```python
import torch

def _softmax(x, axis=-1):
    return torch.softmax(x, dim=axis)

def _gelu(x):
    return torch.nn.functional.gelu(x, approximate='tanh')

def _silu(x):
    return torch.nn.functional.silu(x)

def _sigmoid(x):
    return torch.sigmoid(x)

def _layernorm(x, axis=-1, eps=1e-05):
    m = x.mean(dim=axis, keepdim=True)
    v = x.var(dim=axis, keepdim=True, unbiased=False)
    return (x - m) / torch.sqrt(v + eps)

def _scatter_add(tgt, idx, src, axis=0):
    return tgt.clone().index_add_(axis, idx, src)

def _segment_sum(src, seg, n):
    out = torch.zeros((n,) + src.shape[1:], dtype=src.dtype)
    return out.index_add_(0, seg, src)

def _pad(x, pw):
    flat = tuple((v for pair in reversed(pw) for v in pair))
    return torch.nn.functional.pad(x, flat)
kernel_name = 'adaLN_cond__spectral_fnet__moe_arch_mod__prompt'

def setup_inputs():

    _w = lambda a: (lambda t: t.to(torch.bfloat16) if t.is_floating_point() else t)(torch.as_tensor(a))

    def _A_setup():
        B, S, D = (1, 2048, 1024)
        ns = torch.arange(S)[:, None] * torch.arange(S)[None, :]
        nd = torch.arange(D)[:, None] * torch.arange(D)[None, :]
        Ws_re = torch.cos(-2.0 * torch.pi * ns / S).to(float)
        Ws_im = torch.sin(-2.0 * torch.pi * ns / S).to(float)
        Wd_re = torch.cos(-2.0 * torch.pi * nd / D).to(float)
        Wd_im = torch.sin(-2.0 * torch.pi * nd / D).to(float)
        return {'x': _w((lambda *s: torch.randn(*s, dtype=torch.bfloat16) * 0.1)(B, S, D)), 'dft_seq_re': _w(Ws_re), 'dft_seq_im': _w(Ws_im), 'dft_hid_re': _w(Wd_re), 'dft_hid_im': _w(Wd_im)}
    _A_in = _A_setup()

    def _B_setup():
        B, S, D, F, C = (1, 2048, 1024, 3584, 2)
        return {'x': _w((lambda *s: torch.randn(*s, dtype=torch.bfloat16) * 0.1)(B, S, D)), 'w_r': _w((lambda *s: torch.randn(*s, dtype=torch.bfloat16) * 1.6)(D)), 'w_gate': _w((lambda *s: torch.randn(*s, dtype=torch.bfloat16) * 0.8)(D, F)), 'w_up': _w((lambda *s: torch.randn(*s, dtype=torch.bfloat16) * 0.8)(D, F)), 'w_down': _w((lambda *s: torch.randn(*s, dtype=torch.bfloat16) * 0.8)(F, D))}
    _B_in = _B_setup()
    return {'A_x': _A_in['x'], 'A_dft_seq_re': _A_in['dft_seq_re'], 'A_dft_seq_im': _A_in['dft_seq_im'], 'A_dft_hid_re': _A_in['dft_hid_re'], 'A_dft_hid_im': _A_in['dft_hid_im'], 'B_x': _B_in['x'], 'B_w_r': _B_in['w_r'], 'B_w_gate': _B_in['w_gate'], 'B_w_up': _B_in['w_up'], 'B_w_down': _B_in['w_down'], 'c': _w((lambda *s: torch.randn(*s, dtype=torch.bfloat16) * 0.1)(1, 1024) * 8.0), 'w_mod': _w((lambda *s: torch.randn(*s, dtype=torch.bfloat16) * 0.1)(1024, 6 * 1024) * 1024 ** (-0.5) * 8.0)}

def reference(A_x, A_dft_seq_re, A_dft_seq_im, A_dft_hid_re, A_dft_hid_im, B_x, B_w_r, B_w_gate, B_w_up, B_w_down, c, w_mod):

    def _A_ref(x, dft_seq_re, dft_seq_im, dft_hid_re, dft_hid_im):
        a = torch.einsum('bsd,ed->bse', x, dft_hid_re)
        b = torch.einsum('bsd,ed->bse', x, dft_hid_im)
        y = torch.einsum('ts,bse->bte', dft_seq_re, a) - torch.einsum('ts,bse->bte', dft_seq_im, b)
        return y

    def _B_ref(x, w_r, w_gate, w_up, w_down):
        B, S, D = x.shape
        C = 2
        r = x @ w_r
        idx = torch.argsort(-r, axis=-1)[:, :C]
        idx_e = (lambda x, n, axis: torch.repeat_interleave(x, n, dim=axis))(idx.reshape(B, C, 1), D, axis=2)
        x_sel = (lambda x, i, axis: torch.take_along_dim(x, i, dim=axis))(x, idx_e, axis=1)
        g = _silu(x_sel @ w_gate)
        u = x_sel @ w_up
        y = g * u @ w_down
        r_sel = _sigmoid((lambda x, i, axis: torch.take_along_dim(x, i, dim=axis))(r, idx, axis=1))
        y = y * r_sel.reshape(B, C, 1)
        b_off = (torch.arange(B) * S).reshape(B, 1)
        flat_idx = (idx + b_off).reshape(B * C)
        zero = (x * 0.0).reshape(B * S, D)
        upd = _scatter_add(zero, flat_idx, y.reshape(B * C, D), axis=0)
        return x + upd.reshape(B, S, D)
    x = A_x
    mod = (_silu(c) @ w_mod).reshape(c.shape[0], 6, 1, -1)
    sa, ca, ga, sb, cb, gb = (mod[:, 0].clone(), mod[:, 1].clone(), mod[:, 2].clone(), mod[:, 3].clone(), mod[:, 4].clone(), mod[:, 5].clone())
    h = _layernorm(x, axis=-1) * (1 + ca) + sa
    x = x + ga * _A_ref(h, A_dft_seq_re, A_dft_seq_im, A_dft_hid_re, A_dft_hid_im)
    h = _layernorm(x, axis=-1) * (1 + cb) + sb
    x = x + gb * _B_ref(h, B_w_r, B_w_gate, B_w_up, B_w_down)
    return x
```

```cpp
#include <hip/hip_runtime.h>
#include <hip/hip_bf16.h>
#include <stdint.h>
#include <utility>

#define NEWLAY 1
typedef unsigned short u16;
typedef unsigned long long u64;
using bf16x8 = __attribute__((ext_vector_type(8))) short;
using bf16v8 = __attribute__((ext_vector_type(8))) __bf16;
using f32x4 = __attribute__((ext_vector_type(4))) float;
using f32x2 = __attribute__((ext_vector_type(2))) float;
using u32x4 = __attribute__((ext_vector_type(4))) unsigned;
using u32x2 = __attribute__((ext_vector_type(2))) unsigned;

constexpr int S = 2048, D = 1024, F = 3584;

__device__ __forceinline__ float bf2f(unsigned v) { return __uint_as_float(v << 16); }
__device__ __forceinline__ float bflo(unsigned v) { return __uint_as_float(v << 16); }
__device__ __forceinline__ float bfhi(unsigned v) { return __uint_as_float(v & 0xffff0000u); }
__device__ __forceinline__ unsigned f2bf(float f) {
  __hip_bfloat16 b = __float2bfloat16(f);
  return (unsigned)*reinterpret_cast<u16*>(&b);
}
__device__ __forceinline__ float rbf(float f) { return bf2f(f2bf(f)); }
using bf16v2 = __attribute__((ext_vector_type(2))) __bf16;
__device__ __forceinline__ unsigned pack2(float lo, float hi) {
  return __builtin_bit_cast(unsigned, __builtin_convertvector(f32x2{lo, hi}, bf16v2));
}

template <int CTRL>
__device__ __forceinline__ float dpp_f(float v) {
  return __int_as_float(__builtin_amdgcn_mov_dpp(__float_as_int(v), CTRL, 0xf, 0xf, true));
}
template <int CTRL>
__device__ __forceinline__ float dpp_add(float v) { return v + dpp_f<CTRL>(v); }
__device__ __forceinline__ float row_sum(float v) { return dpp_add<0x140>(dpp_add<0x141>(dpp_add<0x4E>(dpp_add<0xB1>(v)))); }
template <int CTRL>
__device__ __forceinline__ unsigned dpp_u(unsigned v) { return (unsigned)__builtin_amdgcn_mov_dpp((int)v, CTRL, 0xf, 0xf, true); }
__device__ __forceinline__ u64 mk64(unsigned hi, unsigned lo) { return ((u64)hi << 32) | lo; }
__device__ __forceinline__ float swap32_add(float a, float b) {
  auto r = __builtin_amdgcn_permlane32_swap(__float_as_uint(a), __float_as_uint(b), false, false);
  return __uint_as_float(r[0]) + __uint_as_float(r[1]);
}
__device__ __forceinline__ float swap16_add(float a, float b) {
  auto r = __builtin_amdgcn_permlane16_swap(__float_as_uint(a), __float_as_uint(b), false, false);
  return __uint_as_float(r[0]) + __uint_as_float(r[1]);
}
__device__ __forceinline__ float ar4(float v) {
  const float t = swap32_add(v, v);
  return swap16_add(t, t);
}
__device__ __forceinline__ float wave_sum_s(float v) {
  v = row_sum(v);
  v += __int_as_float(__builtin_amdgcn_update_dpp(0, __float_as_int(v), 0x142, 0xa, 0xf, false));
  v += __int_as_float(__builtin_amdgcn_update_dpp(0, __float_as_int(v), 0x143, 0xc, 0xf, false));
  return __int_as_float(__builtin_amdgcn_readlane(__float_as_int(v), 63));
}
__device__ __forceinline__ float silu_bf(float c) { return rbf(c * __builtin_amdgcn_rcpf(1.0f + __expf(-c))); }
__device__ __forceinline__ float dsum(unsigned v, float a) {
  return __builtin_amdgcn_fdot2_f32_bf16(__builtin_bit_cast(bf16v2, v), __builtin_bit_cast(bf16v2, 0x3f803f80u), a, false);
}
template <class Fn, int... I>
__device__ __forceinline__ void sfor_(Fn&& f, std::integer_sequence<int, I...>) { (f(std::integral_constant<int, I>{}), ...); }
template <int N, class Fn>
__device__ __forceinline__ void sfor(Fn&& f) { sfor_(f, std::make_integer_sequence<int, N>{}); }
constexpr unsigned SENT = 0xFFFFFFFFu;
constexpr int NPART = 224;
__device__ __forceinline__ f32x2 unpk(unsigned v) { return f32x2{bflo(v), bfhi(v)}; }

constexpr size_t WS_EPO = 0;
constexpr size_t WS_XFL = 4096;
constexpr size_t WS_MODP = 40960;
constexpr size_t WS_KEYG = 320u << 10;
constexpr size_t WS_UFL = WS_KEYG + 16384;
constexpr size_t WS_MOD = WS_KEYG + (80u << 10);
constexpr size_t WS_RS = WS_KEYG + (92u << 10);
constexpr size_t WS_PART = WS_KEYG + (316u << 10);
constexpr unsigned O_MODP = WS_MODP, O_XFL = WS_XFL, O_KEY = WS_KEYG, O_MOD = WS_MOD, O_HDR = WS_MOD + 8192, O_RS = WS_RS, O_M2 = WS_RS + 80 * 1024;
constexpr unsigned O_PART = WS_PART, O_GU = O_PART + 56 * 8192, O_STAT = O_GU + 224 * 1024, O_X1C = O_STAT + 262144;
constexpr unsigned O_ST2 = O_X1C + 16384;
constexpr unsigned O_FH = 3584u << 10, O_FY = O_FH + 8192;
constexpr unsigned O_GC = O_M2 + 8704, O_VP = O_M2 + 12288, O_NP = O_VP + 65536, O_K2 = O_NP + 65536, O_G2 = 3u << 20, O_G1 = O_G2 + 262144;
static_assert(O_K2 + 4096 == O_PART && O_ST2 + 1024 <= O_KEY + (512 + 768) * 1024, "armed area");
constexpr size_t WS_H = 4u << 20;
constexpr size_t WS_T = WS_H + (size_t)S * D * 2;
constexpr size_t WS_P = WS_T + (size_t)2 * D * S * 2;

#define TSG(base, i)
#define TSD(i)
#define PCAP (1 << 20)
#define PDBG(it)
#define TS(i)
#define TSE()
#define BT(k)
#define BTE()
#define BAR(k) do { BT(k); BARRIER(); } while (0)
__device__ __forceinline__ void st16_wt(void* p, u32x4 v) {
  asm volatile("global_store_dwordx4 %0, %1, off sc1\n\ts_nop 1" ::"v"(p), "v"(v) : "memory");
}
#define WAIT_V(n) asm volatile("s_waitcnt vmcnt(%0)" ::"n"(n) : "memory")
#define WAIT_L(n) asm volatile("s_waitcnt lgkmcnt(%0)" ::"n"(n) : "memory")
#define BARRIER() asm volatile("s_barrier" ::: "memory")
#define LBAR() asm volatile("s_waitcnt lgkmcnt(0)\n\ts_barrier" ::: "memory")
#define DSR(dst, addr, off) asm volatile("ds_read_b128 %0, %1 offset:%2" : "=v"(dst) : "v"(addr), "n"(off) : "memory")
#define MFMA(c, a, b) asm volatile("v_mfma_f32_16x16x32_bf16 %0, %1, %2, %0" : "+v"(c) : "v"(a), "v"(b))
#define MFMA0(c, a, b) asm volatile("v_mfma_f32_16x16x32_bf16 %0, %1, %2, 0" : "=&v"(c) : "v"(a), "v"(b))
#define GLDS(m0v, voff, gbase) \
  asm volatile("s_mov_b32 m0, %0\n\ts_nop 0\n\tglobal_load_lds_dwordx4 %1, %2" ::"s"(m0v), "v"(voff), "s"(gbase) : "memory")
#define LDG(dst, voff, base) asm volatile("global_load_dwordx4 %0, %1, %2" : "=v"(dst) : "v"(voff), "s"(base) : "memory")
#define WPOL " nt"
#define LDM(dst, voff, base) asm volatile("global_load_dwordx4 %0, %1, %2" WPOL : "=v"(dst) : "v"(voff), "s"(base) : "memory")
#define LDX(dst, voff, base, off) asm volatile("global_load_dwordx4 %0, %1, %2 offset:%3" : "=v"(dst) : "v"(voff), "s"(base), "n"(off) : "memory")
#define LDXS(dst, voff, base, off) asm volatile("global_load_dwordx4 %0, %1, %2 offset:%3 sc1" : "=v"(dst) : "v"(voff), "s"(base), "n"(off) : "memory")
#define BF8(v) __builtin_bit_cast(bf16v8, v)
#define LSPIN(v, p, cond)                                   \
  _Pragma("unroll 1") for (int it_ = 0; it_ < (1 << 26); ++it_) { \
    v = __atomic_load_n(p, __ATOMIC_RELAXED);               \
    if (cond) break;                                        \
    __builtin_amdgcn_s_sleep(1);                            \
  }
#define POLL(ok, sl, reload)                                                  \
  if (__builtin_expect(!(ok), 0)) {                                           \
    _Pragma("unroll 1") for (int it = 0; it < PCAP; ++it) {                   \
      PDBG(it);                                                               \
      __builtin_amdgcn_s_sleep(sl);                                           \
      reload;                                                                 \
      if (ok) break;                                                          \
    }                                                                         \
  }
#define POLL2(bad, sl, cap, load)                                             \
  {                                                                           \
    int it = (cap);                                                           \
    u64 live;                                                                 \
    _Pragma("unroll 1") do {                                                  \
      if (sl) __builtin_amdgcn_s_sleep(sl);                                   \
      asm volatile("s_sub_u32 %0, %0, 1\n\ts_cselect_b64 %1, 0, -1" : "+s"(it), "=s"(live)::"scc"); \
      load;                                                                   \
    } while (__builtin_amdgcn_ballot_w64(bad) & live);                        \
    PDBG(PCAP - 2 - it);                                                      \
  }
__device__ __forceinline__ unsigned tick() {
  u64 t;
  asm volatile("s_memrealtime %0\n\ts_waitcnt lgkmcnt(0)" : "=s"(t)::"memory");
  return (unsigned)t;
}
__device__ __forceinline__ void wait_tick(unsigned t) {
#pragma unroll 1
  for (int i = 0; i < 256; ++i)
    if ((int)(tick() - t) >= 0) break;
}
__device__ __forceinline__ void nap_tick(unsigned t) {
#pragma unroll 1
  for (int i = 0; i < 1024; ++i) {
    if ((int)(tick() - t) >= 0) break;
    __builtin_amdgcn_s_sleep(2);
  }
}
#define T_C1 245
#define T_A 40
#define T_N 2500
#define T_D1 78
#define T_C2 322
#define VW0 "\n\ts_waitcnt vmcnt(0)"
#define W8(a) "+v"(a[0]), "+v"(a[1]), "+v"(a[2]), "+v"(a[3]), "+v"(a[4]), "+v"(a[5]), "+v"(a[6]), "+v"(a[7])
#define PFL(ptr) asm volatile("s_mov_b32 m0, %0\n\ts_nop 0\n\tglobal_load_lds_dword %1, off" ::"s"(pfl), "v"(ptr) : "memory")

#define HHOOK(j)
#define XPV 1557
#define XPA0 (XPV / 1000)
#define XPA1 (XPV / 100 % 10)
#define XPB0 (XPV / 10 % 10)
#define XPB1 (XPV % 10)
#define U_X0 6
#define XLD(dst, XP, XH, i)                                                                                   \
  do {                                                                                                        \
    if (MODE) LDXS(dst, (i) ? xo1 : xo0, XP, ((XH)&3) * 2048 - 4096);                                         \
    else LDXS(dst, ((XH)&4) ? xo1 : xo0, XP, ((XH)&3) * 2048 + (i)*1024 - 4096);                              \
  } while (0)
#define HALF(XC, YA, RD, XN, XP, XH, LD, YP)                                                 \
  do {                                                                                       \
    _Pragma("unroll") for (int j = 0; j < 8; ++j) {                                          \
      if ((j & 1) == 0) { if (RD) WAIT_L(5); else WAIT_L(j == 0 ? 5 : 6 - j); }              \
      if (MODE == 2) MFMA0(acc[0][j], Y[j], XC[0]); else if (MODE) MFMA(acc[0][j], Y[j], XC[0]); else MFMA(acc[0][j], XC[0], Y[j]); \
      if (j == 0) DSR(Y[7], YP, 7 * 2048);                                                   \
      else if (RD) DSR(Y[j - 1], YA, (j - 1) * 2048);                                        \
      if (MODE == 2) MFMA0(acc[1][j], Y[j], XC[1]); else if (MODE) MFMA(acc[1][j], Y[j], XC[1]); else MFMA(acc[1][j], XC[1], Y[j]); \
      if ((LD) && j == (((XH)&1) ? XPA0 : XPB0)) XLD(XN[0], XP, XH, 0);                                             \
      if ((LD) && j == (((XH)&1) ? XPA1 : XPB1)) XLD(XN[1], XP, XH, 1);                                             \
      HHOOK(j);                                                                              \
    }                                                                                        \
  } while (0)

__device__ __forceinline__ int key_row(unsigned k) { return (int)((~k >> 1) & 2047u); }
__device__ __forceinline__ unsigned mkkey(float v) {
  const unsigned rb = pack2(0.f, v);
  return rb ^ ((unsigned)((int)rb >> 31) & 0x7fff0000u) ^ 0x80000000u;
}
__device__ __forceinline__ float key_val(unsigned k) {
  const unsigned ord = k >> 16;
  return bf2f(ord ^ ((ord & 0x8000u) ? 0x8000u : 0xffffu));
}
#define OPT_RD 22
#define OPT_RE 20
#define OPT_GD 2
__device__ __forceinline__ unsigned nsent(float v) { return min(__float_as_uint(v), SENT - 1u); }
__device__ __forceinline__ f32x4 lo4(u32x4 q) { return f32x4{bflo(q[0]), bfhi(q[0]), bflo(q[1]), bfhi(q[1])}; }
__device__ __forceinline__ f32x4 hi4(u32x4 q) { return f32x4{bflo(q[2]), bfhi(q[2]), bflo(q[3]), bfhi(q[3])}; }
__device__ __forceinline__ u32x4 pk8(f32x4 a, f32x4 b) { return u32x4{pack2(a[0], a[1]), pack2(a[2], a[3]), pack2(b[0], b[1]), pack2(b[2], b[3])}; }
__device__ __forceinline__ u32x4 nsent4(u32x4 v) { return u32x4{min(v[0], SENT - 1u), min(v[1], SENT - 1u), min(v[2], SENT - 1u), min(v[3], SENT - 1u)}; }
__device__ __forceinline__ unsigned max4(u32x4 v) { return max(max(v[0], v[1]), max(v[2], v[3])); }
__device__ __forceinline__ u32x4 selu(unsigned mk, u32x4 a, u32x4 b) { return (a & mk) | (b & ~mk); }
#define KSTR 16
#define TOP2(CTRL)                                                  \
  do {                                                              \
    const unsigned o1 = dpp_u<CTRL>(t1), o2 = dpp_u<CTRL>(t2);      \
    t2 = max(min(t1, o1), max(t2, o2));                             \
    t1 = max(t1, o1);                                               \
  } while (0)
#define TOP2S(SW)                                                   \
  do {                                                              \
    auto a_ = SW(t1, t1, false, false), b_ = SW(t2, t2, false, false); \
    t2 = max(min(a_[0], a_[1]), max(b_[0], b_[1]));                 \
    t1 = max(a_[0], a_[1]);                                         \
  } while (0)
#define T_C3 253
#define T_C4 340
#define T_G 9
#define T_U 10
#define E_D 6
#define O_DW 6
#define O_DS 4
#define O_TR 1
#define OPOL " sc1"
#define L_PR 1
#define D_W 1
#define X_PA 2
#define G1_C 2
#define R_AT 23
#define B_TK 8
#define W_T 4
#define V_K 3
#define V_T 5
#define N_K 10
#define N_D 2
#define T_K 0
#define H_AT 23
#define G1_S 1
#define V_F 0
#define J_L 0
#define F_T 128
#define K_S 1
#define K_R 1
#define T_H 60
#define H_P 2
#define T_X 80
#define D_S 1
#define A_PW 8
#define A_PS 6
#define U_S 4
#define C_LD 2
#define C_DP 8
#define F_D 2
#define P_AT 2
#define F_AT 29
#define XQ_N 6
#define RD_E 12
#define PW0 2
#define PW1 1
#define T_W 70
#define P_D2 1
#define P_D4 1
#define XSTR 16
#define P_D3 20
#ifndef M_NS
#define M_NS 6
#endif
#define A_Q1 27
#define B_RAT 7
#define B_MAT 13
#define U_D 4
#define FSTR 16
#define B_J 4
#define RETIRE()                                                                                                                  \
  do {                                                                                                                            \
    if (hw == 3 && lane == 0) {                                                                                                   \
      __hip_atomic_store(reinterpret_cast<u64*>(xfl + b * XSTR), ~tag, __ATOMIC_RELAXED, __HIP_MEMORY_SCOPE_AGENT);               \
      if (b == 255) __hip_atomic_store(reinterpret_cast<u64*>(epo), tag, __ATOMIC_RELAXED, __HIP_MEMORY_SCOPE_AGENT);             \
    }                                                                                                                             \
  } while (0)
#define U_N1 3
#define OPT_RR 2
#define B_QAT 8
#define B_QD 4
#define TP_PS 5
#define TP_XC 20
#define TP_P0 2
#define TP_X0 24
#define TP_XL 52
#define W_UO 3
#define CPF_K 24
#define CPF_B 8
#define B_WN 4
#define B_WK 15
struct BSched {
  int cnt[16], rcnt, mcnt, qcnt, vcnt, ncnt;
  constexpr BSched() : cnt{}, rcnt(0), mcnt(0), qcnt(0), vcnt(0), ncnt(0) {
    int pos[16] = {}, n = 0, rpos = 0, mpos = 0, qpos = 0, npos = 0;
    for (int k = 0; k <= B_J - 2 + M_NS; ++k) pos[k] = -8 * (B_J - 2 + M_NS - k);
    for (int k = B_J + 1; k < 16; ++k) {
      cnt[k] = n - pos[k] > 63 ? 63 : n - pos[k];
      if (k - 2 + M_NS < 16) { n += 4; pos[k - 2 + M_NS] = n; }
      if (k == B_RAT + 2) rcnt = n - rpos;
      if (k == B_QAT + B_QD) qcnt = n - qpos;
      if (k == V_T) vcnt = n;
      if (k == N_K + N_D) ncnt = n - npos;
      if (k == N_K) npos = n;
      if (k == B_RAT) { n += 2; rpos = n; }
      if (k == B_QAT) { n += 3; qpos = n; }
      if (k == B_MAT) { n += 3; mpos = n; }
      if (k == B_WK) n += B_WN;
    }
    mcnt = n - mpos > 63 ? 63 : n - mpos;
  }
};
__global__ __launch_bounds__(512, 2) void k_main(const u16* __restrict__ x, char* ws, const u16* __restrict__ Y0, const u16* __restrict__ Y1,
                                              const u16* __restrict__ cvec, const u16* __restrict__ wmod, const u16* __restrict__ X0,
                                              const u16* __restrict__ X1, const u16* __restrict__ wr_, const u16* __restrict__ wg,
                                              const u16* __restrict__ wu, const u16* __restrict__ wd, u16* __restrict__ out) {
  constexpr int DX = 4, NS = M_NS, STG = NS * 16384, SPARE = STG + 32768;
  constexpr int LST = SPARE + 17664, SLT = LST + 256, XC = SLT + 512, VT = XC + 4096;
  __shared__ __attribute__((aligned(1024))) char shm[VT + 256];
  u16(*svb)[1024] = reinterpret_cast<u16(*)[1024]>(shm + SPARE);
  float* cwj = reinterpret_cast<float*>(shm + SPARE + 10240);
  unsigned* ssel = reinterpret_cast<unsigned*>(shm + SPARE + 10368);
  u16(*acts)[64] = reinterpret_cast<u16(*)[64]>(shm + SPARE + 15872);
  float* smod = reinterpret_cast<float*>(shm + SPARE + 15872);
  u16* ssa = reinterpret_cast<u16*>(smod);
  constexpr int TAB = SPARE + 10752, RKA = TAB + 1536, RKB = TAB + 3584;
  u16(*scw)[128] = reinterpret_cast<u16(*)[128]>(shm + TAB + 512);
  static_assert(42 % NS == 0, "slots 0 and 1 are free behind GEMM_B barrier 13");
  const int b = blockIdx.x, xcd = b & 7, j8 = b >> 3;
  const int cc = xcd >> 2, nt = (xcd & 3) * 4 + (j8 & 3), mt = j8 >> 2, et = mt;
  const int tid = threadIdx.x, lane = tid & 63;
  const int wid = __builtin_amdgcn_readfirstlane(tid >> 6);
  const unsigned lds0 = (unsigned)(size_t)(__attribute__((address_space(3))) char*)shm;
  const char* cpf;
  {
    unsigned long long pc;
    unsigned len;
    asm volatile("s_getpc_b64 %0\n.Lcpf_here:\n\ts_mov_b32 %1, .Lcpf_end-.Lcpf_here-4\n\t.subsection 1\n.Lcpf_end:\n\t.subsection 0" : "=s"(pc), "=s"(len));
    cpf = reinterpret_cast<const char*>(pc) + min((unsigned)((lane * 32 + j8) * 128), len);
  }
  const unsigned pfl = lds0 + SPARE + 17408;
  if (wid == 4 && lane < 12) PFL(cpf);
  u32x4 xv[2];
  {
    const unsigned xo = (unsigned)((b * 8 + wid) * D + lane * 8) * 2;
    asm volatile("global_load_dwordx4 %0, %2, %3\n\tglobal_load_dwordx4 %1, %2, %3 offset:1024" : "=&v"(xv[0]), "=&v"(xv[1]) : "v"(xo), "s"(x) : "memory");
  }
  asm volatile("" ::"s"(ws), "s"(Y0), "s"(Y1), "s"(cvec), "s"(wmod), "s"(wr_));
  u16* xn = reinterpret_cast<u16*>(ws + WS_H);
  u16* U = reinterpret_cast<u16*>(ws + WS_T);
  unsigned* part = reinterpret_cast<unsigned*>(ws + WS_PART);
  const int g = (b & 7) * 28 + (b >> 3);
  unsigned* xch = reinterpret_cast<unsigned*>(ws + WS_P);
  unsigned* ufl = reinterpret_cast<unsigned*>(ws + WS_UFL) + (cc * 16 + nt) * 32 * FSTR;
  unsigned* xfl = reinterpret_cast<unsigned*>(ws + WS_XFL);
  unsigned* epo = reinterpret_cast<unsigned*>(ws + WS_EPO);
#define SKEWPT()
  const int g4 = lane >> 4;
  u64 tag0, tag;
#define TAGW(n) asm volatile("s_waitcnt vmcnt(%2)\n\tv_lshl_add_u64 %0, %1, 0, 1" : "=v"(tag) : "v"(tag0), "n"(n) : "memory")
  u32x4 wv[8], cq;
  const char* pfy = reinterpret_cast<const char*>(cc ? Y1 : Y0) + (size_t)((xcd & 3) * 512 + j8 * 16 + (lane & 15)) * 4096 + g4 * 128;
#define PFG(g) (pfy + ((((g) + xcd) & 7) << 9))
  const int u = b * 2 + (wid & 1), cg0 = u & 31, ks0 = u >> 5;
  unsigned ta = lds0 + STG + ((lane >> 2) & 3) * 2368 + (wid * 128 + (lane >> 5) * 32 + (lane & 3) * 8 + (g4 & 1) * 4) * 2;
  unsigned oo = (unsigned)((wid * 4 + (lane >> 5)) * 131072 + (b >> 2) * 2048 + (g4 & 1) * 1024 + ((b & 3) * 16 + (lane & 15)) * 16), oo2 = oo + 262144;
  asm volatile("" : "+v"(ta), "+v"(oo), "+v"(oo2));
  unsigned wa = lds0 + STG + wid * 2368 + lane * 16;
  asm volatile("" : "+v"(wa));
  const unsigned wo = (unsigned)((ks0 * 64 + (lane >> 3) * 8) * 6144 + 2048 + (cg0 >> 4) * 1024 + cg0 * 64 + (lane & 7) * 8) * 2;
  if (wid == 0) TSG(part, 0);
  BT(50);
  if (wid >= 3) {
    asm volatile("global_load_dwordx2 %0, %1, off sc1" : "=v"(tag0) : "v"(epo) : "memory");
    if (wid >= 6) {
      LDG(cq, (ks0 * 64 + g4 * 16 + (lane & 1) * 8) * 2, cvec);
#pragma unroll
      for (int i = 0; i < 8; ++i) LDM(wv[i], wo + i * 12288, wmod);
      const unsigned av = u < 80 ? 0u : ~0u;
      asm volatile("global_store_dwordx4 %0, %1, %2 sc1\n\ts_nop 1" ::"v"(O_KEY + u * 1024 + lane * 16), "v"(u32x4{av, av, av, av}), "s"(ws) : "memory");
      WAIT_V(11);
    } else if (wid == 4) {
      __builtin_amdgcn_s_sleep(C_DP);
      PFL(PFG(0));
      WAIT_V(2);
    } else {
      WAIT_V(1);
    }
  } else {
    WAIT_V(0);
  }
  {
    asm volatile("" : "+v"(xv[0]), "+v"(xv[1])::"memory");
    if (wid == 1) TSG(part, 2);
    BT(51);
    const float s0 = dsum(xv[0][3], dsum(xv[0][2], dsum(xv[0][1], dsum(xv[0][0], 0.f))));
    const float s1 = dsum(xv[1][3], dsum(xv[1][2], dsum(xv[1][1], dsum(xv[1][0], 0.f))));
    const float mf = wave_sum_s(s0 + s1) * (1.0f / D), m = rbf(mf);
    const f32x4 d0 = lo4(xv[0]) - m, d1 = hi4(xv[0]) - m, d2 = lo4(xv[1]) - m, d3 = hi4(xv[1]) - m;
    const f32x4 q = (d0 * d0 + d1 * d1) + (d2 * d2 + d3 * d3);
    const float vf = wave_sum_s((q[0] + q[1]) + (q[2] + q[3])) * (1.0f / D) - (mf - m) * (mf - m);
    const float rden = __builtin_amdgcn_rcpf(rbf(__builtin_amdgcn_sqrtf(rbf(rbf(vf) + 1e-5f))));
    asm volatile("ds_write_b128 %0, %1" ::"v"(wa), "v"(pk8(d0 * rden, d1 * rden)) : "memory");
    asm volatile("ds_write_b128 %0, %1 offset:1024" ::"v"(wa), "v"(pk8(d2 * rden, d3 * rden)) : "memory");
    if (wid == 1) TSG(part, 12);
    BT(52);
    LBAR();
    if (wid == 1) TSG(part, 10);
    BT(53);
    u32x2 t[4];
    asm volatile(
        "ds_read_b64_tr_b16 %0, %4\n\tds_read_b64_tr_b16 %1, %4 offset:%5\n\t"
        "ds_read_b64_tr_b16 %2, %4 offset:128\n\tds_read_b64_tr_b16 %3, %4 offset:%6\n\ts_waitcnt lgkmcnt(0)"
        : "=&v"(t[0]), "=&v"(t[1]), "=&v"(t[2]), "=&v"(t[3])
        : "v"(ta), "n"(4 * 2368), "n"(4 * 2368 + 128)
        : "memory");
    asm volatile("global_store_dwordx4 %0, %2, %4 sc1\n\tglobal_store_dwordx4 %1, %3, %4 sc1\n\ts_nop 1" ::"v"(oo), "v"(oo2),
                 "v"(u32x4{t[0][0], t[0][1], t[1][0], t[1][1]}), "v"(u32x4{t[2][0], t[2][1], t[3][0], t[3][1]}), "s"(xn)
                 : "memory");
    if (wid == 2) TSG(part, 14);
    BT(54);
  }
  if (wid < 4) {
    const unsigned ya0 = lds0 + (lane & 15) * 128 + (g4 ^ (lane & 7)) * 16;
    bf16x8 X[DX][2][2], Y[8];
    f32x4 acc[2][8];
    {
      constexpr int MODE = 0;
      const unsigned xo0 = lane * 16 + 4096, xo1 = xo0 + 8192;
      const char* xb = reinterpret_cast<const char*>(xn) + (size_t)(mt * 4 + wid) * 131072;
#define XB(i) (xb + (((xcd + (i)) & 7) << 14))
#pragma unroll
      for (int i = 0; i < 2; ++i)
#pragma unroll
        for (int j = 0; j < 8; ++j) acc[i][j] = f32x4{0.f, 0.f, 0.f, 0.f};
      asm volatile("" : W8(acc[0]), W8(acc[1]));
      __builtin_amdgcn_s_setprio(1);
      WAIT_V(0);
      if (wid == 0) TSG(part, 1);
      const char* xpb = XB(0);
      BAR(55);
      if (wid == 3) {
        TAGW(0);
        const unsigned* fp = xfl + ((xcd * 32 + lane) & 255) * XSTR;
        if (P_D3) __builtin_amdgcn_s_sleep(P_D3);
#pragma unroll 1
        for (int it = 0; it < PCAP; ++it) { PDBG(it);
          u64 f;
          asm volatile("global_load_dwordx2 %0, %1, off sc1\n\ts_waitcnt vmcnt(0)" : "=&v"(f) : "v"(fp) : "memory");
          if (__all(f == tag)) break;
          __builtin_amdgcn_s_sleep(P_D4);
        }
      }
      BAR(0);
      if (wid == 1) TSG(part, 3);
#pragma unroll
      for (int h = 0; h < 7; ++h) {
        XLD(X[h >> 1][h & 1][0], xpb, h, 0);
        XLD(X[h >> 1][h & 1][1], xpb, h, 1);
      }
#pragma unroll
      for (int j = 0; j < 7; ++j) DSR(Y[j], ya0, j * 2048);
      unsigned so = 0, a0 = ya0;
      f32x4 c0, c1;
#pragma unroll 1
      for (int it = 0; it < 7; ++it) {
        const char* xpa = xpb;
        xpb = XB(it + 1);
#pragma unroll
        for (int s = 0; s < 4; ++s) {
          const unsigned a1 = (ya0 ^ 64) + so;
          so = so == (NS - 1) * 16384 ? 0 : so + 16384;
          WAIT_V(12);
          HALF(X[s][0], a1, true, X[(s + 3) & 3][1], (s == 0 ? xpa : xpb), (2 * s + 7) & 7, true, a0);
          a0 = ya0 + so;
          BAR(1 + it * 4 + s);
          WAIT_V(12);
          HALF(X[s][1], a0, true, X[s][0], xpb, 2 * s, true, a1);
        }
      }
#pragma unroll
      for (int s = 0; s < 4; ++s) {
        const unsigned a1 = (ya0 ^ 64) + so;
        so = so == (NS - 1) * 16384 ? 0 : so + 16384;
        WAIT_V(s == 0 ? 12 : 14 - 4 * s);
        HALF(X[s][0], a1, true, X[3][1], xpb, 7, s == 0, a0);
        a0 = ya0 + so;
        if (s < 3) BAR(29 + s);
        WAIT_V(12 - 4 * s);
        if (s == 2) {
          DSR(c0, lds0 + SPARE + 15872 + 512 + wid * 128 + g4 * 32, 0);
          DSR(c1, lds0 + SPARE + 15872 + 512 + wid * 128 + g4 * 32, 16);
        }
        if (s < 3) HALF(X[s][1], a0, true, X[s][0], xpb, 0, false, a1);
      }
      const char* up = reinterpret_cast<const char*>(U) + (size_t)cc * 4194304 + (size_t)(nt * 4) * 65536 + (mt * 4 + wid) * 2048;
      const unsigned ul = lds0 + STG + wid * 1024 + lane * 16;
#define UEP(j)                                                                                                                             \
  do {                                                                                                                                     \
    u32x4 v_;                                                                                                                              \
    asm volatile("v_pk_mul_f32 %0, %0, %4\n\tv_pk_mul_f32 %1, %1, %5\n\tv_pk_mul_f32 %2, %2, %6\n\tv_pk_mul_f32 %3, %3, %7"            \
                 : "+v"(acc[0][j].lo), "+v"(acc[0][j].hi), "+v"(acc[1][j].lo), "+v"(acc[1][j].hi)                                          \
                 : "v"(c0.lo), "v"(c0.hi), "v"(c1.lo), "v"(c1.hi));                                                                        \
    asm volatile("v_cvt_pk_bf16_f32 %0, %4, %5\n\tv_cvt_pk_bf16_f32 %1, %6, %7\n\tv_cvt_pk_bf16_f32 %2, %8, %9\n\tv_cvt_pk_bf16_f32 %3, %10, %11" \
                 : "=&v"(v_[0]), "=&v"(v_[1]), "=&v"(v_[2]), "=&v"(v_[3])                                                                  \
                 : "v"(acc[0][j][0]), "v"(acc[0][j][1]), "v"(acc[0][j][2]), "v"(acc[0][j][3]), "v"(acc[1][j][0]), "v"(acc[1][j][1]),       \
                   "v"(acc[1][j][2]), "v"(acc[1][j][3]));                                                                                  \
    asm volatile("global_store_dwordx4 %0, %2, %3 offset:%4 sc1\n\tds_write_b128 %1, %2 offset:%5\n\ts_nop 0"                          \
                 ::"v"(lane * 16), "v"(ul), "v"(v_), "s"(up + ((j) >> 1) * 65536), "n"(((j)&1) * 1024), "n"((j)*4096) : "memory");         \
  } while (0)
      {
        const unsigned a1 = (ya0 ^ 64) + (31 % NS) * 16384;
        DSR(Y[7], a1, 7 * 2048);
        WAIT_L(6);
#pragma unroll
        for (int j = 0; j < 8; ++j) {
          if (j == 2) WAIT_L(0);
          MFMA(acc[0][j], X[3][1][0], Y[j]);
          MFMA(acc[1][j], X[3][1][1], Y[j]);
          if (j >= 2) UEP(j - 2);
        }
      }
      if (wid == 0) TSG(part, 4);
      TS(0);
      SKEWPT();
      asm volatile("s_nop 3");
#pragma unroll
      for (int j = 6; j < 8; ++j) UEP(j);
    }
    const int rg = nt * 8 + wid * 2 + cc, row0 = rg * 16 + (lane & 15), col0 = et * 128 + g4 * 8;
    u32x4 xq[4], pv[4], sa4[4];
    f32x4 ga[8];
    f32x2 cj;
    float sw, ce;
    u32x4 dk;
    unsigned spo, soo, th, slt, slf, sla, tha;
    u32x2 se;
    const char* xr = reinterpret_cast<const char*>(xch + (b * 4 + wid) * 1024);
#define XCHLD()                                                                                                     \
  asm volatile(                                                                                                     \
      "global_load_dwordx4 %0, %4, %5 sc1\n\tglobal_load_dwordx4 %1, %4, %5 offset:1024 sc1\n\t"                   \
      "global_load_dwordx4 %2, %4, %5 offset:2048 sc1\n\tglobal_load_dwordx4 %3, %4, %5 offset:3072 sc1"            \
      : "=&v"(pv[0]), "=&v"(pv[1]), "=&v"(pv[2]), "=&v"(pv[3])                                                      \
      : "v"(lane * 16), "s"(xr)                                                                                     \
      : "memory")
    {
      constexpr int MODE = 1;
      const char* ub = reinterpret_cast<const char*>(U) + (size_t)cc * 4194304 + (size_t)(nt * 4 + wid) * 65536;
      const unsigned xo0 = lane * 16 + cc * 1024 + 4096, xo1 = xo0 ^ 1024;
      BT(32);
      LBAR();
      if (wid == 1) TSG(part, 6);
      {
        const unsigned sa = lds0 + STG + wid * 8192 + lane * 16 + cc * 4096;
#pragma unroll
        for (int q = 0; q < 8; ++q) DSR(X[(q >> 1) & 1][q & 1][q >> 2], (q >> 2) ? sa ^ 4096 : sa, (q & 3) * 1024);
        const unsigned a0 = ya0 + (32 % NS) * 16384;
#pragma unroll
        for (int j = 0; j < 7; ++j) DSR(Y[j], a0, j * 2048);
      }
#define UFLAG()                                                                                                         \
  do {                                                                                                                  \
    WAIT_V(0);                                                                                                          \
    if (lane == 0) __hip_atomic_store(ufl + (mt * 4 + wid) * FSTR, 1u, __ATOMIC_RELAXED, __HIP_MEMORY_SCOPE_AGENT);     \
    if (wid == 0) TSG(part, 5);                                                                                         \
  } while (0)
#define XP(h) (ub + (((mt + ((h) >> 2)) & 7) << 13))
#define XS(h) X[((h) >> 1) & 3][(h)&1]
#define XW(h) do { if ((h) >= 4) WAIT_V(2 * (((h) + 6 > 31 ? 31 : (h) + 6) - (h)) + ((h) >= 25 + XQ_N / 8 ? 4 : 0)); } while (0)
      constexpr int H0 = 24, NP = 64, NE = 2 * NP + 8;
#pragma unroll
      for (int s = 0; s < H0 / 2; ++s) {
        const unsigned a0 = ya0 + ((32 + s) % NS) * 16384, a1 = a0 ^ 64, an = ya0 + ((33 + s) % NS) * 16384;
        XW(2 * s);
        if (s == 0) {
          constexpr int MODE = 2;
          HALF(XS(0), a1, true, XS(7), XP(7), 7, false, a0);
          UFLAG();
        } else {
          HALF(XS(2 * s), a1, true, XS(2 * s + 7), XP(2 * s + 7), 2 * s + 7, 2 * s >= 4, a0);
        }
        if (s == 1 && wid == 2) TSD(12);
        BAR(33 + s);
        if (s == 1) {
          if (wid == 1) TSG(part, 7);
          TS(4);
#pragma unroll
          for (int h = 4; h < U_X0; ++h) {
            XLD(XS(h)[0], XP(h), h, 0);
            XLD(XS(h)[1], XP(h), h, 1);
          }
        }
        XW(2 * s + 1);
#undef HHOOK
#define HHOOK(j) if (s == 1 && j + 3 >= U_X0) { XLD(XS(j + 3)[0], XP(j + 3), j + 3, 0); XLD(XS(j + 3)[1], XP(j + 3), j + 3, 1); }
        HALF(XS(2 * s + 1), an, true, XS(2 * s + 8), XP(2 * s + 8), 2 * s + 8, 2 * s + 1 >= 5, a1);
#undef HHOOK
#define HHOOK(j)
      }
      const char* xw = reinterpret_cast<const char*>(xch + ((b ^ 4) * 4 + wid) * 1024);
      const unsigned xoff = (unsigned)(row0 * D + col0) * 2;
      const unsigned rka = lds0 + RKA + (lane & 15) * 16, rkb = lds0 + RKB + (g4 ? 1024 : wid * 256 + lane * 16);
      const unsigned ea = lds0 + TAB + g4 * 32, eb = lds0 + TAB + 512 + (lane & 3) * 256 + g4 * 16;
      u32x4 ov;
      bf16x8 xk;
#pragma unroll
      for (int q = 0; q < 4; ++q) dk[q] = (lane & 3) == q ? ~0u : 0u;
      spo = O_STAT + rg * 2048 + g4 * 512 + (lane & 15) * 16, soo = O_STAT + rg * 2048 + et * 256 + (lane & 15) * 16;
      unsigned cwa = lds0 + SPARE + 10240 + g4 * 8, cwu = lds0 + SPARE + 10240 + et * 4;
      {
        const unsigned one = g4 == ((lane >> 2) & 3) ? ((lane & 1) ? 0x3f800000u : 0x3f80u) : 0u;
        se = u32x2{(lane & 2) ? 0u : one, (lane & 2) ? one : 0u};
        asm volatile("" : "+v"(se));
      }
      tha = lds0 + SPARE + 10368 + 24;
      sla = lds0 + SLT + (row0 & 127) * 4;
      asm volatile("" : "+v"(dk), "+v"(spo), "+v"(soo), "+v"(cwa), "+v"(cwu), "+v"(tha), "+v"(sla));
#define EH(e) (H0 + (((e) < NP ? (e) : (e) - NP - 8) >> 3))
#define EA(e) ((ya0 ^ ((EH(e) & 1) * 64)) + ((32 + (EH(e) >> 1)) % NS) * 16384)
      sfor<NE>([&](auto nn) __attribute__((always_inline)) {
        constexpr int n = decltype(nn)::value, h = EH(n), j = n & 7, m = n - NP - TP_P0, e = n + 7, z = n - (NE - 8);
        (void)sa4, (void)eb, (void)ga, (void)ea, (void)xk, (void)rka, (void)rkb, (void)xq, (void)xoff, (void)ov, (void)xw, (void)x;
        (void)cj, (void)sw, (void)ce, (void)cwa, (void)cwu, (void)th, (void)tha, (void)slt, (void)sla, (void)slf;
        if (n == 0) TS(2);
        if (n == NP) { TS(3); SKEWPT(); }
        if (n < NP && j == 0) {
          if ((h & 1) && h < 31) BAR(33 + (h >> 1));
          XW(h);
        }
        if (n == NP + TP_X0) { TS(14); BAR(48); TS(11); }
        if (n == NP + TP_XL) XCHLD();
        if constexpr ((j & 1) == 0) WAIT_L(z <= 0 ? 5 : 6 - z + 2 * z);
        if constexpr (n < NP) MFMA(acc[1][j], Y[j], XS(h)[1]);
        else if constexpr (n < NP + 8) MFMA(acc[0][j], Y[j], xk);
        else MFMA(acc[0][j], Y[j], XS(h)[0]);
        if constexpr (n == NP - 16) DSR(xk, rkb, 0);
        if constexpr (e >= NP && e < NP + 8) DSR(Y[e & 7], rka, (e & 7) * 256);
        else if constexpr (e < NE) DSR(Y[e & 7], EA(e), (e & 7) * 2048);
        if constexpr (n == 2 || n == 4) XLD(XS(31)[(j >> 1) - 1], XP(31), 31, (j >> 1) - 1);
        if constexpr (n == XQ_N) {
#pragma unroll
          for (int q = 0; q < 4; ++q) LDX(xq[q], xoff, x, q * 64);
        }
        if constexpr (m >= 0 && m < 4 * TP_PS) {
          constexpr int q = m / TP_PS, r = m % TP_PS;
#pragma unroll
          for (int u = (r * 9 + TP_PS - 1) / TP_PS; u < ((r + 1) * 9 + TP_PS - 1) / TP_PS; ++u) {
            if (u < 4) asm volatile("v_cvt_pk_bf16_f32 %0, %1, %2" : "=v"(ov[u & 3]) : "v"(acc[1][2 * q + ((u >> 1) & 1)][(u & 1) * 2]), "v"(acc[1][2 * q + ((u >> 1) & 1)][(u & 1) * 2 + 1]));
            else if (u < 8) asm volatile("v_min_u32 %0, -2, %0" : "+v"(ov[u & 3]));
            else asm volatile("global_store_dwordx4 %0, %1, %2 offset:%3 sc1\n\ts_nop 1" ::"v"(lane * 16), "v"(ov), "s"(xw), "n"(q * 1024) : "memory");
          }
          if (m == 4 * TP_PS - 1) TS(10);
        }
        if constexpr (m == TP_XC) asm volatile("s_waitcnt vmcnt(%4)" : "+v"(xq[0]), "+v"(xq[1]), "+v"(xq[2]), "+v"(xq[3]) : "n"(n >= NP + TP_XL ? 8 : 4) : "memory");
        if constexpr (m >= TP_XC && m < TP_XC + 32) {
          constexpr int c = m - TP_XC;
          if constexpr (c & 1) asm volatile("v_and_b32 %0, 0xffff0000, %1" : "=v"(acc[1][c >> 2][c & 3]) : "v"(xq[c >> 3][(c >> 1) & 3]));
          else asm volatile("v_lshlrev_b32 %0, 16, %1" : "=v"(acc[1][c >> 2][c & 3]) : "v"(xq[c >> 3][(c >> 1) & 3]));
        }
        if constexpr (z >= 0 && z < 4) {
          DSR(ga[2 * z], ea, z * 128);
          DSR(ga[2 * z + 1], ea, z * 128 + 16);
        } else if constexpr (z >= 4 && z < 6) {
          DSR(sa4[2 * z - 8], eb, (2 * z - 8) * 64);
          DSR(sa4[2 * z - 7], eb, (2 * z - 7) * 64);
        } else if constexpr (z == 6) {
          asm volatile("ds_read_b64 %0, %2\n\tds_read_b32 %1, %3" : "=&v"(cj), "=&v"(ce) : "v"(cwa), "v"(cwu) : "memory");
        } else if constexpr (z == 7) {
          asm volatile("ds_read_b32 %0, %4 offset:32\n\tds_read_b32 %1, %5 offset:4\n\tds_read_b32 %3, %5 offset:56\n\tds_read_b32 %2, %6"
                       : "=&v"(sw), "=&v"(th), "=&v"(slt), "=&v"(slf) : "v"(cwa), "v"(tha), "v"(sla) : "memory");
        }
      });
    }
    if (wid == 0) TSG(part, 8);
    TS(13);
    BT(49);
    BTE();
    SKEWPT();
    u16* op = out + (size_t)row0 * D + col0;
    {
      u32x4 e0 = {se[0], se[1], 0u, 0u}, e1 = {0u, 0u, se[0], se[1]};
      asm volatile("" : "+v"(e0), "+v"(e1));
      TS(26);
#define XCHW() asm volatile("s_waitcnt vmcnt(0)" : "+v"(pv[0]), "+v"(pv[1]), "+v"(pv[2]), "+v"(pv[3])::"memory")
      XCHW();
#define XBAD (max(max(max4(pv[0]), max4(pv[1])), max(max4(pv[2]), max4(pv[3]))) == SENT)
      if (__builtin_expect(__any(XBAD), 1)) POLL2(XBAD, 1, PCAP, XCHLD(); XCHW());
      TS(27);
      asm volatile("s_waitcnt lgkmcnt(0)" : W8(acc[0]), W8(ga), "+v"(sa4[0]), "+v"(sa4[1]), "+v"(sa4[2]), "+v"(sa4[3]), "+v"(cj), "+v"(sw), "+v"(ce), "+v"(th), "+v"(slt), "+v"(slf)::"memory");
#pragma unroll
      for (int q = 0; q < 8; ++q) acc[0][q] = __builtin_amdgcn_mfma_f32_16x16x32_bf16(BF8((q & 1) ? e1 : e0), BF8(pv[q >> 1]), acc[0][q], 0, 0, 0);
      f32x4 s4 = {0.f, 0.f, 0.f, 0.f}, gr = {0.f, 0.f, 0.f, 0.f};
#pragma unroll
      for (int q = 0; q < 4; ++q) {
        xq[q] = pk8(acc[1][2 * q] + ga[2 * q] * acc[0][2 * q], acc[1][2 * q + 1] + ga[2 * q + 1] * acc[0][2 * q + 1]);
        gr = __builtin_amdgcn_mfma_f32_16x16x32_bf16(BF8(xq[q]), BF8(xq[q]), gr, 0, 0, 0);
        s4 = __builtin_amdgcn_mfma_f32_16x16x32_bf16(BF8(sa4[q]), BF8(xq[q]), s4, 0, 0, 0);
      }
#define XCST()                                                                                                                         \
  asm volatile(                                                                                                                        \
      "global_store_dwordx4 %0, %1, %5 sc1\n\tglobal_store_dwordx4 %0, %2, %5 offset:64 sc1\n\t"                                      \
      "global_store_dwordx4 %0, %3, %5 offset:128 sc1\n\tglobal_store_dwordx4 %0, %4, %5 offset:192 sc1\n\ts_nop 1" ::"v"(O_X1C - 2048 + slt * 2048 + col0 * 2), \
      "v"(nsent4(xq[0])), "v"(nsent4(xq[1])), "v"(nsent4(xq[2])), "v"(nsent4(xq[3])), "s"(ws)                                          \
      : "memory")
#define SCST() asm volatile("global_store_dwordx4 %0, %1, %2 sc1\n\ts_nop 1" ::"v"(O_ST2 - 128 + slt * 128 + et * 16), "v"(sg), "s"(ws) : "memory")
      if (slt) XCST();
      const bool dl = ((lane >> 2) & 3) == g4;
      const u32x4 gd = __builtin_bit_cast(u32x4, gr) & dk;
      const float m = s4[2] * (1.0f / 128);
      float q2 = __uint_as_float((gd[0] | gd[1]) | (gd[2] | gd[3])) - s4[2] * m, lg = (s4[0] + s4[1]) - m * ce;
      if (__builtin_expect(__any(dl && !(s4[2] * m <= 256.0f * q2)), 0)) {
        asm volatile("" : "+v"(xq[0]), "+v"(xq[1]), "+v"(xq[2]), "+v"(xq[3]));
        f32x4 a = {0.f, 0.f, 0.f, 0.f}, l = {0.f, 0.f, 0.f, 0.f};
#pragma unroll
        for (int q = 0; q < 4; ++q) {
          const u32x4 ch = *reinterpret_cast<const u32x4*>(&scw[0][g4 * 8 + q * 32]), cl = *reinterpret_cast<const u32x4*>(&scw[1][g4 * 8 + q * 32]);
          const f32x4 d0 = lo4(xq[q]) - m, d1 = hi4(xq[q]) - m;
          a += d0 * d0 + d1 * d1;
          l += d0 * (lo4(ch) + lo4(cl)) + d1 * (hi4(ch) + hi4(cl));
        }
        q2 = ar4((a[0] + a[1]) + (a[2] + a[3]));
        lg = ar4((l[0] + l[1]) + (l[2] + l[3]));
      }
      const u32x4 sg = {nsent(m), nsent(q2), nsent(lg), 0u};
      if (dl) {
        if (slt) SCST();
        asm volatile("global_store_dwordx4 %0, %1, %2 sc1\n\ts_nop 1" ::"v"(soo), "v"(sg), "s"(ws) : "memory");
      }
      if (__any(slf == 0u)) {
        POLL2(slf == 0u, 1, 1 << 26, asm volatile("ds_read_b32 %1, %2 offset:56\n\tds_read_b32 %0, %3\n\ts_waitcnt lgkmcnt(0)" : "=&v"(slt), "=&v"(slf) : "v"(tha), "v"(sla) : "memory"));
        if (slt) {
          XCST();
          if (dl) SCST();
        }
      }
      TS(7);
      SKEWPT();
      const unsigned tp = tick() + T_D1, tq = (unsigned)__builtin_amdgcn_readfirstlane((int)th) + T_C1;
      u32x4 sr[2];
#define STLD(W) asm volatile("global_load_dwordx4 %0, %2, %3 sc1\n\tglobal_load_dwordx4 %1, %2, %3 offset:256 sc1" W : "=&v"(sr[0]), "=&v"(sr[1]) : "v"(spo), "s"(ws) : "memory")
#define SBAD (max(max(max(sr[0][0], sr[0][1]), sr[0][2]), max(max(sr[1][0], sr[1][1]), sr[1][2])) == SENT)
      asm volatile("" : "+v"(xq[0]), "+v"(xq[1]), "+v"(xq[2]), "+v"(xq[3])::"memory");
      f32x4 xf[8], xs[8], gc[8];
      auto PREW = [&](int q0, int q1) __attribute__((always_inline)) {
#pragma unroll
        for (int q = q0; q < q1; ++q) {
          const u32x4 sbq = *reinterpret_cast<const u32x4*>(&svb[1][col0 + q * 32]), gbq = *reinterpret_cast<const u32x4*>(&svb[3][col0 + q * 32]);
          const u32x4 cq = *reinterpret_cast<const u32x4*>(&svb[2][col0 + q * 32]);
          xf[2 * q] = lo4(xq[q]), xf[2 * q + 1] = hi4(xq[q]);
          xs[2 * q] = xf[2 * q] + lo4(gbq) * lo4(sbq), xs[2 * q + 1] = xf[2 * q + 1] + hi4(gbq) * hi4(sbq);
          gc[2 * q] = lo4(gbq) * lo4(cq), gc[2 * q + 1] = hi4(gbq) * hi4(cq);
          asm volatile("" : "+v"(xf[2 * q]), "+v"(xf[2 * q + 1]), "+v"(xs[2 * q]), "+v"(xs[2 * q + 1]), "+v"(gc[2 * q]), "+v"(gc[2 * q + 1]));
        }
      };
      PREW(0, PW0);
      wait_tick((int)(tp - tq) < 0 ? tp : tq);
      STLD("");
      unsigned rk = 0x8ffeu - (unsigned)row0 * 2;
      unsigned twa = lds0 + STG + wid * 8192 + (lane & 15) * 256 + ((g4 ^ lane) & 15) * 16, tra = lds0 + STG + wid * 8192 + g4 * 256 + ((g4 ^ lane) & 15) * 16;
      unsigned oo_ = (unsigned)((rg * 16 + g4) * D + et * 128 + (lane & 15) * 8) * 2;
      asm volatile("" : "+v"(rk), "+v"(op), "+v"(twa), "+v"(tra), "+v"(oo_));
      PREW(PW0, 4);
      asm volatile("s_waitcnt vmcnt(0)" : "+v"(sr[0]), "+v"(sr[1])::"memory");
      if (__builtin_expect(__any(SBAD), 1)) POLL2(SBAD, 1, PCAP, STLD(VW0));
      TS(9);
      const f32x2 mk = {__uint_as_float(sr[0][0]), __uint_as_float(sr[1][0])};
      const f32x2 qk = {__uint_as_float(sr[0][1]), __uint_as_float(sr[1][1])};
      const f32x2 lk = {__uint_as_float(sr[0][2]), __uint_as_float(sr[1][2])};
      const float mean = ar4(mk[0] + mk[1]) * (1.0f / 8);
      const f32x2 dm = mk - mean;
      const f32x2 a = qk + 128.0f * (dm * dm), l = lk + dm * cj;
      const float al = swap32_add(a[0] + a[1], l[0] + l[1]), am = swap16_add(al, al);
      auto r = __builtin_amdgcn_permlane32_swap(__float_as_uint(am), __float_as_uint(am), false, false);
      const float rstd = __builtin_amdgcn_rsqf(__uint_as_float(r[0]) * (1.0f / D) + 1e-5f);
      const unsigned key = mkkey(rstd * __uint_as_float(r[1]) + sw) | rk;
      unsigned t1 = slt ? 0u : key, t2 = 0u;
      TOP2(0xB1); TOP2(0x4E); TOP2(0x141); TOP2(0x140);
      if (lane == 0 && et == (rg & 7)) asm volatile("global_store_dwordx2 %0, %1, %2 sc1" ::"v"(O_KEY + rg * KSTR * 8), "v"(u32x2{t1, t2}), "s"(ws) : "memory");
      u32x4 o[4];
#pragma unroll
      for (int q = 0; q < 4; ++q) o[q] = pk8(xs[2 * q] + (gc[2 * q] * (xf[2 * q] - mean)) * rstd, xs[2 * q + 1] + (gc[2 * q + 1] * (xf[2 * q + 1] - mean)) * rstd);
      {
        asm volatile("ds_write_b128 %0, %4\n\tds_write_b128 %1, %5\n\tds_write_b128 %2, %6\n\tds_write_b128 %3, %7" ::"v"(twa), "v"(twa ^ 64), "v"(twa ^ 128), "v"(twa ^ 192),
                     "v"(o[0]), "v"(o[1]), "v"(o[2]), "v"(o[3]) : "memory");
        asm volatile("ds_read_b128 %0, %4\n\tds_read_b128 %1, %5 offset:1024\n\tds_read_b128 %2, %6 offset:2048\n\tds_read_b128 %3, %7 offset:3072\n\ts_waitcnt lgkmcnt(0)"
                     : "=&v"(o[0]), "=&v"(o[1]), "=&v"(o[2]), "=&v"(o[3]) : "v"(tra), "v"(tra ^ 64), "v"(tra ^ 128), "v"(tra ^ 192) : "memory");
      }
      auto OST = [&](bool on) __attribute__((always_inline)) {
        const unsigned mk_ = (unsigned)__builtin_amdgcn_ballot_w64(on) >> g4;
#pragma unroll
        for (int q = 0; q < 4; ++q)
          if (mk_ & (1u << (4 * q))) asm volatile("global_store_dwordx4 %0, %1, %2" OPOL "\n\ts_nop 1" ::"v"(oo_), "v"(o[q]), "s"(out + q * 4096) : "memory");
      };
      bool df = slt;
      {
        const int tn = (int)(tick() - (tq + (T_C3 - T_C1)));
        if (tn > T_X) df = true;
        else if (tn > -T_W) { unsigned sv; LSPIN(sv, ssel + 16 + wid, sv != 0u); }
      }
      OST(!df);
      TS(1);
      u32x2 ks;
      POLL2(ks[1] == 0u, 1, 1 << 26, asm volatile("ds_read_b64 %0, %1\n\ts_waitcnt lgkmcnt(0)" : "=v"(ks) : "v"(lds0 + SPARE + 10368) : "memory"));
      df = df && (ks[0] & 0xffffu) != rk && (ks[1] & 0xffffu) != rk;
      SKEWPT();
      if (__any(df)) {
        if (b < NPART) {
          unsigned sv;
          LSPIN(sv, ssel + O_DW, sv != 0u);
          if (O_DS) __builtin_amdgcn_s_sleep(O_DS);
        }
        OST(df);
      }
      if (__builtin_expect(__any(t1 > ks[1]), 0)) {
        const unsigned tg = ssel[8];
        u32x2 g0, g1;
        POLL2(min(min(g0[0], g0[1]), min(g1[0], g1[1])) == 0u, 8, PCAP,
              asm volatile("global_load_dwordx2 %0, %2, %4 sc1\n\tglobal_load_dwordx2 %1, %3, %4 sc1" VW0
                           : "=&v"(g0), "=&v"(g1) : "v"(O_KEY + lane * 8 * KSTR), "v"(O_KEY + (lane + 64) * 8 * KSTR), "s"(ws) : "memory"));
        t1 = max(g0[0], g1[0]), t2 = max(min(g0[0], g1[0]), max(g0[1], g1[1]));
        TOP2(0xB1); TOP2(0x4E); TOP2(0x141); TOP2(0x140);
        TOP2S(__builtin_amdgcn_permlane16_swap);
        TOP2S(__builtin_amdgcn_permlane32_swap);
        const unsigned T1 = max(t1, ks[0]), T2 = max(min(t1, ks[0]), max(t2, ks[1]));
        float* hb = reinterpret_cast<float*>(shm + STG + wid * 8192);
        const int c2 = et * 128 + lane * 2;
        const f32x2 sb2 = unpk(*reinterpret_cast<const unsigned*>(&svb[1][c2])), cb2 = unpk(*reinterpret_cast<const unsigned*>(&svb[2][c2]));
        const f32x2 gb2 = unpk(*reinterpret_cast<const unsigned*>(&svb[3][c2]));
        auto GPUT = [&](unsigned off, unsigned v) { __hip_atomic_store(reinterpret_cast<u64*>(ws + off), mk64(tg, v), __ATOMIC_RELAXED, __HIP_MEMORY_SCOPE_AGENT); };
        auto GGET = [&](unsigned off) {
          u64 v = 0;
#pragma unroll 1
          for (int it = 0; it < PCAP; ++it) {
            v = __hip_atomic_load(reinterpret_cast<u64*>(ws + off), __ATOMIC_RELAXED, __HIP_MEMORY_SCOPE_AGENT);
            if (__all((unsigned)(v >> 32) == tg)) break;
            __builtin_amdgcn_s_sleep(16);
          }
          return (unsigned)v;
        };
#pragma unroll 1
        for (int tk = 0; tk < 2; ++tk) {
          const unsigned kk = tk ? T2 : T1;
          const u64 own = __builtin_amdgcn_ballot_w64(!slt && key == kk);
          if (own == 0) continue;
          if (key == kk) {
#pragma unroll
            for (int q = 0; q < 4; ++q) *reinterpret_cast<u32x4*>(hb + 1536 + q * 16 + g4 * 4) = xq[q];
          }
          const int ln = __builtin_ctzll(own);
          const float mn = __int_as_float(__builtin_amdgcn_readlane(__float_as_int(mean), ln)), rd = __int_as_float(__builtin_amdgcn_readlane(__float_as_int(rstd), ln));
          const f32x2 x2 = unpk(reinterpret_cast<const unsigned*>(hb)[1536 + lane]), hf = ((x2 - mn) * rd) * cb2 + sb2;
          const unsigned h2 = pack2(hf[0], hf[1]);
          GPUT(O_FH + (tk * 512 + et * 64 + lane) * 8, h2);
#pragma unroll 1
          for (int i = 0; i < 8; ++i) *reinterpret_cast<f32x2*>(hb + i * 128 + lane * 2) = unpk(GGET(O_FH + (tk * 512 + i * 64 + lane) * 8));
          f32x4 ag[2] = {}, au[2] = {};
          const int fc = et * 448 + min(lane, 55) * 8;
#pragma unroll 8
          for (int k = 0; k < D; ++k) {
            const u32x4 a = *reinterpret_cast<const u32x4*>(wg + (size_t)k * F + fc), c = *reinterpret_cast<const u32x4*>(wu + (size_t)k * F + fc);
            const float h = hb[k];
            ag[0] += lo4(a) * h, ag[1] += hi4(a) * h, au[0] += lo4(c) * h, au[1] += hi4(c) * h;
          }
#pragma unroll
          for (int i = 0; i < 8; ++i) hb[1024 + min(lane, 55) * 8 + i] = rbf(silu_bf(rbf(ag[i >> 2][i & 3])) * rbf(au[i >> 2][i & 3]));
          f32x4 ad[4] = {};
#pragma unroll 8
          for (int k = 0; k < 448; ++k) {
            const u16* wp = wd + (size_t)(et * 448 + k) * D + lane * 8;
            const u32x4 a = *reinterpret_cast<const u32x4*>(wp), c = *reinterpret_cast<const u32x4*>(wp + 512);
            const float h = hb[1024 + k];
            ad[0] += lo4(a) * h, ad[1] += hi4(a) * h, ad[2] += lo4(c) * h, ad[3] += hi4(c) * h;
          }
#pragma unroll
          for (int i = 0; i < 16; ++i) GPUT(O_FY + ((tk * 8 + et) * 1024 + (i >> 3) * 512 + lane * 8 + (i & 7)) * 8, __float_as_uint(ad[i >> 2][i & 3]));
          f32x2 y = {0.f, 0.f};
#pragma unroll 1
          for (int i = 0; i < 8; ++i) y += f32x2{__uint_as_float(GGET(O_FY + ((tk * 8 + i) * 1024 + c2) * 8)), __uint_as_float(GGET(O_FY + ((tk * 8 + i) * 1024 + c2 + 1) * 8))};
          const float rsg = rbf(1.0f / (1.0f + __expf(-key_val(kk))));
          __hip_atomic_store(reinterpret_cast<unsigned*>(out + (size_t)key_row(kk) * D + c2),
                             pack2(x2[0] + gb2[0] * rbf(bflo(h2) + rbf(rbf(y[0]) * rsg)), x2[1] + gb2[1] * rbf(bfhi(h2) + rbf(rbf(y[1]) * rsg))), __ATOMIC_RELAXED, __HIP_MEMORY_SCOPE_AGENT);
        }
      }
    }
    TS(16);
    TSE();
  } else {
    const int hw = wid - 4;
    unsigned ts0 = tick();
    asm volatile("" : "+v"(ts0));
    const char* Hc = reinterpret_cast<const char*>((cc ? X1 : X0) + (size_t)et * 128 * D);
    auto UPOLL = [&](int n) {
      const unsigned fo = (unsigned)WS_UFL + ((cc * 16 + nt) * 32 + ((mt + 1 + (lane >> 2)) & 7) * 4 + (lane & 3)) * FSTR * 4;
      unsigned f;
      POLL2(f == 0u && lane < 4 * n, U_S, PCAP, asm volatile("global_load_dword %0, %1, %2 sc1" VW0 : "=&v"(f) : "v"(fo), "s"(ws) : "memory"));
      return (__builtin_amdgcn_ballot_w64(f == 0u) & 0xFFFFFFFull) == 0;
    };
    bool urdy = true;
    u32x4 gT[8], uT[8], wdv[8];
    unsigned og = (unsigned)(((g & 3) * 256 + hw * 64 + (lane >> 3)) * F + (g >> 2) * 64 + ((lane & 7) ^ ((lane >> 3) & 2)) * 8) * 2;
    unsigned wta = lds0 + STG + (g4 * 8 + ((lane >> 2) & 3)) * 128 + (((hw * 2 + ((lane >> 1) & 1)) ^ ((lane >> 2) & 2) ^ ((g4 & 1) * 4)) << 4) + (lane & 1) * 8;
    unsigned od = (unsigned)((min(g >> 2, 55) * 64 + (lane >> 3) * 8) * D + (g & 3) * 256 + hw * 64 + (lane & 7) * 8) * 2;
    unsigned ou = og + (unsigned)(((mt + W_UO) % 7 - mt) * 128);
    asm volatile("" : "+v"(og), "+v"(ou), "+v"(od), "+v"(wta));
#define LDWI(dst, voff, base, inc) asm volatile("global_load_dwordx4 %0, %1, %2" WPOL "\n\tv_add_u32 %1, %3, %1" : "=v"(dst), "+v"(voff) : "s"(base), "n"(inc) : "memory")
#define WST(o, w)                                                                                                                      \
  if (b < NPART) {                                                                                                                     \
    _Pragma("unroll") for (int i = 0; i < 8; ++i)                                                                                      \
        asm volatile("s_mov_b32 m0, %1\n\ts_nop 0\n\tglobal_load_lds_dwordx4 %0, %2" WPOL "\n\tv_xor_b32 %0, 64, %0\n\tv_add_u32 %0, %3, %0" \
                     : "+v"(o) : "s"(lds0 + STG + hw * 8192 + i * 1024), "s"(w), "n"(F * 16) : "memory");                              \
  }
#define WTR2(i) "ds_read_b64_tr_b16 %" #i ", %16 offset:(" #i "/2)*4096+(" #i "&1)*512\n\t"
#define WTK(T)                                                                                                                         \
  if (b < NPART) {                                                                                                                     \
    u32x2 t_[16];                                                                                                                      \
    asm volatile(WTR2(0) WTR2(1) WTR2(2) WTR2(3) WTR2(4) WTR2(5) WTR2(6) WTR2(7) WTR2(8) WTR2(9) WTR2(10) WTR2(11) WTR2(12) WTR2(13)   \
                     WTR2(14) WTR2(15) "s_waitcnt lgkmcnt(0)"                                                                          \
                 : "=&v"(t_[0]), "=&v"(t_[1]), "=&v"(t_[2]), "=&v"(t_[3]), "=&v"(t_[4]), "=&v"(t_[5]), "=&v"(t_[6]), "=&v"(t_[7]),     \
                   "=&v"(t_[8]), "=&v"(t_[9]), "=&v"(t_[10]), "=&v"(t_[11]), "=&v"(t_[12]), "=&v"(t_[13]), "=&v"(t_[14]), "=&v"(t_[15]) \
                 : "v"(wta) : "memory");                                                                                               \
    _Pragma("unroll") for (int f = 0; f < 8; ++f) T[f] = u32x4{t_[2 * f][0], t_[2 * f][1], t_[2 * f + 1][0], t_[2 * f + 1][1]};        \
  }
#define DLD(q0, q1) _Pragma("unroll") for (int q = q0; q < q1; ++q) LDWI(wdv[q], od, wd, D * 2)
    const int pf0 = A_PS + (j8 & (A_PW - 1)), q1 = A_Q1;
    static_assert(A_PS + 2 * A_PW <= R_AT && R_AT < A_Q1 && A_PS + 2 * A_PW + W_T <= A_Q1 && H_AT + 3 <= A_Q1 && G1_C + G1_S < W_T && W_T < A_PW && W_T >= M_NS - 2 && A_Q1 < F_AT && F_AT <= 29 && V_K <= B_J && V_T > B_J && N_K > V_T + 1, "barrier counts");
#define HOFF(r) (unsigned)((((r) >> 5) * 32 + (((r) >> 2) & 3) * 8 + (((r) >> 4) & 1) * 4 + ((r)&3)) * 2048 + ((lane & 7) ^ (lane >> 3)) * 16)
#define TR8(T, W)                                                                                                    \
  _Pragma("unroll") for (int c = 0; c < 8; ++c) {                                                                    \
    _Pragma("unroll") for (int p = 0; p < 4; ++p)                                                                    \
        T[c][p] = __builtin_amdgcn_perm(W[2 * p + 1][c >> 1], W[2 * p][c >> 1], (c & 1) ? 0x07060302u : 0x05040100u); \
    asm volatile("" : "+v"(T[c]));                                                                                   \
  }
    u32x4 qm;
#pragma unroll
    for (int i = 0; i < 4; ++i) qm[i] = ((lane >> 2) & 3) == i ? ~0u : 0u;
    auto MVR = [&](f32x4 d) __attribute__((always_inline)) { return ((lane & 8) ? d[3] : d[0]) + dpp_f<0x128>((lane & 8) ? d[1] : d[2]); };
    auto MV64 = [&](const u32x4* T, u32x4 a) __attribute__((always_inline)) {
      f32x4 d[2] = {};
#pragma unroll
      for (int c = 0; c < 8; ++c) d[c & 1] = __builtin_amdgcn_mfma_f32_16x16x32_bf16(BF8(a & qm[c >> 1]), BF8(T[c]), d[c & 1], 0, 0, 0);
      return f32x2{MVR(d[0]), MVR(d[1])};
    };
    u32x4 sel, gp[4];
#define GP4(o, W)                                                                                                         \
  asm volatile(                                                                                                           \
      "global_load_dwordx4 %0, %4, %5 sc1\n\tglobal_load_dwordx4 %1, %4, %5 offset:1024 sc1\n\t"                         \
      "global_load_dwordx4 %2, %4, %5 offset:2048 sc1\n\tglobal_load_dwordx4 %3, %4, %5 offset:3072 sc1" W                \
      : "=&v"(gp[0]), "=&v"(gp[1]), "=&v"(gp[2]), "=&v"(gp[3])                                                            \
      : "v"(o), "s"(ws)                                                                                                   \
      : "memory")
#define GPBAD (max(max(max4(gp[0]), max4(gp[1])), max(max4(gp[2]), max4(gp[3]))) == SENT)
      auto GPSUM = [&]() __attribute__((always_inline)) {
        f32x4 d = {0.f, 0.f, 0.f, 0.f};
#pragma unroll
        for (int i = 0; i < 4; ++i) d = __builtin_amdgcn_mfma_f32_16x16x32_bf16(BF8(sel), BF8(gp[i]), d, 0, 0, 0);
        return d;
      };
    const bool rdc = b >= NPART && hw == 3, rnp = b >= NPART && hw == 2 && cc == 0;
    const int gbq = (b >> 4) & 1, cj = (b & 15) * 64 + (lane & 31) * 2;
    unsigned voff[4], ro, ra1, wqa, qo, so_, qa, mo, mo1, mo2, ma2;
    auto JSET = [&]() {
#pragma unroll
      for (int i = 0; i < 4; ++i) {
        voff[i] = HOFF((i * 4 + hw) * 8 + (lane >> 3));
        sel[i] = ((lane & 15) >> 1) == i ? ((lane & 1) ? 0x3f800000u : 0x3f80u) : 0u;
      }
      ro = rdc ? O_G2 + ((lane >> 5) * 16384 + gbq * 1024 + cj) * 8 : (unsigned)(((nt * 128 + hw * 32 + cc * 16 + ((lane >> 1) & 3) + (lane & 8)) * D + et * 128 + (lane & 1) * 64) * 2);
      ra1 = lane < 32 ? O_MOD + 2048 + gbq * 4096 + cj * 2 : O_M2 + 8192 + 64 - gbq * 64 + (b & 15) * 4;
      wqa = lds0 + SPARE + 8192 + cj * 2;
      mo = hw ? O_MOD + hw * 2048 + lane * 16 : O_MOD + 4096, mo1 = hw ? mo + 1024 : mo;
      qo = O_RS + (4096 + ((hw & 1) * 8 + (lane >> 4) * 2 + 1) * D + et * 128 + (hw >> 1) * 64 + (lane & 15) * 4) * 4;
      so_ = O_RS + ((hw & 1) * S + nt * 128 + (lane >> 4) * 32 + cc * 16 + (lane & 15)) * 4;
      qa = lds0 + RKA + ((hw >> 1) * 64 + ((lane >> 3) & 1) * 32 + (lane & 1) * 16 + ((lane >> 1) & 3) * 4 + (lane >> 4)) * 16 + (hw & 1) * 8;
      mo2 = O_M2 + (hw == 2 ? 8448u + (lane & 15) * 16 : hw ? 8192u + (lane & 7) * 16 : lane < 32 ? et * 512 + lane * 16 : 4096u + (lane & 16) * 128 + et * 256 + (lane & 15) * 16);
      ma2 = lds0 + (hw ? SPARE + 10240 + (lane & 7) * 8 : TAB + lane * 16);
      asm volatile("" : "+v"(voff[0]), "+v"(voff[1]), "+v"(voff[2]), "+v"(voff[3]), "+v"(ro), "+v"(ra1), "+v"(wqa), "+v"(qo), "+v"(so_), "+v"(qa), "+v"(mo), "+v"(mo1), "+v"(mo2), "+v"(ma2), "+v"(sel));
      *reinterpret_cast<unsigned*>((hw & 1) ? shm + RKB + 1024 + lane * 4 : reinterpret_cast<char*>(scw[2]) + lane * 4) = (hw & 1) ? 0u : 0x3f803f80u;
      if (hw == 3) {
        if (lane < 8) ssel[lane] = 0u, ssel[16 + lane] = 0u;
        *reinterpret_cast<u32x2*>(shm + SLT + lane * 8) = u32x2{0u, 0u};
        ssel[8] = (unsigned)tag;
      }
    };
    if (hw < 2) {
      constexpr int P = 8;
      __builtin_amdgcn_s_setprio(2);
      unsigned voy[P];
#pragma unroll
      for (int i = 0; i < P; ++i) voy[i] = (unsigned)(((i * 2 + hw) * 8 + (lane >> 3)) * 4096 + ((lane & 7) ^ (lane >> 3)) * 16);
      const unsigned lbase = lds0 + hw * 1024;
      const char* Yc = reinterpret_cast<const char*>((cc ? Y1 : Y0) + (size_t)nt * 128 * S);
      auto stage = [&](const char* yp, int slot) {
        const unsigned l = lbase + slot * 16384;
#pragma unroll
        for (int i = 0; i < P; ++i) GLDS(l + i * 2048, voy[i], yp);
      };
#define YSRC(n) (Yc + ((((n) + xcd * 4) & 31) << 7))
      __builtin_amdgcn_s_sleep(C_LD);
      PFL(hw ? PFG(1) : cpf);
      if (!J_L) JSET();
      TAGW(1);
      BAR(55);
      BT(56);
      if (hw == 1 && lane == 0) __hip_atomic_store(reinterpret_cast<u64*>(xfl + b * XSTR), tag, __ATOMIC_RELAXED, __HIP_MEMORY_SCOPE_AGENT);
      GLDS(lds0 + SPARE + 8192 + hw * 1024, (unsigned)(hw * 1024 + lane * 16), wr_);
#pragma unroll
      for (int s = 0; s < NS - 1; ++s) stage(YSRC(s), s);
      if (J_L) JSET();
      WAIT_V((NS - 2) * P);
      BAR(0);
      int slot = NS - 1;
#pragma unroll 1
      for (int k = 1; k <= 32 + B_J; ++k) {
        const int n = k - 2 + NS;
        if (n == 32) {
#pragma unroll
          for (int i = 0; i < P; ++i) voy[i] = HOFF((i * 2 + hw) * 8 + (lane >> 3));
        }
        if (k == 34 && hw == 0) {
          if (U_D) __builtin_amdgcn_s_sleep(U_D);
          urdy = UPOLL(U_N1);
          TSD(14);
        }
        const char* ys = n < 32 ? YSRC(n) : Hc + ((mt * 2 + n) & 15) * 128;
        asm volatile("" : "+s"(ys));
        WAIT_V((NS - 3) * P);
        BAR(k);
        stage(ys, slot);
        slot = slot == NS - 1 ? 0 : slot + 1;
        if (k == pf0) { WST(og, wg); }
        if (k == pf0 + W_T) { WTK(gT); }
        if (k == pf0 + A_PW) { WST(ou, wu); }
        if (k == pf0 + A_PW + W_T) { WTK(uT); }
        if (k == CPF_K && hw == 1 && lane < 12) PFL(cpf);
        if (k == 32) { DLD(0, 8 - B_WN); }
      }

    } else {
      const int aw = hw - 2;
      unsigned ow = wo - (2048 + (cg0 >> 4) * 1024) * 2;
      const unsigned oq = (unsigned)((nt * 64 + aw * 32 + (lane >> 4)) * D + mt * 128) * 2;
      u32x4 hv[8], wt[8], sc;
      {
        asm volatile("s_waitcnt vmcnt(%1)" : "+v"(cq) : "n"(11) : "memory");
#pragma unroll
        for (int i = 0; i < 4; ++i) sc[i] = pack2(silu_bf(bflo(cq[i])), silu_bf(bfhi(cq[i])));
        asm volatile("" : "+v"(sc));
      }
#define GPUB(o, off)                                                                                   \
  if ((lane & 8) == 0)                                                                                 \
    asm volatile("global_store_dwordx4 %0, %1, %2 sc1 ; ALWAYS\n\ts_nop 1" ::"v"(off),                          \
                 "v"(u32x4{__float_as_uint(o[0]), (unsigned)tag, __float_as_uint(o[1]), (unsigned)tag}), "s"(ws) : "memory")
      const unsigned gpo = ks0 * 2048 + cg0 * 64 + (lane & 7) * 8 + (lane >> 4) * 2;
      {
        unsigned mpo = O_G1 + gpo * 8;
        asm volatile("s_waitcnt vmcnt(3)" : W8(wv), "+v"(mpo)::"memory");
        TR8(wt, wv);
        f32x2 o = MV64(wt, sc);
        asm volatile("" : "+v"(o));
        TAGW(0);
        BAR(55);
        if (P_D2) __builtin_amdgcn_s_sleep(P_D2);
        PFL(PFG(2 + aw));
        GPUB(o, mpo);
      }
      unsigned vta = lds0 + STG + (g4 * 8 + ((lane >> 2) & 3)) * 256 + (((aw * 8 + ((lane >> 1) & 1)) ^ ((g4 & 1) * 8 + ((lane >> 2) & 3))) << 4) + (lane & 1) * 8;
      unsigned vso = O_VP + ((cc * 8 + mt) * 16 + nt) * 256 + aw * 128 + (lane & 3) * 32 + g4 * 8;
      asm volatile("" : "+v"(vta), "+v"(vso), "+v"(ow));
      unsigned f0 = O_XFL + ((xcd * 32 + 64 + aw * 128 + lane) & 255) * XSTR * 4, f1 = O_XFL + ((xcd * 32 + 128 + aw * 64 + lane) & 255) * XSTR * 4;
      unsigned xao = (unsigned)WS_P + (b ^ 4) * 16384 + aw * 8192 + 4096 + lane * 16, lao = O_KEY + (512 + b * 3 + aw) * 1024 + lane * 16;
      const char* pf4 = PFG(4 + aw);
      const char* pf6 = PFG(6 + aw);
      asm volatile("" : "+v"(f0), "+v"(f1), "+v"(xao), "+v"(lao), "+v"(pf4), "+v"(pf6));
      BT(57);
      BAR(0);
      float rs = 0.f, rs2 = 0.f;
      const unsigned ra = lds0 + (mt * 16 + aw * 8) * 128 + lane * 16;
      unsigned so = 0;
#define TSX()
#define RSUM()                                                                                                    \
  do {                                                                                                            \
    TSX();                                                                                                        \
    u32x4 R_;                                                                                                     \
    asm volatile("ds_read_b128 %0, %1\n\ts_waitcnt lgkmcnt(0)" : "=&v"(R_) : "v"(ra + so) : "memory");            \
    _Pragma("unroll") for (int j = 0; j < 4; ++j) {                                                               \
      asm volatile("v_add_f32 %0, %0, %1" : "+v"(rs) : "v"(R_[j] << 16));                                         \
      asm volatile("v_add_f32 %0, %0, %1" : "+v"(rs2) : "v"(R_[j] & 0xffff0000u));                                \
    }                                                                                                             \
    so = so == (NS - 1) * 16384 ? 0 : so + 16384;                                                                 \
  } while (0)
      {
        u64 f[2];
#define FLD() asm volatile("global_load_dwordx2 %0, %2, %4 sc1\n\tglobal_load_dwordx2 %1, %3, %4 sc1" : "=&v"(f[0]), "=&v"(f[1]) : "v"(f0), "v"(f1), "s"(ws) : "memory")
        __builtin_amdgcn_s_sleep(F_D);
        FLD();
#pragma unroll
        for (int q = 1; q < 11; ++q)
          asm volatile("global_store_dwordx4 %0, %1, %2 offset:%3 sc1" ::"v"(q < 3 ? lao : xao), "v"(u32x4{~0u, ~0u, ~0u, ~0u}), "s"(ws),
                       "n"(q < 3 ? (q - 1) * 1024 : (q - 7) * 1024) : "memory");
        asm volatile("s_nop 1");
        RSUM();
        BAR(1);
        RSUM();
        asm volatile("s_waitcnt vmcnt(%2)" : "+v"(f[0]), "+v"(f[1]) : "n"(10) : "memory");
        POLL(__all(f[0] == tag && f[1] == tag), 1, FLD(); asm volatile("s_waitcnt vmcnt(0)" : "+v"(f[0]), "+v"(f[1])::"memory"));
      }
      BAR(2);
      if (X_PA) __builtin_amdgcn_s_setprio(X_PA);
#define ARUN(b0, b1) _Pragma("unroll 1") for (int bi = (b0); bi < (b1); ++bi) { RSUM(); BAR(bi + 1); }
#define GLD() _Pragma("unroll") for (int i = 0; i < 8; ++i) LDWI(wv[i], ow, wmod, 12288)
      ARUN(2, pf0);
      PFL(pf4);
      GLD();
#define HVLD() _Pragma("unroll") for (int i = 0; i < 8; ++i) GLDS(lds0 + STG + aw * 8192 + i * 1024, oq + i * 8192 + (((lane & 15) ^ ((i * 4 + (lane >> 4)) & 15)) << 4), cc ? X1 : X0)
      WST(og, wg);
      ARUN(pf0, pf0 + G1_C);
      {
        if (b >= NPART) WAIT_V(0);
        asm volatile("s_waitcnt vmcnt(8)" : W8(wv)::"memory");
        TR8(wt, wv);
        if (G1_S) { RSUM(); BAR(pf0 + G1_C + 1); }
        const f32x2 o = MV64(wt, sc);
        GPUB(o, O_MODP + gpo * 8);
        ow = wo + 2048;
      }
      ARUN(pf0 + G1_C + G1_S, pf0 + W_T - 1);
      WAIT_V(1);
      RSUM();
      BAR(pf0 + W_T);
      WTK(gT);
      ARUN(pf0 + W_T, pf0 + A_PW);
      PFL(pf6);
      WST(ou, wu);
      const int rb_ = b < NPART ? pf0 + A_PW + W_T : R_AT, hb_ = max(rb_ + 1, H_AT + (j8 & 3));
      ARUN(pf0 + A_PW, rb_ - 1);
      WAIT_V(0);
      RSUM();
      BAR(rb_);
      WTK(uT);
      if (b >= NPART) {
        const unsigned c0 = (b - NPART) * 64 + aw * 32 + (lane & 15) * 2, c1 = (b - NPART) * 32 + aw * 16 + (lane & 7) * 2;
        const unsigned wq = *reinterpret_cast<const unsigned*>(&svb[4][c1]);
#define TG2(r, i) ((r[i][1] ^ (unsigned)tag) | (r[i][3] ^ (unsigned)tag) | (r[i + 1][1] ^ (unsigned)tag) | (r[i + 1][3] ^ (unsigned)tag))
        POLL2((TG2(wt, 0) | TG2(wt, 2) | TG2(wt, 4) | TG2(wt, 6)) != 0u, 2, PCAP,
              asm volatile("global_load_dwordx4 %0, %8, %10 sc1\n\tglobal_load_dwordx4 %1, %8, %11 sc1\n\t"
                           "global_load_dwordx4 %2, %8, %12 sc1\n\tglobal_load_dwordx4 %3, %8, %13 sc1\n\t"
                           "global_load_dwordx4 %4, %9, %10 sc1\n\tglobal_load_dwordx4 %5, %9, %11 sc1\n\t"
                           "global_load_dwordx4 %6, %9, %12 sc1\n\tglobal_load_dwordx4 %7, %9, %13 sc1" VW0
                           : "=&v"(wt[0]), "=&v"(wt[1]), "=&v"(wt[2]), "=&v"(wt[3]), "=&v"(wt[4]), "=&v"(wt[5]), "=&v"(wt[6]), "=&v"(wt[7])
                           : "v"(O_MODP + (g4 * 8192 + c0) * 8), "v"(O_G1 + (g4 * 8192 + (lane & 8) * 128 + c1) * 8), "s"(ws), "s"(ws + 16384), "s"(ws + 32768), "s"(ws + 49152)
                           : "memory"));
        auto FIN = [&](int q, int i, bool inc) __attribute__((always_inline)) {
          return rbf((inc ? 1.0f : 0.0f) + rbf(ar4((__uint_as_float(wt[q][i]) + __uint_as_float(wt[q + 1][i])) + (__uint_as_float(wt[q + 2][i]) + __uint_as_float(wt[q + 3][i])))));
        };
        const float s0 = FIN(0, 0, gbq), s1 = FIN(0, 2, gbq);
        if (lane < 16)
          asm volatile("global_store_dwordx4 %0, %1, %2 sc1\n\ts_nop 1" ::"v"(O_MODP + (16 * 2048 + c0) * 8),
                       "v"(u32x4{__float_as_uint(s0), (unsigned)tag, __float_as_uint(s1), (unsigned)tag}), "s"(ws) : "memory");
        const bool hi = lane & 8;
        const float t0 = FIN(4, 0, hi), t1 = FIN(4, 2, hi), o0 = dpp_f<0x128>(t0), o1 = dpp_f<0x128>(t1);
        const float a0 = hi ? o0 : t0, a1 = hi ? o1 : t1, e0 = hi ? t0 : o0, e1 = hi ? t1 : o1, p0 = e0 * bflo(wq), p1 = e1 * bfhi(wq);
        const unsigned h = pack2(p0, p1);
        const unsigned v = g4 == 0 ? __float_as_uint(hi ? a1 : a0) : g4 == 1 ? (hi ? h : pack2(e0, e1)) : hi ? pack2(a0 * p0, a1 * p1) : pack2(p0 - bflo(h), p1 - bfhi(h));
        const unsigned po = g4 == 0 ? O_M2 + c1 * 4 + (hi ? 4 : 0) : (g4 == 1 ? (hi ? O_M2 + 4096 : O_MOD + 4096) : hi ? O_GC : O_M2 + 6144) + c1 * 2;
        if (lane < 48) asm volatile("global_store_dword %0, %1, %2 sc1" ::"v"(po), "v"(min(v, SENT - 1u)), "s"(ws) : "memory");
      }
      ARUN(rb_, hb_);
      HVLD();
      ARUN(hb_, q1);
      u32x4 fm, g0, g1;
#define FMLD(W)                                                                                                                                     \
  asm volatile("global_load_dwordx4 %0, %3, %5 sc1\n\tglobal_load_dwordx4 %1, %4, %5 sc1\n\tglobal_load_dwordx4 %2, %4, %5 offset:64 sc1" W        \
               : "=&v"(fm), "=&v"(g0), "=&v"(g1) : "v"(O_MODP + (16 * 2048 + aw * 1024 + mt * 128) * 8 + lane * 16), "v"(O_GC + nt * 128 + g4 * 16), "s"(ws) : "memory")
      FMLD("");
      RSUM();
      BAR(q1 + 1);
      unsigned ha[4], sma = lds0 + SPARE + 15872 + (aw ? 512 + lane * 8 : lane * 4);
#pragma unroll
      for (int i = 0; i < 4; ++i) ha[i] = lds0 + STG + aw * 8192 + (lane & 15) * 256 + (((i * 4 + (lane >> 4)) ^ (lane & 15)) << 4);
      asm volatile("" : "+v"(ha[0]), "+v"(ha[1]), "+v"(ha[2]), "+v"(ha[3]), "+v"(sma), "+v"(ow));
      ARUN(q1 + 1, F_AT);
      asm volatile("s_waitcnt vmcnt(0)" : "+v"(fm), "+v"(g0), "+v"(g1)::"memory");
#pragma unroll
      for (int i = 0; i < 8; ++i) DSR(hv[i], ha[i & 3], (i >> 2) * 4096);
      POLL(__all(fm[1] == (unsigned)tag && fm[3] == (unsigned)tag && max(max4(g0), max4(g1)) != SENT), 1, FMLD(VW0));
      {
        const unsigned sg = cc ? 0x80000000u : 0u;
        asm volatile("ds_write_b32 %0, %2\n\tds_write_b32 %1, %3" ::"v"(sma), "v"(sma + (aw ? 4 : 256)), "v"(aw ? fm[0] ^ sg : (fm[0] >> 16) | (fm[2] & 0xffff0000u)), "v"(fm[2] ^ sg) : "memory");
      }
      ARUN(F_AT, 29);
      GLD();
      RSUM();
      BAR(30);
        u32x2 tv[16];
#pragma unroll
        for (int q = 0; q < 16; ++q)
          asm volatile("ds_read_b64_tr_b16 %0, %1 offset:%2" : "=v"(tv[q]) : "v"(vta ^ ((((q >> 2) * 2) ^ ((q & 1) * 4)) << 4)), "n"((q & 1) * 1024 + ((q >> 1) & 1) * 8192) : "memory");
      RSUM();
      BAR(31);
      RSUM();
      {
        const float v = dpp_add<0x141>(dpp_add<0x4E>(dpp_add<0xB1>(rs + rs2)));
        if ((lane & 7) == 0)
          asm volatile("global_store_dword %0, %1, %2 sc1 ; ALWAYS" ::"v"(O_RS + (cc * S + nt * 128 + mt * 16 + aw * 8 + (lane >> 3)) * 4), "v"(nsent(v)), "s"(ws) : "memory");
      }
      BAR(32);
      if (!V_F) {
        __builtin_amdgcn_s_sleep(2);
        DLD(0, 8 - B_WN);
      }
      __builtin_amdgcn_s_setprio(2);
      {
        asm volatile("" : W8(tv), W8((tv + 8))::"memory");
        f32x4 d[4];
#pragma unroll
        for (int j = 0; j < 4; ++j) {
          d[j] = __builtin_amdgcn_mfma_f32_16x16x32_bf16(BF8((u32x4{tv[4 * j][0], tv[4 * j][1], tv[4 * j + 1][0], tv[4 * j + 1][1]})), BF8(g0), f32x4{0.f, 0.f, 0.f, 0.f}, 0, 0, 0);
          d[j] = __builtin_amdgcn_mfma_f32_16x16x32_bf16(BF8((u32x4{tv[4 * j + 2][0], tv[4 * j + 2][1], tv[4 * j + 3][0], tv[4 * j + 3][1]})), BF8(g1), d[j], 0, 0, 0);
        }
        u32x2 vo;
#pragma unroll
        for (int i = 0; i < 2; ++i) {
          const unsigned e0 = pack2(d[0][2 * i], d[0][2 * i + 1]), e1 = pack2(d[1][2 * i], d[1][2 * i + 1]), e2 = pack2(d[2][2 * i], d[2][2 * i + 1]), e3 = pack2(d[3][2 * i], d[3][2 * i + 1]);
          vo[i] = min((lane & 2) ? ((lane & 1) ? e3 : e2) : ((lane & 1) ? e1 : e0), SENT - 1u);
        }
        if ((lane & 12) == 0) asm volatile("global_store_dwordx2 %0, %1, %2 sc1 ; ALWAYS" ::"v"(vso), "v"(vo), "s"(ws) : "memory");
      }
      if (V_F) { DLD(0, 8 - B_WN); }
      asm volatile("" : W8(hv)::"memory");
      {
        f32x4 qa[2] = {};
#pragma unroll
        for (int kk = 0; kk < 4; ++kk) {
          const bf16x8 sb8 = *reinterpret_cast<const bf16x8*>(ssa + kk * 32 + (lane >> 4) * 8);
#pragma unroll
          for (int r = 0; r < 2; ++r)
            qa[r] = __builtin_amdgcn_mfma_f32_16x16x32_bf16(__builtin_bit_cast(bf16v8, hv[r * 4 + kk]), __builtin_bit_cast(bf16v8, sb8), qa[r], 0, 0, 0);
        }
        if ((lane & 15) < 2)
          asm volatile("global_store_dwordx4 %0, %1, %2 sc1 ; ALWAYS\n\ts_nop 1" ::"v"(O_RS + (4096 + (cc * 8 + mt) * D + nt * 64 + aw * 32 + (lane & 1) * 16 + (lane >> 4) * 4) * 4),
                       "v"(nsent4(selu((lane & 1) ? ~0u : 0u, __builtin_bit_cast(u32x4, qa[1]), __builtin_bit_cast(u32x4, qa[0])))), "s"(ws) : "memory");
      }
      BAR(33);
      {
        asm volatile("s_waitcnt vmcnt(%8)" : W8(wv) : "n"(8 - B_WN + 3) : "memory");
        TR8(wt, wv);
        const f32x2 o = MV64(wt, sc);
        GPUB(o, O_G2 + gpo * 8);
      }
      JSET();
      BAR(34);
#pragma unroll
      for (int k = 3; k <= B_J; ++k) {
        BAR(32 + k);
        if (k == V_K && !aw) GP4(O_VP + (cc * 8 + mt) * 4096 + lane * 16, "");
      }
    }
    u32x4 dT[8];
    u32x4 m[3];
    u32x2 kk;
#define K2LD(W) asm volatile("global_load_dwordx2 %0, %1, %2 sc1" W : "=&v"(kk) : "v"(O_K2 + (lane & 15) * 64 + (xcd & (K_R - 1)) * 1024), "s"(ws) : "memory")
    {
      constexpr BSched sch;

      const unsigned lbase = lds0 + hw * 1024;
      u32x4 rq[2];
      u32x4 rr[8];
      unsigned cbv;
#define RDLD() asm volatile("global_load_dwordx4 %0, %2, %3 sc1\n\tglobal_load_dwordx4 %1, %2, %4 sc1" : "=&v"(rq[0]), "=&v"(rq[1]) : "v"(ro), "s"(x), "s"(x + 4096) : "memory")
#define RRLD(W)                                                                                                                               \
  asm volatile("global_load_dwordx4 %0, %9, %10 sc1\n\tglobal_load_dwordx4 %1, %9, %11 sc1\n\t"                                              \
               "global_load_dwordx4 %2, %9, %12 sc1\n\tglobal_load_dwordx4 %3, %9, %13 sc1\n\t"                                              \
               "global_load_dwordx4 %4, %14, %10 sc1\n\tglobal_load_dwordx4 %5, %14, %11 sc1\n\t"                                            \
               "global_load_dwordx4 %6, %14, %12 sc1\n\tglobal_load_dwordx4 %7, %14, %13 sc1\n\tglobal_load_dword %8, %15, %10 sc1" W         \
               : "=&v"(rr[0]), "=&v"(rr[1]), "=&v"(rr[2]), "=&v"(rr[3]), "=&v"(rr[4]), "=&v"(rr[5]), "=&v"(rr[6]), "=&v"(rr[7]), "=&v"(cbv)  \
               : "v"(ro), "s"(ws), "s"(ws + 16384), "s"(ws + 32768), "s"(ws + 49152), "v"(ro + 65536), "v"(O_MOD + 4096 + cj * 2)            \
               : "memory")
      unsigned wq;
      unsigned mr;
#define QLD(W)                                                                                                           \
  asm volatile("global_load_dwordx4 %0, %3, %5 offset:-4096 sc1\n\tglobal_load_dwordx4 %1, %3, %5 sc1\n\tglobal_load_dword %2, %4, %5 sc1" W \
               : "=&v"(m[0]), "=&v"(m[1]), "=&v"(mr)                                                                     \
               : "v"(qo), "v"(so_), "s"(ws)                                                                              \
               : "memory")
#define MODLD()                                                                                                                \
  asm volatile("global_load_dwordx4 %0, %3, %6 sc1\n\tglobal_load_dwordx4 %1, %4, %6 sc1\n\tglobal_load_dwordx4 %2, %5, %6 sc1" \
               : "=&v"(m[0]), "=&v"(m[1]), "=&v"(m[2])                                                                         \
               : "v"(mo), "v"(mo1), "v"(mo2), "s"(ws)                                                                          \
               : "memory")
#define MODW(n) asm volatile("s_waitcnt vmcnt(%3)" : "+v"(m[0]), "+v"(m[1]), "+v"(m[2]) : "n"(n) : "memory")
#pragma unroll
      for (int k = B_J; k < 16; ++k) {
        if (k > B_J) {
          if (k == 2 * U_N1 - 1 && !urdy) UPOLL(7);
          WAIT_V(sch.cnt[k]);
          if (k == B_QAT + B_QD + 1) WAIT_L(0);
          BAR(32 + k);
        }
        if (k > B_J && k - 2 + NS < 16) {
          const char* yp = Hc + ((mt * 2 + k - 2 + NS) & 15) * 128;
          const unsigned l = lbase + ((30 + k + NS) % NS) * 16384;
#pragma unroll
          for (int i = 0; i < 4; ++i) GLDS(l + i * 4096, voff[i], yp);
        }
        if (k == B_TK && hw == 1 && b >= 192) {
          const unsigned t = tick();
          if (lane == 0) asm volatile("global_store_dword %0, %1, %2 sc1" ::"v"(O_M2 + 8448 + (b - 192) * 4), "v"(min(t, SENT - 1u)), "s"(ws) : "memory");
        }
        if (k == B_RAT + 2) {
          WAIT_V(sch.rcnt);
          if (rdc) {
            asm volatile("" : W8(rr), "+v"(cbv));
            POLL(__all((TG2(rr, 0) | TG2(rr, 2) | TG2(rr, 4) | TG2(rr, 6)) == 0u && cbv != SENT), 2, RRLD(VW0));
            float t[2];
#pragma unroll
            for (int j = 0; j < 2; ++j) {
#define RF(q) __uint_as_float(rr[q][2 * j])
              t[j] = ((RF(0) + RF(1)) + (RF(2) + RF(3))) + ((RF(4) + RF(5)) + (RF(6) + RF(7)));
            }
            const unsigned o = pack2(swap32_add(t[0], t[0]), swap32_add(t[1], t[1])), f = gbq ? cbv : o;
            const float pr = row_sum(bflo(f) * bflo(wq) + bfhi(f) * bfhi(wq)), ps = swap16_add(pr, pr);
            if (lane <= 32) asm volatile("global_store_dword %0, %1, %2 sc1" ::"v"(ra1), "v"(min(lane < 32 ? o : __float_as_uint(ps), SENT - 1u)), "s"(ws) : "memory");
            TS(12);
          } else {
            asm volatile("" ::"v"(rq[0]), "v"(rq[1]));
          }
        }
        if (k == V_T && hw == 2) {
          WAIT_V(sch.vcnt);
          asm volatile("" : "+v"(gp[0]), "+v"(gp[1]), "+v"(gp[2]), "+v"(gp[3]));
          POLL(!__any(GPBAD), 2, GP4(O_VP + (cc * 8 + mt) * 4096 + lane * 16, VW0));
          const f32x4 d = GPSUM();
          if (lane < 32) *reinterpret_cast<u32x2*>(shm + VT + (lane & 15) * 16 + g4 * 8) = u32x2{pack2(d[0], d[1]), pack2(d[2], d[3])};
          WAIT_L(0);
        }
        if (k == V_T + 1) {
          f32x4 n[2] = {};
#pragma unroll
          for (int q = 0; q < 8; ++q)
            n[q >> 2] = __builtin_amdgcn_mfma_f32_16x16x32_bf16(BF8(*reinterpret_cast<const u32x4*>(shm + VT + (q & 3) * 64 + g4 * 16)),
                                                                BF8(*reinterpret_cast<const u32x4*>(shm + STG + hw * 8192 + q * 1024 + lane * 16)), n[q >> 2], 0, 0, 0);
          if (lane < 16)
            asm volatile("global_store_dword %0, %1, %2 sc1" ::"v"(O_NP + nt * 4096 + (cc * 8 + mt) * 256 + hw * 64 + lane * 4), "v"(min(pack2(n[0][0], n[1][0]), SENT - 1u)), "s"(ws) : "memory");
          TS(33);
        }
        if (k == N_K + N_D && rnp) {
            WAIT_V(sch.ncnt);
            asm volatile("" : "+v"(gp[0]), "+v"(gp[1]), "+v"(gp[2]), "+v"(gp[3]));
            POLL(!__any(GPBAD), 2, GP4(O_NP + nt * 4096 + lane * 16, VW0));
            const unsigned r0 = nt * 128 + ((lane >> 2) & 3) * 32 + (lane & 3) * 4 + (g4 & 1) * 2;
        const f32x4 d = GPSUM();
        unsigned ky[4];
#pragma unroll
        for (int i = 0; i < 4; ++i) {
          const unsigned f = __float_as_uint(d[i]);
          ky[i] = (((f ^ ((unsigned)((int)f >> 31) | 0x80000000u)) >> 1) & ~2047u) | (r0 + (i & 1) * 16 + (i >> 1));
        }
        unsigned t1 = max(max(ky[0], ky[1]), max(ky[2], ky[3])), t2 = max(min(max(ky[0], ky[1]), max(ky[2], ky[3])), max(min(ky[0], ky[1]), min(ky[2], ky[3])));
        TOP2(0xB1); TOP2(0x4E); TOP2(0x141); TOP2(0x140);
        TOP2S(__builtin_amdgcn_permlane16_swap);
            if (lane < K_R) asm volatile("global_store_dwordx2 %0, %1, %2 sc1" ::"v"(O_K2 + nt * 64 + lane * 1024), "v"(u32x2{t1, t2}), "s"(ws) : "memory");
            TS(34);
        }
        if (k == N_K && rnp) GP4(O_NP + nt * 4096 + lane * 16, "");
        if (k == B_QAT + B_QD) {
          asm volatile("s_waitcnt vmcnt(%3)" : "+v"(m[0]), "+v"(m[1]), "+v"(mr) : "n"(sch.qcnt) : "memory");
          POLL(__all(max(max(max4(m[0]), max4(m[1])), mr) != SENT), 2, QLD(VW0));
          const f32x4 t = __builtin_bit_cast(f32x4, m[0]) + __builtin_bit_cast(f32x4, m[1]);
          const float r = swap16_add(swap32_add(t[0], t[2]), swap32_add(t[1], t[3]));
          const unsigned a0 = pack2(r, r);
          asm volatile("ds_write_b64 %0, %1" ::"v"(qa), "v"(u32x2{a0, pack2(r - bflo(a0), r - bflo(a0))}) : "memory");
          const float rs = __uint_as_float(mr ^ ((hw & 1) ? 0x80000000u : 0u));
          const unsigned bb = pack2(rs, rs - rbf(rs));
          *reinterpret_cast<u32x2*>(shm + RKB + lane * 16 + (hw & 1) * 8) = u32x2{bb, bb};
        }
        if (k == CPF_B && hw == 2 && lane < 12) PFL(cpf);
        if (k == B_RAT) {
          if (rdc) RRLD(""); else RDLD();
          asm volatile("ds_read_b32 %0, %1\n\ts_waitcnt lgkmcnt(0)" : "=v"(wq) : "v"(wqa) : "memory");
        }
        if (k == B_QAT) QLD("");
        if (k == B_MAT) MODLD();
        if (k == B_WK) {
#pragma unroll
          for (int q = 8 - B_WN; q < 8; ++q) LDWI(wdv[q], od, wd, D * 2);
        }
      }
      MODW(sch.mcnt);
      TS(30);
      SKEWPT();
      POLL(__all(max(max(max4(m[0]), max4(m[1])), max4(m[2])) != SENT), 4, MODLD(); MODW(0));
      *reinterpret_cast<u32x4*>(&svb[hw][lane * 8]) = m[0];
      *reinterpret_cast<u32x4*>(&svb[hw][512 + lane * 8]) = m[1];
      if (hw == 0) {
        asm volatile("ds_write_b128 %0, %1" ::"v"(ma2), "v"(m[2]) : "memory");
      } else if (hw == 2) {
        int d = max(max((int)(m[2][0] - ts0), (int)(m[2][1] - ts0)), max((int)(m[2][2] - ts0), (int)(m[2][3] - ts0)));
        d = max(d, (int)dpp_u<0xB1>(d));
        d = max(d, (int)dpp_u<0x4E>(d));
        d = max(d, (int)dpp_u<0x141>(d));
        d = max(d, (int)dpp_u<0x140>(d));
        ssel[7] = ts0 + d + 235 + ((d - T_N) * T_A >> 8);
      } else {
        const f32x4 v = __builtin_bit_cast(f32x4, m[2]);
        const f32x2 c = {v[0] + v[1], v[2] + v[3]};
        const float s4 = dpp_add<0x4E>(dpp_add<0xB1>(c[0] + c[1]));
        if (lane < 8) asm volatile("ds_write_b64 %0, %1" ::"v"(ma2), "v"(lane < 4 ? c : f32x2{s4, s4}) : "memory");
      }
    }
    if (hw || !L_PR) __builtin_amdgcn_s_setprio(0);
    TS(17);
    BT(48);
    LBAR();
    BTE();
    TS(18);
    if (hw == 0) {
      nap_tick((unsigned)__builtin_amdgcn_readfirstlane((int)ssel[7]) + T_K);
      POLL2(max(kk[0], kk[1]) == SENT, K_S, PCAP, K2LD(VW0));
      TS(35);
      const unsigned me = lane < 32 ? kk[0] : kk[1], ot = (lane & 16) ? kk[1] : kk[0];
      unsigned rk = ot > me;
      sfor<15>([&](auto r) __attribute__((always_inline)) { rk += dpp_u<0x121 + decltype(r)::value>(ot) > me; });
      auto rs_ = __builtin_amdgcn_permlane16_swap(rk, rk, false, false);
      rk = rs_[0] + rs_[1];
      if (rk < 8u) {
        *reinterpret_cast<unsigned*>(shm + LST + rk * 4) = me & 2047u;
        if ((lane & 15) == nt) *reinterpret_cast<unsigned*>(shm + SLT + (me & 127u) * 4) = rk + 1;
      }
      asm volatile("" ::: "memory");
      ssel[20] = 1u;
    }
    TS(19);
    unsigned sfa = lds0 + SPARE + 10368, sv, one = 1u;
    RETIRE();
    LSPIN(sv, ssel + 20, sv != 0u);
    asm volatile("" ::: "memory");
    const int kc = (g & 3) * 256 + hw * 64 + (lane & 7) * 8;
    const u32x4 cbq = *reinterpret_cast<const u32x4*>(&svb[2][kc]), sbq = *reinterpret_cast<const u32x4*>(&svb[1][kc]);
    f32x4 cb2[2] = {lo4(cbq), hi4(cbq)}, sb2[2] = {lo4(sbq), hi4(sbq)};
    float sw = cwj[8], cj8 = cwj[lane & 7];
    unsigned rk2 = 0x8ffeu - (*reinterpret_cast<const unsigned*>(shm + LST + (lane >> 3) * 4) & 2047u) * 2;
    unsigned xo_ = O_X1C + (lane >> 3) * 2048 + kc * 2, hwa = lds0 + XC + hw * 1024 + (lane & 7) * 128 + (((lane >> 3) ^ lane) & 7) * 16;
    unsigned hra = lds0 + XC + g4 * 128 + ((lane ^ g4) & 7) * 16, hrb = hra ^ 64, ara = lds0 + SPARE + 15872 + (lane & 2) * 64 + g4 * 32 + (lane & 1) * 16;
    unsigned pso = O_PART + ((g >> 2) * 2048 + (lane & 8) * 128 + (g & 3) * 256 + hw * 64 + (lane & 7) * 8 + g4 * 2) * 4;
    unsigned gso = O_GU + g * 1024 + (lane >> 5) * 256 + hw * 64 + (lane & 15) * 4, awa = lds0 + SPARE + 15872 + lane * 4, sfw = sfa + hw * 4;
    unsigned gl0 = O_GU + (g >> 2) * 4096 + lane * 8, gl1 = O_GU + (xcd * 7 + (mt + 7 - W_UO) % 7) * 4096 + 512 + lane * 8;
    asm volatile("" : "+v"(sfa), "+v"(one), "+v"(hra), "+v"(hrb), "+v"(ara), "+v"(pso), "+v"(gso), "+v"(awa), "+v"(gl0), "+v"(gl1), "+v"(sfw), "+v"(qm));
    asm volatile("" : "+v"(cb2[0]), "+v"(cb2[1]), "+v"(sb2[0]), "+v"(sb2[1]), "+v"(hwa), "+v"(sw), "+v"(cj8), "+v"(rk2), "+v"(xo_));
    TS(21);
    nap_tick((unsigned)__builtin_amdgcn_readfirstlane((int)ssel[7]) + T_C3 - 10);
    __builtin_amdgcn_s_setprio(H_P);
    wait_tick((unsigned)__builtin_amdgcn_readfirstlane((int)ssel[7]) + T_C3);
    u32x4 st, xv_;
    POLL2(max(max4(xv_), max4(st)) == SENT, D_S, PCAP,
          asm volatile("global_load_dwordx4 %0, %2, %4 sc1\n\tglobal_load_dwordx4 %1, %3, %4 sc1" VW0 : "=&v"(st), "=&v"(xv_) : "v"(O_ST2 + lane * 16), "v"(xo_), "s"(ws) : "memory"));
    TS(22);
    SKEWPT();
#define SUM8(v) dpp_add<0x141>(dpp_add<0x4E>(dpp_add<0xB1>(v)))
    const f32x4 sf = __builtin_bit_cast(f32x4, st);
    const float mean = SUM8(sf[0]) * 0.125f, dm = sf[0] - mean;
    const float rstd = __builtin_amdgcn_rsqf(SUM8(sf[1] + 128.0f * (dm * dm)) * (1.0f / D) + 1e-5f);
    asm volatile("ds_write_b128 %0, %1\n\tds_write_b32 %2, %3 offset:64" ::"v"(hwa),
                 "v"(pk8(((lo4(xv_) - mean) * rstd) * cb2[0] + sb2[0], ((hi4(xv_) - mean) * rstd) * cb2[1] + sb2[1])), "v"(sfw), "v"(one) : "memory");
    TS(3);
    u32x4 ha[8], fg;
#define HSLD(W)                                                                                                                             \
  asm volatile(                                                                                                                             \
      "ds_read_b128 %8, %11 offset:64\n\tds_read_b128 %0, %9\n\tds_read_b128 %1, %10 offset:512\n\tds_read_b128 %2, %9 offset:1024\n\t"       \
      "ds_read_b128 %3, %10 offset:1536\n\tds_read_b128 %4, %9 offset:2048\n\tds_read_b128 %5, %10 offset:2560\n\t"                          \
      "ds_read_b128 %6, %9 offset:3072\n\tds_read_b128 %7, %10 offset:3584" W                                                               \
      : "=&v"(ha[0]), "=&v"(ha[1]), "=&v"(ha[2]), "=&v"(ha[3]), "=&v"(ha[4]), "=&v"(ha[5]), "=&v"(ha[6]), "=&v"(ha[7]), "=&v"(fg)           \
      : "v"(hra), "v"(hrb), "v"(sfa)                                                                                                        \
      : "memory")
    unsigned key = mkkey(rstd * SUM8(sf[2] + dm * cj8) + sw) | rk2;
    asm volatile("" : "+v"(key));
    HSLD("");
    unsigned t1 = key, t2 = 0u;
    TOP2(0x140);
    TOP2S(__builtin_amdgcn_permlane16_swap);
    TOP2S(__builtin_amdgcn_permlane32_swap);
    const int l1 = __builtin_ctzll(__builtin_amdgcn_ballot_w64(key == t1)), l2 = __builtin_ctzll(__builtin_amdgcn_ballot_w64(key == t2));
    unsigned sl = lane < 32 ? l1 : l2;
    asm volatile("s_waitcnt lgkmcnt(0)" : W8(ha), "+v"(fg), "+v"(sl)::"memory");
    TS(2);
    if (b < NPART) {
#define HBAD ((fg[0] & fg[1] & fg[2] & fg[3]) == 0u)
      if (__any(HBAD)) POLL2(HBAD, 0, 1 << 26, HSLD("\n\ts_waitcnt lgkmcnt(0)"));
      TS(23);
      f32x4 dg = {0.f, 0.f, 0.f, 0.f}, du = {0.f, 0.f, 0.f, 0.f};
#pragma unroll
      for (int f = 0; f < 8; ++f) {
        dg = __builtin_amdgcn_mfma_f32_16x16x32_bf16(BF8(ha[f]), BF8(gT[f]), dg, 0, 0, 0);
        du = __builtin_amdgcn_mfma_f32_16x16x32_bf16(BF8(ha[f]), BF8(uT[f]), du, 0, 0, 0);
      }
      const bool s0 = sl & 8, s1 = sl & 16;
      if (((lane ^ (sl >> 1)) & 16) == 0)
        asm volatile("global_store_dword %0, %1, %3 sc1 ; ALWAYS\n\tglobal_store_dword %0, %2, %3 offset:512 sc1" ::"v"(gso),
                     "v"(nsent(s1 ? (s0 ? dg[3] : dg[2]) : (s0 ? dg[1] : dg[0]))), "v"(nsent(s1 ? (s0 ? du[3] : du[2]) : (s0 ? du[1] : du[0]))), "s"(ws) : "memory");
    }
    if (hw == 0) asm volatile("ds_write_b64 %0, %1" ::"v"(sfa), "v"(u32x2{t1, t2}) : "memory");
    if (b < NPART) {
      TS(24);
      asm volatile("" : W8(wdv)::"memory");
      TR8(dT, wdv);
      if (hw == 0) {
        u32x2 rr[8];
        if (E_D) __builtin_amdgcn_s_sleep(E_D);
#define MX2(i) max(max(rr[i][0], rr[i][1]), max(rr[i + 1][0], rr[i + 1][1]))
        POLL2(max(max(MX2(0), MX2(2)), max(MX2(4), MX2(6))) == SENT, 1, PCAP,
              asm volatile("global_load_dwordx2 %0, %8, %10 sc1\n\tglobal_load_dwordx2 %1, %8, %10 offset:1024 sc1\n\t"
                           "global_load_dwordx2 %2, %8, %10 offset:2048 sc1\n\tglobal_load_dwordx2 %3, %8, %10 offset:3072 sc1\n\t"
                           "global_load_dwordx2 %4, %9, %10 sc1\n\tglobal_load_dwordx2 %5, %9, %10 offset:1024 sc1\n\t"
                           "global_load_dwordx2 %6, %9, %10 offset:2048 sc1\n\tglobal_load_dwordx2 %7, %9, %10 offset:3072 sc1" VW0
                           : "=&v"(rr[0]), "=&v"(rr[1]), "=&v"(rr[2]), "=&v"(rr[3]), "=&v"(rr[4]), "=&v"(rr[5]), "=&v"(rr[6]), "=&v"(rr[7])
                           : "v"(gl0), "v"(gl1), "s"(ws)
                           : "memory"));
        TS(8);
#define R2(i) __builtin_bit_cast(f32x2, rr[i])
        const f32x2 gs = (R2(0) + R2(1)) + (R2(2) + R2(3)), us = (R2(4) + R2(5)) + (R2(6) + R2(7));
        const unsigned gp = pack2(gs[0], gs[1]), up = pack2(us[0], us[1]);
        const unsigned sp = pack2(bflo(gp) * __builtin_amdgcn_rcpf(1.0f + __expf(-bflo(gp))), bfhi(gp) * __builtin_amdgcn_rcpf(1.0f + __expf(-bfhi(gp))));
        asm volatile("ds_write_b32 %0, %1\n\tds_write_b32 %2, %3 offset:24" ::"v"(awa), "v"(pack2(bflo(sp) * bflo(up), bfhi(sp) * bfhi(up))), "v"(sfa), "v"(one) : "memory");
      }
      TS(25);
      SKEWPT();
      POLL2(sv == 0u, 0, 1 << 26, asm volatile("ds_read_b32 %1, %3 offset:24\n\tds_read_b128 %0, %2\n\ts_waitcnt lgkmcnt(0)" : "=&v"(fg), "=&v"(sv) : "v"(ara), "v"(sfa) : "memory"));
      const f32x2 dd = MV64(dT, fg);
      asm volatile("global_store_dwordx2 %0, %1, %2 sc1" ::"v"(pso), "v"(u32x2{nsent(dd[0]), nsent(dd[1])}), "s"(ws) : "memory");
      TS(6);
    } else if (hw == 3) {
      u32x2 g0, g1;
      nap_tick((unsigned)__builtin_amdgcn_readfirstlane((int)ssel[7]) + T_C4);
      POLL2(min(min(g0[0], g0[1]), min(g1[0], g1[1])) == 0u, 2, PCAP,
            asm volatile("global_load_dwordx2 %0, %2, %4 sc1\n\tglobal_load_dwordx2 %1, %3, %4 sc1" VW0
                         : "=&v"(g0), "=&v"(g1) : "v"(O_KEY + lane * 8 * KSTR), "v"(O_KEY + (lane + 64) * 8 * KSTR), "s"(ws) : "memory"));
      const unsigned kk = (b & 1) ? t2 : t1;
      const int nb = __builtin_popcountll(__builtin_amdgcn_ballot_w64(g0[0] > kk)) + __builtin_popcountll(__builtin_amdgcn_ballot_w64(g0[1] > kk)) +
                     __builtin_popcountll(__builtin_amdgcn_ballot_w64(g1[0] > kk)) + __builtin_popcountll(__builtin_amdgcn_ballot_w64(g1[1] > kk));
      ssel[3] = nb + (b & 1) >= 2 ? 2u : 1u;
      TS(29);
    } else if (hw < 2) {
      const int tok = b & 1, c0 = ((b - NPART) >> 1) * 64 + hw * 32 + (lane & 7) * 4, col = c0 + g4;
      const unsigned td = tick(), kk = tok ? t2 : t1;
      const int ln = tok ? l2 : l1;
      unsigned v;
      POLL2(v == SENT, 1, PCAP, asm volatile("global_load_dword %0, %1, %2 sc1" VW0 : "=&v"(v) : "v"(O_X1C + (ln >> 3) * 2048 + (col >> 1) * 4), "s"(ws) : "memory"));
      float x1 = (col & 1) ? bfhi(v) : bflo(v);
      float h2 = rbf(((x1 - __int_as_float(__builtin_amdgcn_readlane(__float_as_int(mean), ln))) * __int_as_float(__builtin_amdgcn_readlane(__float_as_int(rstd), ln))) * bf2f(svb[2][col]) + bf2f(svb[1][col]));
      float gb = bf2f(svb[3][col]), rsg = rbf(1.0f / (1.0f + __expf(-key_val(kk))));
      unsigned oo_ = (key_row(kk) * D + col) * 2, po = O_PART + ((lane >> 3) * 2048 + tok * 1024 + c0) * 4, p1 = po + 4 * 65536;
      asm volatile("" : "+v"(x1), "+v"(h2), "+v"(gb), "+v"(rsg), "+v"(oo_), "+v"(po), "+v"(p1));
      u32x4 y[7];
      nap_tick(td + F_T);
      POLL2(max(max(max(max4(y[0]), max4(y[1])), max(max4(y[2]), max4(y[3]))), max(max(max4(y[4]), max4(y[5])), max4(y[6]))) == SENT || sv == 0u, OPT_RR, PCAP,
            asm volatile("global_load_dwordx4 %0, %7, %9 sc1\n\tglobal_load_dwordx4 %1, %7, %10 sc1\n\tglobal_load_dwordx4 %2, %7, %11 sc1\n\t"
                         "global_load_dwordx4 %3, %7, %12 sc1\n\tglobal_load_dwordx4 %4, %8, %9 sc1\n\tglobal_load_dwordx4 %5, %8, %10 sc1\n\t"
                         "global_load_dwordx4 %6, %8, %11 sc1"
                         : "=&v"(y[0]), "=&v"(y[1]), "=&v"(y[2]), "=&v"(y[3]), "=&v"(y[4]), "=&v"(y[5]), "=&v"(y[6])
                         : "v"(po), "v"(p1), "s"(ws), "s"(ws + 65536), "s"(ws + 131072), "s"(ws + 196608)
                         : "memory");
            asm volatile("s_waitcnt vmcnt(2)\n\tds_read_b32 %7, %8 offset:12\n\ts_waitcnt vmcnt(0) lgkmcnt(0)"
                         : "+v"(y[0]), "+v"(y[1]), "+v"(y[2]), "+v"(y[3]), "+v"(y[4]), "+v"(y[5]), "+v"(y[6]), "=&v"(sv) : "v"(sfa) : "memory"));
      TS(28);
#define YF(i) __builtin_bit_cast(f32x4, y[i])
      const f32x4 t = ((YF(0) + YF(1)) + (YF(2) + YF(3))) + ((YF(4) + YF(5)) + YF(6));
      const float sum = dpp_add<0x128>(swap16_add(swap32_add(t[0], t[2]), swap32_add(t[1], t[3])));
      asm volatile("global_store_short %0, %1, %2" ::"v"(oo_), "v"(pack2(x1 + gb * (sv == 2u ? h2 : rbf(h2 + rbf(rbf(sum) * rsg))), 0.f)), "s"(out) : "memory");
      TS(15);
    }
    TSE();
  }
}

extern "C" void kernel_launch(void* const* d_in, const int* in_sizes, int n_in, void* d_out, int out_size, void* d_ws, size_t ws_size,
                              hipStream_t stream) {
  const u16* x = (const u16*)d_in[0];
  const u16* wsr = (const u16*)d_in[1];
  const u16* wsi = (const u16*)d_in[2];
  const u16* whr = (const u16*)d_in[3];
  const u16* whi = (const u16*)d_in[4];
  const u16* w_r = (const u16*)d_in[6];
  const u16* w_gate = (const u16*)d_in[7];
  const u16* w_up = (const u16*)d_in[8];
  const u16* w_down = (const u16*)d_in[9];
  const u16* c = (const u16*)d_in[10];
  const u16* w_mod = (const u16*)d_in[11];
  u16* out = (u16*)d_out;
  k_main<<<256, 512, 0, stream>>>(x, (char*)d_ws, wsr, wsi, c, w_mod, whr, whi, w_r, w_gate, w_up, w_down, out);
}
```

```cpp
#include <hip/hip_runtime.h>
#include <hip/hip_bf16.h>
#include <stdint.h>
#include <utility>

#define NEWLAY 1
typedef unsigned short u16;
typedef unsigned long long u64;
using bf16x8 = __attribute__((ext_vector_type(8))) short;
using bf16v8 = __attribute__((ext_vector_type(8))) __bf16;
using f32x4 = __attribute__((ext_vector_type(4))) float;
using f32x2 = __attribute__((ext_vector_type(2))) float;
using u32x4 = __attribute__((ext_vector_type(4))) unsigned;
using u32x2 = __attribute__((ext_vector_type(2))) unsigned;

constexpr int S = 2048, D = 1024, F = 3584;

__device__ __forceinline__ float bf2f(unsigned v) { return __uint_as_float(v << 16); }
__device__ __forceinline__ float bflo(unsigned v) { return __uint_as_float(v << 16); }
__device__ __forceinline__ float bfhi(unsigned v) { return __uint_as_float(v & 0xffff0000u); }
__device__ __forceinline__ unsigned f2bf(float f) {
  __hip_bfloat16 b = __float2bfloat16(f);
  return (unsigned)*reinterpret_cast<u16*>(&b);
}
__device__ __forceinline__ float rbf(float f) { return bf2f(f2bf(f)); }
using bf16v2 = __attribute__((ext_vector_type(2))) __bf16;
__device__ __forceinline__ unsigned pack2(float lo, float hi) {
  return __builtin_bit_cast(unsigned, __builtin_convertvector(f32x2{lo, hi}, bf16v2));
}

template <int CTRL>
__device__ __forceinline__ float dpp_f(float v) {
  return __int_as_float(__builtin_amdgcn_mov_dpp(__float_as_int(v), CTRL, 0xf, 0xf, true));
}
template <int CTRL>
__device__ __forceinline__ float dpp_add(float v) { return v + dpp_f<CTRL>(v); }
__device__ __forceinline__ float row_sum(float v) { return dpp_add<0x140>(dpp_add<0x141>(dpp_add<0x4E>(dpp_add<0xB1>(v)))); }
template <int CTRL>
__device__ __forceinline__ unsigned dpp_u(unsigned v) { return (unsigned)__builtin_amdgcn_mov_dpp((int)v, CTRL, 0xf, 0xf, true); }
__device__ __forceinline__ u64 mk64(unsigned hi, unsigned lo) { return ((u64)hi << 32) | lo; }
__device__ __forceinline__ float swap32_add(float a, float b) {
  auto r = __builtin_amdgcn_permlane32_swap(__float_as_uint(a), __float_as_uint(b), false, false);
  return __uint_as_float(r[0]) + __uint_as_float(r[1]);
}
__device__ __forceinline__ float swap16_add(float a, float b) {
  auto r = __builtin_amdgcn_permlane16_swap(__float_as_uint(a), __float_as_uint(b), false, false);
  return __uint_as_float(r[0]) + __uint_as_float(r[1]);
}
__device__ __forceinline__ float ar4(float v) {
  const float t = swap32_add(v, v);
  return swap16_add(t, t);
}
__device__ __forceinline__ float wave_sum_s(float v) {
  v = row_sum(v);
  v += __int_as_float(__builtin_amdgcn_update_dpp(0, __float_as_int(v), 0x142, 0xa, 0xf, false));
  v += __int_as_float(__builtin_amdgcn_update_dpp(0, __float_as_int(v), 0x143, 0xc, 0xf, false));
  return __int_as_float(__builtin_amdgcn_readlane(__float_as_int(v), 63));
}
__device__ __forceinline__ float silu_bf(float c) { return rbf(c * __builtin_amdgcn_rcpf(1.0f + __expf(-c))); }
__device__ __forceinline__ float dsum(unsigned v, float a) {
  return __builtin_amdgcn_fdot2_f32_bf16(__builtin_bit_cast(bf16v2, v), __builtin_bit_cast(bf16v2, 0x3f803f80u), a, false);
}
template <class Fn, int... I>
__device__ __forceinline__ void sfor_(Fn&& f, std::integer_sequence<int, I...>) { (f(std::integral_constant<int, I>{}), ...); }
template <int N, class Fn>
__device__ __forceinline__ void sfor(Fn&& f) { sfor_(f, std::make_integer_sequence<int, N>{}); }
constexpr unsigned SENT = 0xFFFFFFFFu;
constexpr int NPART = 224;
__device__ __forceinline__ f32x2 unpk(unsigned v) { return f32x2{bflo(v), bfhi(v)}; }

constexpr size_t WS_EPO = 0;
constexpr size_t WS_XFL = 4096;
constexpr size_t WS_MODP = 40960;
constexpr size_t WS_KEYG = 320u << 10;
constexpr size_t WS_UFL = WS_KEYG + 16384;
constexpr size_t WS_MOD = WS_KEYG + (80u << 10);
constexpr size_t WS_RS = WS_KEYG + (92u << 10);
constexpr size_t WS_PART = WS_KEYG + (316u << 10);
constexpr unsigned O_MODP = WS_MODP, O_XFL = WS_XFL, O_KEY = WS_KEYG, O_MOD = WS_MOD, O_HDR = WS_MOD + 8192, O_RS = WS_RS, O_M2 = WS_RS + 80 * 1024;
constexpr unsigned O_PART = WS_PART, O_GU = O_PART + 56 * 8192, O_STAT = O_GU + 224 * 1024, O_X1C = O_STAT + 262144;
constexpr unsigned O_ST2 = O_X1C + 20480, O_VD = O_ST2 + 2048, O_P2 = 1536u << 10, O_F2 = 3584u << 10;
constexpr unsigned O_GC = O_M2 + 8704, O_VP = O_M2 + 12288, O_NP = O_VP + 65536, O_K2 = O_NP + 65536, O_G2 = 3u << 20, O_G1 = O_G2 + 262144;
static_assert(O_K2 + 4096 == O_PART && O_VD + 4096 <= O_KEY + (512 + 768) * 1024 && O_PART + O_P2 >= O_VD + 4096 && O_STAT + O_P2 <= O_G2, "armed area");
constexpr size_t WS_H = 4u << 20;
constexpr size_t WS_T = WS_H + (size_t)S * D * 2;
constexpr size_t WS_P = WS_T + (size_t)2 * D * S * 2;

#define TSG(base, i)
#define TSD(i)
#define PCAP (1 << 20)
#define PDBG(it)
#define TS(i)
#define TSE()
#define BT(k)
#define BTE()
#define BAR(k) do { BT(k); BARRIER(); } while (0)
__device__ __forceinline__ void st16_wt(void* p, u32x4 v) {
  asm volatile("global_store_dwordx4 %0, %1, off sc1\n\ts_nop 1" ::"v"(p), "v"(v) : "memory");
}
#define WAIT_V(n) asm volatile("s_waitcnt vmcnt(%0)" ::"n"(n) : "memory")
#define WAIT_L(n) asm volatile("s_waitcnt lgkmcnt(%0)" ::"n"(n) : "memory")
#define BARRIER() asm volatile("s_barrier" ::: "memory")
#define LBAR() asm volatile("s_waitcnt lgkmcnt(0)\n\ts_barrier" ::: "memory")
#define DSR(dst, addr, off) asm volatile("ds_read_b128 %0, %1 offset:%2" : "=v"(dst) : "v"(addr), "n"(off) : "memory")
#define MFMA(c, a, b) asm volatile("v_mfma_f32_16x16x32_bf16 %0, %1, %2, %0" : "+v"(c) : "v"(a), "v"(b))
#define MFMA0(c, a, b) asm volatile("v_mfma_f32_16x16x32_bf16 %0, %1, %2, 0" : "=&v"(c) : "v"(a), "v"(b))
#define GLDS(m0v, voff, gbase) \
  asm volatile("s_mov_b32 m0, %0\n\ts_nop 0\n\tglobal_load_lds_dwordx4 %1, %2" ::"s"(m0v), "v"(voff), "s"(gbase) : "memory")
#define LDG(dst, voff, base) asm volatile("global_load_dwordx4 %0, %1, %2" : "=v"(dst) : "v"(voff), "s"(base) : "memory")
#define WPOL " nt"
#define LDM(dst, voff, base) asm volatile("global_load_dwordx4 %0, %1, %2" WPOL : "=v"(dst) : "v"(voff), "s"(base) : "memory")
#define LDX(dst, voff, base, off) asm volatile("global_load_dwordx4 %0, %1, %2 offset:%3" : "=v"(dst) : "v"(voff), "s"(base), "n"(off) : "memory")
#define LDXS(dst, voff, base, off) asm volatile("global_load_dwordx4 %0, %1, %2 offset:%3 sc1" : "=v"(dst) : "v"(voff), "s"(base), "n"(off) : "memory")
#define BF8(v) __builtin_bit_cast(bf16v8, v)
#define LSPIN(v, p, cond)                                   \
  _Pragma("unroll 1") for (int it_ = 0; it_ < (1 << 26); ++it_) { \
    v = __atomic_load_n(p, __ATOMIC_RELAXED);               \
    if (cond) break;                                        \
    __builtin_amdgcn_s_sleep(1);                            \
  }
#define POLL(ok, sl, reload)                                                  \
  if (__builtin_expect(!(ok), 0)) {                                           \
    _Pragma("unroll 1") for (int it = 0; it < PCAP; ++it) {                   \
      PDBG(it);                                                               \
      __builtin_amdgcn_s_sleep(sl);                                           \
      reload;                                                                 \
      if (ok) break;                                                          \
    }                                                                         \
  }
#define POLL2(bad, sl, cap, load)                                             \
  {                                                                           \
    int it = (cap);                                                           \
    u64 live;                                                                 \
    _Pragma("unroll 1") do {                                                  \
      if (sl) __builtin_amdgcn_s_sleep(sl);                                   \
      asm volatile("s_sub_u32 %0, %0, 1\n\ts_cselect_b64 %1, 0, -1" : "+s"(it), "=s"(live)::"scc"); \
      load;                                                                   \
    } while (__builtin_amdgcn_ballot_w64(bad) & live);                        \
    PDBG(PCAP - 2 - it);                                                      \
  }
__device__ __forceinline__ unsigned tick() {
  u64 t;
  asm volatile("s_memrealtime %0\n\ts_waitcnt lgkmcnt(0)" : "=s"(t)::"memory");
  return (unsigned)t;
}
__device__ __forceinline__ void wait_tick(unsigned t) {
#pragma unroll 1
  for (int i = 0; i < 256; ++i)
    if ((int)(tick() - t) >= 0) break;
}
__device__ __forceinline__ void nap_tick(unsigned t) {
#pragma unroll 1
  for (int i = 0; i < 1024; ++i) {
    if ((int)(tick() - t) >= 0) break;
    __builtin_amdgcn_s_sleep(2);
  }
}
#define T_C1 245
#define T_A 40
#define T_N 2500
#define T_D1 78
#define T_C2 322
#define VW0 "\n\ts_waitcnt vmcnt(0)"
#define W8(a) "+v"(a[0]), "+v"(a[1]), "+v"(a[2]), "+v"(a[3]), "+v"(a[4]), "+v"(a[5]), "+v"(a[6]), "+v"(a[7])
#define PFL(ptr) asm volatile("s_mov_b32 m0, %0\n\ts_nop 0\n\tglobal_load_lds_dword %1, off" ::"s"(pfl), "v"(ptr) : "memory")

#define HHOOK(j)
#define XPV 1557
#define XPA0 (XPV / 1000)
#define XPA1 (XPV / 100 % 10)
#define XPB0 (XPV / 10 % 10)
#define XPB1 (XPV % 10)
#define U_X0 6
#define XLD(dst, XP, XH, i)                                                                                   \
  do {                                                                                                        \
    if (MODE) LDXS(dst, (i) ? xo1 : xo0, XP, ((XH)&3) * 2048 - 4096);                                         \
    else LDXS(dst, ((XH)&4) ? xo1 : xo0, XP, ((XH)&3) * 2048 + (i)*1024 - 4096);                              \
  } while (0)
#define HALF(XC, YA, RD, XN, XP, XH, LD, YP)                                                 \
  do {                                                                                       \
    _Pragma("unroll") for (int j = 0; j < 8; ++j) {                                          \
      if ((j & 1) == 0) { if (RD) WAIT_L(5); else WAIT_L(j == 0 ? 5 : 6 - j); }              \
      if (MODE == 2) MFMA0(acc[0][j], Y[j], XC[0]); else if (MODE) MFMA(acc[0][j], Y[j], XC[0]); else MFMA(acc[0][j], XC[0], Y[j]); \
      if (j == 0) DSR(Y[7], YP, 7 * 2048);                                                   \
      else if (RD) DSR(Y[j - 1], YA, (j - 1) * 2048);                                        \
      if (MODE == 2) MFMA0(acc[1][j], Y[j], XC[1]); else if (MODE) MFMA(acc[1][j], Y[j], XC[1]); else MFMA(acc[1][j], XC[1], Y[j]); \
      if ((LD) && j == (((XH)&1) ? XPA0 : XPB0)) XLD(XN[0], XP, XH, 0);                                             \
      if ((LD) && j == (((XH)&1) ? XPA1 : XPB1)) XLD(XN[1], XP, XH, 1);                                             \
      HHOOK(j);                                                                              \
    }                                                                                        \
  } while (0)

__device__ __forceinline__ int key_row(unsigned k) { return (int)((~k >> 1) & 2047u); }
__device__ __forceinline__ unsigned mkkey(float v) {
  const unsigned rb = pack2(0.f, v);
  return rb ^ ((unsigned)((int)rb >> 31) & 0x7fff0000u) ^ 0x80000000u;
}
__device__ __forceinline__ float key_val(unsigned k) {
  const unsigned ord = k >> 16;
  return bf2f(ord ^ ((ord & 0x8000u) ? 0x8000u : 0xffffu));
}
#define OPT_RD 22
#define OPT_RE 20
#define OPT_GD 2
__device__ __forceinline__ unsigned nsent(float v) { return min(__float_as_uint(v), SENT - 1u); }
__device__ __forceinline__ f32x4 lo4(u32x4 q) { return f32x4{bflo(q[0]), bfhi(q[0]), bflo(q[1]), bfhi(q[1])}; }
__device__ __forceinline__ f32x4 hi4(u32x4 q) { return f32x4{bflo(q[2]), bfhi(q[2]), bflo(q[3]), bfhi(q[3])}; }
__device__ __forceinline__ u32x4 pk8(f32x4 a, f32x4 b) { return u32x4{pack2(a[0], a[1]), pack2(a[2], a[3]), pack2(b[0], b[1]), pack2(b[2], b[3])}; }
__device__ __forceinline__ u32x4 nsent4(u32x4 v) { return u32x4{min(v[0], SENT - 1u), min(v[1], SENT - 1u), min(v[2], SENT - 1u), min(v[3], SENT - 1u)}; }
__device__ __forceinline__ unsigned max4(u32x4 v) { return max(max(v[0], v[1]), max(v[2], v[3])); }
__device__ __forceinline__ u32x4 selu(unsigned mk, u32x4 a, u32x4 b) { return (a & mk) | (b & ~mk); }
#define KSTR 16
#define TOP2(CTRL)                                                  \
  do {                                                              \
    const unsigned o1 = dpp_u<CTRL>(t1), o2 = dpp_u<CTRL>(t2);      \
    t2 = max(min(t1, o1), max(t2, o2));                             \
    t1 = max(t1, o1);                                               \
  } while (0)
#define TOP2S(SW)                                                   \
  do {                                                              \
    auto a_ = SW(t1, t1, false, false), b_ = SW(t2, t2, false, false); \
    t2 = max(min(a_[0], a_[1]), max(b_[0], b_[1]));                 \
    t1 = max(a_[0], a_[1]);                                         \
  } while (0)
#define T_C3 253
#define T_C4 340
#define T_G 9
#define T_U 10
#define E_D 6
#define O_DW 6
#define O_DS 4
#define O_TR 1
#define OPOL " sc1"
#define L_PR 1
#define D_W 1
#define X_PA 2
#define G1_C 2
#define R_AT 23
#define B_TK 8
#define W_T 4
#define V_K 3
#define V_T 5
#define N_K 10
#define N_D 2
#define T_K 0
#define H_AT 23
#define G1_S 1
#define V_F 0
#define J_L 0
#define F_T 132
#define K_S 1
#define K_R 1
#define T_C5 470
#define VD_E 1
#define K_E 1
#define T_H 60
#define H_P 2
#define T_X 80
#define D_S 1
#define A_PW 8
#define A_PS 6
#define U_S 4
#define C_LD 2
#define C_DP 8
#define F_D 2
#define P_AT 2
#define F_AT 29
#define XQ_N 6
#define RD_E 12
#define PW0 2
#define PW1 1
#define T_W 70
#define P_D2 1
#define P_D4 1
#define XSTR 16
#define P_D3 20
#ifndef M_NS
#define M_NS 6
#endif
#define A_Q1 27
#define B_RAT 7
#define B_MAT 13
#define U_D 4
#define FSTR 16
#define B_J 4
#define RETIRE()                                                                                                                  \
  do {                                                                                                                            \
    if (hw == 3 && lane == 0) {                                                                                                   \
      __hip_atomic_store(reinterpret_cast<u64*>(xfl + b * XSTR), ~tag, __ATOMIC_RELAXED, __HIP_MEMORY_SCOPE_AGENT);               \
      if (b == 255) __hip_atomic_store(reinterpret_cast<u64*>(epo), tag, __ATOMIC_RELAXED, __HIP_MEMORY_SCOPE_AGENT);             \
    }                                                                                                                             \
  } while (0)
#define U_N1 3
#define OPT_RR 2
#define B_QAT 8
#define B_QD 4
#define TP_PS 5
#define TP_XC 20
#define TP_P0 2
#define TP_X0 24
#define TP_XL 52
#define W_UO 3
#define CPF_K 24
#define CPF_B 8
#define B_WN 4
#define B_WK 15
struct BSched {
  int cnt[16], rcnt, mcnt, qcnt, vcnt, ncnt;
  constexpr BSched() : cnt{}, rcnt(0), mcnt(0), qcnt(0), vcnt(0), ncnt(0) {
    int pos[16] = {}, n = 0, rpos = 0, mpos = 0, qpos = 0, npos = 0;
    for (int k = 0; k <= B_J - 2 + M_NS; ++k) pos[k] = -8 * (B_J - 2 + M_NS - k);
    for (int k = B_J + 1; k < 16; ++k) {
      cnt[k] = n - pos[k] > 63 ? 63 : n - pos[k];
      if (k - 2 + M_NS < 16) { n += 4; pos[k - 2 + M_NS] = n; }
      if (k == B_RAT + 2) rcnt = n - rpos;
      if (k == B_QAT + B_QD) qcnt = n - qpos;
      if (k == V_T) vcnt = n;
      if (k == N_K + N_D) ncnt = n - npos;
      if (k == N_K) npos = n;
      if (k == B_RAT) { n += 2; rpos = n; }
      if (k == B_QAT) { n += 3; qpos = n; }
      if (k == B_MAT) { n += 3; mpos = n; }
      if (k == B_WK) n += B_WN;
    }
    mcnt = n - mpos > 63 ? 63 : n - mpos;
  }
};
__global__ __launch_bounds__(512, 2) void k_main(const u16* __restrict__ x, char* ws, const u16* __restrict__ Y0, const u16* __restrict__ Y1,
                                              const u16* __restrict__ cvec, const u16* __restrict__ wmod, const u16* __restrict__ X0,
                                              const u16* __restrict__ X1, const u16* __restrict__ wr_, const u16* __restrict__ wg,
                                              const u16* __restrict__ wu, const u16* __restrict__ wd, u16* __restrict__ out) {
  constexpr int DX = 4, NS = M_NS, STG = NS * 16384, SPARE = STG + 32768;
  constexpr int LST = SPARE + 17664, SLT = LST + 256, XC = SLT + 512, VT = XC + 4096;
  __shared__ __attribute__((aligned(1024))) char shm[VT + 256];
  u16(*svb)[1024] = reinterpret_cast<u16(*)[1024]>(shm + SPARE);
  float* cwj = reinterpret_cast<float*>(shm + SPARE + 10240);
  unsigned* ssel = reinterpret_cast<unsigned*>(shm + SPARE + 10368);
  u16(*acts)[64] = reinterpret_cast<u16(*)[64]>(shm + SPARE + 15872);
  float* smod = reinterpret_cast<float*>(shm + SPARE + 15872);
  u16* ssa = reinterpret_cast<u16*>(smod);
  constexpr int TAB = SPARE + 10752, RKA = TAB + 1536, RKB = TAB + 3584;
  u16(*scw)[128] = reinterpret_cast<u16(*)[128]>(shm + TAB + 512);
  static_assert(42 % NS == 0, "slots 0 and 1 are free behind GEMM_B barrier 13");
  const int b = blockIdx.x, xcd = b & 7, j8 = b >> 3;
  const int cc = xcd >> 2, nt = (xcd & 3) * 4 + (j8 & 3), mt = j8 >> 2, et = mt;
  const int tid = threadIdx.x, lane = tid & 63;
  const int wid = __builtin_amdgcn_readfirstlane(tid >> 6);
  const unsigned lds0 = (unsigned)(size_t)(__attribute__((address_space(3))) char*)shm;
  const char* cpf;
  {
    unsigned long long pc;
    unsigned len;
    asm volatile("s_getpc_b64 %0\n.Lcpf_here:\n\ts_mov_b32 %1, .Lcpf_end-.Lcpf_here-4\n\t.subsection 1\n.Lcpf_end:\n\t.subsection 0" : "=s"(pc), "=s"(len));
    cpf = reinterpret_cast<const char*>(pc) + min((unsigned)((lane * 32 + j8) * 128), len);
  }
  const unsigned pfl = lds0 + SPARE + 17408;
  if (wid == 4 && lane < 12) PFL(cpf);
  u32x4 xv[2];
  {
    const unsigned xo = (unsigned)((b * 8 + wid) * D + lane * 8) * 2;
    asm volatile("global_load_dwordx4 %0, %2, %3\n\tglobal_load_dwordx4 %1, %2, %3 offset:1024" : "=&v"(xv[0]), "=&v"(xv[1]) : "v"(xo), "s"(x) : "memory");
  }
  asm volatile("" ::"s"(ws), "s"(Y0), "s"(Y1), "s"(cvec), "s"(wmod), "s"(wr_));
  u16* xn = reinterpret_cast<u16*>(ws + WS_H);
  u16* U = reinterpret_cast<u16*>(ws + WS_T);
  unsigned* part = reinterpret_cast<unsigned*>(ws + WS_PART);
  const int g = (b & 7) * 28 + (b >> 3);
  unsigned* xch = reinterpret_cast<unsigned*>(ws + WS_P);
  unsigned* ufl = reinterpret_cast<unsigned*>(ws + WS_UFL) + (cc * 16 + nt) * 32 * FSTR;
  unsigned* xfl = reinterpret_cast<unsigned*>(ws + WS_XFL);
  unsigned* epo = reinterpret_cast<unsigned*>(ws + WS_EPO);
#define SKEWPT()
  const int g4 = lane >> 4;
  u64 tag0, tag;
#define TAGW(n) asm volatile("s_waitcnt vmcnt(%2)\n\tv_lshl_add_u64 %0, %1, 0, 1" : "=v"(tag) : "v"(tag0), "n"(n) : "memory")
  u32x4 wv[8], cq;
  const char* pfy = reinterpret_cast<const char*>(cc ? Y1 : Y0) + (size_t)((xcd & 3) * 512 + j8 * 16 + (lane & 15)) * 4096 + g4 * 128;
#define PFG(g) (pfy + ((((g) + xcd) & 7) << 9))
  const int u = b * 2 + (wid & 1), cg0 = u & 31, ks0 = u >> 5;
  unsigned ta = lds0 + STG + ((lane >> 2) & 3) * 2368 + (wid * 128 + (lane >> 5) * 32 + (lane & 3) * 8 + (g4 & 1) * 4) * 2;
  unsigned oo = (unsigned)((wid * 4 + (lane >> 5)) * 131072 + (b >> 2) * 2048 + (g4 & 1) * 1024 + ((b & 3) * 16 + (lane & 15)) * 16), oo2 = oo + 262144;
  asm volatile("" : "+v"(ta), "+v"(oo), "+v"(oo2));
  unsigned wa = lds0 + STG + wid * 2368 + lane * 16;
  asm volatile("" : "+v"(wa));
  const unsigned wo = (unsigned)((ks0 * 64 + (lane >> 3) * 8) * 6144 + 2048 + (cg0 >> 4) * 1024 + cg0 * 64 + (lane & 7) * 8) * 2;
  if (wid == 0) TSG(part, 0);
  BT(50);
  if (wid >= 3) {
    asm volatile("global_load_dwordx2 %0, %1, off sc1" : "=v"(tag0) : "v"(epo) : "memory");
    if (wid >= 6) {
      LDG(cq, (ks0 * 64 + g4 * 16 + (lane & 1) * 8) * 2, cvec);
#pragma unroll
      for (int i = 0; i < 8; ++i) LDM(wv[i], wo + i * 12288, wmod);
      const unsigned av = u < 80 ? 0u : ~0u;
      asm volatile("global_store_dwordx4 %0, %1, %2 sc1\n\ts_nop 1" ::"v"(O_KEY + u * 1024 + lane * 16), "v"(u32x4{av, av, av, av}), "s"(ws) : "memory");
      WAIT_V(11);
    } else if (wid == 4) {
      __builtin_amdgcn_s_sleep(C_DP);
      PFL(PFG(0));
      WAIT_V(2);
    } else {
      WAIT_V(1);
    }
  } else {
    WAIT_V(0);
  }
  {
    asm volatile("" : "+v"(xv[0]), "+v"(xv[1])::"memory");
    if (wid == 1) TSG(part, 2);
    BT(51);
    const float s0 = dsum(xv[0][3], dsum(xv[0][2], dsum(xv[0][1], dsum(xv[0][0], 0.f))));
    const float s1 = dsum(xv[1][3], dsum(xv[1][2], dsum(xv[1][1], dsum(xv[1][0], 0.f))));
    const float mf = wave_sum_s(s0 + s1) * (1.0f / D), m = rbf(mf);
    const f32x4 d0 = lo4(xv[0]) - m, d1 = hi4(xv[0]) - m, d2 = lo4(xv[1]) - m, d3 = hi4(xv[1]) - m;
    const f32x4 q = (d0 * d0 + d1 * d1) + (d2 * d2 + d3 * d3);
    const float vf = wave_sum_s((q[0] + q[1]) + (q[2] + q[3])) * (1.0f / D) - (mf - m) * (mf - m);
    const float rden = __builtin_amdgcn_rcpf(rbf(__builtin_amdgcn_sqrtf(rbf(rbf(vf) + 1e-5f))));
    asm volatile("ds_write_b128 %0, %1" ::"v"(wa), "v"(pk8(d0 * rden, d1 * rden)) : "memory");
    asm volatile("ds_write_b128 %0, %1 offset:1024" ::"v"(wa), "v"(pk8(d2 * rden, d3 * rden)) : "memory");
    if (wid == 1) TSG(part, 12);
    BT(52);
    LBAR();
    if (wid == 1) TSG(part, 10);
    BT(53);
    u32x2 t[4];
    asm volatile(
        "ds_read_b64_tr_b16 %0, %4\n\tds_read_b64_tr_b16 %1, %4 offset:%5\n\t"
        "ds_read_b64_tr_b16 %2, %4 offset:128\n\tds_read_b64_tr_b16 %3, %4 offset:%6\n\ts_waitcnt lgkmcnt(0)"
        : "=&v"(t[0]), "=&v"(t[1]), "=&v"(t[2]), "=&v"(t[3])
        : "v"(ta), "n"(4 * 2368), "n"(4 * 2368 + 128)
        : "memory");
    asm volatile("global_store_dwordx4 %0, %2, %4 sc1\n\tglobal_store_dwordx4 %1, %3, %4 sc1\n\ts_nop 1" ::"v"(oo), "v"(oo2),
                 "v"(u32x4{t[0][0], t[0][1], t[1][0], t[1][1]}), "v"(u32x4{t[2][0], t[2][1], t[3][0], t[3][1]}), "s"(xn)
                 : "memory");
    if (wid == 2) TSG(part, 14);
    BT(54);
  }
  u32x2 vd;
#define VDLD(sl) POLL2(max(vd[0], vd[1]) == SENT, sl, PCAP, asm volatile("global_load_dwordx2 %0, %1, %2 sc1" VW0 : "=&v"(vd) : "v"(O_VD + (b & 31) * 128), "s"(ws) : "memory"))
  if (wid < 4) {
    const unsigned ya0 = lds0 + (lane & 15) * 128 + (g4 ^ (lane & 7)) * 16;
    bf16x8 X[DX][2][2], Y[8];
    f32x4 acc[2][8];
    {
      constexpr int MODE = 0;
      const unsigned xo0 = lane * 16 + 4096, xo1 = xo0 + 8192;
      const char* xb = reinterpret_cast<const char*>(xn) + (size_t)(mt * 4 + wid) * 131072;
#define XB(i) (xb + (((xcd + (i)) & 7) << 14))
#pragma unroll
      for (int i = 0; i < 2; ++i)
#pragma unroll
        for (int j = 0; j < 8; ++j) acc[i][j] = f32x4{0.f, 0.f, 0.f, 0.f};
      asm volatile("" : W8(acc[0]), W8(acc[1]));
      __builtin_amdgcn_s_setprio(1);
      WAIT_V(0);
      if (wid == 0) TSG(part, 1);
      const char* xpb = XB(0);
      BAR(55);
      if (wid == 3) {
        TAGW(0);
        const unsigned* fp = xfl + ((xcd * 32 + lane) & 255) * XSTR;
        if (P_D3) __builtin_amdgcn_s_sleep(P_D3);
#pragma unroll 1
        for (int it = 0; it < PCAP; ++it) { PDBG(it);
          u64 f;
          asm volatile("global_load_dwordx2 %0, %1, off sc1\n\ts_waitcnt vmcnt(0)" : "=&v"(f) : "v"(fp) : "memory");
          if (__all(f == tag)) break;
          __builtin_amdgcn_s_sleep(P_D4);
        }
      }
      BAR(0);
      if (wid == 1) TSG(part, 3);
#pragma unroll
      for (int h = 0; h < 7; ++h) {
        XLD(X[h >> 1][h & 1][0], xpb, h, 0);
        XLD(X[h >> 1][h & 1][1], xpb, h, 1);
      }
#pragma unroll
      for (int j = 0; j < 7; ++j) DSR(Y[j], ya0, j * 2048);
      unsigned so = 0, a0 = ya0;
      f32x4 c0, c1;
#pragma unroll 1
      for (int it = 0; it < 7; ++it) {
        const char* xpa = xpb;
        xpb = XB(it + 1);
#pragma unroll
        for (int s = 0; s < 4; ++s) {
          const unsigned a1 = (ya0 ^ 64) + so;
          so = so == (NS - 1) * 16384 ? 0 : so + 16384;
          WAIT_V(12);
          HALF(X[s][0], a1, true, X[(s + 3) & 3][1], (s == 0 ? xpa : xpb), (2 * s + 7) & 7, true, a0);
          a0 = ya0 + so;
          BAR(1 + it * 4 + s);
          WAIT_V(12);
          HALF(X[s][1], a0, true, X[s][0], xpb, 2 * s, true, a1);
        }
      }
#pragma unroll
      for (int s = 0; s < 4; ++s) {
        const unsigned a1 = (ya0 ^ 64) + so;
        so = so == (NS - 1) * 16384 ? 0 : so + 16384;
        WAIT_V(s == 0 ? 12 : 14 - 4 * s);
        HALF(X[s][0], a1, true, X[3][1], xpb, 7, s == 0, a0);
        a0 = ya0 + so;
        if (s < 3) BAR(29 + s);
        WAIT_V(12 - 4 * s);
        if (s == 2) {
          DSR(c0, lds0 + SPARE + 15872 + 512 + wid * 128 + g4 * 32, 0);
          DSR(c1, lds0 + SPARE + 15872 + 512 + wid * 128 + g4 * 32, 16);
        }
        if (s < 3) HALF(X[s][1], a0, true, X[s][0], xpb, 0, false, a1);
      }
      const char* up = reinterpret_cast<const char*>(U) + (size_t)cc * 4194304 + (size_t)(nt * 4) * 65536 + (mt * 4 + wid) * 2048;
      const unsigned ul = lds0 + STG + wid * 1024 + lane * 16;
#define UEP(j)                                                                                                                             \
  do {                                                                                                                                     \
    u32x4 v_;                                                                                                                              \
    asm volatile("v_pk_mul_f32 %0, %0, %4\n\tv_pk_mul_f32 %1, %1, %5\n\tv_pk_mul_f32 %2, %2, %6\n\tv_pk_mul_f32 %3, %3, %7"            \
                 : "+v"(acc[0][j].lo), "+v"(acc[0][j].hi), "+v"(acc[1][j].lo), "+v"(acc[1][j].hi)                                          \
                 : "v"(c0.lo), "v"(c0.hi), "v"(c1.lo), "v"(c1.hi));                                                                        \
    asm volatile("v_cvt_pk_bf16_f32 %0, %4, %5\n\tv_cvt_pk_bf16_f32 %1, %6, %7\n\tv_cvt_pk_bf16_f32 %2, %8, %9\n\tv_cvt_pk_bf16_f32 %3, %10, %11" \
                 : "=&v"(v_[0]), "=&v"(v_[1]), "=&v"(v_[2]), "=&v"(v_[3])                                                                  \
                 : "v"(acc[0][j][0]), "v"(acc[0][j][1]), "v"(acc[0][j][2]), "v"(acc[0][j][3]), "v"(acc[1][j][0]), "v"(acc[1][j][1]),       \
                   "v"(acc[1][j][2]), "v"(acc[1][j][3]));                                                                                  \
    asm volatile("global_store_dwordx4 %0, %2, %3 offset:%4 sc1\n\tds_write_b128 %1, %2 offset:%5\n\ts_nop 0"                          \
                 ::"v"(lane * 16), "v"(ul), "v"(v_), "s"(up + ((j) >> 1) * 65536), "n"(((j)&1) * 1024), "n"((j)*4096) : "memory");         \
  } while (0)
      {
        const unsigned a1 = (ya0 ^ 64) + (31 % NS) * 16384;
        DSR(Y[7], a1, 7 * 2048);
        WAIT_L(6);
#pragma unroll
        for (int j = 0; j < 8; ++j) {
          if (j == 2) WAIT_L(0);
          MFMA(acc[0][j], X[3][1][0], Y[j]);
          MFMA(acc[1][j], X[3][1][1], Y[j]);
          if (j >= 2) UEP(j - 2);
        }
      }
      if (wid == 0) TSG(part, 4);
      TS(0);
      SKEWPT();
      asm volatile("s_nop 3");
#pragma unroll
      for (int j = 6; j < 8; ++j) UEP(j);
    }
    const int rg = nt * 8 + wid * 2 + cc, row0 = rg * 16 + (lane & 15), col0 = et * 128 + g4 * 8;
    u32x4 xq[4], pv[4], sa4[4];
    f32x4 ga[8];
    f32x2 cj;
    float sw, ce;
    u32x4 dk;
    unsigned spo, soo, th, slt, slf, sla, tha;
    u32x2 se;
    const char* xr = reinterpret_cast<const char*>(xch + (b * 4 + wid) * 1024);
#define XCHLD()                                                                                                     \
  asm volatile(                                                                                                     \
      "global_load_dwordx4 %0, %4, %5 sc1\n\tglobal_load_dwordx4 %1, %4, %5 offset:1024 sc1\n\t"                   \
      "global_load_dwordx4 %2, %4, %5 offset:2048 sc1\n\tglobal_load_dwordx4 %3, %4, %5 offset:3072 sc1"            \
      : "=&v"(pv[0]), "=&v"(pv[1]), "=&v"(pv[2]), "=&v"(pv[3])                                                      \
      : "v"(lane * 16), "s"(xr)                                                                                     \
      : "memory")
    {
      constexpr int MODE = 1;
      const char* ub = reinterpret_cast<const char*>(U) + (size_t)cc * 4194304 + (size_t)(nt * 4 + wid) * 65536;
      const unsigned xo0 = lane * 16 + cc * 1024 + 4096, xo1 = xo0 ^ 1024;
      BT(32);
      LBAR();
      if (wid == 1) TSG(part, 6);
      {
        const unsigned sa = lds0 + STG + wid * 8192 + lane * 16 + cc * 4096;
#pragma unroll
        for (int q = 0; q < 8; ++q) DSR(X[(q >> 1) & 1][q & 1][q >> 2], (q >> 2) ? sa ^ 4096 : sa, (q & 3) * 1024);
        const unsigned a0 = ya0 + (32 % NS) * 16384;
#pragma unroll
        for (int j = 0; j < 7; ++j) DSR(Y[j], a0, j * 2048);
      }
#define UFLAG()                                                                                                         \
  do {                                                                                                                  \
    WAIT_V(0);                                                                                                          \
    if (lane == 0) __hip_atomic_store(ufl + (mt * 4 + wid) * FSTR, 1u, __ATOMIC_RELAXED, __HIP_MEMORY_SCOPE_AGENT);     \
    if (wid == 0) TSG(part, 5);                                                                                         \
  } while (0)
#define XP(h) (ub + (((mt + ((h) >> 2)) & 7) << 13))
#define XS(h) X[((h) >> 1) & 3][(h)&1]
#define XW(h) do { if ((h) >= 4) WAIT_V(2 * (((h) + 6 > 31 ? 31 : (h) + 6) - (h)) + ((h) >= 25 + XQ_N / 8 ? 4 : 0)); } while (0)
      constexpr int H0 = 24, NP = 64, NE = 2 * NP + 8;
#pragma unroll
      for (int s = 0; s < H0 / 2; ++s) {
        const unsigned a0 = ya0 + ((32 + s) % NS) * 16384, a1 = a0 ^ 64, an = ya0 + ((33 + s) % NS) * 16384;
        XW(2 * s);
        if (s == 0) {
          constexpr int MODE = 2;
          HALF(XS(0), a1, true, XS(7), XP(7), 7, false, a0);
          UFLAG();
        } else {
          HALF(XS(2 * s), a1, true, XS(2 * s + 7), XP(2 * s + 7), 2 * s + 7, 2 * s >= 4, a0);
        }
        if (s == 1 && wid == 2) TSD(12);
        BAR(33 + s);
        if (s == 1) {
          if (wid == 1) TSG(part, 7);
          TS(4);
#pragma unroll
          for (int h = 4; h < U_X0; ++h) {
            XLD(XS(h)[0], XP(h), h, 0);
            XLD(XS(h)[1], XP(h), h, 1);
          }
        }
        XW(2 * s + 1);
#undef HHOOK
#define HHOOK(j) if (s == 1 && j + 3 >= U_X0) { XLD(XS(j + 3)[0], XP(j + 3), j + 3, 0); XLD(XS(j + 3)[1], XP(j + 3), j + 3, 1); }
        HALF(XS(2 * s + 1), an, true, XS(2 * s + 8), XP(2 * s + 8), 2 * s + 8, 2 * s + 1 >= 5, a1);
#undef HHOOK
#define HHOOK(j)
      }
      const char* xw = reinterpret_cast<const char*>(xch + ((b ^ 4) * 4 + wid) * 1024);
      const unsigned xoff = (unsigned)(row0 * D + col0) * 2;
      const unsigned rka = lds0 + RKA + (lane & 15) * 16, rkb = lds0 + RKB + (g4 ? 1024 : wid * 256 + lane * 16);
      const unsigned ea = lds0 + TAB + g4 * 32, eb = lds0 + TAB + 512 + (lane & 3) * 256 + g4 * 16;
      u32x4 ov;
      bf16x8 xk;
#pragma unroll
      for (int q = 0; q < 4; ++q) dk[q] = (lane & 3) == q ? ~0u : 0u;
      spo = O_STAT + rg * 2048 + g4 * 512 + (lane & 15) * 16, soo = O_STAT + rg * 2048 + et * 256 + (lane & 15) * 16;
      unsigned cwa = lds0 + SPARE + 10240 + g4 * 8, cwu = lds0 + SPARE + 10240 + et * 4;
      {
        const unsigned one = g4 == ((lane >> 2) & 3) ? ((lane & 1) ? 0x3f800000u : 0x3f80u) : 0u;
        se = u32x2{(lane & 2) ? 0u : one, (lane & 2) ? one : 0u};
        asm volatile("" : "+v"(se));
      }
      tha = lds0 + SPARE + 10368 + 24;
      sla = lds0 + SLT + (row0 & 127) * 4;
      asm volatile("" : "+v"(dk), "+v"(spo), "+v"(soo), "+v"(cwa), "+v"(cwu), "+v"(tha), "+v"(sla));
#define EH(e) (H0 + (((e) < NP ? (e) : (e) - NP - 8) >> 3))
#define EA(e) ((ya0 ^ ((EH(e) & 1) * 64)) + ((32 + (EH(e) >> 1)) % NS) * 16384)
      sfor<NE>([&](auto nn) __attribute__((always_inline)) {
        constexpr int n = decltype(nn)::value, h = EH(n), j = n & 7, m = n - NP - TP_P0, e = n + 7, z = n - (NE - 8);
        (void)sa4, (void)eb, (void)ga, (void)ea, (void)xk, (void)rka, (void)rkb, (void)xq, (void)xoff, (void)ov, (void)xw, (void)x;
        (void)cj, (void)sw, (void)ce, (void)cwa, (void)cwu, (void)th, (void)tha, (void)slt, (void)sla, (void)slf;
        if (n == 0) TS(2);
        if (n == NP) { TS(3); SKEWPT(); }
        if (n < NP && j == 0) {
          if ((h & 1) && h < 31) BAR(33 + (h >> 1));
          XW(h);
        }
        if (n == NP + TP_X0) { TS(14); BAR(48); TS(11); }
        if (n == NP + TP_XL) XCHLD();
        if constexpr ((j & 1) == 0) WAIT_L(z <= 0 ? 5 : 6 - z + 2 * z);
        if constexpr (n < NP) MFMA(acc[1][j], Y[j], XS(h)[1]);
        else if constexpr (n < NP + 8) MFMA(acc[0][j], Y[j], xk);
        else MFMA(acc[0][j], Y[j], XS(h)[0]);
        if constexpr (n == NP - 16) DSR(xk, rkb, 0);
        if constexpr (e >= NP && e < NP + 8) DSR(Y[e & 7], rka, (e & 7) * 256);
        else if constexpr (e < NE) DSR(Y[e & 7], EA(e), (e & 7) * 2048);
        if constexpr (n == 2 || n == 4) XLD(XS(31)[(j >> 1) - 1], XP(31), 31, (j >> 1) - 1);
        if constexpr (n == XQ_N) {
#pragma unroll
          for (int q = 0; q < 4; ++q) LDX(xq[q], xoff, x, q * 64);
        }
        if constexpr (m >= 0 && m < 4 * TP_PS) {
          constexpr int q = m / TP_PS, r = m % TP_PS;
#pragma unroll
          for (int u = (r * 9 + TP_PS - 1) / TP_PS; u < ((r + 1) * 9 + TP_PS - 1) / TP_PS; ++u) {
            if (u < 4) asm volatile("v_cvt_pk_bf16_f32 %0, %1, %2" : "=v"(ov[u & 3]) : "v"(acc[1][2 * q + ((u >> 1) & 1)][(u & 1) * 2]), "v"(acc[1][2 * q + ((u >> 1) & 1)][(u & 1) * 2 + 1]));
            else if (u < 8) asm volatile("v_min_u32 %0, -2, %0" : "+v"(ov[u & 3]));
            else asm volatile("global_store_dwordx4 %0, %1, %2 offset:%3 sc1\n\ts_nop 1" ::"v"(lane * 16), "v"(ov), "s"(xw), "n"(q * 1024) : "memory");
          }
          if (m == 4 * TP_PS - 1) TS(10);
        }
        if constexpr (m == TP_XC) asm volatile("s_waitcnt vmcnt(%4)" : "+v"(xq[0]), "+v"(xq[1]), "+v"(xq[2]), "+v"(xq[3]) : "n"(n >= NP + TP_XL ? 8 : 4) : "memory");
        if constexpr (m >= TP_XC && m < TP_XC + 32) {
          constexpr int c = m - TP_XC;
          if constexpr (c & 1) asm volatile("v_and_b32 %0, 0xffff0000, %1" : "=v"(acc[1][c >> 2][c & 3]) : "v"(xq[c >> 3][(c >> 1) & 3]));
          else asm volatile("v_lshlrev_b32 %0, 16, %1" : "=v"(acc[1][c >> 2][c & 3]) : "v"(xq[c >> 3][(c >> 1) & 3]));
        }
        if constexpr (z >= 0 && z < 4) {
          DSR(ga[2 * z], ea, z * 128);
          DSR(ga[2 * z + 1], ea, z * 128 + 16);
        } else if constexpr (z >= 4 && z < 6) {
          DSR(sa4[2 * z - 8], eb, (2 * z - 8) * 64);
          DSR(sa4[2 * z - 7], eb, (2 * z - 7) * 64);
        } else if constexpr (z == 6) {
          asm volatile("ds_read_b64 %0, %2\n\tds_read_b32 %1, %3" : "=&v"(cj), "=&v"(ce) : "v"(cwa), "v"(cwu) : "memory");
        } else if constexpr (z == 7) {
          asm volatile("ds_read_b32 %0, %4 offset:32\n\tds_read_b32 %1, %5 offset:4\n\tds_read_b32 %3, %5 offset:56\n\tds_read_b32 %2, %6"
                       : "=&v"(sw), "=&v"(th), "=&v"(slt), "=&v"(slf) : "v"(cwa), "v"(tha), "v"(sla) : "memory");
        }
      });
    }
    if (wid == 0) TSG(part, 8);
    TS(13);
    BT(49);
    BTE();
    SKEWPT();
    u16* op = out + (size_t)row0 * D + col0;
    {
      u32x4 e0 = {se[0], se[1], 0u, 0u}, e1 = {0u, 0u, se[0], se[1]};
      asm volatile("" : "+v"(e0), "+v"(e1));
      TS(26);
#define XCHW() asm volatile("s_waitcnt vmcnt(0)" : "+v"(pv[0]), "+v"(pv[1]), "+v"(pv[2]), "+v"(pv[3])::"memory")
      XCHW();
#define XBAD (max(max(max4(pv[0]), max4(pv[1])), max(max4(pv[2]), max4(pv[3]))) == SENT)
      if (__builtin_expect(__any(XBAD), 1)) POLL2(XBAD, 1, PCAP, XCHLD(); XCHW());
      TS(27);
      asm volatile("s_waitcnt lgkmcnt(0)" : W8(acc[0]), W8(ga), "+v"(sa4[0]), "+v"(sa4[1]), "+v"(sa4[2]), "+v"(sa4[3]), "+v"(cj), "+v"(sw), "+v"(ce), "+v"(th), "+v"(slt), "+v"(slf)::"memory");
#pragma unroll
      for (int q = 0; q < 8; ++q) acc[0][q] = __builtin_amdgcn_mfma_f32_16x16x32_bf16(BF8((q & 1) ? e1 : e0), BF8(pv[q >> 1]), acc[0][q], 0, 0, 0);
      f32x4 s4 = {0.f, 0.f, 0.f, 0.f}, gr = {0.f, 0.f, 0.f, 0.f};
#pragma unroll
      for (int q = 0; q < 4; ++q) {
        xq[q] = pk8(acc[1][2 * q] + ga[2 * q] * acc[0][2 * q], acc[1][2 * q + 1] + ga[2 * q + 1] * acc[0][2 * q + 1]);
        gr = __builtin_amdgcn_mfma_f32_16x16x32_bf16(BF8(xq[q]), BF8(xq[q]), gr, 0, 0, 0);
        s4 = __builtin_amdgcn_mfma_f32_16x16x32_bf16(BF8(sa4[q]), BF8(xq[q]), s4, 0, 0, 0);
      }
#define XCST()                                                                                                                         \
  asm volatile(                                                                                                                        \
      "global_store_dwordx4 %0, %1, %5 sc1\n\tglobal_store_dwordx4 %0, %2, %5 offset:64 sc1\n\t"                                      \
      "global_store_dwordx4 %0, %3, %5 offset:128 sc1\n\tglobal_store_dwordx4 %0, %4, %5 offset:192 sc1\n\ts_nop 1" ::"v"(O_X1C - 2048 + slt * 2048 + col0 * 2), \
      "v"(nsent4(xq[0])), "v"(nsent4(xq[1])), "v"(nsent4(xq[2])), "v"(nsent4(xq[3])), "s"(ws)                                          \
      : "memory")
#define SCST() asm volatile("global_store_dwordx4 %0, %1, %2 sc1\n\ts_nop 1" ::"v"(O_ST2 - 128 + slt * 128 + et * 16), "v"(sg), "s"(ws) : "memory")
      if (slt) XCST();
      const bool dl = ((lane >> 2) & 3) == g4;
      const u32x4 gd = __builtin_bit_cast(u32x4, gr) & dk;
      const float m = s4[2] * (1.0f / 128);
      float q2 = __uint_as_float((gd[0] | gd[1]) | (gd[2] | gd[3])) - s4[2] * m, lg = (s4[0] + s4[1]) - m * ce;
      if (__builtin_expect(__any(dl && !(s4[2] * m <= 256.0f * q2)), 0)) {
        asm volatile("" : "+v"(xq[0]), "+v"(xq[1]), "+v"(xq[2]), "+v"(xq[3]));
        f32x4 a = {0.f, 0.f, 0.f, 0.f}, l = {0.f, 0.f, 0.f, 0.f};
#pragma unroll
        for (int q = 0; q < 4; ++q) {
          const u32x4 ch = *reinterpret_cast<const u32x4*>(&scw[0][g4 * 8 + q * 32]), cl = *reinterpret_cast<const u32x4*>(&scw[1][g4 * 8 + q * 32]);
          const f32x4 d0 = lo4(xq[q]) - m, d1 = hi4(xq[q]) - m;
          a += d0 * d0 + d1 * d1;
          l += d0 * (lo4(ch) + lo4(cl)) + d1 * (hi4(ch) + hi4(cl));
        }
        q2 = ar4((a[0] + a[1]) + (a[2] + a[3]));
        lg = ar4((l[0] + l[1]) + (l[2] + l[3]));
      }
      const u32x4 sg = {nsent(m), nsent(q2), nsent(lg), 0u};
      if (dl) {
        if (slt) SCST();
        asm volatile("global_store_dwordx4 %0, %1, %2 sc1\n\ts_nop 1" ::"v"(soo), "v"(sg), "s"(ws) : "memory");
      }
      if (__any(slf == 0u)) {
        POLL2(slf == 0u, 1, 1 << 26, asm volatile("ds_read_b32 %1, %2 offset:56\n\tds_read_b32 %0, %3\n\ts_waitcnt lgkmcnt(0)" : "=&v"(slt), "=&v"(slf) : "v"(tha), "v"(sla) : "memory"));
        if (slt) {
          XCST();
          if (dl) SCST();
        }
      }
      TS(7);
      SKEWPT();
      const unsigned tp = tick() + T_D1, tq = (unsigned)__builtin_amdgcn_readfirstlane((int)th) + T_C1;
      u32x4 sr[2];
#define STLD(W) asm volatile("global_load_dwordx4 %0, %2, %3 sc1\n\tglobal_load_dwordx4 %1, %2, %3 offset:256 sc1" W : "=&v"(sr[0]), "=&v"(sr[1]) : "v"(spo), "s"(ws) : "memory")
#define SBAD (max(max(max(sr[0][0], sr[0][1]), sr[0][2]), max(max(sr[1][0], sr[1][1]), sr[1][2])) == SENT)
      asm volatile("" : "+v"(xq[0]), "+v"(xq[1]), "+v"(xq[2]), "+v"(xq[3])::"memory");
      f32x4 xf[8], xs[8], gc[8];
      auto PREW = [&](int q0, int q1) __attribute__((always_inline)) {
#pragma unroll
        for (int q = q0; q < q1; ++q) {
          const u32x4 sbq = *reinterpret_cast<const u32x4*>(&svb[1][col0 + q * 32]), gbq = *reinterpret_cast<const u32x4*>(&svb[3][col0 + q * 32]);
          const u32x4 cq = *reinterpret_cast<const u32x4*>(&svb[2][col0 + q * 32]);
          xf[2 * q] = lo4(xq[q]), xf[2 * q + 1] = hi4(xq[q]);
          xs[2 * q] = xf[2 * q] + lo4(gbq) * lo4(sbq), xs[2 * q + 1] = xf[2 * q + 1] + hi4(gbq) * hi4(sbq);
          gc[2 * q] = lo4(gbq) * lo4(cq), gc[2 * q + 1] = hi4(gbq) * hi4(cq);
          asm volatile("" : "+v"(xf[2 * q]), "+v"(xf[2 * q + 1]), "+v"(xs[2 * q]), "+v"(xs[2 * q + 1]), "+v"(gc[2 * q]), "+v"(gc[2 * q + 1]));
        }
      };
      PREW(0, PW0);
      wait_tick((int)(tp - tq) < 0 ? tp : tq);
      STLD("");
      unsigned rk = 0x8ffeu - (unsigned)row0 * 2;
      unsigned twa = lds0 + STG + wid * 8192 + (lane & 15) * 256 + ((g4 ^ lane) & 15) * 16, tra = lds0 + STG + wid * 8192 + g4 * 256 + ((g4 ^ lane) & 15) * 16;
      unsigned oo_ = (unsigned)((rg * 16 + g4) * D + et * 128 + (lane & 15) * 8) * 2;
      asm volatile("" : "+v"(rk), "+v"(op), "+v"(twa), "+v"(tra), "+v"(oo_));
      PREW(PW0, 4);
      asm volatile("s_waitcnt vmcnt(0)" : "+v"(sr[0]), "+v"(sr[1])::"memory");
      if (__builtin_expect(__any(SBAD), 1)) POLL2(SBAD, 1, PCAP, STLD(VW0));
      TS(9);
      const f32x2 mk = {__uint_as_float(sr[0][0]), __uint_as_float(sr[1][0])};
      const f32x2 qk = {__uint_as_float(sr[0][1]), __uint_as_float(sr[1][1])};
      const f32x2 lk = {__uint_as_float(sr[0][2]), __uint_as_float(sr[1][2])};
      const float mean = ar4(mk[0] + mk[1]) * (1.0f / 8);
      const f32x2 dm = mk - mean;
      const f32x2 a = qk + 128.0f * (dm * dm), l = lk + dm * cj;
      const float al = swap32_add(a[0] + a[1], l[0] + l[1]), am = swap16_add(al, al);
      auto r = __builtin_amdgcn_permlane32_swap(__float_as_uint(am), __float_as_uint(am), false, false);
      const float rstd = __builtin_amdgcn_rsqf(__uint_as_float(r[0]) * (1.0f / D) + 1e-5f);
      const unsigned key = mkkey(rstd * __uint_as_float(r[1]) + sw) | rk;
      unsigned t1 = slt ? 0u : key, t2 = 0u;
      TOP2(0xB1); TOP2(0x4E); TOP2(0x141); TOP2(0x140);
      if (lane == 0 && et == (rg & 7)) asm volatile("global_store_dwordx2 %0, %1, %2 sc1" ::"v"(O_KEY + rg * KSTR * 8), "v"(u32x2{t1, t2}), "s"(ws) : "memory");
      u32x4 o[4];
#pragma unroll
      for (int q = 0; q < 4; ++q) o[q] = pk8(xs[2 * q] + (gc[2 * q] * (xf[2 * q] - mean)) * rstd, xs[2 * q + 1] + (gc[2 * q + 1] * (xf[2 * q + 1] - mean)) * rstd);
      {
        asm volatile("ds_write_b128 %0, %4\n\tds_write_b128 %1, %5\n\tds_write_b128 %2, %6\n\tds_write_b128 %3, %7" ::"v"(twa), "v"(twa ^ 64), "v"(twa ^ 128), "v"(twa ^ 192),
                     "v"(o[0]), "v"(o[1]), "v"(o[2]), "v"(o[3]) : "memory");
        asm volatile("ds_read_b128 %0, %4\n\tds_read_b128 %1, %5 offset:1024\n\tds_read_b128 %2, %6 offset:2048\n\tds_read_b128 %3, %7 offset:3072\n\ts_waitcnt lgkmcnt(0)"
                     : "=&v"(o[0]), "=&v"(o[1]), "=&v"(o[2]), "=&v"(o[3]) : "v"(tra), "v"(tra ^ 64), "v"(tra ^ 128), "v"(tra ^ 192) : "memory");
      }
      auto OST = [&](bool on) __attribute__((always_inline)) {
        const unsigned mk_ = (unsigned)__builtin_amdgcn_ballot_w64(on) >> g4;
#pragma unroll
        for (int q = 0; q < 4; ++q)
          if (mk_ & (1u << (4 * q))) asm volatile("global_store_dwordx4 %0, %1, %2" OPOL "\n\ts_nop 1" ::"v"(oo_), "v"(o[q]), "s"(out + q * 4096) : "memory");
      };
      bool df = slt;
      {
        const int tn = (int)(tick() - (tq + (T_C3 - T_C1)));
        if (tn > T_X) df = true;
        else if (tn > -T_W) { unsigned sv; LSPIN(sv, ssel + 16 + wid, sv != 0u); }
      }
      OST(!df);
      TS(1);
      u32x2 ks;
      POLL2(ks[1] == 0u, 1, 1 << 26, asm volatile("ds_read_b64 %0, %1\n\ts_waitcnt lgkmcnt(0)" : "=v"(ks) : "v"(lds0 + SPARE + 10368) : "memory"));
      df = df && (ks[0] & 0xffffu) != rk && (ks[1] & 0xffffu) != rk;
      SKEWPT();
      if (__any(df)) {
        if (b < NPART) {
          unsigned sv;
          LSPIN(sv, ssel + O_DW, sv != 0u);
          if (O_DS) __builtin_amdgcn_s_sleep(O_DS);
        }
        OST(df);
      }
      if (__builtin_expect(__any(t1 > ks[1]), 0)) {
        VDLD(8);
        slt = slt ? 0u : key == vd[0] ? 9u : key == vd[1] ? 10u : 0u;
        if (slt) {
          XCST();
          if (dl) SCST();
        }
      }
    }
    TS(16);
    TSE();
  } else {
    const int hw = wid - 4;
    unsigned ts0 = tick();
    asm volatile("" : "+v"(ts0));
    const char* Hc = reinterpret_cast<const char*>((cc ? X1 : X0) + (size_t)et * 128 * D);
    auto UPOLL = [&](int n) {
      const unsigned fo = (unsigned)WS_UFL + ((cc * 16 + nt) * 32 + ((mt + 1 + (lane >> 2)) & 7) * 4 + (lane & 3)) * FSTR * 4;
      unsigned f;
      POLL2(f == 0u && lane < 4 * n, U_S, PCAP, asm volatile("global_load_dword %0, %1, %2 sc1" VW0 : "=&v"(f) : "v"(fo), "s"(ws) : "memory"));
      return (__builtin_amdgcn_ballot_w64(f == 0u) & 0xFFFFFFFull) == 0;
    };
    bool urdy = true;
    u32x4 gT[8], uT[8], wdv[8];
    unsigned og = (unsigned)(((g & 3) * 256 + hw * 64 + (lane >> 3)) * F + (g >> 2) * 64 + ((lane & 7) ^ ((lane >> 3) & 2)) * 8) * 2;
    unsigned wta = lds0 + STG + (g4 * 8 + ((lane >> 2) & 3)) * 128 + (((hw * 2 + ((lane >> 1) & 1)) ^ ((lane >> 2) & 2) ^ ((g4 & 1) * 4)) << 4) + (lane & 1) * 8;
    unsigned od = (unsigned)((min(g >> 2, 55) * 64 + (lane >> 3) * 8) * D + (g & 3) * 256 + hw * 64 + (lane & 7) * 8) * 2;
    unsigned ou = og + (unsigned)(((mt + W_UO) % 7 - mt) * 128);
    asm volatile("" : "+v"(og), "+v"(ou), "+v"(od), "+v"(wta));
#define LDWI(dst, voff, base, inc) asm volatile("global_load_dwordx4 %0, %1, %2" WPOL "\n\tv_add_u32 %1, %3, %1" : "=v"(dst), "+v"(voff) : "s"(base), "n"(inc) : "memory")
#define WST(o, w)                                                                                                                      \
  if (b < NPART) {                                                                                                                     \
    _Pragma("unroll") for (int i = 0; i < 8; ++i)                                                                                      \
        asm volatile("s_mov_b32 m0, %1\n\ts_nop 0\n\tglobal_load_lds_dwordx4 %0, %2" WPOL "\n\tv_xor_b32 %0, 64, %0\n\tv_add_u32 %0, %3, %0" \
                     : "+v"(o) : "s"(lds0 + STG + hw * 8192 + i * 1024), "s"(w), "n"(F * 16) : "memory");                              \
  }
#define WTR2(i) "ds_read_b64_tr_b16 %" #i ", %16 offset:(" #i "/2)*4096+(" #i "&1)*512\n\t"
#define WTK(T)                                                                                                                         \
  if (b < NPART) {                                                                                                                     \
    u32x2 t_[16];                                                                                                                      \
    asm volatile(WTR2(0) WTR2(1) WTR2(2) WTR2(3) WTR2(4) WTR2(5) WTR2(6) WTR2(7) WTR2(8) WTR2(9) WTR2(10) WTR2(11) WTR2(12) WTR2(13)   \
                     WTR2(14) WTR2(15) "s_waitcnt lgkmcnt(0)"                                                                          \
                 : "=&v"(t_[0]), "=&v"(t_[1]), "=&v"(t_[2]), "=&v"(t_[3]), "=&v"(t_[4]), "=&v"(t_[5]), "=&v"(t_[6]), "=&v"(t_[7]),     \
                   "=&v"(t_[8]), "=&v"(t_[9]), "=&v"(t_[10]), "=&v"(t_[11]), "=&v"(t_[12]), "=&v"(t_[13]), "=&v"(t_[14]), "=&v"(t_[15]) \
                 : "v"(wta) : "memory");                                                                                               \
    _Pragma("unroll") for (int f = 0; f < 8; ++f) T[f] = u32x4{t_[2 * f][0], t_[2 * f][1], t_[2 * f + 1][0], t_[2 * f + 1][1]};        \
  }
#define DLD(q0, q1) _Pragma("unroll") for (int q = q0; q < q1; ++q) LDWI(wdv[q], od, wd, D * 2)
    const int pf0 = A_PS + (j8 & (A_PW - 1)), q1 = A_Q1;
    static_assert(A_PS + 2 * A_PW <= R_AT && R_AT < A_Q1 && A_PS + 2 * A_PW + W_T <= A_Q1 && H_AT + 3 <= A_Q1 && G1_C + G1_S < W_T && W_T < A_PW && W_T >= M_NS - 2 && A_Q1 < F_AT && F_AT <= 29 && V_K <= B_J && V_T > B_J && N_K > V_T + 1, "barrier counts");
#define HOFF(r) (unsigned)((((r) >> 5) * 32 + (((r) >> 2) & 3) * 8 + (((r) >> 4) & 1) * 4 + ((r)&3)) * 2048 + ((lane & 7) ^ (lane >> 3)) * 16)
#define TR8(T, W)                                                                                                    \
  _Pragma("unroll") for (int c = 0; c < 8; ++c) {                                                                    \
    _Pragma("unroll") for (int p = 0; p < 4; ++p)                                                                    \
        T[c][p] = __builtin_amdgcn_perm(W[2 * p + 1][c >> 1], W[2 * p][c >> 1], (c & 1) ? 0x07060302u : 0x05040100u); \
    asm volatile("" : "+v"(T[c]));                                                                                   \
  }
    u32x4 qm;
#pragma unroll
    for (int i = 0; i < 4; ++i) qm[i] = ((lane >> 2) & 3) == i ? ~0u : 0u;
    auto MVR = [&](f32x4 d) __attribute__((always_inline)) { return ((lane & 8) ? d[3] : d[0]) + dpp_f<0x128>((lane & 8) ? d[1] : d[2]); };
    auto MV64 = [&](const u32x4* T, u32x4 a) __attribute__((always_inline)) {
      f32x4 d[2] = {};
#pragma unroll
      for (int c = 0; c < 8; ++c) d[c & 1] = __builtin_amdgcn_mfma_f32_16x16x32_bf16(BF8(a & qm[c >> 1]), BF8(T[c]), d[c & 1], 0, 0, 0);
      return f32x2{MVR(d[0]), MVR(d[1])};
    };
    u32x4 sel, gp[4];
#define GP4(o, W)                                                                                                         \
  asm volatile(                                                                                                           \
      "global_load_dwordx4 %0, %4, %5 sc1\n\tglobal_load_dwordx4 %1, %4, %5 offset:1024 sc1\n\t"                         \
      "global_load_dwordx4 %2, %4, %5 offset:2048 sc1\n\tglobal_load_dwordx4 %3, %4, %5 offset:3072 sc1" W                \
      : "=&v"(gp[0]), "=&v"(gp[1]), "=&v"(gp[2]), "=&v"(gp[3])                                                            \
      : "v"(o), "s"(ws)                                                                                                   \
      : "memory")
#define GPBAD (max(max(max4(gp[0]), max4(gp[1])), max(max4(gp[2]), max4(gp[3]))) == SENT)
      auto GPSUM = [&]() __attribute__((always_inline)) {
        f32x4 d = {0.f, 0.f, 0.f, 0.f};
#pragma unroll
        for (int i = 0; i < 4; ++i) d = __builtin_amdgcn_mfma_f32_16x16x32_bf16(BF8(sel), BF8(gp[i]), d, 0, 0, 0);
        return d;
      };
    const bool rdc = b >= NPART && hw == 3, rnp = b >= NPART && hw == 2 && cc == 0;
    const int gbq = (b >> 4) & 1, cj = (b & 15) * 64 + (lane & 31) * 2;
    unsigned voff[4], ro, ra1, wqa, qo, so_, qa, mo, mo1, mo2, ma2;
    auto JSET = [&]() {
#pragma unroll
      for (int i = 0; i < 4; ++i) {
        voff[i] = HOFF((i * 4 + hw) * 8 + (lane >> 3));
        sel[i] = ((lane & 15) >> 1) == i ? ((lane & 1) ? 0x3f800000u : 0x3f80u) : 0u;
      }
      ro = rdc ? O_G2 + ((lane >> 5) * 16384 + gbq * 1024 + cj) * 8 : (unsigned)(((nt * 128 + hw * 32 + cc * 16 + ((lane >> 1) & 3) + (lane & 8)) * D + et * 128 + (lane & 1) * 64) * 2);
      ra1 = lane < 32 ? O_MOD + 2048 + gbq * 4096 + cj * 2 : O_M2 + 8192 + 64 - gbq * 64 + (b & 15) * 4;
      wqa = lds0 + SPARE + 8192 + cj * 2;
      mo = hw ? O_MOD + hw * 2048 + lane * 16 : O_MOD + 4096, mo1 = hw ? mo + 1024 : mo;
      qo = O_RS + (4096 + ((hw & 1) * 8 + (lane >> 4) * 2 + 1) * D + et * 128 + (hw >> 1) * 64 + (lane & 15) * 4) * 4;
      so_ = O_RS + ((hw & 1) * S + nt * 128 + (lane >> 4) * 32 + cc * 16 + (lane & 15)) * 4;
      qa = lds0 + RKA + ((hw >> 1) * 64 + ((lane >> 3) & 1) * 32 + (lane & 1) * 16 + ((lane >> 1) & 3) * 4 + (lane >> 4)) * 16 + (hw & 1) * 8;
      mo2 = O_M2 + (hw == 2 ? 8448u + (lane & 15) * 16 : hw ? 8192u + (lane & 7) * 16 : lane < 32 ? et * 512 + lane * 16 : 4096u + (lane & 16) * 128 + et * 256 + (lane & 15) * 16);
      ma2 = lds0 + (hw ? SPARE + 10240 + (lane & 7) * 8 : TAB + lane * 16);
      asm volatile("" : "+v"(voff[0]), "+v"(voff[1]), "+v"(voff[2]), "+v"(voff[3]), "+v"(ro), "+v"(ra1), "+v"(wqa), "+v"(qo), "+v"(so_), "+v"(qa), "+v"(mo), "+v"(mo1), "+v"(mo2), "+v"(ma2), "+v"(sel));
      *reinterpret_cast<unsigned*>((hw & 1) ? shm + RKB + 1024 + lane * 4 : reinterpret_cast<char*>(scw[2]) + lane * 4) = (hw & 1) ? 0u : 0x3f803f80u;
      if (hw == 3) {
        if (lane < 8) ssel[lane] = 0u, ssel[16 + lane] = 0u;
        *reinterpret_cast<u32x2*>(shm + SLT + lane * 8) = u32x2{0u, 0u};
      }
    };
    if (hw < 2) {
      constexpr int P = 8;
      __builtin_amdgcn_s_setprio(2);
      unsigned voy[P];
#pragma unroll
      for (int i = 0; i < P; ++i) voy[i] = (unsigned)(((i * 2 + hw) * 8 + (lane >> 3)) * 4096 + ((lane & 7) ^ (lane >> 3)) * 16);
      const unsigned lbase = lds0 + hw * 1024;
      const char* Yc = reinterpret_cast<const char*>((cc ? Y1 : Y0) + (size_t)nt * 128 * S);
      auto stage = [&](const char* yp, int slot) {
        const unsigned l = lbase + slot * 16384;
#pragma unroll
        for (int i = 0; i < P; ++i) GLDS(l + i * 2048, voy[i], yp);
      };
#define YSRC(n) (Yc + ((((n) + xcd * 4) & 31) << 7))
      __builtin_amdgcn_s_sleep(C_LD);
      PFL(hw ? PFG(1) : cpf);
      if (!J_L) JSET();
      TAGW(1);
      BAR(55);
      BT(56);
      if (hw == 1 && lane == 0) __hip_atomic_store(reinterpret_cast<u64*>(xfl + b * XSTR), tag, __ATOMIC_RELAXED, __HIP_MEMORY_SCOPE_AGENT);
      GLDS(lds0 + SPARE + 8192 + hw * 1024, (unsigned)(hw * 1024 + lane * 16), wr_);
#pragma unroll
      for (int s = 0; s < NS - 1; ++s) stage(YSRC(s), s);
      if (J_L) JSET();
      WAIT_V((NS - 2) * P);
      BAR(0);
      int slot = NS - 1;
#pragma unroll 1
      for (int k = 1; k <= 32 + B_J; ++k) {
        const int n = k - 2 + NS;
        if (n == 32) {
#pragma unroll
          for (int i = 0; i < P; ++i) voy[i] = HOFF((i * 2 + hw) * 8 + (lane >> 3));
        }
        if (k == 34 && hw == 0) {
          if (U_D) __builtin_amdgcn_s_sleep(U_D);
          urdy = UPOLL(U_N1);
          TSD(14);
        }
        const char* ys = n < 32 ? YSRC(n) : Hc + ((mt * 2 + n) & 15) * 128;
        asm volatile("" : "+s"(ys));
        WAIT_V((NS - 3) * P);
        BAR(k);
        stage(ys, slot);
        slot = slot == NS - 1 ? 0 : slot + 1;
        if (k == pf0) { WST(og, wg); }
        if (k == pf0 + W_T) { WTK(gT); }
        if (k == pf0 + A_PW) { WST(ou, wu); }
        if (k == pf0 + A_PW + W_T) { WTK(uT); }
        if (k == CPF_K && hw == 1 && lane < 12) PFL(cpf);
        if (k == 32) { DLD(0, 8 - B_WN); }
      }

    } else {
      const int aw = hw - 2;
      unsigned ow = wo - (2048 + (cg0 >> 4) * 1024) * 2;
      const unsigned oq = (unsigned)((nt * 64 + aw * 32 + (lane >> 4)) * D + mt * 128) * 2;
      u32x4 hv[8], wt[8], sc;
      {
        asm volatile("s_waitcnt vmcnt(%1)" : "+v"(cq) : "n"(11) : "memory");
#pragma unroll
        for (int i = 0; i < 4; ++i) sc[i] = pack2(silu_bf(bflo(cq[i])), silu_bf(bfhi(cq[i])));
        asm volatile("" : "+v"(sc));
      }
#define GPUB(o, off)                                                                                   \
  if ((lane & 8) == 0)                                                                                 \
    asm volatile("global_store_dwordx4 %0, %1, %2 sc1 ; ALWAYS\n\ts_nop 1" ::"v"(off),                          \
                 "v"(u32x4{__float_as_uint(o[0]), (unsigned)tag, __float_as_uint(o[1]), (unsigned)tag}), "s"(ws) : "memory")
      const unsigned gpo = ks0 * 2048 + cg0 * 64 + (lane & 7) * 8 + (lane >> 4) * 2;
      {
        unsigned mpo = O_G1 + gpo * 8;
        asm volatile("s_waitcnt vmcnt(3)" : W8(wv), "+v"(mpo)::"memory");
        TR8(wt, wv);
        f32x2 o = MV64(wt, sc);
        asm volatile("" : "+v"(o));
        TAGW(0);
        BAR(55);
        if (P_D2) __builtin_amdgcn_s_sleep(P_D2);
        PFL(PFG(2 + aw));
        GPUB(o, mpo);
      }
      unsigned vta = lds0 + STG + (g4 * 8 + ((lane >> 2) & 3)) * 256 + (((aw * 8 + ((lane >> 1) & 1)) ^ ((g4 & 1) * 8 + ((lane >> 2) & 3))) << 4) + (lane & 1) * 8;
      unsigned vso = O_VP + ((cc * 8 + mt) * 16 + nt) * 256 + aw * 128 + (lane & 3) * 32 + g4 * 8;
      asm volatile("" : "+v"(vta), "+v"(vso), "+v"(ow));
      unsigned f0 = O_XFL + ((xcd * 32 + 64 + aw * 128 + lane) & 255) * XSTR * 4, f1 = O_XFL + ((xcd * 32 + 128 + aw * 64 + lane) & 255) * XSTR * 4;
      unsigned xao = (unsigned)WS_P + (b ^ 4) * 16384 + aw * 8192 + 4096 + lane * 16, lao = O_KEY + (512 + b * 3 + aw) * 1024 + lane * 16;
      const char* pf4 = PFG(4 + aw);
      const char* pf6 = PFG(6 + aw);
      asm volatile("" : "+v"(f0), "+v"(f1), "+v"(xao), "+v"(lao), "+v"(pf4), "+v"(pf6));
      BT(57);
      BAR(0);
      float rs = 0.f, rs2 = 0.f;
      const unsigned ra = lds0 + (mt * 16 + aw * 8) * 128 + lane * 16;
      unsigned so = 0;
#define TSX()
#define RSUM()                                                                                                    \
  do {                                                                                                            \
    TSX();                                                                                                        \
    u32x4 R_;                                                                                                     \
    asm volatile("ds_read_b128 %0, %1\n\ts_waitcnt lgkmcnt(0)" : "=&v"(R_) : "v"(ra + so) : "memory");            \
    _Pragma("unroll") for (int j = 0; j < 4; ++j) {                                                               \
      asm volatile("v_add_f32 %0, %0, %1" : "+v"(rs) : "v"(R_[j] << 16));                                         \
      asm volatile("v_add_f32 %0, %0, %1" : "+v"(rs2) : "v"(R_[j] & 0xffff0000u));                                \
    }                                                                                                             \
    so = so == (NS - 1) * 16384 ? 0 : so + 16384;                                                                 \
  } while (0)
      {
        u64 f[2];
#define FLD() asm volatile("global_load_dwordx2 %0, %2, %4 sc1\n\tglobal_load_dwordx2 %1, %3, %4 sc1" : "=&v"(f[0]), "=&v"(f[1]) : "v"(f0), "v"(f1), "s"(ws) : "memory")
        __builtin_amdgcn_s_sleep(F_D);
        FLD();
#pragma unroll
        for (int q = 1; q < 11; ++q)
          asm volatile("global_store_dwordx4 %0, %1, %2 offset:%3 sc1" ::"v"(q < 3 ? lao : xao), "v"(u32x4{~0u, ~0u, ~0u, ~0u}), "s"(ws),
                       "n"(q < 3 ? (q - 1) * 1024 : (q - 7) * 1024) : "memory");
        asm volatile("s_nop 1");
        RSUM();
        BAR(1);
        RSUM();
        asm volatile("s_waitcnt vmcnt(%2)" : "+v"(f[0]), "+v"(f[1]) : "n"(10) : "memory");
        POLL(__all(f[0] == tag && f[1] == tag), 1, FLD(); asm volatile("s_waitcnt vmcnt(0)" : "+v"(f[0]), "+v"(f[1])::"memory"));
      }
      BAR(2);
      if (X_PA) __builtin_amdgcn_s_setprio(X_PA);
#define ARUN(b0, b1) _Pragma("unroll 1") for (int bi = (b0); bi < (b1); ++bi) { RSUM(); BAR(bi + 1); }
#define GLD() _Pragma("unroll") for (int i = 0; i < 8; ++i) LDWI(wv[i], ow, wmod, 12288)
      ARUN(2, pf0);
      PFL(pf4);
      GLD();
#define HVLD() _Pragma("unroll") for (int i = 0; i < 8; ++i) GLDS(lds0 + STG + aw * 8192 + i * 1024, oq + i * 8192 + (((lane & 15) ^ ((i * 4 + (lane >> 4)) & 15)) << 4), cc ? X1 : X0)
      WST(og, wg);
      ARUN(pf0, pf0 + G1_C);
      {
        if (b >= NPART) WAIT_V(0);
        asm volatile("s_waitcnt vmcnt(8)" : W8(wv)::"memory");
        TR8(wt, wv);
        if (G1_S) { RSUM(); BAR(pf0 + G1_C + 1); }
        const f32x2 o = MV64(wt, sc);
        GPUB(o, O_MODP + gpo * 8);
        ow = wo + 2048;
      }
      ARUN(pf0 + G1_C + G1_S, pf0 + W_T - 1);
      WAIT_V(1);
      RSUM();
      BAR(pf0 + W_T);
      WTK(gT);
      ARUN(pf0 + W_T, pf0 + A_PW);
      PFL(pf6);
      WST(ou, wu);
      const int rb_ = b < NPART ? pf0 + A_PW + W_T : R_AT, hb_ = max(rb_ + 1, H_AT + (j8 & 3));
      ARUN(pf0 + A_PW, rb_ - 1);
      WAIT_V(0);
      RSUM();
      BAR(rb_);
      WTK(uT);
      if (b >= NPART) {
        const unsigned c0 = (b - NPART) * 64 + aw * 32 + (lane & 15) * 2, c1 = (b - NPART) * 32 + aw * 16 + (lane & 7) * 2;
        const unsigned wq = *reinterpret_cast<const unsigned*>(&svb[4][c1]);
#define TG2(r, i) ((r[i][1] ^ (unsigned)tag) | (r[i][3] ^ (unsigned)tag) | (r[i + 1][1] ^ (unsigned)tag) | (r[i + 1][3] ^ (unsigned)tag))
        POLL2((TG2(wt, 0) | TG2(wt, 2) | TG2(wt, 4) | TG2(wt, 6)) != 0u, 2, PCAP,
              asm volatile("global_load_dwordx4 %0, %8, %10 sc1\n\tglobal_load_dwordx4 %1, %8, %11 sc1\n\t"
                           "global_load_dwordx4 %2, %8, %12 sc1\n\tglobal_load_dwordx4 %3, %8, %13 sc1\n\t"
                           "global_load_dwordx4 %4, %9, %10 sc1\n\tglobal_load_dwordx4 %5, %9, %11 sc1\n\t"
                           "global_load_dwordx4 %6, %9, %12 sc1\n\tglobal_load_dwordx4 %7, %9, %13 sc1" VW0
                           : "=&v"(wt[0]), "=&v"(wt[1]), "=&v"(wt[2]), "=&v"(wt[3]), "=&v"(wt[4]), "=&v"(wt[5]), "=&v"(wt[6]), "=&v"(wt[7])
                           : "v"(O_MODP + (g4 * 8192 + c0) * 8), "v"(O_G1 + (g4 * 8192 + (lane & 8) * 128 + c1) * 8), "s"(ws), "s"(ws + 16384), "s"(ws + 32768), "s"(ws + 49152)
                           : "memory"));
        auto FIN = [&](int q, int i, bool inc) __attribute__((always_inline)) {
          return rbf((inc ? 1.0f : 0.0f) + rbf(ar4((__uint_as_float(wt[q][i]) + __uint_as_float(wt[q + 1][i])) + (__uint_as_float(wt[q + 2][i]) + __uint_as_float(wt[q + 3][i])))));
        };
        const float s0 = FIN(0, 0, gbq), s1 = FIN(0, 2, gbq);
        if (lane < 16)
          asm volatile("global_store_dwordx4 %0, %1, %2 sc1\n\ts_nop 1" ::"v"(O_MODP + (16 * 2048 + c0) * 8),
                       "v"(u32x4{__float_as_uint(s0), (unsigned)tag, __float_as_uint(s1), (unsigned)tag}), "s"(ws) : "memory");
        const bool hi = lane & 8;
        const float t0 = FIN(4, 0, hi), t1 = FIN(4, 2, hi), o0 = dpp_f<0x128>(t0), o1 = dpp_f<0x128>(t1);
        const float a0 = hi ? o0 : t0, a1 = hi ? o1 : t1, e0 = hi ? t0 : o0, e1 = hi ? t1 : o1, p0 = e0 * bflo(wq), p1 = e1 * bfhi(wq);
        const unsigned h = pack2(p0, p1);
        const unsigned v = g4 == 0 ? __float_as_uint(hi ? a1 : a0) : g4 == 1 ? (hi ? h : pack2(e0, e1)) : hi ? pack2(a0 * p0, a1 * p1) : pack2(p0 - bflo(h), p1 - bfhi(h));
        const unsigned po = g4 == 0 ? O_M2 + c1 * 4 + (hi ? 4 : 0) : (g4 == 1 ? (hi ? O_M2 + 4096 : O_MOD + 4096) : hi ? O_GC : O_M2 + 6144) + c1 * 2;
        if (lane < 48) asm volatile("global_store_dword %0, %1, %2 sc1" ::"v"(po), "v"(min(v, SENT - 1u)), "s"(ws) : "memory");
      }
      ARUN(rb_, hb_);
      HVLD();
      ARUN(hb_, q1);
      u32x4 fm, g0, g1;
#define FMLD(W)                                                                                                                                     \
  asm volatile("global_load_dwordx4 %0, %3, %5 sc1\n\tglobal_load_dwordx4 %1, %4, %5 sc1\n\tglobal_load_dwordx4 %2, %4, %5 offset:64 sc1" W        \
               : "=&v"(fm), "=&v"(g0), "=&v"(g1) : "v"(O_MODP + (16 * 2048 + aw * 1024 + mt * 128) * 8 + lane * 16), "v"(O_GC + nt * 128 + g4 * 16), "s"(ws) : "memory")
      FMLD("");
      RSUM();
      BAR(q1 + 1);
      unsigned ha[4], sma = lds0 + SPARE + 15872 + (aw ? 512 + lane * 8 : lane * 4);
#pragma unroll
      for (int i = 0; i < 4; ++i) ha[i] = lds0 + STG + aw * 8192 + (lane & 15) * 256 + (((i * 4 + (lane >> 4)) ^ (lane & 15)) << 4);
      asm volatile("" : "+v"(ha[0]), "+v"(ha[1]), "+v"(ha[2]), "+v"(ha[3]), "+v"(sma), "+v"(ow));
      ARUN(q1 + 1, F_AT);
      asm volatile("s_waitcnt vmcnt(0)" : "+v"(fm), "+v"(g0), "+v"(g1)::"memory");
#pragma unroll
      for (int i = 0; i < 8; ++i) DSR(hv[i], ha[i & 3], (i >> 2) * 4096);
      POLL(__all(fm[1] == (unsigned)tag && fm[3] == (unsigned)tag && max(max4(g0), max4(g1)) != SENT), 1, FMLD(VW0));
      {
        const unsigned sg = cc ? 0x80000000u : 0u;
        asm volatile("ds_write_b32 %0, %2\n\tds_write_b32 %1, %3" ::"v"(sma), "v"(sma + (aw ? 4 : 256)), "v"(aw ? fm[0] ^ sg : (fm[0] >> 16) | (fm[2] & 0xffff0000u)), "v"(fm[2] ^ sg) : "memory");
      }
      ARUN(F_AT, 29);
      GLD();
      RSUM();
      BAR(30);
        u32x2 tv[16];
#pragma unroll
        for (int q = 0; q < 16; ++q)
          asm volatile("ds_read_b64_tr_b16 %0, %1 offset:%2" : "=v"(tv[q]) : "v"(vta ^ ((((q >> 2) * 2) ^ ((q & 1) * 4)) << 4)), "n"((q & 1) * 1024 + ((q >> 1) & 1) * 8192) : "memory");
      RSUM();
      BAR(31);
      RSUM();
      {
        const float v = dpp_add<0x141>(dpp_add<0x4E>(dpp_add<0xB1>(rs + rs2)));
        if ((lane & 7) == 0)
          asm volatile("global_store_dword %0, %1, %2 sc1 ; ALWAYS" ::"v"(O_RS + (cc * S + nt * 128 + mt * 16 + aw * 8 + (lane >> 3)) * 4), "v"(nsent(v)), "s"(ws) : "memory");
      }
      BAR(32);
      if (!V_F) {
        __builtin_amdgcn_s_sleep(2);
        DLD(0, 8 - B_WN);
      }
      __builtin_amdgcn_s_setprio(2);
      {
        asm volatile("" : W8(tv), W8((tv + 8))::"memory");
        f32x4 d[4];
#pragma unroll
        for (int j = 0; j < 4; ++j) {
          d[j] = __builtin_amdgcn_mfma_f32_16x16x32_bf16(BF8((u32x4{tv[4 * j][0], tv[4 * j][1], tv[4 * j + 1][0], tv[4 * j + 1][1]})), BF8(g0), f32x4{0.f, 0.f, 0.f, 0.f}, 0, 0, 0);
          d[j] = __builtin_amdgcn_mfma_f32_16x16x32_bf16(BF8((u32x4{tv[4 * j + 2][0], tv[4 * j + 2][1], tv[4 * j + 3][0], tv[4 * j + 3][1]})), BF8(g1), d[j], 0, 0, 0);
        }
        u32x2 vo;
#pragma unroll
        for (int i = 0; i < 2; ++i) {
          const unsigned e0 = pack2(d[0][2 * i], d[0][2 * i + 1]), e1 = pack2(d[1][2 * i], d[1][2 * i + 1]), e2 = pack2(d[2][2 * i], d[2][2 * i + 1]), e3 = pack2(d[3][2 * i], d[3][2 * i + 1]);
          vo[i] = min((lane & 2) ? ((lane & 1) ? e3 : e2) : ((lane & 1) ? e1 : e0), SENT - 1u);
        }
        if ((lane & 12) == 0) asm volatile("global_store_dwordx2 %0, %1, %2 sc1 ; ALWAYS" ::"v"(vso), "v"(vo), "s"(ws) : "memory");
      }
      if (V_F) { DLD(0, 8 - B_WN); }
      asm volatile("" : W8(hv)::"memory");
      {
        f32x4 qa[2] = {};
#pragma unroll
        for (int kk = 0; kk < 4; ++kk) {
          const bf16x8 sb8 = *reinterpret_cast<const bf16x8*>(ssa + kk * 32 + (lane >> 4) * 8);
#pragma unroll
          for (int r = 0; r < 2; ++r)
            qa[r] = __builtin_amdgcn_mfma_f32_16x16x32_bf16(__builtin_bit_cast(bf16v8, hv[r * 4 + kk]), __builtin_bit_cast(bf16v8, sb8), qa[r], 0, 0, 0);
        }
        if ((lane & 15) < 2)
          asm volatile("global_store_dwordx4 %0, %1, %2 sc1 ; ALWAYS\n\ts_nop 1" ::"v"(O_RS + (4096 + (cc * 8 + mt) * D + nt * 64 + aw * 32 + (lane & 1) * 16 + (lane >> 4) * 4) * 4),
                       "v"(nsent4(selu((lane & 1) ? ~0u : 0u, __builtin_bit_cast(u32x4, qa[1]), __builtin_bit_cast(u32x4, qa[0])))), "s"(ws) : "memory");
      }
      BAR(33);
      {
        asm volatile("s_waitcnt vmcnt(%8)" : W8(wv) : "n"(8 - B_WN + 3) : "memory");
        TR8(wt, wv);
        const f32x2 o = MV64(wt, sc);
        GPUB(o, O_G2 + gpo * 8);
      }
      JSET();
      BAR(34);
#pragma unroll
      for (int k = 3; k <= B_J; ++k) {
        BAR(32 + k);
        if (k == V_K && !aw) GP4(O_VP + (cc * 8 + mt) * 4096 + lane * 16, "");
      }
    }
    u32x4 dT[8];
    u32x4 m[3];
    u32x2 kk = {SENT, SENT};
#define K2LD(W) asm volatile("global_load_dwordx2 %0, %1, %2 sc1" W : "+v"(kk) : "v"(O_K2 + (lane & 15) * 64 + (xcd & (K_R - 1)) * 1024), "s"(ws) : "memory")
    {
      constexpr BSched sch;

      const unsigned lbase = lds0 + hw * 1024;
      u32x4 rq[2];
      u32x4 rr[8];
      unsigned cbv;
#define RDLD() asm volatile("global_load_dwordx4 %0, %2, %3 sc1\n\tglobal_load_dwordx4 %1, %2, %4 sc1" : "=&v"(rq[0]), "=&v"(rq[1]) : "v"(ro), "s"(x), "s"(x + 4096) : "memory")
#define RRLD(W)                                                                                                                               \
  asm volatile("global_load_dwordx4 %0, %9, %10 sc1\n\tglobal_load_dwordx4 %1, %9, %11 sc1\n\t"                                              \
               "global_load_dwordx4 %2, %9, %12 sc1\n\tglobal_load_dwordx4 %3, %9, %13 sc1\n\t"                                              \
               "global_load_dwordx4 %4, %14, %10 sc1\n\tglobal_load_dwordx4 %5, %14, %11 sc1\n\t"                                            \
               "global_load_dwordx4 %6, %14, %12 sc1\n\tglobal_load_dwordx4 %7, %14, %13 sc1\n\tglobal_load_dword %8, %15, %10 sc1" W         \
               : "=&v"(rr[0]), "=&v"(rr[1]), "=&v"(rr[2]), "=&v"(rr[3]), "=&v"(rr[4]), "=&v"(rr[5]), "=&v"(rr[6]), "=&v"(rr[7]), "=&v"(cbv)  \
               : "v"(ro), "s"(ws), "s"(ws + 16384), "s"(ws + 32768), "s"(ws + 49152), "v"(ro + 65536), "v"(O_MOD + 4096 + cj * 2)            \
               : "memory")
      unsigned wq;
      unsigned mr;
#define QLD(W)                                                                                                           \
  asm volatile("global_load_dwordx4 %0, %3, %5 offset:-4096 sc1\n\tglobal_load_dwordx4 %1, %3, %5 sc1\n\tglobal_load_dword %2, %4, %5 sc1" W \
               : "=&v"(m[0]), "=&v"(m[1]), "=&v"(mr)                                                                     \
               : "v"(qo), "v"(so_), "s"(ws)                                                                              \
               : "memory")
#define MODLD()                                                                                                                \
  asm volatile("global_load_dwordx4 %0, %3, %6 sc1\n\tglobal_load_dwordx4 %1, %4, %6 sc1\n\tglobal_load_dwordx4 %2, %5, %6 sc1" \
               : "=&v"(m[0]), "=&v"(m[1]), "=&v"(m[2])                                                                         \
               : "v"(mo), "v"(mo1), "v"(mo2), "s"(ws)                                                                          \
               : "memory")
#define MODW(n) asm volatile("s_waitcnt vmcnt(%3)" : "+v"(m[0]), "+v"(m[1]), "+v"(m[2]) : "n"(n) : "memory")
#pragma unroll
      for (int k = B_J; k < 16; ++k) {
        if (k > B_J) {
          if (k == 2 * U_N1 - 1 && !urdy) UPOLL(7);
          WAIT_V(sch.cnt[k]);
          if (k == B_QAT + B_QD + 1) WAIT_L(0);
          BAR(32 + k);
        }
        if (k > B_J && k - 2 + NS < 16) {
          const char* yp = Hc + ((mt * 2 + k - 2 + NS) & 15) * 128;
          const unsigned l = lbase + ((30 + k + NS) % NS) * 16384;
#pragma unroll
          for (int i = 0; i < 4; ++i) GLDS(l + i * 4096, voff[i], yp);
        }
        if (k == B_TK && hw == 1 && b >= 192) {
          const unsigned t = tick();
          if (lane == 0) asm volatile("global_store_dword %0, %1, %2 sc1" ::"v"(O_M2 + 8448 + (b - 192) * 4), "v"(min(t, SENT - 1u)), "s"(ws) : "memory");
        }
        if (k == B_RAT + 2) {
          WAIT_V(sch.rcnt);
          if (rdc) {
            asm volatile("" : W8(rr), "+v"(cbv));
            POLL(__all((TG2(rr, 0) | TG2(rr, 2) | TG2(rr, 4) | TG2(rr, 6)) == 0u && cbv != SENT), 2, RRLD(VW0));
            float t[2];
#pragma unroll
            for (int j = 0; j < 2; ++j) {
#define RF(q) __uint_as_float(rr[q][2 * j])
              t[j] = ((RF(0) + RF(1)) + (RF(2) + RF(3))) + ((RF(4) + RF(5)) + (RF(6) + RF(7)));
            }
            const unsigned o = pack2(swap32_add(t[0], t[0]), swap32_add(t[1], t[1])), f = gbq ? cbv : o;
            const float pr = row_sum(bflo(f) * bflo(wq) + bfhi(f) * bfhi(wq)), ps = swap16_add(pr, pr);
            if (lane <= 32) asm volatile("global_store_dword %0, %1, %2 sc1" ::"v"(ra1), "v"(min(lane < 32 ? o : __float_as_uint(ps), SENT - 1u)), "s"(ws) : "memory");
            TS(12);
          } else {
            asm volatile("" ::"v"(rq[0]), "v"(rq[1]));
          }
        }
        if (k == V_T && hw == 2) {
          WAIT_V(sch.vcnt);
          asm volatile("" : "+v"(gp[0]), "+v"(gp[1]), "+v"(gp[2]), "+v"(gp[3]));
          POLL(!__any(GPBAD), 2, GP4(O_VP + (cc * 8 + mt) * 4096 + lane * 16, VW0));
          const f32x4 d = GPSUM();
          if (lane < 32) *reinterpret_cast<u32x2*>(shm + VT + (lane & 15) * 16 + g4 * 8) = u32x2{pack2(d[0], d[1]), pack2(d[2], d[3])};
          WAIT_L(0);
        }
        if (k == V_T + 1) {
          f32x4 n[2] = {};
#pragma unroll
          for (int q = 0; q < 8; ++q)
            n[q >> 2] = __builtin_amdgcn_mfma_f32_16x16x32_bf16(BF8(*reinterpret_cast<const u32x4*>(shm + VT + (q & 3) * 64 + g4 * 16)),
                                                                BF8(*reinterpret_cast<const u32x4*>(shm + STG + hw * 8192 + q * 1024 + lane * 16)), n[q >> 2], 0, 0, 0);
          if (lane < 16)
            asm volatile("global_store_dword %0, %1, %2 sc1" ::"v"(O_NP + nt * 4096 + (cc * 8 + mt) * 256 + hw * 64 + lane * 4), "v"(min(pack2(n[0][0], n[1][0]), SENT - 1u)), "s"(ws) : "memory");
          TS(33);
        }
        if (k == N_K + N_D && rnp) {
            WAIT_V(sch.ncnt);
            asm volatile("" : "+v"(gp[0]), "+v"(gp[1]), "+v"(gp[2]), "+v"(gp[3]));
            POLL(!__any(GPBAD), 2, GP4(O_NP + nt * 4096 + lane * 16, VW0));
            const unsigned r0 = nt * 128 + ((lane >> 2) & 3) * 32 + (lane & 3) * 4 + (g4 & 1) * 2;
        const f32x4 d = GPSUM();
        unsigned ky[4];
#pragma unroll
        for (int i = 0; i < 4; ++i) {
          const unsigned f = __float_as_uint(d[i]);
          ky[i] = (((f ^ ((unsigned)((int)f >> 31) | 0x80000000u)) >> 1) & ~2047u) | (r0 + (i & 1) * 16 + (i >> 1));
        }
        unsigned t1 = max(max(ky[0], ky[1]), max(ky[2], ky[3])), t2 = max(min(max(ky[0], ky[1]), max(ky[2], ky[3])), max(min(ky[0], ky[1]), min(ky[2], ky[3])));
        TOP2(0xB1); TOP2(0x4E); TOP2(0x141); TOP2(0x140);
        TOP2S(__builtin_amdgcn_permlane16_swap);
            if (lane < K_R) asm volatile("global_store_dwordx2 %0, %1, %2 sc1" ::"v"(O_K2 + nt * 64 + lane * 1024), "v"(u32x2{t1, t2}), "s"(ws) : "memory");
            TS(34);
        }
        if (k == N_K && rnp) GP4(O_NP + nt * 4096 + lane * 16, "");
        if (k == B_QAT + B_QD) {
          asm volatile("s_waitcnt vmcnt(%3)" : "+v"(m[0]), "+v"(m[1]), "+v"(mr) : "n"(sch.qcnt) : "memory");
          POLL(__all(max(max(max4(m[0]), max4(m[1])), mr) != SENT), 2, QLD(VW0));
          const f32x4 t = __builtin_bit_cast(f32x4, m[0]) + __builtin_bit_cast(f32x4, m[1]);
          const float r = swap16_add(swap32_add(t[0], t[2]), swap32_add(t[1], t[3]));
          const unsigned a0 = pack2(r, r);
          asm volatile("ds_write_b64 %0, %1" ::"v"(qa), "v"(u32x2{a0, pack2(r - bflo(a0), r - bflo(a0))}) : "memory");
          const float rs = __uint_as_float(mr ^ ((hw & 1) ? 0x80000000u : 0u));
          const unsigned bb = pack2(rs, rs - rbf(rs));
          *reinterpret_cast<u32x2*>(shm + RKB + lane * 16 + (hw & 1) * 8) = u32x2{bb, bb};
        }
        if (k == CPF_B && hw == 2 && lane < 12) PFL(cpf);
        if (k == B_RAT) {
          if (rdc) RRLD(""); else RDLD();
          asm volatile("ds_read_b32 %0, %1\n\ts_waitcnt lgkmcnt(0)" : "=v"(wq) : "v"(wqa) : "memory");
        }
        if (k == B_QAT) QLD("");
        if (k == B_MAT) MODLD();
        if (k == B_WK) {
#pragma unroll
          for (int q = 8 - B_WN; q < 8; ++q) LDWI(wdv[q], od, wd, D * 2);
        }
      }
      MODW(sch.mcnt);
      TS(30);
      SKEWPT();
      POLL(__all(max(max(max4(m[0]), max4(m[1])), max4(m[2])) != SENT), 4, MODLD(); MODW(0));
      *reinterpret_cast<u32x4*>(&svb[hw][lane * 8]) = m[0];
      *reinterpret_cast<u32x4*>(&svb[hw][512 + lane * 8]) = m[1];
      if (hw == 0) {
        asm volatile("ds_write_b128 %0, %1" ::"v"(ma2), "v"(m[2]) : "memory");
      } else if (hw == 2) {
        int d = max(max((int)(m[2][0] - ts0), (int)(m[2][1] - ts0)), max((int)(m[2][2] - ts0), (int)(m[2][3] - ts0)));
        d = max(d, (int)dpp_u<0xB1>(d));
        d = max(d, (int)dpp_u<0x4E>(d));
        d = max(d, (int)dpp_u<0x141>(d));
        d = max(d, (int)dpp_u<0x140>(d));
        const unsigned hh = ts0 + d + 235 + ((d - T_N) * T_A >> 8);
        ssel[7] = hh;
        if (K_E && (int)(tick() - (unsigned)__builtin_amdgcn_readfirstlane((int)hh) - T_K) >= 0) K2LD("");
      } else {
        const f32x4 v = __builtin_bit_cast(f32x4, m[2]);
        const f32x2 c = {v[0] + v[1], v[2] + v[3]};
        const float s4 = dpp_add<0x4E>(dpp_add<0xB1>(c[0] + c[1]));
        if (lane < 8) asm volatile("ds_write_b64 %0, %1" ::"v"(ma2), "v"(lane < 4 ? c : f32x2{s4, s4}) : "memory");
      }
    }
    if (hw != 2 || !L_PR) __builtin_amdgcn_s_setprio(0);
    TS(17);
    BT(48);
    LBAR();
    BTE();
    TS(18);
    if (hw == 2) {
      if (K_E) WAIT_V(0);
      asm volatile("" : "+v"(kk));
      if (__any(max(kk[0], kk[1]) == SENT)) {
        nap_tick((unsigned)__builtin_amdgcn_readfirstlane((int)ssel[7]) + T_K);
        POLL2(max(kk[0], kk[1]) == SENT, K_S, PCAP, K2LD(VW0));
      }
      TS(35);
      const unsigned me = lane < 32 ? kk[0] : kk[1], ot = (lane & 16) ? kk[1] : kk[0];
      unsigned rk = ot > me;
      sfor<15>([&](auto r) __attribute__((always_inline)) { rk += dpp_u<0x121 + decltype(r)::value>(ot) > me; });
      auto rs_ = __builtin_amdgcn_permlane16_swap(rk, rk, false, false);
      rk = rs_[0] + rs_[1];
      if (rk < 8u) {
        *reinterpret_cast<unsigned*>(shm + LST + rk * 4) = 0x8ffeu - (me & 2047u) * 2;
        if ((lane & 15) == nt) *reinterpret_cast<unsigned*>(shm + SLT + (me & 127u) * 4) = rk + 1;
      }
      asm volatile("" ::: "memory");
      ssel[20] = 1u;
      if (L_PR == 2) __builtin_amdgcn_s_setprio(0);
    }
    TS(19);
    unsigned sfa = lds0 + SPARE + 10368, sv, one = 1u;
    RETIRE();
    const int kc = (g & 3) * 256 + hw * 64 + (lane & 7) * 8;
    const u32x4 cbq = *reinterpret_cast<const u32x4*>(&svb[2][kc]), sbq = *reinterpret_cast<const u32x4*>(&svb[1][kc]);
    f32x4 cb2[2] = {lo4(cbq), hi4(cbq)}, sb2[2] = {lo4(sbq), hi4(sbq)};
    float sw = cwj[8], cj8 = cwj[lane & 7];
    unsigned rk2 = lds0 + LST + (lane >> 3) * 4;
    unsigned xo_ = O_X1C + (lane >> 3) * 2048 + kc * 2, hwa = lds0 + XC + hw * 1024 + (lane & 7) * 128 + (((lane >> 3) ^ lane) & 7) * 16;
    unsigned hra = lds0 + XC + g4 * 128 + ((lane ^ g4) & 7) * 16, hrb = hra ^ 64, ara = lds0 + SPARE + 15872 + (lane & 2) * 64 + g4 * 32 + (lane & 1) * 16;
    unsigned pso = O_PART + ((g >> 2) * 2048 + (lane & 8) * 128 + (g & 3) * 256 + hw * 64 + (lane & 7) * 8 + g4 * 2) * 4;
    unsigned gso = O_GU + g * 1024 + (lane >> 5) * 256 + hw * 64 + (lane & 15) * 4, awa = lds0 + SPARE + 15872 + lane * 4, sfw = sfa + hw * 4;
    unsigned gl0 = O_GU + (g >> 2) * 4096 + lane * 8, gl1 = O_GU + (xcd * 7 + (mt + 7 - W_UO) % 7) * 4096 + 512 + lane * 8;
    asm volatile("" : "+v"(sfa), "+v"(one), "+v"(hra), "+v"(hrb), "+v"(ara), "+v"(pso), "+v"(gso), "+v"(awa), "+v"(gl0), "+v"(gl1), "+v"(sfw), "+v"(qm));
    asm volatile("" : "+v"(cb2[0]), "+v"(cb2[1]), "+v"(sb2[0]), "+v"(sb2[1]), "+v"(hwa), "+v"(sw), "+v"(cj8), "+v"(rk2), "+v"(xo_));
    unsigned sto = O_ST2 + lane * 16, kmk = ~0u;
    asm volatile("" : "+v"(sto), "+v"(kmk));
    const unsigned th = (unsigned)__builtin_amdgcn_readfirstlane((int)ssel[7]);
    const char* wb = ws;
#define P2PUT(i) if (__builtin_expect(wb != ws, 0)) { WAIT_V(0); if (lane == 0) asm volatile("global_store_dwordx2 %0, %1, %2 sc1" ::"v"(O_F2 + (i)*8192 + (g * 4 + hw) * 8), "v"(tag), "s"(ws) : "memory"); }
#define P2GET(o) if (__builtin_expect(wb != ws, 0)) { u64 f; POLL2(f != tag, 4, PCAP, asm volatile("global_load_dwordx2 %0, %1, %2 sc1" VW0 : "=&v"(f) : "v"(O_F2 + (o)), "s"(ws) : "memory")); }
#pragma unroll 1
    for (;;) {
    TS(20);
    if (wb == ws) {
      unsigned r_;
      POLL2(sv == 0u, 2, 1 << 26, asm volatile("ds_read_b32 %0, %2 offset:80\n\tds_read_b32 %1, %3\n\ts_waitcnt lgkmcnt(0)" : "=&v"(sv), "=&v"(r_) : "v"(sfa), "v"(rk2) : "memory"));
      rk2 = r_;
    }
    TS(21);
    nap_tick(th + T_C3 - 10);
    __builtin_amdgcn_s_setprio(H_P);
    wait_tick(th + T_C3);
    u32x4 st, xv_;
    POLL2(max(max4(xv_), max4(st)) == SENT, D_S, PCAP,
          asm volatile("global_load_dwordx4 %0, %2, %4 sc1\n\tglobal_load_dwordx4 %1, %3, %4 sc1" VW0 : "=&v"(st), "=&v"(xv_) : "v"(sto), "v"(xo_), "s"(ws) : "memory"));
    TS(22);
    SKEWPT();
#define SUM8(v) dpp_add<0x141>(dpp_add<0x4E>(dpp_add<0xB1>(v)))
    const f32x4 sf = __builtin_bit_cast(f32x4, st);
    const float mean = SUM8(sf[0]) * 0.125f, dm = sf[0] - mean;
    const float rstd = __builtin_amdgcn_rsqf(SUM8(sf[1] + 128.0f * (dm * dm)) * (1.0f / D) + 1e-5f);
    asm volatile("ds_write_b128 %0, %1\n\tds_write_b32 %2, %3 offset:64" ::"v"(hwa),
                 "v"(pk8(((lo4(xv_) - mean) * rstd) * cb2[0] + sb2[0], ((hi4(xv_) - mean) * rstd) * cb2[1] + sb2[1])), "v"(sfw), "v"(one) : "memory");
    TS(3);
    u32x4 ha[8], fg;
#define HSLD(W)                                                                                                                             \
  asm volatile(                                                                                                                             \
      "ds_read_b128 %8, %11 offset:64\n\tds_read_b128 %0, %9\n\tds_read_b128 %1, %10 offset:512\n\tds_read_b128 %2, %9 offset:1024\n\t"       \
      "ds_read_b128 %3, %10 offset:1536\n\tds_read_b128 %4, %9 offset:2048\n\tds_read_b128 %5, %10 offset:2560\n\t"                          \
      "ds_read_b128 %6, %9 offset:3072\n\tds_read_b128 %7, %10 offset:3584" W                                                               \
      : "=&v"(ha[0]), "=&v"(ha[1]), "=&v"(ha[2]), "=&v"(ha[3]), "=&v"(ha[4]), "=&v"(ha[5]), "=&v"(ha[6]), "=&v"(ha[7]), "=&v"(fg)           \
      : "v"(hra), "v"(hrb), "v"(sfa)                                                                                                        \
      : "memory")
    unsigned key = (mkkey(rstd * SUM8(sf[2] + dm * cj8) + sw) | rk2) & kmk;
    asm volatile("" : "+v"(key));
    HSLD("");
    unsigned t1 = key, t2 = 0u;
    TOP2(0x140);
    TOP2S(__builtin_amdgcn_permlane16_swap);
    TOP2S(__builtin_amdgcn_permlane32_swap);
    const int l1 = __builtin_ctzll(__builtin_amdgcn_ballot_w64(key == t1)), l2 = __builtin_ctzll(__builtin_amdgcn_ballot_w64(key == t2));
    unsigned mq[6];
    asm volatile("s_bfe_i32 %0, %6, 0x10003\n\ts_bfe_i32 %1, %7, 0x10003\n\ts_bfe_i32 %2, %6, 0x10004\n\ts_bfe_i32 %3, %7, 0x10004\n\t"
                 "s_bfe_i32 %4, %6, 0x10005\n\ts_bfe_i32 %5, %7, 0x10005\n\ts_xor_b32 %4, %4, 0xffff\n\ts_xor_b32 %5, %5, 0xffff"
                 : "=&s"(mq[0]), "=&s"(mq[1]), "=&s"(mq[2]), "=&s"(mq[3]), "=&s"(mq[4]), "=&s"(mq[5]) : "s"(l1), "s"(l2) : "scc");
    const u64 m0 = mk64(mq[1], mq[0]), m1 = mk64(mq[3], mq[2]), m2 = mk64(mq[5], mq[4]);
    asm volatile("s_waitcnt lgkmcnt(0)" : W8(ha), "+v"(fg)::"memory");
    TS(2);
    if (b < NPART) {
#define HBAD ((fg[0] & fg[1] & fg[2] & fg[3]) != one)
      if (__any(HBAD)) POLL2(HBAD, 0, 1 << 26, HSLD("\n\ts_waitcnt lgkmcnt(0)"));
      TS(23);
      f32x4 dg = {0.f, 0.f, 0.f, 0.f}, du = {0.f, 0.f, 0.f, 0.f};
#pragma unroll
      for (int f = 0; f < 8; ++f) {
        dg = __builtin_amdgcn_mfma_f32_16x16x32_bf16(BF8(ha[f]), BF8(gT[f]), dg, 0, 0, 0);
        du = __builtin_amdgcn_mfma_f32_16x16x32_bf16(BF8(ha[f]), BF8(uT[f]), du, 0, 0, 0);
      }
      const bool s0 = __builtin_amdgcn_inverse_ballot_w64(m0), s1 = __builtin_amdgcn_inverse_ballot_w64(m1);
      if (__builtin_amdgcn_inverse_ballot_w64(m2))
        asm volatile("global_store_dword %0, %1, %3 sc1 ; ALWAYS\n\tglobal_store_dword %0, %2, %3 offset:512 sc1" ::"v"(gso),
                     "v"(nsent(s1 ? (s0 ? dg[3] : dg[2]) : (s0 ? dg[1] : dg[0]))), "v"(nsent(s1 ? (s0 ? du[3] : du[2]) : (s0 ? du[1] : du[0]))), "s"(wb) : "memory");
    }
    if (hw == 0 && wb == ws) asm volatile("ds_write_b64 %0, %1" ::"v"(sfa), "v"(u32x2{t1, t2}) : "memory");
    if (b < NPART) {
      TS(24);
      asm volatile("" : W8(wdv)::"memory");
      TR8(dT, wdv);
      P2PUT(0);
      if (hw == 0) {
        u32x2 rr[8];
        if (E_D) __builtin_amdgcn_s_sleep(E_D);
        P2GET(((((lane & 16) ? gl1 : gl0) - O_GU) >> 12) * 128 + (lane & 15) * 8);
#define MX2(i) max(max(rr[i][0], rr[i][1]), max(rr[i + 1][0], rr[i + 1][1]))
        POLL2(max(max(MX2(0), MX2(2)), max(MX2(4), MX2(6))) == SENT, 1, PCAP,
              asm volatile("global_load_dwordx2 %0, %8, %10 sc1\n\tglobal_load_dwordx2 %1, %8, %10 offset:1024 sc1\n\t"
                           "global_load_dwordx2 %2, %8, %10 offset:2048 sc1\n\tglobal_load_dwordx2 %3, %8, %10 offset:3072 sc1\n\t"
                           "global_load_dwordx2 %4, %9, %10 sc1\n\tglobal_load_dwordx2 %5, %9, %10 offset:1024 sc1\n\t"
                           "global_load_dwordx2 %6, %9, %10 offset:2048 sc1\n\tglobal_load_dwordx2 %7, %9, %10 offset:3072 sc1" VW0
                           : "=&v"(rr[0]), "=&v"(rr[1]), "=&v"(rr[2]), "=&v"(rr[3]), "=&v"(rr[4]), "=&v"(rr[5]), "=&v"(rr[6]), "=&v"(rr[7])
                           : "v"(gl0), "v"(gl1), "s"(wb)
                           : "memory"));
        TS(8);
#define R2(i) __builtin_bit_cast(f32x2, rr[i])
        const f32x2 gs = (R2(0) + R2(1)) + (R2(2) + R2(3)), us = (R2(4) + R2(5)) + (R2(6) + R2(7));
        const unsigned gp = pack2(gs[0], gs[1]), up = pack2(us[0], us[1]);
        const unsigned sp = pack2(bflo(gp) * __builtin_amdgcn_rcpf(1.0f + __expf(-bflo(gp))), bfhi(gp) * __builtin_amdgcn_rcpf(1.0f + __expf(-bfhi(gp))));
        asm volatile("ds_write_b32 %0, %1\n\tds_write_b32 %2, %3 offset:24" ::"v"(awa), "v"(pack2(bflo(sp) * bflo(up), bfhi(sp) * bfhi(up))), "v"(sfa), "v"(one) : "memory");
      }
      TS(25);
      SKEWPT();
      POLL2(sv != one, 0, 1 << 26, asm volatile("ds_read_b32 %1, %3 offset:24\n\tds_read_b128 %0, %2\n\ts_waitcnt lgkmcnt(0)" : "=&v"(fg), "=&v"(sv) : "v"(ara), "v"(sfa) : "memory"));
      if (VD_E == 1) asm volatile("global_load_dwordx2 %0, %1, %2 sc1" : "=&v"(vd) : "v"(O_VD + (b & 31) * 128), "s"(ws) : "memory");
      const f32x2 dd = MV64(dT, fg);
      asm volatile("global_store_dwordx2 %0, %1, %2 sc1" ::"v"(pso), "v"(u32x2{nsent(dd[0]), nsent(dd[1])}), "s"(wb) : "memory");
      TS(6);
      P2PUT(1);
      if (VD_E == 1) {
        asm volatile("s_waitcnt vmcnt(1)" : "+v"(vd)::"memory");
        if (__any(max(vd[0], vd[1]) == SENT)) VDLD(2);
      } else if (VD_E == 2) {
        if (hw == 1) {
          nap_tick(th + T_C5);
          VDLD(2);
          asm volatile("ds_write_b64 %0, %1 offset:8" ::"v"(sfa), "v"(vd) : "memory");
        } else {
          POLL2(vd[1] == 0u, 2, 1 << 26, asm volatile("ds_read_b64 %0, %1 offset:8\n\ts_waitcnt lgkmcnt(0)" : "=v"(vd) : "v"(sfa) : "memory"));
        }
      } else if (VD_E == 3) {
        break;
      } else {
        nap_tick(th + T_C5);
        VDLD(2);
      }
      TS(5);
    } else if (hw == 3) {
      u32x2 g0, g1;
      nap_tick(th + T_C4);
      POLL2(min(min(g0[0], g0[1]), min(g1[0], g1[1])) == 0u, 2, PCAP,
            asm volatile("global_load_dwordx2 %0, %2, %4 sc1\n\tglobal_load_dwordx2 %1, %3, %4 sc1" VW0
                         : "=&v"(g0), "=&v"(g1) : "v"(O_KEY + lane * 8 * KSTR), "v"(O_KEY + (lane + 64) * 8 * KSTR), "s"(ws) : "memory"));
      const unsigned k1 = t1, k2 = t2;
      t1 = max(g0[0], g1[0]), t2 = max(min(g0[0], g1[0]), max(g0[1], g1[1]));
      TOP2(0xB1); TOP2(0x4E); TOP2(0x141); TOP2(0x140);
      TOP2S(__builtin_amdgcn_permlane16_swap);
      TOP2S(__builtin_amdgcn_permlane32_swap);
      vd = u32x2{max(t1, k1), max(min(t1, k1), max(t2, k2))};
      if (lane == 0) asm volatile("global_store_dwordx2 %0, %1, %2 sc1\n\tds_write_b64 %3, %1 offset:8" ::"v"(O_VD + (b & 31) * 128), "v"(vd), "s"(ws), "v"(sfa) : "memory");
      TS(29);
      break;
    } else if (hw < 2) {
      const int tok = b & 1, c0 = ((b - NPART) >> 1) * 64 + hw * 32 + (lane & 7) * 4, col = c0 + g4;
      const unsigned td = tick(), kk = tok ? t2 : t1;
      const int ln = tok ? l2 : l1;
      unsigned v;
      POLL2(v == SENT, 1, PCAP, asm volatile("global_load_dword %0, %1, %2 sc1" VW0 : "=&v"(v) : "v"(O_X1C + ((__builtin_amdgcn_readlane(sto, ln) - O_ST2) >> 7) * 2048 + (col >> 1) * 4), "s"(ws) : "memory"));
      float x1 = (col & 1) ? bfhi(v) : bflo(v);
      float h2 = rbf(((x1 - __int_as_float(__builtin_amdgcn_readlane(__float_as_int(mean), ln))) * __int_as_float(__builtin_amdgcn_readlane(__float_as_int(rstd), ln))) * bf2f(svb[2][col]) + bf2f(svb[1][col]));
      float gb = bf2f(svb[3][col]), rsg = rbf(1.0f / (1.0f + __expf(-key_val(kk))));
      unsigned oo_ = (key_row(kk) * D + col) * 2, po = O_PART + ((lane >> 3) * 2048 + tok * 1024 + c0) * 4, p1 = po + 4 * 65536;
      asm volatile("" : "+v"(x1), "+v"(h2), "+v"(gb), "+v"(rsg), "+v"(oo_), "+v"(po), "+v"(p1));
      u32x4 y[7];
      nap_tick(td + F_T);
      P2GET(8192 + (min(lane, 55) * 16 + ((b - NPART) >> 1)) * 8);
      POLL2(max(max(max(max4(y[0]), max4(y[1])), max(max4(y[2]), max4(y[3]))), max(max(max4(y[4]), max4(y[5])), max4(y[6]))) == SENT || vd[1] == 0u, OPT_RR, PCAP,
            asm volatile("global_load_dwordx4 %0, %7, %9 sc1\n\tglobal_load_dwordx4 %1, %7, %10 sc1\n\tglobal_load_dwordx4 %2, %7, %11 sc1\n\t"
                         "global_load_dwordx4 %3, %7, %12 sc1\n\tglobal_load_dwordx4 %4, %8, %9 sc1\n\tglobal_load_dwordx4 %5, %8, %10 sc1\n\t"
                         "global_load_dwordx4 %6, %8, %11 sc1"
                         : "=&v"(y[0]), "=&v"(y[1]), "=&v"(y[2]), "=&v"(y[3]), "=&v"(y[4]), "=&v"(y[5]), "=&v"(y[6])
                         : "v"(po), "v"(p1), "s"(wb), "s"(wb + 65536), "s"(wb + 131072), "s"(wb + 196608)
                         : "memory");
            asm volatile("s_waitcnt vmcnt(2)\n\tds_read_b64 %7, %8 offset:8\n\ts_waitcnt vmcnt(0) lgkmcnt(0)"
                         : "+v"(y[0]), "+v"(y[1]), "+v"(y[2]), "+v"(y[3]), "+v"(y[4]), "+v"(y[5]), "+v"(y[6]), "=&v"(vd) : "v"(sfa) : "memory"));
      TS(28);
#define YF(i) __builtin_bit_cast(f32x4, y[i])
      const f32x4 t = ((YF(0) + YF(1)) + (YF(2) + YF(3))) + ((YF(4) + YF(5)) + YF(6));
      const float sum = dpp_add<0x128>(swap16_add(swap32_add(t[0], t[2]), swap32_add(t[1], t[3])));
      asm volatile("global_store_short %0, %1, %2" ::"v"(oo_), "v"(pack2(x1 + gb * (wb == ws && kk < vd[1] ? h2 : rbf(h2 + rbf(rbf(sum) * rsg))), 0.f)), "s"(out) : "memory");
      TS(15);
    } else {
      break;
    }
    if (__builtin_expect(wb != ws || __all(vd[1] == t2), 1)) break;
    const unsigned tk = (lane & 8) ? vd[1] : vd[0], ns = tk == t1 ? l1 >> 3 : 8 + ((lane >> 3) & 1);
    rk2 = tk & 0xffffu, kmk = lane < 16 ? ~0u : 0u, one = 3u;
    xo_ = O_X1C + ns * 2048 + kc * 2, sto = O_ST2 + ns * 128 + (lane & 7) * 16;
    wb = ws + O_P2;
    }
    TSE();
  }
}

extern "C" void kernel_launch(void* const* d_in, const int* in_sizes, int n_in, void* d_out, int out_size, void* d_ws, size_t ws_size,
                              hipStream_t stream) {
  const u16* x = (const u16*)d_in[0];
  const u16* wsr = (const u16*)d_in[1];
  const u16* wsi = (const u16*)d_in[2];
  const u16* whr = (const u16*)d_in[3];
  const u16* whi = (const u16*)d_in[4];
  const u16* w_r = (const u16*)d_in[6];
  const u16* w_gate = (const u16*)d_in[7];
  const u16* w_up = (const u16*)d_in[8];
  const u16* w_down = (const u16*)d_in[9];
  const u16* c = (const u16*)d_in[10];
  const u16* w_mod = (const u16*)d_in[11];
  u16* out = (u16*)d_out;
  k_main<<<256, 512, 0, stream>>>(x, (char*)d_ws, wsr, wsi, c, w_mod, whr, whi, w_r, w_gate, w_up, w_down, out);
}
```

```cpp
#include <hip/hip_runtime.h>
#include <hip/hip_bf16.h>
#include <stdint.h>
#include <utility>

#define NEWLAY 1
typedef unsigned short u16;
typedef unsigned long long u64;
using bf16x8 = __attribute__((ext_vector_type(8))) short;
using bf16v8 = __attribute__((ext_vector_type(8))) __bf16;
using f32x4 = __attribute__((ext_vector_type(4))) float;
using f32x2 = __attribute__((ext_vector_type(2))) float;
using u32x4 = __attribute__((ext_vector_type(4))) unsigned;
using u32x2 = __attribute__((ext_vector_type(2))) unsigned;

constexpr int S = 2048, D = 1024, F = 3584;

__device__ __forceinline__ float bf2f(unsigned v) { return __uint_as_float(v << 16); }
__device__ __forceinline__ float bflo(unsigned v) { return __uint_as_float(v << 16); }
__device__ __forceinline__ float bfhi(unsigned v) { return __uint_as_float(v & 0xffff0000u); }
__device__ __forceinline__ unsigned f2bf(float f) {
  __hip_bfloat16 b = __float2bfloat16(f);
  return (unsigned)*reinterpret_cast<u16*>(&b);
}
__device__ __forceinline__ float rbf(float f) { return bf2f(f2bf(f)); }
using bf16v2 = __attribute__((ext_vector_type(2))) __bf16;
__device__ __forceinline__ unsigned pack2(float lo, float hi) {
  return __builtin_bit_cast(unsigned, __builtin_convertvector(f32x2{lo, hi}, bf16v2));
}

template <int CTRL>
__device__ __forceinline__ float dpp_f(float v) {
  return __int_as_float(__builtin_amdgcn_mov_dpp(__float_as_int(v), CTRL, 0xf, 0xf, true));
}
template <int CTRL>
__device__ __forceinline__ float dpp_add(float v) { return v + dpp_f<CTRL>(v); }
__device__ __forceinline__ float row_sum(float v) { return dpp_add<0x140>(dpp_add<0x141>(dpp_add<0x4E>(dpp_add<0xB1>(v)))); }
template <int CTRL>
__device__ __forceinline__ unsigned dpp_u(unsigned v) { return (unsigned)__builtin_amdgcn_mov_dpp((int)v, CTRL, 0xf, 0xf, true); }
__device__ __forceinline__ u64 mk64(unsigned hi, unsigned lo) { return ((u64)hi << 32) | lo; }
__device__ __forceinline__ float swap32_add(float a, float b) {
  auto r = __builtin_amdgcn_permlane32_swap(__float_as_uint(a), __float_as_uint(b), false, false);
  return __uint_as_float(r[0]) + __uint_as_float(r[1]);
}
__device__ __forceinline__ float swap16_add(float a, float b) {
  auto r = __builtin_amdgcn_permlane16_swap(__float_as_uint(a), __float_as_uint(b), false, false);
  return __uint_as_float(r[0]) + __uint_as_float(r[1]);
}
__device__ __forceinline__ float ar4(float v) {
  const float t = swap32_add(v, v);
  return swap16_add(t, t);
}
__device__ __forceinline__ float wave_sum_s(float v) {
  v = row_sum(v);
  v += __int_as_float(__builtin_amdgcn_update_dpp(0, __float_as_int(v), 0x142, 0xa, 0xf, false));
  v += __int_as_float(__builtin_amdgcn_update_dpp(0, __float_as_int(v), 0x143, 0xc, 0xf, false));
  return __int_as_float(__builtin_amdgcn_readlane(__float_as_int(v), 63));
}
__device__ __forceinline__ float silu_bf(float c) { return rbf(c * __builtin_amdgcn_rcpf(1.0f + __expf(-c))); }
__device__ __forceinline__ float dsum(unsigned v, float a) {
  return __builtin_amdgcn_fdot2_f32_bf16(__builtin_bit_cast(bf16v2, v), __builtin_bit_cast(bf16v2, 0x3f803f80u), a, false);
}
template <class Fn, int... I>
__device__ __forceinline__ void sfor_(Fn&& f, std::integer_sequence<int, I...>) { (f(std::integral_constant<int, I>{}), ...); }
template <int N, class Fn>
__device__ __forceinline__ void sfor(Fn&& f) { sfor_(f, std::make_integer_sequence<int, N>{}); }
constexpr unsigned SENT = 0xFFFFFFFFu;
constexpr int NPART = 224;
__device__ __forceinline__ f32x2 unpk(unsigned v) { return f32x2{bflo(v), bfhi(v)}; }

constexpr size_t WS_EPO = 0;
constexpr size_t WS_XFL = 4096;
constexpr size_t WS_MODP = 40960;
constexpr size_t WS_KEYG = 320u << 10;
constexpr size_t WS_UFL = WS_KEYG + 16384;
constexpr size_t WS_MOD = WS_KEYG + (80u << 10);
constexpr size_t WS_RS = WS_KEYG + (92u << 10);
constexpr size_t WS_PART = WS_KEYG + (316u << 10);
constexpr unsigned O_MODP = WS_MODP, O_XFL = WS_XFL, O_KEY = WS_KEYG, O_MOD = WS_MOD, O_HDR = WS_MOD + 8192, O_RS = WS_RS, O_M2 = WS_RS + 80 * 1024;
constexpr unsigned O_PART = WS_PART, O_GU = O_PART + 56 * 8192, O_STAT = O_GU + 224 * 1024, O_X1C = O_STAT + 262144;
#define D_R 1
constexpr unsigned O_ST2 = O_X1C + 20480, X_CS = 22528, O_VD = O_X1C + D_R * X_CS, O_P2 = 1536u << 10, O_F2 = 3584u << 10;
constexpr unsigned O_GC = O_M2 + 8704, O_VP = O_M2 + 12288, O_NP = O_VP + 65536, O_K2 = O_NP + 65536, O_G2 = 3u << 20, O_G1 = O_G2 + 262144;
static_assert(O_K2 + 4096 == O_PART && O_VD + 4096 <= O_KEY + (512 + 896) * 1024 && O_PART + O_P2 >= O_VD + 4096 && O_STAT + O_P2 <= O_G2, "armed area");
constexpr size_t WS_H = 4u << 20;
constexpr size_t WS_T = WS_H + (size_t)S * D * 2;
constexpr size_t WS_P = WS_T + (size_t)2 * D * S * 2;

#define TSG(base, i)
#define TSD(i)
#define PCAP (1 << 20)
#define PDBG(it)
#define TS(i)
#define TSE()
#define BT(k)
#define BTE()
#define BAR(k) do { BT(k); BARRIER(); } while (0)
__device__ __forceinline__ void st16_wt(void* p, u32x4 v) {
  asm volatile("global_store_dwordx4 %0, %1, off sc1\n\ts_nop 1" ::"v"(p), "v"(v) : "memory");
}
#define WAIT_V(n) asm volatile("s_waitcnt vmcnt(%0)" ::"n"(n) : "memory")
#define WAIT_L(n) asm volatile("s_waitcnt lgkmcnt(%0)" ::"n"(n) : "memory")
#define BARRIER() asm volatile("s_barrier" ::: "memory")
#define LBAR() asm volatile("s_waitcnt lgkmcnt(0)\n\ts_barrier" ::: "memory")
#define DSR(dst, addr, off) asm volatile("ds_read_b128 %0, %1 offset:%2" : "=v"(dst) : "v"(addr), "n"(off) : "memory")
#define MFMA(c, a, b) asm volatile("v_mfma_f32_16x16x32_bf16 %0, %1, %2, %0" : "+v"(c) : "v"(a), "v"(b))
#define MFMA0(c, a, b) asm volatile("v_mfma_f32_16x16x32_bf16 %0, %1, %2, 0" : "=&v"(c) : "v"(a), "v"(b))
#define GLDS(m0v, voff, gbase) \
  asm volatile("s_mov_b32 m0, %0\n\ts_nop 0\n\tglobal_load_lds_dwordx4 %1, %2" ::"s"(m0v), "v"(voff), "s"(gbase) : "memory")
#define GLDS4(m0v, v, gbase)                                                                                                        \
  asm volatile("s_mov_b32 m0, %0\n\ts_nop 0\n\tglobal_load_lds_dwordx4 %1, %5\n\tglobal_load_lds_dwordx4 %2, %5 offset:1024\n\t"    \
               "global_load_lds_dwordx4 %3, %5 offset:2048\n\tglobal_load_lds_dwordx4 %4, %5 offset:3072" ::"s"(m0v),               \
               "v"((v)[0]), "v"((v)[1]), "v"((v)[2]), "v"((v)[3]), "s"(gbase) : "memory")
#define LDG(dst, voff, base) asm volatile("global_load_dwordx4 %0, %1, %2" : "=v"(dst) : "v"(voff), "s"(base) : "memory")
#define WPOL " nt"
#define LDM(dst, voff, base) asm volatile("global_load_dwordx4 %0, %1, %2" WPOL : "=v"(dst) : "v"(voff), "s"(base) : "memory")
#define LDX(dst, voff, base, off) asm volatile("global_load_dwordx4 %0, %1, %2 offset:%3" : "=v"(dst) : "v"(voff), "s"(base), "n"(off) : "memory")
#define LDXS(dst, voff, base, off) asm volatile("global_load_dwordx4 %0, %1, %2 offset:%3 sc1" : "=v"(dst) : "v"(voff), "s"(base), "n"(off) : "memory")
#define BF8(v) __builtin_bit_cast(bf16v8, v)
#define LSPIN(v, p, cond)                                   \
  _Pragma("unroll 1") for (int it_ = 0; it_ < (1 << 26); ++it_) { \
    v = __atomic_load_n(p, __ATOMIC_RELAXED);               \
    if (cond) break;                                        \
    __builtin_amdgcn_s_sleep(1);                            \
  }
#define POLL(ok, sl, reload)                                                  \
  if (__builtin_expect(!(ok), 0)) {                                           \
    _Pragma("unroll 1") for (int it = 0; it < PCAP; ++it) {                   \
      PDBG(it);                                                               \
      __builtin_amdgcn_s_sleep(sl);                                           \
      reload;                                                                 \
      if (ok) break;                                                          \
    }                                                                         \
  }
#define POLL2(bad, sl, cap, load)                                             \
  {                                                                           \
    int it = (cap);                                                           \
    u64 live;                                                                 \
    _Pragma("unroll 1") do {                                                  \
      if (sl) __builtin_amdgcn_s_sleep(sl);                                   \
      asm volatile("s_sub_u32 %0, %0, 1\n\ts_cselect_b64 %1, 0, -1" : "+s"(it), "=s"(live)::"scc"); \
      load;                                                                   \
    } while (__builtin_amdgcn_ballot_w64(bad) & live);                        \
    PDBG(PCAP - 2 - it);                                                      \
  }
__device__ __forceinline__ unsigned tick() {
  u64 t;
  asm volatile("s_memrealtime %0\n\ts_waitcnt lgkmcnt(0)" : "=s"(t)::"memory");
  return (unsigned)t;
}
__device__ __forceinline__ void wait_tick(unsigned t) {
#pragma unroll 1
  for (int i = 0; i < 256; ++i)
    if ((int)(tick() - t) >= 0) break;
}
__device__ __forceinline__ void nap_tick(unsigned t) {
#pragma unroll 1
  for (int i = 0; i < 1024; ++i) {
    if ((int)(tick() - t) >= 0) break;
    __builtin_amdgcn_s_sleep(2);
  }
}
#define T_C1 245
#define T_A 40
#define T_N 2500
#define T_D1 78
#define T_C2 322
#define VW0 "\n\ts_waitcnt vmcnt(0)"
#define W8(a) "+v"(a[0]), "+v"(a[1]), "+v"(a[2]), "+v"(a[3]), "+v"(a[4]), "+v"(a[5]), "+v"(a[6]), "+v"(a[7])
#define PFL(ptr) asm volatile("s_mov_b32 m0, %0\n\ts_nop 0\n\tglobal_load_lds_dword %1, off" ::"s"(pfl), "v"(ptr) : "memory")

#define HHOOK(j)
#define XPV 1557
#define XPW 1557
#define XPM (MODE ? XPW : XPV)
#define XPA0 (XPM / 1000)
#define XPA1 (XPM / 100 % 10)
#define XPB0 (XPM / 10 % 10)
#define XPB1 (XPM % 10)
#define U_X0 6
#define XLD(dst, XP, XH, i)                                                                                   \
  do {                                                                                                        \
    if (MODE) LDXS(dst, (i) ? xo1 : xo0, XP, ((XH)&3) * 2048 - 4096);                                         \
    else LDXS(dst, ((XH)&4) ? xo1 : xo0, XP, ((XH)&3) * 2048 + (i)*1024 - 4096);                              \
  } while (0)
#define HALF(XC, YA, RD, XN, XP, XH, LD, YP)                                                 \
  do {                                                                                       \
    _Pragma("unroll") for (int j = 0; j < 8; ++j) {                                          \
      if ((j & 1) == 0) { if (RD) WAIT_L(5); else WAIT_L(j == 0 ? 5 : 6 - j); }              \
      if (MODE == 2) MFMA0(acc[0][j], Y[j], XC[0]); else if (MODE) MFMA(acc[0][j], Y[j], XC[0]); else MFMA(acc[0][j], XC[0], Y[j]); \
      if (j == 0) DSR(Y[7], YP, 7 * 2048);                                                   \
      else if (RD) DSR(Y[j - 1], YA, (j - 1) * 2048);                                        \
      if (MODE == 2) MFMA0(acc[1][j], Y[j], XC[1]); else if (MODE) MFMA(acc[1][j], Y[j], XC[1]); else MFMA(acc[1][j], XC[1], Y[j]); \
      if ((LD) && j == (((XH)&1) ? XPA0 : XPB0)) XLD(XN[0], XP, XH, 0);                                             \
      if ((LD) && j == (((XH)&1) ? XPA1 : XPB1)) XLD(XN[1], XP, XH, 1);                                             \
      HHOOK(j);                                                                              \
    }                                                                                        \
  } while (0)

__device__ __forceinline__ int key_row(unsigned k) { return (int)((~k >> 1) & 2047u); }
__device__ __forceinline__ unsigned mkkey(float v) {
  const unsigned rb = pack2(0.f, v);
  return rb ^ ((unsigned)((int)rb >> 31) & 0x7fff0000u) ^ 0x80000000u;
}
__device__ __forceinline__ float key_val(unsigned k) {
  const unsigned ord = k >> 16;
  return bf2f(ord ^ ((ord & 0x8000u) ? 0x8000u : 0xffffu));
}
#define OPT_RD 22
#define OPT_RE 20
#define OPT_GD 2
__device__ __forceinline__ unsigned nsent(float v) { return min(__float_as_uint(v), SENT - 1u); }
__device__ __forceinline__ f32x4 lo4(u32x4 q) { return f32x4{bflo(q[0]), bfhi(q[0]), bflo(q[1]), bfhi(q[1])}; }
__device__ __forceinline__ f32x4 hi4(u32x4 q) { return f32x4{bflo(q[2]), bfhi(q[2]), bflo(q[3]), bfhi(q[3])}; }
__device__ __forceinline__ u32x4 pk8(f32x4 a, f32x4 b) { return u32x4{pack2(a[0], a[1]), pack2(a[2], a[3]), pack2(b[0], b[1]), pack2(b[2], b[3])}; }
__device__ __forceinline__ u32x4 nsent4(u32x4 v) { return u32x4{min(v[0], SENT - 1u), min(v[1], SENT - 1u), min(v[2], SENT - 1u), min(v[3], SENT - 1u)}; }
__device__ __forceinline__ unsigned max4(u32x4 v) { return max(max(v[0], v[1]), max(v[2], v[3])); }
__device__ __forceinline__ float amin4(u32x4 v, float m) {
  asm("v_min3_f32 %0, |%1|, |%2|, %0\n\tv_min3_f32 %0, |%3|, |%4|, %0" : "+v"(m) : "v"(v[0]), "v"(v[1]), "v"(v[2]), "v"(v[3]));
  return m;
}
__device__ __forceinline__ u32x4 selu(unsigned mk, u32x4 a, u32x4 b) { return (a & mk) | (b & ~mk); }
#define KSTR 16
#define TOP2(CTRL)                                                  \
  do {                                                              \
    const unsigned o1 = dpp_u<CTRL>(t1), o2 = dpp_u<CTRL>(t2);      \
    t2 = max(min(t1, o1), max(t2, o2));                             \
    t1 = max(t1, o1);                                               \
  } while (0)
#define TOP2S(SW)                                                   \
  do {                                                              \
    auto a_ = SW(t1, t1, false, false), b_ = SW(t2, t2, false, false); \
    t2 = max(min(a_[0], a_[1]), max(b_[0], b_[1]));                 \
    t1 = max(a_[0], a_[1]);                                         \
  } while (0)
#define T_C3 253
#define T_C4 340
#define T_G 9
#define T_U 10
#define E_D 6
#define O_DW 6
#define O_DS 4
#define O_TR 1
#define OPOL " sc1"
#define L_PR 1
#define D_W 1
#define X_PA 2
#define G1_C 2
#define R_AT 24
#define B_TK 8
#define W_T 4
#define V_K 3
#define V_T 5
#define N_K 10
#define N_D 2
#define T_K 0
#define H_AT 23
#define G1_S 1
#define V_F 0
#define J_L 0
#define F_T 132
#define K_S 1
#define K_R 1
#define T_C5 470
#define VD_E 1
#define K_E 1
#define Q_L 0
#define N_C (V_T + 1)
#define N_P 2
#define R_S 0
#define N_S 0
#define T_O 252
#define L_H 0
#define A_W 0
#define A_R 1
#define PF23 0
#define P_D5 2
#define S_N 5
#define S_D 2
#define J_D 0
#define W_L 0
#define W_D 2
#define D_S2 1
#define D_G 1
#define T_H 60
#define H_P 2
#define T_X 80
#define D_S 1
#define A_PW 8
#define A_PS 6
#define U_S 4
#define C_LD 2
#define C_DP 8
#define F_D 2
#define P_AT 2
#define F_AT 29
#define XQ_N 6
#define RD_E 12
#define PW0 2
#define PW1 1
#define T_W 70
#define P_D2 1
#define P_D4 1
#define XSTR 16
#define P_D3 20
#ifndef M_NS
#define M_NS 6
#endif
#define A_Q1 27
#define B_RAT 7
#define B_MAT 13
#define U_D 4
#define FSTR 16
#define B_J 4
#define RETIRE()                                                                                                                  \
  do {                                                                                                                            \
    if (hw == 3 && lane == 0) {                                                                                                   \
      __hip_atomic_store(reinterpret_cast<u64*>(xfl + b * XSTR), ~tag, __ATOMIC_RELAXED, __HIP_MEMORY_SCOPE_AGENT);               \
      if (b == 255) __hip_atomic_store(reinterpret_cast<u64*>(epo), tag, __ATOMIC_RELAXED, __HIP_MEMORY_SCOPE_AGENT);             \
    }                                                                                                                             \
  } while (0)
#define U_N1 3
#define OPT_RR 2
#define B_QAT 8
#define B_QD 4
#define TP_PS 5
#define TP_XC (4 * TP_PS)
#define TP_P0 2
#define TP_X0 24
#define TP_XL 58
#define W_UO 3
#define CPF_K 24
#define CPF_B 8
#define B_WN 4
#define B_WK 15
struct BSched {
  int cnt[16], rcnt, mcnt, qcnt, vcnt, ncnt;
  constexpr BSched() : cnt{}, rcnt(0), mcnt(0), qcnt(0), vcnt(0), ncnt(0) {
    int pos[16] = {}, n = 0, rpos = 0, mpos = 0, qpos = 0, npos = 0;
    for (int k = 0; k <= B_J - 2 + M_NS; ++k) pos[k] = -8 * (B_J - 2 + M_NS - k);
    for (int k = B_J + 1; k < 16; ++k) {
      cnt[k] = n - pos[k] > 63 ? 63 : n - pos[k];
      if (k - 2 + M_NS < 16) { n += 4; pos[k - 2 + M_NS] = n; }
      if (k == B_RAT + 2) rcnt = n - rpos;
      if (k == B_QAT + B_QD) qcnt = n - qpos;
      if (k == V_T) vcnt = n;
      if (k == N_K + N_D) ncnt = n - npos;
      if (k == N_K) npos = n;
      if (k == B_RAT) { n += 2; rpos = n; }
      if (k == B_QAT) { n += 3; qpos = n; }
      if (k == B_MAT) { n += 3; mpos = n; }
      if (k == B_WK) n += B_WN;
    }
    mcnt = n - mpos > 63 ? 63 : n - mpos;
  }
};
__global__ __launch_bounds__(512, 2) void k_main(const u16* __restrict__ x, char* ws, const u16* __restrict__ Y0, const u16* __restrict__ Y1,
                                              const u16* __restrict__ cvec, const u16* __restrict__ wmod, const u16* __restrict__ X0,
                                              const u16* __restrict__ X1, const u16* __restrict__ wr_, const u16* __restrict__ wg,
                                              const u16* __restrict__ wu, const u16* __restrict__ wd, u16* __restrict__ out) {
  constexpr int DX = 4, NS = M_NS, STG = NS * 16384, SPARE = STG + 32768;
  constexpr int LST = SPARE + 17664, SLT = LST + 256, XC = SLT + 512, VT = XC + 4096;
  __shared__ __attribute__((aligned(1024))) char shm[VT + 256];
  u16(*svb)[1024] = reinterpret_cast<u16(*)[1024]>(shm + SPARE);
  float* cwj = reinterpret_cast<float*>(shm + SPARE + 10240);
  unsigned* ssel = reinterpret_cast<unsigned*>(shm + SPARE + 10368);
  u16(*acts)[64] = reinterpret_cast<u16(*)[64]>(shm + SPARE + 15872);
  float* smod = reinterpret_cast<float*>(shm + SPARE + 15872);
  u16* ssa = reinterpret_cast<u16*>(smod);
  constexpr int TAB = SPARE + 10752, RKA = TAB + 1536, RKB = TAB + 3584;
  u16(*scw)[128] = reinterpret_cast<u16(*)[128]>(shm + TAB + 512);
  static_assert(42 % NS == 0, "slots 0 and 1 are free behind GEMM_B barrier 13");
  const int b = blockIdx.x, xcd = b & 7, j8 = b >> 3;
  const int cc = xcd >> 2, nt = (xcd & 3) * 4 + (j8 & 3), mt = j8 >> 2, et = mt;
  const int tid = threadIdx.x, lane = tid & 63;
  const int wid = __builtin_amdgcn_readfirstlane(tid >> 6);
  const unsigned lds0 = (unsigned)(size_t)(__attribute__((address_space(3))) char*)shm;
  const char* cpf;
  {
    unsigned long long pc;
    unsigned len;
    asm volatile("s_getpc_b64 %0\n.Lcpf_here:\n\ts_mov_b32 %1, .Lcpf_end-.Lcpf_here-4\n\t.subsection 1\n.Lcpf_end:\n\t.subsection 0" : "=s"(pc), "=s"(len));
    cpf = reinterpret_cast<const char*>(pc) + min((unsigned)((lane * 32 + j8) * 128), len);
  }
  const unsigned pfl = lds0 + SPARE + 17408;
  if (wid == 4 && lane < 12) PFL(cpf);
  u32x4 xv[2];
  {
    const unsigned xo = (unsigned)((b * 8 + wid) * D + lane * 8) * 2;
    asm volatile("global_load_dwordx4 %0, %2, %3\n\tglobal_load_dwordx4 %1, %2, %3 offset:1024" : "=&v"(xv[0]), "=&v"(xv[1]) : "v"(xo), "s"(x) : "memory");
  }
  asm volatile("" ::"s"(ws), "s"(Y0), "s"(Y1), "s"(cvec), "s"(wmod), "s"(wr_));
  u16* xn = reinterpret_cast<u16*>(ws + WS_H);
  u16* U = reinterpret_cast<u16*>(ws + WS_T);
  unsigned* part = reinterpret_cast<unsigned*>(ws + WS_PART);
  const int g = (b & 7) * 28 + (b >> 3);
  unsigned* ufl = reinterpret_cast<unsigned*>(ws + WS_UFL) + (cc * 16 + nt) * 32 * FSTR;
  unsigned* xfl = reinterpret_cast<unsigned*>(ws + WS_XFL);
  unsigned* epo = reinterpret_cast<unsigned*>(ws + WS_EPO);
#define SKEWPT()
  const int g4 = lane >> 4;
  u64 tag0, tag;
#define TAGW(n) asm volatile("s_waitcnt vmcnt(%2)\n\tv_lshl_add_u64 %0, %1, 0, 1" : "=v"(tag) : "v"(tag0), "n"(n) : "memory")
  u32x4 wv[8], cq;
  const char* pfy = reinterpret_cast<const char*>(cc ? Y1 : Y0) + (size_t)((xcd & 3) * 512 + j8 * 16 + (lane & 15)) * 4096 + g4 * 128;
#define PFG(g) (pfy + ((((g) + xcd) & 7) << 9))
  const int u = b * 2 + (wid & 1), cg0 = u & 31, ks0 = u >> 5;
  unsigned ta = lds0 + STG + ((lane >> 2) & 3) * 2368 + (wid * 128 + (lane >> 5) * 32 + (lane & 3) * 8 + (g4 & 1) * 4) * 2;
  unsigned oo = (unsigned)((wid * 4 + (lane >> 5)) * 131072 + (b >> 2) * 2048 + (g4 & 1) * 1024 + ((b & 3) * 16 + (lane & 15)) * 16), oo2 = oo + 262144;
  asm volatile("" : "+v"(ta), "+v"(oo), "+v"(oo2));
  unsigned wa = lds0 + STG + wid * 2368 + lane * 16;
  asm volatile("" : "+v"(wa));
  const unsigned wo = (unsigned)((ks0 * 64 + (lane >> 3) * 8) * 6144 + 2048 + (cg0 >> 4) * 1024 + cg0 * 64 + (lane & 7) * 8) * 2;
  if (wid == 0) TSG(part, 0);
  BT(50);
  if (wid >= 3) {
    asm volatile("global_load_dwordx2 %0, %1, off sc1" : "=v"(tag0) : "v"(epo) : "memory");
    if (wid >= 6) {
      LDG(cq, (ks0 * 64 + g4 * 16 + (lane & 1) * 8) * 2, cvec);
      if (W_L == 2) __builtin_amdgcn_s_sleep(W_D);
      if (W_L != 1) {
#pragma unroll
        for (int i = 0; i < 8; ++i) LDM(wv[i], wo + i * 12288, wmod);
      }
      const unsigned av = u < 80 ? 0u : ~0u;
      asm volatile("global_store_dwordx4 %0, %1, %2 sc1\n\ts_nop 1" ::"v"(O_KEY + u * 1024 + lane * 16), "v"(u32x4{av, av, av, av}), "s"(ws) : "memory");
      WAIT_V(W_L == 1 ? 3 : 11);
    } else if (wid == 4 || (W_L == 1 && wid == 5)) {
      __builtin_amdgcn_s_sleep(C_DP);
      PFL(PFG(wid & 1));
      WAIT_V(2);
    } else {
      WAIT_V(1);
    }
  } else {
    WAIT_V(0);
  }
  {
    asm volatile("" : "+v"(xv[0]), "+v"(xv[1])::"memory");
    if (wid == 1) TSG(part, 2);
    BT(51);
    const float s0 = dsum(xv[0][3], dsum(xv[0][2], dsum(xv[0][1], dsum(xv[0][0], 0.f))));
    const float s1 = dsum(xv[1][3], dsum(xv[1][2], dsum(xv[1][1], dsum(xv[1][0], 0.f))));
    const float mf = wave_sum_s(s0 + s1) * (1.0f / D), m = rbf(mf);
    const f32x4 d0 = lo4(xv[0]) - m, d1 = hi4(xv[0]) - m, d2 = lo4(xv[1]) - m, d3 = hi4(xv[1]) - m;
    const f32x4 q = (d0 * d0 + d1 * d1) + (d2 * d2 + d3 * d3);
    const float vf = wave_sum_s((q[0] + q[1]) + (q[2] + q[3])) * (1.0f / D) - (mf - m) * (mf - m);
    const float rden = __builtin_amdgcn_rcpf(rbf(__builtin_amdgcn_sqrtf(rbf(rbf(vf) + 1e-5f))));
    asm volatile("ds_write_b128 %0, %1" ::"v"(wa), "v"(pk8(d0 * rden, d1 * rden)) : "memory");
    asm volatile("ds_write_b128 %0, %1 offset:1024" ::"v"(wa), "v"(pk8(d2 * rden, d3 * rden)) : "memory");
    if (wid == 1) TSG(part, 12);
    BT(52);
    LBAR();
    if (wid == 1) TSG(part, 10);
    BT(53);
    u32x2 t[4];
    asm volatile(
        "ds_read_b64_tr_b16 %0, %4\n\tds_read_b64_tr_b16 %1, %4 offset:%5\n\t"
        "ds_read_b64_tr_b16 %2, %4 offset:128\n\tds_read_b64_tr_b16 %3, %4 offset:%6\n\ts_waitcnt lgkmcnt(0)"
        : "=&v"(t[0]), "=&v"(t[1]), "=&v"(t[2]), "=&v"(t[3])
        : "v"(ta), "n"(4 * 2368), "n"(4 * 2368 + 128)
        : "memory");
    asm volatile("global_store_dwordx4 %0, %2, %4 sc1\n\tglobal_store_dwordx4 %1, %3, %4 sc1\n\ts_nop 1" ::"v"(oo), "v"(oo2),
                 "v"(u32x4{t[0][0], t[0][1], t[1][0], t[1][1]}), "v"(u32x4{t[2][0], t[2][1], t[3][0], t[3][1]}), "s"(xn)
                 : "memory");
    if (wid == 2) TSG(part, 14);
    BT(54);
  }
  u32x2 vd;
#define VDLD(sl) POLL2(max(vd[0], vd[1]) == SENT, sl, PCAP, asm volatile("global_load_dwordx2 %0, %1, %2 sc1" VW0 : "=&v"(vd) : "v"(O_VD + (b & 31) * 128), "s"(ws) : "memory"))
  if (wid < 4) {
    const unsigned ya0 = lds0 + (lane & 15) * 128 + (g4 ^ (lane & 7)) * 16;
    bf16x8 X[DX][2][2], Y[8];
    f32x4 acc[2][8];
    {
      constexpr int MODE = 0;
      const unsigned xo0 = lane * 16 + 4096, xo1 = xo0 + 8192;
      const char* xb = reinterpret_cast<const char*>(xn) + (size_t)(mt * 4 + wid) * 131072;
#define XB(i) (xb + (((xcd + (i)) & 7) << 14))
#pragma unroll
      for (int i = 0; i < 2; ++i)
#pragma unroll
        for (int j = 0; j < 8; ++j) acc[i][j] = f32x4{0.f, 0.f, 0.f, 0.f};
      asm volatile("" : W8(acc[0]), W8(acc[1]));
      __builtin_amdgcn_s_setprio(1);
      WAIT_V(0);
      if (wid == 0) TSG(part, 1);
      const char* xpb = XB(0);
      BAR(55);
      if (wid == 3) {
        TAGW(0);
        const unsigned* fp = xfl + ((xcd * 32 + lane) & 255) * XSTR;
        if (P_D3) __builtin_amdgcn_s_sleep(P_D3);
#pragma unroll 1
        for (int it = 0; it < PCAP; ++it) { PDBG(it);
          u64 f;
          if (it < 2) BT(58 + 2 * it);
          asm volatile("global_load_dwordx2 %0, %1, off sc1\n\ts_waitcnt vmcnt(0)" : "=&v"(f) : "v"(fp) : "memory");
          if (it < 2) BT(59 + 2 * it);
          if (__all(f == tag)) break;
          __builtin_amdgcn_s_sleep(P_D4);
        }
      }
      BAR(0);
      if (wid == 1) TSG(part, 3);
#pragma unroll
      for (int h = 0; h < 7; ++h) {
        XLD(X[h >> 1][h & 1][0], xpb, h, 0);
        XLD(X[h >> 1][h & 1][1], xpb, h, 1);
      }
#pragma unroll
      for (int j = 0; j < 7; ++j) DSR(Y[j], ya0, j * 2048);
      unsigned so = 0, a0 = ya0;
      f32x4 c0, c1;
#pragma unroll 1
      for (int it = 0; it < 7; ++it) {
        const char* xpa = xpb;
        xpb = XB(it + 1);
#pragma unroll
        for (int s = 0; s < 4; ++s) {
          const unsigned a1 = (ya0 ^ 64) + so;
          so = so == (NS - 1) * 16384 ? 0 : so + 16384;
          WAIT_V(12);
          HALF(X[s][0], a1, true, X[(s + 3) & 3][1], (s == 0 ? xpa : xpb), (2 * s + 7) & 7, true, a0);
          a0 = ya0 + so;
          BAR(1 + it * 4 + s);
          WAIT_V(12);
          HALF(X[s][1], a0, true, X[s][0], xpb, 2 * s, true, a1);
        }
      }
#pragma unroll
      for (int s = 0; s < 4; ++s) {
        const unsigned a1 = (ya0 ^ 64) + so;
        so = so == (NS - 1) * 16384 ? 0 : so + 16384;
        WAIT_V(s == 0 ? 12 : 14 - 4 * s);
        HALF(X[s][0], a1, true, X[3][1], xpb, 7, s == 0, a0);
        a0 = ya0 + so;
        if (s < 3) BAR(29 + s);
        WAIT_V(12 - 4 * s);
        if (s == 2) {
          DSR(c0, lds0 + SPARE + 15872 + 512 + wid * 128 + g4 * 32, 0);
          DSR(c1, lds0 + SPARE + 15872 + 512 + wid * 128 + g4 * 32, 16);
        }
        if (s < 3) HALF(X[s][1], a0, true, X[s][0], xpb, 0, false, a1);
      }
      const char* up = reinterpret_cast<const char*>(U) + (size_t)cc * 4194304 + (size_t)(nt * 4) * 65536 + (mt * 4 + wid) * 2048;
      const unsigned ul = lds0 + STG + wid * 1024 + lane * 16;
#define UEP(j)                                                                                                                             \
  do {                                                                                                                                     \
    u32x4 v_;                                                                                                                              \
    asm volatile("v_pk_mul_f32 %0, %0, %4\n\tv_pk_mul_f32 %1, %1, %5\n\tv_pk_mul_f32 %2, %2, %6\n\tv_pk_mul_f32 %3, %3, %7"            \
                 : "+v"(acc[0][j].lo), "+v"(acc[0][j].hi), "+v"(acc[1][j].lo), "+v"(acc[1][j].hi)                                          \
                 : "v"(c0.lo), "v"(c0.hi), "v"(c1.lo), "v"(c1.hi));                                                                        \
    asm volatile("v_cvt_pk_bf16_f32 %0, %4, %5\n\tv_cvt_pk_bf16_f32 %1, %6, %7\n\tv_cvt_pk_bf16_f32 %2, %8, %9\n\tv_cvt_pk_bf16_f32 %3, %10, %11" \
                 : "=&v"(v_[0]), "=&v"(v_[1]), "=&v"(v_[2]), "=&v"(v_[3])                                                                  \
                 : "v"(acc[0][j][0]), "v"(acc[0][j][1]), "v"(acc[0][j][2]), "v"(acc[0][j][3]), "v"(acc[1][j][0]), "v"(acc[1][j][1]),       \
                   "v"(acc[1][j][2]), "v"(acc[1][j][3]));                                                                                  \
    asm volatile("global_store_dwordx4 %0, %2, %3 offset:%4 sc1\n\tds_write_b128 %1, %2 offset:%5\n\ts_nop 0"                          \
                 ::"v"(lane * 16), "v"(ul), "v"(v_), "s"(up + ((j) >> 1) * 65536), "n"(((j)&1) * 1024), "n"((j)*4096) : "memory");         \
  } while (0)
      {
        const unsigned a1 = (ya0 ^ 64) + (31 % NS) * 16384;
        DSR(Y[7], a1, 7 * 2048);
        WAIT_L(6);
#pragma unroll
        for (int j = 0; j < 8; ++j) {
          if (j == 2) WAIT_L(0);
          MFMA(acc[0][j], X[3][1][0], Y[j]);
          MFMA(acc[1][j], X[3][1][1], Y[j]);
          if (j >= 2) UEP(j - 2);
        }
      }
      if (wid == 0) TSG(part, 4);
      TS(0);
      SKEWPT();
      asm volatile("s_nop 3");
#pragma unroll
      for (int j = 6; j < 8; ++j) UEP(j);
    }
    const int rg = nt * 8 + wid * 2 + cc, row0 = rg * 16 + (lane & 15), col0 = et * 128 + g4 * 8;
    u32x4 xq[4], pv[4], sa4[4];
    f32x4 ga[8];
    f32x2 cj;
    float sw, ce;
    u32x4 dk;
    unsigned spo, soo, th, slt, slf, sla, tha;
    u32x2 se;
    const char* xr = reinterpret_cast<const char*>(xn) + mt * 524288 + (cc * 16 + nt) * 16384 + wid * 4096;
#define XCHLD()                                                                                                     \
  asm volatile(                                                                                                     \
      "global_load_dwordx4 %0, %4, %5 sc1\n\tglobal_load_dwordx4 %1, %4, %5 offset:1024 sc1\n\t"                   \
      "global_load_dwordx4 %2, %4, %5 offset:2048 sc1\n\tglobal_load_dwordx4 %3, %4, %5 offset:3072 sc1"            \
      : "=&v"(pv[0]), "=&v"(pv[1]), "=&v"(pv[2]), "=&v"(pv[3])                                                      \
      : "v"(lane * 16), "s"(xr)                                                                                     \
      : "memory")
    {
      constexpr int MODE = 1;
      const char* ub = reinterpret_cast<const char*>(U) + (size_t)cc * 4194304 + (size_t)(nt * 4 + wid) * 65536;
      const unsigned xo0 = lane * 16 + cc * 1024 + 4096, xo1 = xo0 ^ 1024;
      BT(32);
      LBAR();
      if (wid == 1) TSG(part, 6);
      {
        const unsigned sa = lds0 + STG + wid * 8192 + lane * 16 + cc * 4096;
#pragma unroll
        for (int q = 0; q < 8; ++q) DSR(X[(q >> 1) & 1][q & 1][q >> 2], (q >> 2) ? sa ^ 4096 : sa, (q & 3) * 1024);
        const unsigned a0 = ya0 + (32 % NS) * 16384;
#pragma unroll
        for (int j = 0; j < 7; ++j) DSR(Y[j], a0, j * 2048);
      }
#define UFLAG()                                                                                                         \
  do {                                                                                                                  \
    WAIT_V(0);                                                                                                          \
    if (lane == 0) __hip_atomic_store(ufl + (mt * 4 + wid) * FSTR, 1u, __ATOMIC_RELAXED, __HIP_MEMORY_SCOPE_AGENT);     \
    if (wid == 0) TSG(part, 5);                                                                                         \
  } while (0)
#define XP(h) (ub + (((mt + ((h) >> 2)) & 7) << 13))
#define XS(h) X[((h) >> 1) & 3][(h)&1]
#define XW(h) do { if ((h) >= 4) WAIT_V(2 * (((h) + 6 > 31 ? 31 : (h) + 6) - (h)) + ((h) >= 25 + XQ_N / 8 ? 4 : 0)); } while (0)
      constexpr int H0 = 24, NP = 64, NE = 2 * NP + 8;
#pragma unroll
      for (int s = 0; s < H0 / 2; ++s) {
        const unsigned a0 = ya0 + ((32 + s) % NS) * 16384, a1 = a0 ^ 64, an = ya0 + ((33 + s) % NS) * 16384;
        XW(2 * s);
        if (s == 0) {
          constexpr int MODE = 2;
          HALF(XS(0), a1, true, XS(7), XP(7), 7, false, a0);
          UFLAG();
        } else {
          HALF(XS(2 * s), a1, true, XS(2 * s + 7), XP(2 * s + 7), 2 * s + 7, 2 * s >= 4, a0);
        }
        if (s == 1 && wid == 2) TSD(12);
        BAR(33 + s);
        if (s == 1) {
          if (wid == 1) TSG(part, 7);
          TS(4);
#pragma unroll
          for (int h = 4; h < U_X0; ++h) {
            XLD(XS(h)[0], XP(h), h, 0);
            XLD(XS(h)[1], XP(h), h, 1);
          }
        }
        XW(2 * s + 1);
#undef HHOOK
#define HHOOK(j) if (s == 1 && j + 3 >= U_X0) { XLD(XS(j + 3)[0], XP(j + 3), j + 3, 0); XLD(XS(j + 3)[1], XP(j + 3), j + 3, 1); }
        HALF(XS(2 * s + 1), an, true, XS(2 * s + 8), XP(2 * s + 8), 2 * s + 8, 2 * s + 1 >= 5, a1);
#undef HHOOK
#define HHOOK(j)
      }
      const char* xw = reinterpret_cast<const char*>(xn) + mt * 524288 + ((cc ^ 1) * 16 + nt) * 16384 + wid * 4096;
      unsigned c128 = 0x43000000u;
      asm volatile("" : "+v"(c128));
      const unsigned xoff = (unsigned)(row0 * D + col0) * 2;
      const unsigned rka = lds0 + RKA + (lane & 15) * 16, rkb = lds0 + RKB + (g4 ? 1024 : wid * 256 + lane * 16);
      const unsigned ea = lds0 + TAB + g4 * 32, eb = lds0 + TAB + 512 + (lane & 3) * 256 + g4 * 16;
      u32x4 ov;
      bf16x8 xk;
#pragma unroll
      for (int q = 0; q < 4; ++q) dk[q] = (lane & 3) == q ? ~0u : 0u;
      spo = O_STAT + rg * 2048 + g4 * 512 + (lane & 15) * 16, soo = O_STAT + rg * 2048 + et * 256 + (lane & 15) * 16;
      unsigned cwa = lds0 + SPARE + 10240 + g4 * 8, cwu = lds0 + SPARE + 10240 + et * 4;
      {
        const unsigned one = g4 == ((lane >> 2) & 3) ? ((lane & 1) ? 0x1f800000u : 0x3f80u) : 0u;
        se = u32x2{(lane & 2) ? 0u : one, (lane & 2) ? one : 0u};
        asm volatile("" : "+v"(se));
      }
      tha = lds0 + SPARE + 10368 + 24;
      sla = lds0 + SLT + (row0 & 127) * 4;
      asm volatile("" : "+v"(dk), "+v"(spo), "+v"(soo), "+v"(cwa), "+v"(cwu), "+v"(tha), "+v"(sla));
#define EH(e) (H0 + (((e) < NP ? (e) : (e) - NP - 8) >> 3))
#define EA(e) ((ya0 ^ ((EH(e) & 1) * 64)) + ((32 + (EH(e) >> 1)) % NS) * 16384)
      sfor<NE>([&](auto nn) __attribute__((always_inline)) {
        constexpr int n = decltype(nn)::value, h = EH(n), j = n & 7, m = n - NP - TP_P0, e = n + 7, z = n - (NE - 8);
        (void)sa4, (void)eb, (void)ga, (void)ea, (void)xk, (void)rka, (void)rkb, (void)xq, (void)xoff, (void)ov, (void)xw, (void)x;
        (void)cj, (void)sw, (void)ce, (void)cwa, (void)cwu, (void)th, (void)tha, (void)slt, (void)sla, (void)slf, (void)c128;
        if (n == 0) TS(2);
        if (n == NP) { TS(3); SKEWPT(); }
        if (n < NP && j == 0) {
          if ((h & 1) && h < 31) BAR(33 + (h >> 1));
          XW(h);
        }
        if (n == NP + TP_X0) { TS(14); BAR(48); TS(11); }
        if (n == NP + TP_XL) XCHLD();
        if constexpr ((j & 1) == 0) WAIT_L(z <= 0 ? 5 : 6 - z + 2 * z);
        if constexpr (n < NP) MFMA(acc[1][j], Y[j], XS(h)[1]);
        else if constexpr (n < NP + 8) MFMA(acc[0][j], Y[j], xk);
        else MFMA(acc[0][j], Y[j], XS(h)[0]);
        if constexpr (n == NP - 16) DSR(xk, rkb, 0);
        if constexpr (e >= NP && e < NP + 8) DSR(Y[e & 7], rka, (e & 7) * 256);
        else if constexpr (e < NE) DSR(Y[e & 7], EA(e), (e & 7) * 2048);
        if constexpr (n == 2 || n == 4) XLD(XS(31)[(j >> 1) - 1], XP(31), 31, (j >> 1) - 1);
        if constexpr (n == XQ_N) {
#pragma unroll
          for (int q = 0; q < 4; ++q) LDX(xq[q], xoff, x, q * 64);
        }
        if constexpr (m >= 0 && m < 4 * TP_PS) {
          constexpr int q = m / TP_PS, r = m % TP_PS;
#pragma unroll
          for (int u = (r * 13 + TP_PS - 1) / TP_PS; u < ((r + 1) * 13 + TP_PS - 1) / TP_PS; ++u) {
#define AO(i) acc[1][2 * q + ((u >> 1) & 1)][(u & 1) * 2 + (i)]
            if (u < 4) asm volatile("v_and_or_b32 %0, %1, %2, %3" : "=v"(ov[u & 3]) : "v"(AO(1)), "s"(0x80000000u), "v"(c128));
            else if (u < 8) asm volatile("v_fmamk_f32 %0, %1, 0x5f800000, %0" : "+v"(ov[u & 3]) : "v"(AO(1)));
            else if (u < 12) asm volatile("v_cvt_pk_bf16_f32 %0, %1, %0" : "+v"(ov[u & 3]) : "v"(AO(0)));
            else asm volatile("global_store_dwordx4 %0, %1, %2 offset:%3 sc1\n\ts_nop 1" ::"v"(lane * 16), "v"(ov), "s"(xw), "n"(q * 1024) : "memory");
          }
          if (m == 4 * TP_PS - 1) TS(10);
        }
        if constexpr (m == TP_XC) asm volatile("s_waitcnt vmcnt(%4)" : "+v"(xq[0]), "+v"(xq[1]), "+v"(xq[2]), "+v"(xq[3]) : "n"(n >= NP + TP_XL ? 8 : 4) : "memory");
        if constexpr (m >= TP_XC && m < TP_XC + 32) {
          constexpr int c = m - TP_XC;
          if constexpr (c & 1) asm volatile("v_and_b32 %0, 0xffff0000, %1" : "=v"(acc[1][c >> 2][c & 3]) : "v"(xq[c >> 3][(c >> 1) & 3]));
          else asm volatile("v_lshlrev_b32 %0, 16, %1" : "=v"(acc[1][c >> 2][c & 3]) : "v"(xq[c >> 3][(c >> 1) & 3]));
        }
        if constexpr (z >= 0 && z < 4) {
          DSR(ga[2 * z], ea, z * 128);
          DSR(ga[2 * z + 1], ea, z * 128 + 16);
        } else if constexpr (z >= 4 && z < 6) {
          DSR(sa4[2 * z - 8], eb, (2 * z - 8) * 64);
          DSR(sa4[2 * z - 7], eb, (2 * z - 7) * 64);
        } else if constexpr (z == 6) {
          asm volatile("ds_read_b64 %0, %2\n\tds_read_b32 %1, %3" : "=&v"(cj), "=&v"(ce) : "v"(cwa), "v"(cwu) : "memory");
        } else if constexpr (z == 7) {
          asm volatile("ds_read_b32 %0, %4 offset:32\n\tds_read_b32 %1, %5 offset:4\n\tds_read_b32 %3, %5 offset:56\n\tds_read_b32 %2, %6"
                       : "=&v"(sw), "=&v"(th), "=&v"(slt), "=&v"(slf) : "v"(cwa), "v"(tha), "v"(sla) : "memory");
        }
      });
    }
    if (wid == 0) TSG(part, 8);
    TS(13);
    BT(49);
    BTE();
    SKEWPT();
    u16* op = out + (size_t)row0 * D + col0;
    {
      u32x4 e0 = {se[0], se[1], 0u, 0u}, e1 = {0u, 0u, se[0], se[1]};
      asm volatile("" : "+v"(e0), "+v"(e1));
      TS(26);
#define XCHW() asm volatile("s_waitcnt vmcnt(0)" : "+v"(pv[0]), "+v"(pv[1]), "+v"(pv[2]), "+v"(pv[3])::"memory")
      XCHW();
#define XBAD (amin4(pv[3], amin4(pv[2], 128.0f)) < 128.0f || amin4(pv[1], amin4(pv[0], 128.0f)) < 128.0f)
      if (__builtin_expect(__any(XBAD), 1)) POLL2(XBAD, 1, PCAP, XCHLD(); XCHW());
      TS(27);
      asm volatile("s_waitcnt lgkmcnt(0)" : W8(acc[0]), W8(ga), "+v"(sa4[0]), "+v"(sa4[1]), "+v"(sa4[2]), "+v"(sa4[3]), "+v"(cj), "+v"(sw), "+v"(ce), "+v"(th), "+v"(slt), "+v"(slf)::"memory");
#pragma unroll
      for (int q = 0; q < 8; ++q) acc[0][q] = __builtin_amdgcn_mfma_f32_16x16x32_bf16(BF8((q & 1) ? e1 : e0), BF8(pv[q >> 1]), acc[0][q], 0, 0, 0);
      f32x4 s4 = {0.f, 0.f, 0.f, 0.f}, gr = {0.f, 0.f, 0.f, 0.f};
#pragma unroll
      for (int q = 0; q < 4; ++q) {
        xq[q] = pk8(acc[1][2 * q] + ga[2 * q] * acc[0][2 * q], acc[1][2 * q + 1] + ga[2 * q + 1] * acc[0][2 * q + 1]);
        gr = __builtin_amdgcn_mfma_f32_16x16x32_bf16(BF8(xq[q]), BF8(xq[q]), gr, 0, 0, 0);
        s4 = __builtin_amdgcn_mfma_f32_16x16x32_bf16(BF8(sa4[q]), BF8(xq[q]), s4, 0, 0, 0);
      }
#define XCST()                                                                                                                         \
  do {                                                                                                                                 \
    const u32x4 n0 = nsent4(xq[0]), n1 = nsent4(xq[1]), n2 = nsent4(xq[2]), n3 = nsent4(xq[3]);                                        \
    _Pragma("unroll") for (int r = D_R - 1; r >= 0; --r) asm volatile(                                                                      \
        "global_store_dwordx4 %0, %1, %5 sc1\n\tglobal_store_dwordx4 %0, %2, %5 offset:64 sc1\n\t"                                    \
        "global_store_dwordx4 %0, %3, %5 offset:128 sc1\n\tglobal_store_dwordx4 %0, %4, %5 offset:192 sc1\n\ts_nop 1" ::"v"(O_X1C - 2048 + r * X_CS + slt * 2048 + col0 * 2), \
        "v"(n0), "v"(n1), "v"(n2), "v"(n3), "s"(ws) : "memory");                                                                       \
  } while (0)
#define SCST()                                                                                                                         \
  do {                                                                                                                                 \
    _Pragma("unroll") for (int r = D_R - 1; r >= 0; --r)                                                                                    \
        asm volatile("global_store_dwordx4 %0, %1, %2 sc1\n\ts_nop 1" ::"v"(O_ST2 - 128 + r * X_CS + slt * 128 + et * 16), "v"(sg), "s"(ws) : "memory"); \
  } while (0)
      if (slt) XCST();
      const bool dl = ((lane >> 2) & 3) == g4;
      const u32x4 gd = __builtin_bit_cast(u32x4, gr) & dk;
      const float m = s4[2] * (1.0f / 128);
      float q2 = __uint_as_float((gd[0] | gd[1]) | (gd[2] | gd[3])) - s4[2] * m, lg = (s4[0] + s4[1]) - m * ce;
      if (__builtin_expect(__any(dl && !(s4[2] * m <= 256.0f * q2)), 0)) {
        asm volatile("" : "+v"(xq[0]), "+v"(xq[1]), "+v"(xq[2]), "+v"(xq[3]));
        f32x4 a = {0.f, 0.f, 0.f, 0.f}, l = {0.f, 0.f, 0.f, 0.f};
#pragma unroll
        for (int q = 0; q < 4; ++q) {
          const u32x4 ch = *reinterpret_cast<const u32x4*>(&scw[0][g4 * 8 + q * 32]), cl = *reinterpret_cast<const u32x4*>(&scw[1][g4 * 8 + q * 32]);
          const f32x4 d0 = lo4(xq[q]) - m, d1 = hi4(xq[q]) - m;
          a += d0 * d0 + d1 * d1;
          l += d0 * (lo4(ch) + lo4(cl)) + d1 * (hi4(ch) + hi4(cl));
        }
        q2 = ar4((a[0] + a[1]) + (a[2] + a[3]));
        lg = ar4((l[0] + l[1]) + (l[2] + l[3]));
      }
      const u32x4 sg = {nsent(m), nsent(q2), nsent(lg), 0u};
      if (dl) {
        if (slt) SCST();
        asm volatile("global_store_dwordx4 %0, %1, %2 sc1\n\ts_nop 1" ::"v"(soo), "v"(sg), "s"(ws) : "memory");
      }
      if (__any(slf == 0u)) {
        POLL2(slf == 0u, 1, 1 << 26, asm volatile("ds_read_b32 %1, %2 offset:56\n\tds_read_b32 %0, %3\n\ts_waitcnt lgkmcnt(0)" : "=&v"(slt), "=&v"(slf) : "v"(tha), "v"(sla) : "memory"));
        if (slt) {
          XCST();
          if (dl) SCST();
        }
      }
      if (D_G) asm volatile("ds_write_b32 %0, %1 offset:72" ::"v"(tha + wid * 4), "v"(1u) : "memory");
      TS(7);
      SKEWPT();
      const unsigned tp = tick() + T_D1, tq = (unsigned)__builtin_amdgcn_readfirstlane((int)th) + T_C1;
      u32x4 sr[2];
#define STLD(W) asm volatile("global_load_dwordx4 %0, %2, %3 sc1\n\tglobal_load_dwordx4 %1, %2, %3 offset:256 sc1" W : "=&v"(sr[0]), "=&v"(sr[1]) : "v"(spo), "s"(ws) : "memory")
#define SBAD (max(max(max(sr[0][0], sr[0][1]), sr[0][2]), max(max(sr[1][0], sr[1][1]), sr[1][2])) == SENT)
      asm volatile("" : "+v"(xq[0]), "+v"(xq[1]), "+v"(xq[2]), "+v"(xq[3])::"memory");
      f32x4 xf[8], xs[8], gc[8];
      auto PREW = [&](int q0, int q1) __attribute__((always_inline)) {
#pragma unroll
        for (int q = q0; q < q1; ++q) {
          const u32x4 sbq = *reinterpret_cast<const u32x4*>(&svb[1][col0 + q * 32]), gbq = *reinterpret_cast<const u32x4*>(&svb[3][col0 + q * 32]);
          const u32x4 cq = *reinterpret_cast<const u32x4*>(&svb[2][col0 + q * 32]);
          xf[2 * q] = lo4(xq[q]), xf[2 * q + 1] = hi4(xq[q]);
          xs[2 * q] = xf[2 * q] + lo4(gbq) * lo4(sbq), xs[2 * q + 1] = xf[2 * q + 1] + hi4(gbq) * hi4(sbq);
          gc[2 * q] = lo4(gbq) * lo4(cq), gc[2 * q + 1] = hi4(gbq) * hi4(cq);
          asm volatile("" : "+v"(xf[2 * q]), "+v"(xf[2 * q + 1]), "+v"(xs[2 * q]), "+v"(xs[2 * q + 1]), "+v"(gc[2 * q]), "+v"(gc[2 * q + 1]));
        }
      };
      PREW(0, PW0);
      wait_tick((int)(tp - tq) < 0 ? tp : tq);
      STLD("");
      unsigned rk = 0x8ffeu - (unsigned)row0 * 2;
      unsigned twa = lds0 + STG + wid * 8192 + (lane & 15) * 256 + ((g4 ^ lane) & 15) * 16, tra = lds0 + STG + wid * 8192 + g4 * 256 + ((g4 ^ lane) & 15) * 16;
      unsigned oo_ = (unsigned)((rg * 16 + g4) * D + et * 128 + (lane & 15) * 8) * 2;
      asm volatile("" : "+v"(rk), "+v"(op), "+v"(twa), "+v"(tra), "+v"(oo_));
      PREW(PW0, 4);
      asm volatile("s_waitcnt vmcnt(0)" : "+v"(sr[0]), "+v"(sr[1])::"memory");
      if (__builtin_expect(__any(SBAD), 1)) POLL2(SBAD, 1, PCAP, STLD(VW0));
      TS(9);
      const f32x2 mk = {__uint_as_float(sr[0][0]), __uint_as_float(sr[1][0])};
      const f32x2 qk = {__uint_as_float(sr[0][1]), __uint_as_float(sr[1][1])};
      const f32x2 lk = {__uint_as_float(sr[0][2]), __uint_as_float(sr[1][2])};
      const float mean = ar4(mk[0] + mk[1]) * (1.0f / 8);
      const f32x2 dm = mk - mean;
      const f32x2 a = qk + 128.0f * (dm * dm), l = lk + dm * cj;
      const float al = swap32_add(a[0] + a[1], l[0] + l[1]), am = swap16_add(al, al);
      auto r = __builtin_amdgcn_permlane32_swap(__float_as_uint(am), __float_as_uint(am), false, false);
      const float rstd = __builtin_amdgcn_rsqf(__uint_as_float(r[0]) * (1.0f / D) + 1e-5f);
      const unsigned key = mkkey(rstd * __uint_as_float(r[1]) + sw) | rk;
      unsigned t1 = slt ? 0u : key, t2 = 0u;
      TOP2(0xB1); TOP2(0x4E); TOP2(0x141); TOP2(0x140);
      if (lane == 0 && et == (rg & 7)) asm volatile("global_store_dwordx2 %0, %1, %2 sc1" ::"v"(O_KEY + rg * KSTR * 8), "v"(u32x2{t1, t2}), "s"(ws) : "memory");
      u32x4 o[4];
#pragma unroll
      for (int q = 0; q < 4; ++q) o[q] = pk8(xs[2 * q] + (gc[2 * q] * (xf[2 * q] - mean)) * rstd, xs[2 * q + 1] + (gc[2 * q + 1] * (xf[2 * q + 1] - mean)) * rstd);
      {
        asm volatile("ds_write_b128 %0, %4\n\tds_write_b128 %1, %5\n\tds_write_b128 %2, %6\n\tds_write_b128 %3, %7" ::"v"(twa), "v"(twa ^ 64), "v"(twa ^ 128), "v"(twa ^ 192),
                     "v"(o[0]), "v"(o[1]), "v"(o[2]), "v"(o[3]) : "memory");
        asm volatile("ds_read_b128 %0, %4\n\tds_read_b128 %1, %5 offset:1024\n\tds_read_b128 %2, %6 offset:2048\n\tds_read_b128 %3, %7 offset:3072\n\ts_waitcnt lgkmcnt(0)"
                     : "=&v"(o[0]), "=&v"(o[1]), "=&v"(o[2]), "=&v"(o[3]) : "v"(tra), "v"(tra ^ 64), "v"(tra ^ 128), "v"(tra ^ 192) : "memory");
      }
      auto OST = [&](bool on) __attribute__((always_inline)) {
        const unsigned mk_ = (unsigned)__builtin_amdgcn_ballot_w64(on) >> g4;
#pragma unroll
        for (int q = 0; q < 4; ++q)
          if (mk_ & (1u << (4 * q))) asm volatile("global_store_dwordx4 %0, %1, %2" OPOL "\n\ts_nop 1" ::"v"(oo_), "v"(o[q]), "s"(out + q * 4096) : "memory");
      };
      bool df = slt;
      {
        const int tn = (int)(tick() - (tq + (T_C3 - T_C1)));
        if (tn > T_X) df = true;
        else if (tn > -T_W) { unsigned sv; LSPIN(sv, ssel + 16 + wid, sv != 0u); }
      }
      OST(!df);
      TS(1);
      u32x2 ks;
      POLL2(ks[1] == 0u, 1, 1 << 26, asm volatile("ds_read_b64 %0, %1\n\ts_waitcnt lgkmcnt(0)" : "=v"(ks) : "v"(lds0 + SPARE + 10368) : "memory"));
      df = df && (ks[0] & 0xffffu) != rk && (ks[1] & 0xffffu) != rk;
      SKEWPT();
      if (__any(df)) {
        if (b < NPART) {
          unsigned sv;
          LSPIN(sv, ssel + O_DW, sv != 0u);
          if (O_DS) __builtin_amdgcn_s_sleep(O_DS);
        }
        OST(df);
      }
      if (__builtin_expect(__any(t1 > ks[1]), 0)) {
        VDLD(8);
        slt = slt ? 0u : key == vd[0] ? 9u : key == vd[1] ? 10u : 0u;
        if (slt) {
          XCST();
          if (dl) SCST();
        }
      }
    }
    TS(16);
    TSE();
  } else {
    const int hw = wid - 4;
    unsigned ts0 = tick();
    asm volatile("" : "+v"(ts0));
    const char* Hc = reinterpret_cast<const char*>((cc ? X1 : X0) + (size_t)et * 128 * D);
    auto UPOLL = [&](int n) {
      const unsigned fo = (unsigned)WS_UFL + ((cc * 16 + nt) * 32 + ((mt + 1 + (lane >> 2)) & 7) * 4 + (lane & 3)) * FSTR * 4;
      unsigned f;
      POLL2(f == 0u && lane < 4 * n, U_S, PCAP, asm volatile("global_load_dword %0, %1, %2 sc1" VW0 : "=&v"(f) : "v"(fo), "s"(ws) : "memory"));
      return (__builtin_amdgcn_ballot_w64(f == 0u) & 0xFFFFFFFull) == 0;
    };
    bool urdy = true;
    u32x4 gT[8], uT[8], wdv[8];
    unsigned og = (unsigned)(((g & 3) * 256 + hw * 64 + (lane >> 3)) * F + (g >> 2) * 64 + ((lane & 7) ^ ((lane >> 3) & 2) ^ 4) * 8) * 2;
    unsigned wta = lds0 + STG + (g4 * 8 + ((lane >> 2) & 3)) * 128 + (((hw * 2 + ((lane >> 1) & 1)) ^ ((lane >> 2) & 2) ^ ((g4 & 1) * 4)) << 4) + (lane & 1) * 8;
    unsigned od = (unsigned)(((g >> 2) * 64 + (lane >> 3) * 8) * D + (g & 3) * 256 + hw * 64 + (lane & 7) * 8) * 2;
    unsigned ou = og + (unsigned)(((mt + W_UO) % 7 - mt) * 128);
    asm volatile("" : "+v"(og), "+v"(ou), "+v"(od), "+v"(wta));
#define LDWI(dst, voff, base, inc) asm volatile("global_load_dwordx4 %0, %1, %2" WPOL "\n\tv_add_u32 %1, %3, %1" : "=v"(dst), "+v"(voff) : "s"(base), "n"(inc) : "memory")
#define WST(o, w)                                                                                                          \
  if (b < NPART) {                                                                                                         \
    asm volatile("s_mov_b32 m0, %0" ::"s"(lds0 + STG + hw * 8192));                                                        \
    _Pragma("unroll") for (int i = 0; i < 8; ++i) {                                                                        \
      asm volatile("v_xor_b32 %0, 64, %0\n\tglobal_load_lds_dwordx4 %0, %1 offset:%2" WPOL "\n\tv_add_u32 %0, %3, %0"       \
                   : "+v"(o) : "s"(w), "n"((i & 3) * 1024), "n"(F * 16 + (i == 3 ? 3072 : -1024)) : "memory");             \
      if (i == 3) asm volatile("s_add_u32 m0, m0, 4096" ::: "scc");                                                        \
    }                                                                                                                      \
  }
#define WTR2(i) "ds_read_b64_tr_b16 %" #i ", %16 offset:(" #i "/2)*4096+(" #i "&1)*512\n\t"
#define WTK(T)                                                                                                                         \
  if (b < NPART) {                                                                                                                     \
    u32x2 t_[16];                                                                                                                      \
    asm volatile(WTR2(0) WTR2(1) WTR2(2) WTR2(3) WTR2(4) WTR2(5) WTR2(6) WTR2(7) WTR2(8) WTR2(9) WTR2(10) WTR2(11) WTR2(12) WTR2(13)   \
                     WTR2(14) WTR2(15) "s_waitcnt lgkmcnt(0)"                                                                          \
                 : "=&v"(t_[0]), "=&v"(t_[1]), "=&v"(t_[2]), "=&v"(t_[3]), "=&v"(t_[4]), "=&v"(t_[5]), "=&v"(t_[6]), "=&v"(t_[7]),     \
                   "=&v"(t_[8]), "=&v"(t_[9]), "=&v"(t_[10]), "=&v"(t_[11]), "=&v"(t_[12]), "=&v"(t_[13]), "=&v"(t_[14]), "=&v"(t_[15]) \
                 : "v"(wta) : "memory");                                                                                               \
    _Pragma("unroll") for (int f = 0; f < 8; ++f) T[f] = u32x4{t_[2 * f][0], t_[2 * f][1], t_[2 * f + 1][0], t_[2 * f + 1][1]};        \
  }
#define DLD(q0, q1) if (b < NPART) { _Pragma("unroll") for (int q = q0; q < q1; ++q) LDWI(wdv[q], od, wd, D * 2); }
    const int pf0 = A_PS + (j8 & (A_PW - 1)), q1 = A_Q1;
    static_assert(A_PS + 2 * A_PW + 2 <= R_AT && R_AT < A_Q1 && A_PS + 2 * A_PW + W_T <= A_Q1 && H_AT + 3 <= A_Q1 && G1_C + G1_S < W_T && W_T < A_PW && W_T >= M_NS - 2 && A_Q1 < F_AT && F_AT <= 29 && V_K <= B_J && V_T > B_J && B_WK == 15 && N_K > N_C && N_C > V_T && B_RAT > B_J && B_QAT > B_J && B_QAT + B_QD < B_MAT, "barrier counts");
#define HOFF(r) (unsigned)((((r) >> 5) * 32 + (((r) >> 2) & 3) * 8 + (((r) >> 4) & 1) * 4 + ((r)&3)) * 2048 + ((lane & 7) ^ (lane >> 3)) * 16)
#define TR8(T, W)                                                                                                    \
  _Pragma("unroll") for (int c = 0; c < 8; ++c) {                                                                    \
    _Pragma("unroll") for (int p = 0; p < 4; ++p)                                                                    \
        T[c][p] = __builtin_amdgcn_perm(W[2 * p + 1][c >> 1], W[2 * p][c >> 1], (c & 1) ? 0x07060302u : 0x05040100u); \
    asm volatile("" : "+v"(T[c]));                                                                                   \
  }
    u32x4 qm;
#pragma unroll
    for (int i = 0; i < 4; ++i) qm[i] = ((lane >> 2) & 3) == i ? ~0u : 0u;
    auto MVR = [&](f32x4 d) __attribute__((always_inline)) { return ((lane & 8) ? d[3] : d[0]) + dpp_f<0x128>((lane & 8) ? d[1] : d[2]); };
    auto MV64 = [&](const u32x4* T, u32x4 a) __attribute__((always_inline)) {
      f32x4 d[2] = {};
#pragma unroll
      for (int c = 0; c < 8; ++c) d[c & 1] = __builtin_amdgcn_mfma_f32_16x16x32_bf16(BF8(a & qm[c >> 1]), BF8(T[c]), d[c & 1], 0, 0, 0);
      return f32x2{MVR(d[0]), MVR(d[1])};
    };
    u32x4 sel, gp[4];
    unsigned vc;
#define VCLD(W) asm volatile("global_load_dword %0, %1, %2 sc1" W : "=&v"(vc) : "v"(O_VP + ((cc ^ 1) * 8 + mt) * 4096 + (lane & 15) * 256), "s"(ws) : "memory")
#define GP4(o, W)                                                                                                         \
  asm volatile(                                                                                                           \
      "global_load_dwordx4 %0, %4, %5 sc1\n\tglobal_load_dwordx4 %1, %4, %5 offset:1024 sc1\n\t"                         \
      "global_load_dwordx4 %2, %4, %5 offset:2048 sc1\n\tglobal_load_dwordx4 %3, %4, %5 offset:3072 sc1" W                \
      : "=&v"(gp[0]), "=&v"(gp[1]), "=&v"(gp[2]), "=&v"(gp[3])                                                            \
      : "v"(o), "s"(ws)                                                                                                   \
      : "memory")
#define GPBAD (max(max(max4(gp[0]), max4(gp[1])), max(max4(gp[2]), max4(gp[3]))) == SENT)
      auto GPSUM = [&]() __attribute__((always_inline)) {
        f32x4 d = {0.f, 0.f, 0.f, 0.f};
#pragma unroll
        for (int i = 0; i < 4; ++i) d = __builtin_amdgcn_mfma_f32_16x16x32_bf16(BF8(sel), BF8(gp[i]), d, 0, 0, 0);
        return d;
      };
    const bool rdc = b >= NPART && hw == 3, rnp = b >= NPART && hw == 2 && cc == 0;
    const int gbq = (b >> 4) & 1, cj = (b & 15) * 64 + (lane & 31) * 2;
    unsigned voff[4], ro, ra1, wqa, qo, so_, qa, mo, mo1, mo2, ma2;
    auto JSET = [&]() {
#pragma unroll
      for (int i = 0; i < 4; ++i) {
        voff[i] = HOFF((hw * 4 + i) * 8 + (lane >> 3)) - i * 1024;
        sel[i] = ((lane & 15) >> 1) == i ? ((lane & 1) ? 0x3f800000u : 0x3f80u) : 0u;
      }
      ro = rdc ? O_G2 + ((lane >> 5) * 16384 + gbq * 1024 + cj) * 8 : (unsigned)(((nt * 128 + hw * 32 + cc * 16 + ((lane >> 1) & 3) + (lane & 8)) * D + et * 128 + (lane & 1) * 64) * 2);
      ra1 = lane < 32 ? O_MOD + 2048 + gbq * 4096 + cj * 2 : O_M2 + 8192 + 64 - gbq * 64 + (b & 15) * 4;
      wqa = lds0 + SPARE + 8192 + cj * 2;
      mo = hw ? O_MOD + hw * 2048 + lane * 16 : O_MOD + 4096, mo1 = hw ? mo + 1024 : mo;
      qo = O_RS + (4096 + ((hw & 1) * 8 + (lane >> 4) * 2 + 1) * D + et * 128 + (hw >> 1) * 64 + (lane & 15) * 4) * 4;
      so_ = O_RS + ((hw & 1) * S + nt * 128 + (lane >> 4) * 32 + cc * 16 + (lane & 15)) * 4;
      qa = lds0 + RKA + ((hw >> 1) * 64 + ((lane >> 3) & 1) * 32 + (lane & 1) * 16 + ((lane >> 1) & 3) * 4 + (lane >> 4)) * 16 + (hw & 1) * 8;
      mo2 = O_M2 + (hw == 2 ? 8448u + (lane & 15) * 16 : hw ? 8192u + (lane & 7) * 16 : lane < 32 ? et * 512 + lane * 16 : 4096u + (lane & 16) * 128 + et * 256 + (lane & 15) * 16);
      ma2 = lds0 + (hw ? SPARE + 10240 + (lane & 7) * 8 : TAB + lane * 16);
      asm volatile("" : "+v"(voff[0]), "+v"(voff[1]), "+v"(voff[2]), "+v"(voff[3]), "+v"(ro), "+v"(ra1), "+v"(wqa), "+v"(qo), "+v"(so_), "+v"(qa), "+v"(mo), "+v"(mo1), "+v"(mo2), "+v"(ma2), "+v"(sel));
      *reinterpret_cast<unsigned*>((hw & 1) ? shm + RKB + 1024 + lane * 4 : reinterpret_cast<char*>(scw[2]) + lane * 4) = (hw & 1) ? 0u : 0x3f803f80u;
      if (hw == 3) {
        if (lane < 32) ssel[lane] = 0u;
        *reinterpret_cast<u32x2*>(shm + SLT + lane * 8) = u32x2{0u, 0u};
      }
    };
    if (hw < 2) {
      constexpr int P = 8;
      __builtin_amdgcn_s_setprio(2);
      unsigned voy[P];
#pragma unroll
      for (int i = 0; i < P; ++i) voy[i] = (unsigned)(((hw * 8 + i) * 8 + (lane >> 3)) * 4096 + ((lane & 7) ^ (lane >> 3)) * 16 - (i & 3) * 1024);
      const unsigned lbase = lds0 + hw * 8192;
      const char* Yc = reinterpret_cast<const char*>((cc ? Y1 : Y0) + (size_t)nt * 128 * S);
      auto stage = [&](const char* yp, unsigned l, bool pace) {
        GLDS4(l, voy, yp);
        if (L_H && pace) __builtin_amdgcn_s_sleep(L_H);
        GLDS4(l + 4096, voy + 4, yp);
      };
#define YSRC(n) (Yc + ((((n) + xcd * 4) & 31) << 7))
      __builtin_amdgcn_s_sleep(C_LD);
      if (W_L == 1) { if (hw) WAIT_V(0); else PFL(cpf); }
      else PFL(hw ? PFG(1) : cpf);
      if (!J_L) JSET();
      if (J_L && J_D) __builtin_amdgcn_s_sleep(J_D);
      TAGW(1);
      BAR(55);
      BT(56);
      if (hw == 1 && lane == 0) __hip_atomic_store(reinterpret_cast<u64*>(xfl + b * XSTR), tag, __ATOMIC_RELAXED, __HIP_MEMORY_SCOPE_AGENT);
#pragma unroll
      for (int s = 0; s < S_N; ++s) stage(YSRC(s), lbase + s * 16384, false);
      BT(57);
      WAIT_V((S_N - 1) * P);
      BT(62);
      BAR(0);
      if (S_N < NS - 1) {
        if (S_D) __builtin_amdgcn_s_sleep(S_D);
#pragma unroll
        for (int s = S_N; s < NS - 1; ++s) stage(YSRC(s), lbase + s * 16384, false);
      }
      unsigned slot = lbase + (NS - 1) * 16384;
      const u64 evm = ((u64)(1u | 1u << W_T | 1u << A_PW | 1u << (A_PW + W_T)) << pf0) | 1ull << CPF_K | 1ull << (33 - NS) | 3ull << 32;
      const char* yb = Yc;
      unsigned yo = (NS - 1 + xcd * 4) << 7, ym = 31 << 7;
#pragma unroll 1
      for (int k = 1; k <= 32 + B_J; ++k) {
        const char* ys = yb + (yo & ym);
        yo += 128;
        asm volatile("" : "+s"(ys));
        WAIT_V((NS - 3) * P);
        BAR(k);
        stage(ys, slot, true);
        slot = slot == lbase + (NS - 1) * 16384 ? lbase : slot + 16384;
        if (__builtin_expect((evm >> k) & 1, 0)) {
          if (k == pf0) {
            WST(og, wg);
            if (b >= NPART) GLDS(lds0 + SPARE + 8192 + hw * 1024, (unsigned)(hw * 1024 + lane * 16), wr_);
          }
          if (k == pf0 + W_T) { WTK(gT); }
          if (k == pf0 + A_PW) { WST(ou, wu); }
          if (k == pf0 + A_PW + W_T) { WTK(uT); }
          if (k == CPF_K && hw == 1 && lane < 12) PFL(cpf);
          if (k == 33 - NS) {
#pragma unroll
            for (int i = 0; i < P; ++i) voy[i] = HOFF((hw * 8 + i) * 8 + (lane >> 3)) - (i & 3) * 1024;
            yb = Hc, yo = mt << 8, ym = 15 << 7;
          }
          if (k == 32) {
            DLD(0, 8 - B_WN);
            if (J_L) JSET();
          }
          if (k == 33 && hw == 0) {
            if (U_D) __builtin_amdgcn_s_sleep(U_D);
            urdy = UPOLL(U_N1);
          }
        }
      }
    } else {
      const int aw = hw - 2;
      unsigned ow = wo - (2048 + (cg0 >> 4) * 1024) * 2;
      const unsigned oq = (unsigned)((nt * 64 + aw * 32 + (lane >> 4)) * D + mt * 128) * 2;
      u32x4 hv[8], wt[8], sc;
      {
        if (W_L == 1) {
          if (W_D) __builtin_amdgcn_s_sleep(W_D);
#pragma unroll
          for (int i = 0; i < 8; ++i) LDM(wv[i], wo + i * 12288, wmod);
        }
        if (PF23 == 1) {
          if (P_D5) __builtin_amdgcn_s_sleep(P_D5);
          PFL(PFG(2 + aw));
        }
        asm volatile("s_waitcnt vmcnt(%1)" : "+v"(cq) : "n"(11 + (PF23 == 1)) : "memory");
#pragma unroll
        for (int i = 0; i < 4; ++i) sc[i] = pack2(silu_bf(bflo(cq[i])), silu_bf(bfhi(cq[i])));
        asm volatile("" : "+v"(sc));
      }
#define GPUB(o, off)                                                                                   \
  if ((lane & 8) == 0)                                                                                 \
    asm volatile("global_store_dwordx4 %0, %1, %2 sc1 ; ALWAYS\n\ts_nop 1" ::"v"(off),                          \
                 "v"(u32x4{__float_as_uint(o[0]), (unsigned)tag, __float_as_uint(o[1]), (unsigned)tag}), "s"(ws) : "memory")
      const unsigned gpo = ks0 * 2048 + cg0 * 64 + (lane & 7) * 8 + (lane >> 4) * 2;
      {
        unsigned mpo = O_G1 + gpo * 8;
        if (W_L == 1) {
          TAGW(8);
          BAR(55);
          if (P_D2) __builtin_amdgcn_s_sleep(P_D2);
          PFL(PFG(2 + aw));
        }
        asm volatile("s_waitcnt vmcnt(%9)" : W8(wv), "+v"(mpo) : "n"(W_L == 1 ? 1 : 3 + (PF23 == 1)) : "memory");
        TR8(wt, wv);
        f32x2 o = MV64(wt, sc);
        asm volatile("" : "+v"(o));
        if (W_L != 1) {
          TAGW(PF23 == 1);
          BAR(55);
          if (P_D2) __builtin_amdgcn_s_sleep(P_D2);
          if (PF23 == 0) PFL(PFG(2 + aw));
        }
        GPUB(o, mpo);
      }
      unsigned vta = lds0 + STG + (g4 * 8 + ((lane >> 2) & 3)) * 256 + (((aw * 8 + ((lane >> 1) & 1)) ^ ((g4 & 1) * 8 + ((lane >> 2) & 3))) << 4) + (lane & 1) * 8;
      unsigned vso = O_VP + ((cc * 8 + mt) * 16 + nt) * 256 + aw * 128 + (lane & 3) * 32 + g4 * 8;
      asm volatile("" : "+v"(vta), "+v"(vso), "+v"(ow));
      unsigned f0 = O_XFL + ((xcd * 32 + 64 + aw * 128 + lane) & 255) * XSTR * 4, f1 = O_XFL + ((xcd * 32 + 128 + aw * 64 + lane) & 255) * XSTR * 4;
      unsigned lao = O_KEY + 512 * 1024 + b * 3584 + aw * 1536 + lane * 16;
      unsigned wao = (lane < 32 ? O_MODP : O_G2 - 512) + (ks0 * 2048 + cg0 * 64) * 8 + lane * 16;
      asm volatile("" : "+v"(wao));
      const char* pf4 = PFG(4 + aw);
      const char* pf6 = PFG(6 + aw);
      asm volatile("" : "+v"(f0), "+v"(f1), "+v"(lao), "+v"(pf4), "+v"(pf6));
      BT(57);
      BAR(0);
      float rs = 0.f, rs2 = 0.f;
      const unsigned ra = lds0 + (mt * 16 + aw * 8) * 128 + lane * 16;
      unsigned so = 0;
#define TSX()
#define RSUM()                                                                                                    \
  do {                                                                                                            \
    TSX();                                                                                                        \
    u32x4 R_;                                                                                                     \
    asm volatile("ds_read_b128 %0, %1\n\ts_waitcnt lgkmcnt(0)" : "=&v"(R_) : "v"(ra + so) : "memory");            \
    _Pragma("unroll") for (int j = 0; j < 4; ++j) {                                                               \
      asm volatile("v_add_f32 %0, %0, %1" : "+v"(rs) : "v"(R_[j] << 16));                                         \
      asm volatile("v_add_f32 %0, %0, %1" : "+v"(rs2) : "v"(R_[j] & 0xffff0000u));                                \
    }                                                                                                             \
    so = so == (NS - 1) * 16384 ? 0 : so + 16384;                                                                 \
  } while (0)
      {
        u64 f[2];
#define FLD() asm volatile("global_load_dwordx2 %0, %2, %4 sc1\n\tglobal_load_dwordx2 %1, %3, %4 sc1" : "=&v"(f[0]), "=&v"(f[1]) : "v"(f0), "v"(f1), "s"(ws) : "memory")
        __builtin_amdgcn_s_sleep(F_D);
        FLD();
        asm volatile("global_store_dwordx4 %0, %1, %2 offset:1024 sc1" ::"v"(lao), "v"(u32x4{~0u, ~0u, ~0u, ~0u}), "s"(ws) : "memory");
        if (!aw || lane >= 32) asm volatile("global_store_dwordx4 %0, %1, %2 sc1 ; ALWAYS" ::"v"(lao), "v"(u32x4{~0u, ~0u, ~0u, ~0u}), "s"(ws) : "memory");
        if (A_W) asm volatile("global_store_dwordx4 %0, %1, %2 sc1" ::"v"(wao), "v"(u32x4{0u, ~(unsigned)tag, 0u, ~(unsigned)tag}), "s"(ws) : "memory");
        asm volatile("s_nop 1");
        if (PF23 == 2) PFL(PFG(2 + aw));
        RSUM();
        BAR(1);
        RSUM();
        asm volatile("s_waitcnt vmcnt(%2)" : "+v"(f[0]), "+v"(f[1]) : "n"(2 + A_W + (PF23 == 2)) : "memory");
        POLL(__all(f[0] == tag && f[1] == tag), 1, FLD(); asm volatile("s_waitcnt vmcnt(0)" : "+v"(f[0]), "+v"(f[1])::"memory"));
      }
      BAR(2);
      if (X_PA) __builtin_amdgcn_s_setprio(X_PA);
#define ARUN(b0, b1) _Pragma("unroll 1") for (int bi = (b0); bi < (b1); ++bi) { RSUM(); BAR(bi + 1); }
#define GLD() _Pragma("unroll") for (int i = 0; i < 8; ++i) LDWI(wv[i], ow, wmod, 12288)
      ARUN(2, pf0);
      PFL(pf4);
      GLD();
#define HVLD() _Pragma("unroll") for (int i = 0; i < 8; ++i) GLDS(lds0 + STG + aw * 8192 + i * 1024, oq + i * 8192 + (((lane & 15) ^ ((i * 4 + (lane >> 4)) & 15)) << 4), cc ? X1 : X0)
      WST(og, wg);
      ARUN(pf0, pf0 + G1_C);
      {
        if (b >= NPART) WAIT_V(0);
        asm volatile("s_waitcnt vmcnt(8)" : W8(wv)::"memory");
        TR8(wt, wv);
        if (G1_S) { RSUM(); BAR(pf0 + G1_C + 1); }
        const f32x2 o = MV64(wt, sc);
        GPUB(o, O_MODP + gpo * 8);
        ow = wo + 2048;
      }
      ARUN(pf0 + G1_C + G1_S, pf0 + W_T - 1);
      WAIT_V(1);
      RSUM();
      BAR(pf0 + W_T);
      WTK(gT);
      ARUN(pf0 + W_T, pf0 + A_PW);
      PFL(pf6);
      WST(ou, wu);
      const int rb_ = b < NPART ? pf0 + A_PW + W_T : R_AT, hb_ = max(rb_ + 1, H_AT + (j8 & 3));
      const unsigned c0 = (b - NPART) * 64 + aw * 32 + (lane & 15) * 2, c1 = (b - NPART) * 32 + aw * 16 + (lane & 7) * 2;
#define ERLD(W)                                                                                                                     \
  asm volatile("global_load_dwordx4 %0, %8, %10 sc1\n\tglobal_load_dwordx4 %1, %8, %11 sc1\n\t"                                    \
               "global_load_dwordx4 %2, %8, %12 sc1\n\tglobal_load_dwordx4 %3, %8, %13 sc1\n\t"                                    \
               "global_load_dwordx4 %4, %9, %10 sc1\n\tglobal_load_dwordx4 %5, %9, %11 sc1\n\t"                                    \
               "global_load_dwordx4 %6, %9, %12 sc1\n\tglobal_load_dwordx4 %7, %9, %13 sc1" W                                      \
               : "=&v"(wt[0]), "=&v"(wt[1]), "=&v"(wt[2]), "=&v"(wt[3]), "=&v"(wt[4]), "=&v"(wt[5]), "=&v"(wt[6]), "=&v"(wt[7])     \
               : "v"(O_MODP + (g4 * 8192 + c0) * 8), "v"(O_G1 + (g4 * 8192 + (lane & 8) * 128 + c1) * 8), "s"(ws), "s"(ws + 16384), \
                 "s"(ws + 32768), "s"(ws + 49152)                                                                                   \
               : "memory")
      ARUN(pf0 + A_PW, rb_ - 3);
      if (b >= NPART) ERLD("");
      ARUN(rb_ - 3, rb_ - 1);
      WAIT_V(0);
      RSUM();
      BAR(rb_);
      WTK(uT);
      if (b >= NPART) {
        const unsigned wq = *reinterpret_cast<const unsigned*>(&svb[4][c1]);
#define TG2(r, i) ((r[i][1] ^ (unsigned)tag) | (r[i][3] ^ (unsigned)tag) | (r[i + 1][1] ^ (unsigned)tag) | (r[i + 1][3] ^ (unsigned)tag))
        asm volatile("" : W8(wt));
        POLL(__all((TG2(wt, 0) | TG2(wt, 2) | TG2(wt, 4) | TG2(wt, 6)) == 0u), 2, ERLD(VW0));
        auto FIN = [&](int q, int i, bool inc) __attribute__((always_inline)) {
          return rbf((inc ? 1.0f : 0.0f) + rbf(ar4((__uint_as_float(wt[q][i]) + __uint_as_float(wt[q + 1][i])) + (__uint_as_float(wt[q + 2][i]) + __uint_as_float(wt[q + 3][i])))));
        };
        const float s0 = FIN(0, 0, gbq), s1 = FIN(0, 2, gbq);
        if (lane < 16)
          asm volatile("global_store_dwordx4 %0, %1, %2 sc1\n\ts_nop 1" ::"v"(O_MODP + (16 * 2048 + c0) * 8),
                       "v"(u32x4{__float_as_uint(s0), (unsigned)tag, __float_as_uint(s1), (unsigned)tag}), "s"(ws) : "memory");
        const bool hi = lane & 8;
        const float t0 = FIN(4, 0, hi), t1 = FIN(4, 2, hi), o0 = dpp_f<0x128>(t0), o1 = dpp_f<0x128>(t1);
        const float a0 = hi ? o0 : t0, a1 = hi ? o1 : t1, e0 = hi ? t0 : o0, e1 = hi ? t1 : o1, p0 = e0 * bflo(wq), p1 = e1 * bfhi(wq);
        const unsigned h = pack2(p0, p1);
        const unsigned v = g4 == 0 ? __float_as_uint(hi ? a1 : a0) : g4 == 1 ? (hi ? h : pack2(e0, e1)) : hi ? pack2(a0 * p0, a1 * p1) : pack2(p0 - bflo(h), p1 - bfhi(h));
        const unsigned po = g4 == 0 ? O_M2 + c1 * 4 + (hi ? 4 : 0) : (g4 == 1 ? (hi ? O_M2 + 4096 : O_MOD + 4096) : hi ? O_GC : O_M2 + 6144) + c1 * 2;
        if (lane < 48) asm volatile("global_store_dword %0, %1, %2 sc1" ::"v"(po), "v"(min(v, SENT - 1u)), "s"(ws) : "memory");
      }
      ARUN(rb_, hb_);
      HVLD();
      ARUN(hb_, q1);
      u32x4 fm, g0, g1;
#define FMLD(W)                                                                                                                                     \
  asm volatile("global_load_dwordx4 %0, %3, %5 sc1\n\tglobal_load_dwordx4 %1, %4, %5 sc1\n\tglobal_load_dwordx4 %2, %4, %5 offset:64 sc1" W        \
               : "=&v"(fm), "=&v"(g0), "=&v"(g1) : "v"(O_MODP + (16 * 2048 + aw * 1024 + mt * 128) * 8 + lane * 16), "v"(O_GC + nt * 128 + g4 * 16), "s"(ws) : "memory")
      FMLD("");
      RSUM();
      BAR(q1 + 1);
      unsigned ha[4], sma = lds0 + SPARE + 15872 + (aw ? 512 + lane * 8 : lane * 4);
#pragma unroll
      for (int i = 0; i < 4; ++i) ha[i] = lds0 + STG + aw * 8192 + (lane & 15) * 256 + (((i * 4 + (lane >> 4)) ^ (lane & 15)) << 4);
      asm volatile("" : "+v"(ha[0]), "+v"(ha[1]), "+v"(ha[2]), "+v"(ha[3]), "+v"(sma), "+v"(ow));
      ARUN(q1 + 1, F_AT);
      asm volatile("s_waitcnt vmcnt(0)" : "+v"(fm), "+v"(g0), "+v"(g1)::"memory");
#pragma unroll
      for (int i = 0; i < 8; ++i) DSR(hv[i], ha[i & 3], (i >> 2) * 4096);
      POLL(__all(fm[1] == (unsigned)tag && fm[3] == (unsigned)tag && max(max4(g0), max4(g1)) != SENT), 1, FMLD(VW0));
      {
        const unsigned sg = cc ? 0x80000000u : 0u;
        asm volatile("ds_write_b32 %0, %2\n\tds_write_b32 %1, %3" ::"v"(sma), "v"(sma + (aw ? 4 : 256)), "v"(aw ? fm[0] ^ sg : (fm[0] >> 16) | (fm[2] & 0xffff0000u)), "v"(fm[2] ^ sg) : "memory");
      }
      ARUN(F_AT, 29);
      GLD();
      RSUM();
      BAR(30);
        u32x2 tv[16];
#pragma unroll
        for (int q = 0; q < 16; ++q)
          asm volatile("ds_read_b64_tr_b16 %0, %1 offset:%2" : "=v"(tv[q]) : "v"(vta ^ ((((q >> 2) * 2) ^ ((q & 1) * 4)) << 4)), "n"((q & 1) * 1024 + ((q >> 1) & 1) * 8192) : "memory");
      RSUM();
      BAR(31);
      RSUM();
      {
        const float v = dpp_add<0x141>(dpp_add<0x4E>(dpp_add<0xB1>(rs + rs2)));
        if ((lane & 7) == 0)
          asm volatile("global_store_dword %0, %1, %2 sc1 ; ALWAYS" ::"v"(O_RS + (cc * S + nt * 128 + mt * 16 + aw * 8 + (lane >> 3)) * 4), "v"(nsent(v)), "s"(ws) : "memory");
      }
      BAR(32);
      if (!V_F) {
        __builtin_amdgcn_s_sleep(2);
        DLD(0, 8 - B_WN);
      }
      __builtin_amdgcn_s_setprio(2);
      {
        asm volatile("" : W8(tv), W8((tv + 8))::"memory");
        f32x4 d[4];
#pragma unroll
        for (int j = 0; j < 4; ++j) {
          d[j] = __builtin_amdgcn_mfma_f32_16x16x32_bf16(BF8((u32x4{tv[4 * j][0], tv[4 * j][1], tv[4 * j + 1][0], tv[4 * j + 1][1]})), BF8(g0), f32x4{0.f, 0.f, 0.f, 0.f}, 0, 0, 0);
          d[j] = __builtin_amdgcn_mfma_f32_16x16x32_bf16(BF8((u32x4{tv[4 * j + 2][0], tv[4 * j + 2][1], tv[4 * j + 3][0], tv[4 * j + 3][1]})), BF8(g1), d[j], 0, 0, 0);
        }
        u32x2 vo;
#pragma unroll
        for (int i = 0; i < 2; ++i) {
          const unsigned e0 = pack2(d[0][2 * i], d[0][2 * i + 1]), e1 = pack2(d[1][2 * i], d[1][2 * i + 1]), e2 = pack2(d[2][2 * i], d[2][2 * i + 1]), e3 = pack2(d[3][2 * i], d[3][2 * i + 1]);
          vo[i] = min((lane & 2) ? ((lane & 1) ? e3 : e2) : ((lane & 1) ? e1 : e0), SENT - 1u);
        }
        if ((lane & 12) == 0) asm volatile("global_store_dwordx2 %0, %1, %2 sc1 ; ALWAYS" ::"v"(vso), "v"(vo), "s"(ws) : "memory");
      }
      if (V_F) { DLD(0, 8 - B_WN); }
      if (Q_L) BAR(33);
      asm volatile("" : W8(hv)::"memory");
      {
        f32x4 qa[2] = {};
#pragma unroll
        for (int kk = 0; kk < 4; ++kk) {
          const bf16x8 sb8 = *reinterpret_cast<const bf16x8*>(ssa + kk * 32 + (lane >> 4) * 8);
#pragma unroll
          for (int r = 0; r < 2; ++r)
            qa[r] = __builtin_amdgcn_mfma_f32_16x16x32_bf16(__builtin_bit_cast(bf16v8, hv[r * 4 + kk]), __builtin_bit_cast(bf16v8, sb8), qa[r], 0, 0, 0);
        }
        if ((lane & 15) < 2)
          asm volatile("global_store_dwordx4 %0, %1, %2 sc1 ; ALWAYS\n\ts_nop 1" ::"v"(O_RS + (4096 + (cc * 8 + mt) * D + nt * 64 + aw * 32 + (lane & 1) * 16 + (lane >> 4) * 4) * 4),
                       "v"(nsent4(selu((lane & 1) ? ~0u : 0u, __builtin_bit_cast(u32x4, qa[1]), __builtin_bit_cast(u32x4, qa[0])))), "s"(ws) : "memory");
      }
      if (!Q_L) BAR(33);
      {
        if (b >= NPART) WAIT_V(3);
        asm volatile("s_waitcnt vmcnt(%8)" : W8(wv) : "n"(8 - B_WN + 3) : "memory");
        TR8(wt, wv);
        const f32x2 o = MV64(wt, sc);
        GPUB(o, O_G2 + gpo * 8);
      }
      JSET();
      BAR(34);
#pragma unroll
      for (int k = 3; k <= B_J; ++k) {
        BAR(32 + k);
        if (k == V_K) {
          if (!aw) GP4(O_VP + (cc * 8 + mt) * 4096 + lane * 16, "");
          else VCLD("");
        }
      }
    }
    u32x4 dT[8];
    u32x4 m[3];
    u32x2 kk = {SENT, SENT};
#define K2LD(W) asm volatile("global_load_dwordx2 %0, %1, %2 sc1" W : "+v"(kk) : "v"(O_K2 + (lane & 15) * 64 + (xcd & (K_R - 1)) * 1024), "s"(ws) : "memory")
    {
      constexpr BSched sch;

      const unsigned lbase = lds0 + hw * 4096;
      u32x4 rq[2];
      u32x4 rr[8];
      unsigned cbv, t1, t2;
      float t[2];
#define RDLD() asm volatile("global_load_dwordx4 %0, %2, %3 sc1\n\tglobal_load_dwordx4 %1, %2, %4 sc1" : "=&v"(rq[0]), "=&v"(rq[1]) : "v"(ro), "s"(x), "s"(x + 4096) : "memory")
#define RRLD(W)                                                                                                                               \
  asm volatile("global_load_dwordx4 %0, %9, %10 sc1\n\tglobal_load_dwordx4 %1, %9, %11 sc1\n\t"                                              \
               "global_load_dwordx4 %2, %9, %12 sc1\n\tglobal_load_dwordx4 %3, %9, %13 sc1\n\t"                                              \
               "global_load_dwordx4 %4, %14, %10 sc1\n\tglobal_load_dwordx4 %5, %14, %11 sc1\n\t"                                            \
               "global_load_dwordx4 %6, %14, %12 sc1\n\tglobal_load_dwordx4 %7, %14, %13 sc1\n\tglobal_load_dword %8, %15, %10 sc1" W         \
               : "=&v"(rr[0]), "=&v"(rr[1]), "=&v"(rr[2]), "=&v"(rr[3]), "=&v"(rr[4]), "=&v"(rr[5]), "=&v"(rr[6]), "=&v"(rr[7]), "=&v"(cbv)  \
               : "v"(ro), "s"(ws), "s"(ws + 16384), "s"(ws + 32768), "s"(ws + 49152), "v"(ro + 65536), "v"(O_MOD + 4096 + cj * 2)            \
               : "memory")
      unsigned wq;
      unsigned mr;
#define QLD(W)                                                                                                           \
  asm volatile("global_load_dwordx4 %0, %3, %5 offset:-4096 sc1\n\tglobal_load_dwordx4 %1, %3, %5 sc1\n\tglobal_load_dword %2, %4, %5 sc1" W \
               : "=&v"(m[0]), "=&v"(m[1]), "=&v"(mr)                                                                     \
               : "v"(qo), "v"(so_), "s"(ws)                                                                              \
               : "memory")
#define MODLD()                                                                                                                \
  asm volatile("global_load_dwordx4 %0, %3, %6 sc1\n\tglobal_load_dwordx4 %1, %4, %6 sc1\n\tglobal_load_dwordx4 %2, %5, %6 sc1" \
               : "=&v"(m[0]), "=&v"(m[1]), "=&v"(m[2])                                                                         \
               : "v"(mo), "v"(mo1), "v"(mo2), "s"(ws)                                                                          \
               : "memory")
#define MODW(n) asm volatile("s_waitcnt vmcnt(%3)" : "+v"(m[0]), "+v"(m[1]), "+v"(m[2]) : "n"(n) : "memory")
#pragma unroll
      for (int k = B_J; k < 16; ++k) {
        if (k > B_J) {
          if (k == 2 * U_N1 - 1 && !urdy) UPOLL(7);
          WAIT_V(sch.cnt[k]);
          if (k == B_QAT + B_QD + 1) WAIT_L(0);
          BAR(32 + k);
        }
        if (k > B_J && k - 2 + NS < 16) {
          const char* yp = Hc + ((mt * 2 + k - 2 + NS) & 15) * 128;
          const unsigned l = lbase + ((30 + k + NS) % NS) * 16384;
          GLDS4(l, voff, yp);
        }
        if (k == B_TK && hw == 1 && b >= 192) {
          const unsigned t = tick();
          if (lane == 0) asm volatile("global_store_dword %0, %1, %2 sc1" ::"v"(O_M2 + 8448 + (b - 192) * 4), "v"(min(t, SENT - 1u)), "s"(ws) : "memory");
        }
        if (k == B_RAT + 2) {
          WAIT_V(sch.rcnt);
          if (rdc) {
            asm volatile("" : W8(rr), "+v"(cbv));
            POLL(__all((TG2(rr, 0) | TG2(rr, 2) | TG2(rr, 4) | TG2(rr, 6)) == 0u && cbv != SENT), 2, RRLD(VW0));
#pragma unroll
            for (int j = 0; j < 2; ++j) {
#define RF(q) __uint_as_float(rr[q][2 * j])
              t[j] = ((RF(0) + RF(1)) + (RF(2) + RF(3))) + ((RF(4) + RF(5)) + (RF(6) + RF(7)));
            }
            asm volatile("" : "+v"(t[0]), "+v"(t[1]));
          } else {
            asm volatile("" ::"v"(rq[0]), "v"(rq[1]));
          }
        }
        if (k == B_RAT + 2 + R_S && rdc) {
          const unsigned o = pack2(swap32_add(t[0], t[0]), swap32_add(t[1], t[1])), f = gbq ? cbv : o;
          const float pr = row_sum(bflo(f) * bflo(wq) + bfhi(f) * bfhi(wq)), ps = swap16_add(pr, pr);
          if (lane <= 32) asm volatile("global_store_dword %0, %1, %2 sc1" ::"v"(ra1), "v"(min(lane < 32 ? o : __float_as_uint(ps), SENT - 1u)), "s"(ws) : "memory");
          TS(12);
        }
        if (k == V_T && hw == 3) {
          WAIT_V(sch.vcnt);
          asm volatile("" : "+v"(vc));
          POLL(__all(vc != SENT), 2, VCLD(VW0));
        }
        if (k == V_T && hw == 2) {
          WAIT_V(sch.vcnt);
          asm volatile("" : "+v"(gp[0]), "+v"(gp[1]), "+v"(gp[2]), "+v"(gp[3]));
          POLL(!__any(GPBAD), 2, GP4(O_VP + (cc * 8 + mt) * 4096 + lane * 16, VW0));
          const f32x4 d = GPSUM();
          if (lane < 32) *reinterpret_cast<u32x2*>(shm + VT + (lane & 15) * 16 + g4 * 8) = u32x2{pack2(d[0], d[1]), pack2(d[2], d[3])};
          WAIT_L(0);
        }
        if (k == N_C) {
          if (N_P != 2) __builtin_amdgcn_s_setprio(N_P);
          f32x4 n[2] = {};
#pragma unroll
          for (int q = 0; q < 8; ++q)
            n[q >> 2] = __builtin_amdgcn_mfma_f32_16x16x32_bf16(BF8(*reinterpret_cast<const u32x4*>(shm + VT + (q & 3) * 64 + g4 * 16)),
                                                                BF8(*reinterpret_cast<const u32x4*>(shm + STG + hw * 8192 + q * 1024 + lane * 16)), n[q >> 2], 0, 0, 0);
          if (lane < 16)
            asm volatile("global_store_dword %0, %1, %2 sc1" ::"v"(O_NP + nt * 4096 + (cc * 8 + mt) * 256 + hw * 64 + lane * 4), "v"(min(pack2(n[0][0], n[1][0]), SENT - 1u)), "s"(ws) : "memory");
          if (N_P != 2) __builtin_amdgcn_s_setprio(2);
          TS(33);
        }
        if (k == N_K + N_D && rnp) {
            WAIT_V(sch.ncnt);
            asm volatile("" : "+v"(gp[0]), "+v"(gp[1]), "+v"(gp[2]), "+v"(gp[3]));
            POLL(!__any(GPBAD), 2, GP4(O_NP + nt * 4096 + lane * 16, VW0));
            const unsigned r0 = nt * 128 + ((lane >> 2) & 3) * 32 + (lane & 3) * 4 + (g4 & 1) * 2;
        const f32x4 d = GPSUM();
        unsigned ky[4];
#pragma unroll
        for (int i = 0; i < 4; ++i) {
          const unsigned f = __float_as_uint(d[i]);
          ky[i] = (((f ^ ((unsigned)((int)f >> 31) | 0x80000000u)) >> 1) & ~2047u) | (r0 + (i & 1) * 16 + (i >> 1));
        }
        t1 = max(max(ky[0], ky[1]), max(ky[2], ky[3])), t2 = max(min(max(ky[0], ky[1]), max(ky[2], ky[3])), max(min(ky[0], ky[1]), min(ky[2], ky[3])));
        asm volatile("" : "+v"(t1), "+v"(t2));
        }
        if (k == N_K + N_D + N_S && rnp) {
        TOP2(0xB1); TOP2(0x4E); TOP2(0x141); TOP2(0x140);
        TOP2S(__builtin_amdgcn_permlane16_swap);
            if (lane < K_R) asm volatile("global_store_dwordx2 %0, %1, %2 sc1" ::"v"(O_K2 + nt * 64 + lane * 1024), "v"(u32x2{t1, t2}), "s"(ws) : "memory");
            TS(34);
        }
        if (k == N_K && rnp) GP4(O_NP + nt * 4096 + lane * 16, "");
        if (k == B_QAT + B_QD) {
          asm volatile("s_waitcnt vmcnt(%3)" : "+v"(m[0]), "+v"(m[1]), "+v"(mr) : "n"(sch.qcnt) : "memory");
          POLL(__all(max(max(max4(m[0]), max4(m[1])), mr) != SENT), 2, QLD(VW0));
          const f32x4 t = __builtin_bit_cast(f32x4, m[0]) + __builtin_bit_cast(f32x4, m[1]);
          const float r = swap16_add(swap32_add(t[0], t[2]), swap32_add(t[1], t[3]));
          const unsigned a0 = pack2(r, r);
          asm volatile("ds_write_b64 %0, %1" ::"v"(qa), "v"(u32x2{a0, pack2(r - bflo(a0), r - bflo(a0))}) : "memory");
          const float rs = __uint_as_float(mr ^ ((hw & 1) ? 0x80000000u : 0u));
          const unsigned bb = pack2(rs, rs - rbf(rs));
          *reinterpret_cast<u32x2*>(shm + RKB + lane * 16 + (hw & 1) * 8) = u32x2{bb, bb};
        }
        if (k == CPF_B && hw == 2 && lane < 12) PFL(cpf);
        if (k == B_RAT) {
          if (rdc) RRLD(""); else RDLD();
          asm volatile("ds_read_b32 %0, %1\n\ts_waitcnt lgkmcnt(0)" : "=v"(wq) : "v"(wqa) : "memory");
        }
        if (k == B_QAT) QLD("");
        if (k == B_MAT) MODLD();
        if (k == B_WK) { DLD(8 - B_WN, 8); }
      }
      if (b >= NPART) WAIT_V(0);
      MODW(sch.mcnt);
      TS(30);
      SKEWPT();
      POLL(__all(max(max(max4(m[0]), max4(m[1])), max4(m[2])) != SENT), 4, MODLD(); MODW(0));
      *reinterpret_cast<u32x4*>(&svb[hw][lane * 8]) = m[0];
      *reinterpret_cast<u32x4*>(&svb[hw][512 + lane * 8]) = m[1];
      if (hw == 0) {
        asm volatile("ds_write_b128 %0, %1" ::"v"(ma2), "v"(m[2]) : "memory");
      } else if (hw == 2) {
        int d = max(max((int)(m[2][0] - ts0), (int)(m[2][1] - ts0)), max((int)(m[2][2] - ts0), (int)(m[2][3] - ts0)));
        d = max(d, (int)dpp_u<0xB1>(d));
        d = max(d, (int)dpp_u<0x4E>(d));
        d = max(d, (int)dpp_u<0x141>(d));
        d = max(d, (int)dpp_u<0x140>(d));
        const unsigned hh = ts0 + d + T_O + ((d - T_N) * T_A >> 8);
        ssel[7] = hh;
        if (K_E && (int)(tick() - (unsigned)__builtin_amdgcn_readfirstlane((int)hh) - T_K) >= 0) K2LD("");
      } else {
        const f32x4 v = __builtin_bit_cast(f32x4, m[2]);
        const f32x2 c = {v[0] + v[1], v[2] + v[3]};
        const float s4 = dpp_add<0x4E>(dpp_add<0xB1>(c[0] + c[1]));
        if (lane < 8) asm volatile("ds_write_b64 %0, %1" ::"v"(ma2), "v"(lane < 4 ? c : f32x2{s4, s4}) : "memory");
      }
    }
    if (hw != 2 || !L_PR) __builtin_amdgcn_s_setprio(0);
    TS(17);
    BT(48);
    LBAR();
    BTE();
    TS(18);
    if (hw == 2) {
      if (K_E) WAIT_V(0);
      asm volatile("" : "+v"(kk));
      if (__any(max(kk[0], kk[1]) == SENT)) {
        nap_tick((unsigned)__builtin_amdgcn_readfirstlane((int)ssel[7]) + T_K);
        POLL2(max(kk[0], kk[1]) == SENT, K_S, PCAP, K2LD(VW0));
      }
      TS(35);
      const unsigned me = lane < 32 ? kk[0] : kk[1], ot = (lane & 16) ? kk[1] : kk[0];
      unsigned rk = ot > me;
      sfor<15>([&](auto r) __attribute__((always_inline)) { rk += dpp_u<0x121 + decltype(r)::value>(ot) > me; });
      auto rs_ = __builtin_amdgcn_permlane16_swap(rk, rk, false, false);
      rk = rs_[0] + rs_[1];
      if (rk < 8u) {
        *reinterpret_cast<unsigned*>(shm + LST + rk * 4) = 0x8ffeu - (me & 2047u) * 2;
        if ((lane & 15) == nt) *reinterpret_cast<unsigned*>(shm + SLT + (me & 127u) * 4) = rk + 1;
      }
      asm volatile("" ::: "memory");
      ssel[20] = 1u;
      if (L_PR == 2) __builtin_amdgcn_s_setprio(0);
    }
    TS(19);
    unsigned sfa = lds0 + SPARE + 10368, sv, one = 1u;
    RETIRE();
    const int kc = (g & 3) * 256 + hw * 64 + (lane & 7) * 8;
    const u32x4 cbq = *reinterpret_cast<const u32x4*>(&svb[2][kc]), sbq = *reinterpret_cast<const u32x4*>(&svb[1][kc]);
    f32x4 cb2[2] = {lo4(cbq), hi4(cbq)}, sb2[2] = {lo4(sbq), hi4(sbq)};
    float sw = cwj[8], cj8 = cwj[lane & 7];
    unsigned rk2 = lds0 + LST + (lane >> 3) * 4;
    unsigned xo_ = O_X1C + (lane >> 3) * 2048 + kc * 2, hwa = lds0 + XC + hw * 1024 + (lane & 7) * 128 + (((lane >> 3) ^ lane) & 7) * 16;
    unsigned hra = lds0 + XC + g4 * 128 + ((lane ^ g4) & 7) * 16, hrb = hra ^ 64, ara = lds0 + SPARE + 15872 + (lane & 2) * 64 + g4 * 32 + (lane & 1) * 16;
    unsigned pso = O_PART + ((g >> 2) * 2048 + (lane & 8) * 128 + (g & 3) * 256 + hw * 64 + (lane & 7) * 8 + g4 * 2) * 4;
    unsigned gso = O_GU + g * 1024 + (lane >> 5) * 256 + hw * 64 + (lane & 15) * 4, awa = lds0 + SPARE + 15872 + lane * 4, sfw = sfa + hw * 4;
    unsigned gl0 = O_GU + (g >> 2) * 4096 + lane * 8, gl1 = O_GU + (xcd * 7 + (mt + 7 - W_UO) % 7) * 4096 + 512 + lane * 8;
    asm volatile("" : "+v"(sfa), "+v"(one), "+v"(hra), "+v"(hrb), "+v"(ara), "+v"(pso), "+v"(gso), "+v"(awa), "+v"(gl0), "+v"(gl1), "+v"(sfw), "+v"(qm));
    asm volatile("" : "+v"(cb2[0]), "+v"(cb2[1]), "+v"(sb2[0]), "+v"(sb2[1]), "+v"(hwa), "+v"(sw), "+v"(cj8), "+v"(rk2), "+v"(xo_));
    unsigned sto = O_ST2 + lane * 16, kmk = ~0u;
    asm volatile("" : "+v"(sto), "+v"(kmk));
    const unsigned th = (unsigned)__builtin_amdgcn_readfirstlane((int)ssel[7]);
    const char* wb = ws;
#define P2PUT(i) if (__builtin_expect(wb != ws, 0)) { WAIT_V(0); if (lane == 0) asm volatile("global_store_dwordx2 %0, %1, %2 sc1" ::"v"(O_F2 + (i)*8192 + (g * 4 + hw) * 8), "v"(tag), "s"(ws) : "memory"); }
#define P2GET(o) if (__builtin_expect(wb != ws, 0)) { u64 f; POLL2(f != tag, 4, PCAP, asm volatile("global_load_dwordx2 %0, %1, %2 sc1" VW0 : "=&v"(f) : "v"(O_F2 + (o)), "s"(ws) : "memory")); }
#pragma unroll 1
    for (;;) {
    TS(20);
    if (wb == ws) {
      unsigned r_;
      POLL2(sv == 0u, 2, 1 << 26, asm volatile("ds_read_b32 %0, %2 offset:80\n\tds_read_b32 %1, %3\n\ts_waitcnt lgkmcnt(0)" : "=&v"(sv), "=&v"(r_) : "v"(sfa), "v"(rk2) : "memory"));
      rk2 = r_;
    }
    TS(21);
    nap_tick(th + T_C3 - 10);
    if (D_G) {
      u32x4 dn;
      POLL2((dn[0] & dn[1] & dn[2] & dn[3]) == 0u, 1, 1 << 26, asm volatile("ds_read_b128 %0, %1 offset:96\n\ts_waitcnt lgkmcnt(0)" : "=v"(dn) : "v"(sfa) : "memory"));
      if (D_G > 1) __builtin_amdgcn_s_sleep(D_G);
    }
    __builtin_amdgcn_s_setprio(H_P);
    wait_tick(th + T_C3);
    TS(36);
    u32x4 st, xv_;
    unsigned cpy = 0;
    POLL2(max(max4(xv_), max4(st)) == SENT, 0, PCAP,
          if (cpy == D_R * X_CS) { __builtin_amdgcn_s_sleep(D_S2); cpy -= X_CS; } else if (D_S && cpy) __builtin_amdgcn_s_sleep(D_S);
          asm volatile("global_load_dwordx4 %0, %2, %4 sc1\n\tglobal_load_dwordx4 %1, %3, %4 sc1" VW0 : "=&v"(st), "=&v"(xv_) : "v"(sto), "v"(xo_), "s"(ws + cpy) : "memory");
          cpy += X_CS);
    TS(22);
    SKEWPT();
#define SUM8(v) dpp_add<0x141>(dpp_add<0x4E>(dpp_add<0xB1>(v)))
    const f32x4 sf = __builtin_bit_cast(f32x4, st);
    const float mean = SUM8(sf[0]) * 0.125f, dm = sf[0] - mean;
    const float rstd = __builtin_amdgcn_rsqf(SUM8(sf[1] + 128.0f * (dm * dm)) * (1.0f / D) + 1e-5f);
    asm volatile("ds_write_b128 %0, %1\n\tds_write_b32 %2, %3 offset:64" ::"v"(hwa),
                 "v"(pk8(((lo4(xv_) - mean) * rstd) * cb2[0] + sb2[0], ((hi4(xv_) - mean) * rstd) * cb2[1] + sb2[1])), "v"(sfw), "v"(one) : "memory");
    TS(3);
    u32x4 ha[8], fg;
#define HSLD(W)                                                                                                                             \
  asm volatile(                                                                                                                             \
      "ds_read_b128 %8, %11 offset:64\n\tds_read_b128 %0, %9\n\tds_read_b128 %1, %10 offset:512\n\tds_read_b128 %2, %9 offset:1024\n\t"       \
      "ds_read_b128 %3, %10 offset:1536\n\tds_read_b128 %4, %9 offset:2048\n\tds_read_b128 %5, %10 offset:2560\n\t"                          \
      "ds_read_b128 %6, %9 offset:3072\n\tds_read_b128 %7, %10 offset:3584" W                                                               \
      : "=&v"(ha[0]), "=&v"(ha[1]), "=&v"(ha[2]), "=&v"(ha[3]), "=&v"(ha[4]), "=&v"(ha[5]), "=&v"(ha[6]), "=&v"(ha[7]), "=&v"(fg)           \
      : "v"(hra), "v"(hrb), "v"(sfa)                                                                                                        \
      : "memory")
    unsigned key = (mkkey(rstd * SUM8(sf[2] + dm * cj8) + sw) | rk2) & kmk;
    asm volatile("" : "+v"(key));
    HSLD("");
    unsigned t1 = key, t2 = 0u;
    TOP2(0x140);
    TOP2S(__builtin_amdgcn_permlane16_swap);
    TOP2S(__builtin_amdgcn_permlane32_swap);
    const int l1 = __builtin_ctzll(__builtin_amdgcn_ballot_w64(key == t1)), l2 = __builtin_ctzll(__builtin_amdgcn_ballot_w64(key == t2));
    unsigned mq[6];
    asm volatile("s_bfe_i32 %0, %6, 0x10003\n\ts_bfe_i32 %1, %7, 0x10003\n\ts_bfe_i32 %2, %6, 0x10004\n\ts_bfe_i32 %3, %7, 0x10004\n\t"
                 "s_bfe_i32 %4, %6, 0x10005\n\ts_bfe_i32 %5, %7, 0x10005\n\ts_xor_b32 %4, %4, 0xffff\n\ts_xor_b32 %5, %5, 0xffff"
                 : "=&s"(mq[0]), "=&s"(mq[1]), "=&s"(mq[2]), "=&s"(mq[3]), "=&s"(mq[4]), "=&s"(mq[5]) : "s"(l1), "s"(l2) : "scc");
    const u64 m0 = mk64(mq[1], mq[0]), m1 = mk64(mq[3], mq[2]), m2 = mk64(mq[5], mq[4]);
    asm volatile("s_waitcnt lgkmcnt(0)" : W8(ha), "+v"(fg)::"memory");
    TS(2);
    if (b < NPART) {
#define HBAD ((fg[0] & fg[1] & fg[2] & fg[3]) != one)
      if (__any(HBAD)) POLL2(HBAD, 0, 1 << 26, HSLD("\n\ts_waitcnt lgkmcnt(0)"));
      TS(23);
      f32x4 dg = {0.f, 0.f, 0.f, 0.f}, du = {0.f, 0.f, 0.f, 0.f};
#pragma unroll
      for (int f = 0; f < 8; ++f) {
        dg = __builtin_amdgcn_mfma_f32_16x16x32_bf16(BF8(ha[f]), BF8(gT[f]), dg, 0, 0, 0);
        du = __builtin_amdgcn_mfma_f32_16x16x32_bf16(BF8(ha[f]), BF8(uT[f]), du, 0, 0, 0);
      }
      const bool s0 = __builtin_amdgcn_inverse_ballot_w64(m0), s1 = __builtin_amdgcn_inverse_ballot_w64(m1);
      if (__builtin_amdgcn_inverse_ballot_w64(m2))
        asm volatile("global_store_dword %0, %1, %3 sc1 ; ALWAYS\n\tglobal_store_dword %0, %2, %3 offset:512 sc1" ::"v"(gso),
                     "v"(nsent(s1 ? (s0 ? dg[3] : dg[2]) : (s0 ? dg[1] : dg[0]))), "v"(nsent(s1 ? (s0 ? du[3] : du[2]) : (s0 ? du[1] : du[0]))), "s"(wb) : "memory");
    }
    if (hw == 0 && wb == ws) asm volatile("ds_write_b64 %0, %1" ::"v"(sfa), "v"(u32x2{t1, t2}) : "memory");
    if (b < NPART) {
      TS(24);
      asm volatile("" : W8(wdv)::"memory");
      TR8(dT, wdv);
      P2PUT(0);
      if (hw == 0) {
        u32x2 rr[8];
        if (E_D) __builtin_amdgcn_s_sleep(E_D);
        P2GET(((((lane & 16) ? gl1 : gl0) - O_GU) >> 12) * 128 + (lane & 15) * 8);
#define MX2(i) max(max(rr[i][0], rr[i][1]), max(rr[i + 1][0], rr[i + 1][1]))
        POLL2(max(max(MX2(0), MX2(2)), max(MX2(4), MX2(6))) == SENT, 1, PCAP,
              asm volatile("global_load_dwordx2 %0, %8, %10 sc1\n\tglobal_load_dwordx2 %1, %8, %10 offset:1024 sc1\n\t"
                           "global_load_dwordx2 %2, %8, %10 offset:2048 sc1\n\tglobal_load_dwordx2 %3, %8, %10 offset:3072 sc1\n\t"
                           "global_load_dwordx2 %4, %9, %10 sc1\n\tglobal_load_dwordx2 %5, %9, %10 offset:1024 sc1\n\t"
                           "global_load_dwordx2 %6, %9, %10 offset:2048 sc1\n\tglobal_load_dwordx2 %7, %9, %10 offset:3072 sc1" VW0
                           : "=&v"(rr[0]), "=&v"(rr[1]), "=&v"(rr[2]), "=&v"(rr[3]), "=&v"(rr[4]), "=&v"(rr[5]), "=&v"(rr[6]), "=&v"(rr[7])
                           : "v"(gl0), "v"(gl1), "s"(wb)
                           : "memory"));
        TS(8);
#define R2(i) __builtin_bit_cast(f32x2, rr[i])
        const f32x2 gs = (R2(0) + R2(1)) + (R2(2) + R2(3)), us = (R2(4) + R2(5)) + (R2(6) + R2(7));
        const unsigned gp = pack2(gs[0], gs[1]), up = pack2(us[0], us[1]);
        const unsigned sp = pack2(bflo(gp) * __builtin_amdgcn_rcpf(1.0f + __expf(-bflo(gp))), bfhi(gp) * __builtin_amdgcn_rcpf(1.0f + __expf(-bfhi(gp))));
        asm volatile("ds_write_b32 %0, %1\n\tds_write_b32 %2, %3 offset:24" ::"v"(awa), "v"(pack2(bflo(sp) * bflo(up), bfhi(sp) * bfhi(up))), "v"(sfa), "v"(one) : "memory");
      }
      TS(25);
      SKEWPT();
      POLL2(sv != one, 0, 1 << 26, asm volatile("ds_read_b32 %1, %3 offset:24\n\tds_read_b128 %0, %2\n\ts_waitcnt lgkmcnt(0)" : "=&v"(fg), "=&v"(sv) : "v"(ara), "v"(sfa) : "memory"));
      if (VD_E == 1) asm volatile("global_load_dwordx2 %0, %1, %2 sc1" : "=&v"(vd) : "v"(O_VD + (b & 31) * 128), "s"(ws) : "memory");
      const f32x2 dd = MV64(dT, fg);
      asm volatile("global_store_dwordx2 %0, %1, %2 sc1" ::"v"(pso), "v"(u32x2{nsent(dd[0]), nsent(dd[1])}), "s"(wb) : "memory");
      TS(6);
      P2PUT(1);
      if (VD_E == 1) {
        asm volatile("s_waitcnt vmcnt(1)" : "+v"(vd)::"memory");
        if (__any(max(vd[0], vd[1]) == SENT)) VDLD(2);
      } else if (VD_E == 2) {
        if (hw == 1) {
          nap_tick(th + T_C5);
          VDLD(2);
          asm volatile("ds_write_b64 %0, %1 offset:8" ::"v"(sfa), "v"(vd) : "memory");
        } else {
          POLL2(vd[1] == 0u, 2, 1 << 26, asm volatile("ds_read_b64 %0, %1 offset:8\n\ts_waitcnt lgkmcnt(0)" : "=v"(vd) : "v"(sfa) : "memory"));
        }
      } else if (VD_E == 3) {
        break;
      } else {
        nap_tick(th + T_C5);
        VDLD(2);
      }
      TS(5);
    } else if (hw == 3) {
      u32x2 g0, g1;
      nap_tick(th + T_C4);
      POLL2(min(min(g0[0], g0[1]), min(g1[0], g1[1])) == 0u, 2, PCAP,
            asm volatile("global_load_dwordx2 %0, %2, %4 sc1\n\tglobal_load_dwordx2 %1, %3, %4 sc1" VW0
                         : "=&v"(g0), "=&v"(g1) : "v"(O_KEY + lane * 8 * KSTR), "v"(O_KEY + (lane + 64) * 8 * KSTR), "s"(ws) : "memory"));
      const unsigned k1 = t1, k2 = t2;
      t1 = max(g0[0], g1[0]), t2 = max(min(g0[0], g1[0]), max(g0[1], g1[1]));
      TOP2(0xB1); TOP2(0x4E); TOP2(0x141); TOP2(0x140);
      TOP2S(__builtin_amdgcn_permlane16_swap);
      TOP2S(__builtin_amdgcn_permlane32_swap);
      vd = u32x2{max(t1, k1), max(min(t1, k1), max(t2, k2))};
      if (lane == 0) asm volatile("global_store_dwordx2 %0, %1, %2 sc1\n\tds_write_b64 %3, %1 offset:8" ::"v"(O_VD + (b & 31) * 128), "v"(vd), "s"(ws), "v"(sfa) : "memory");
      TS(29);
      break;
    } else if (hw < 2) {
      const int tok = b & 1, c0 = ((b - NPART) >> 1) * 64 + hw * 32 + (lane & 7) * 4, col = c0 + g4;
      const unsigned td = tick(), kk = tok ? t2 : t1;
      const int ln = tok ? l2 : l1;
      unsigned v;
      POLL2(v == SENT, 1, PCAP, asm volatile("global_load_dword %0, %1, %2 sc1" VW0 : "=&v"(v) : "v"(O_X1C + ((__builtin_amdgcn_readlane(sto, ln) - O_ST2) >> 7) * 2048 + (col >> 1) * 4), "s"(ws) : "memory"));
      float x1 = (col & 1) ? bfhi(v) : bflo(v);
      float h2 = rbf(((x1 - __int_as_float(__builtin_amdgcn_readlane(__float_as_int(mean), ln))) * __int_as_float(__builtin_amdgcn_readlane(__float_as_int(rstd), ln))) * bf2f(svb[2][col]) + bf2f(svb[1][col]));
      float gb = bf2f(svb[3][col]), rsg = rbf(1.0f / (1.0f + __expf(-key_val(kk))));
      unsigned oo_ = (key_row(kk) * D + col) * 2, po = O_PART + ((lane >> 3) * 2048 + tok * 1024 + c0) * 4, p1 = po + 4 * 65536;
      asm volatile("" : "+v"(x1), "+v"(h2), "+v"(gb), "+v"(rsg), "+v"(oo_), "+v"(po), "+v"(p1));
      u32x4 y[7];
      nap_tick(td + F_T);
      P2GET(8192 + (min(lane, 55) * 16 + ((b - NPART) >> 1)) * 8);
      POLL2(max(max(max(max4(y[0]), max4(y[1])), max(max4(y[2]), max4(y[3]))), max(max(max4(y[4]), max4(y[5])), max4(y[6]))) == SENT || vd[1] == 0u, OPT_RR, PCAP,
            asm volatile("global_load_dwordx4 %0, %7, %9 sc1\n\tglobal_load_dwordx4 %1, %7, %10 sc1\n\tglobal_load_dwordx4 %2, %7, %11 sc1\n\t"
                         "global_load_dwordx4 %3, %7, %12 sc1\n\tglobal_load_dwordx4 %4, %8, %9 sc1\n\tglobal_load_dwordx4 %5, %8, %10 sc1\n\t"
                         "global_load_dwordx4 %6, %8, %11 sc1"
                         : "=&v"(y[0]), "=&v"(y[1]), "=&v"(y[2]), "=&v"(y[3]), "=&v"(y[4]), "=&v"(y[5]), "=&v"(y[6])
                         : "v"(po), "v"(p1), "s"(wb), "s"(wb + 65536), "s"(wb + 131072), "s"(wb + 196608)
                         : "memory");
            asm volatile("s_waitcnt vmcnt(2)\n\tds_read_b64 %7, %8 offset:8\n\ts_waitcnt vmcnt(0) lgkmcnt(0)"
                         : "+v"(y[0]), "+v"(y[1]), "+v"(y[2]), "+v"(y[3]), "+v"(y[4]), "+v"(y[5]), "+v"(y[6]), "=&v"(vd) : "v"(sfa) : "memory"));
      TS(28);
#define YF(i) __builtin_bit_cast(f32x4, y[i])
      const f32x4 t = ((YF(0) + YF(1)) + (YF(2) + YF(3))) + ((YF(4) + YF(5)) + YF(6));
      const float sum = dpp_add<0x128>(swap16_add(swap32_add(t[0], t[2]), swap32_add(t[1], t[3])));
      asm volatile("global_store_short %0, %1, %2" ::"v"(oo_), "v"(pack2(x1 + gb * (wb == ws && kk < vd[1] ? h2 : rbf(h2 + rbf(rbf(sum) * rsg))), 0.f)), "s"(out) : "memory");
      TS(15);
    } else {
      break;
    }
    if (__builtin_expect(wb != ws || __all(vd[1] == t2), 1)) break;
    const unsigned tk = (lane & 8) ? vd[1] : vd[0], ns = tk == t1 ? l1 >> 3 : 8 + ((lane >> 3) & 1);
    rk2 = tk & 0xffffu, kmk = lane < 16 ? ~0u : 0u, one = 3u;
    xo_ = O_X1C + ns * 2048 + kc * 2, sto = O_ST2 + ns * 128 + (lane & 7) * 16;
    wb = ws + O_P2;
    }
    TSE();
  }
}

extern "C" void kernel_launch(void* const* d_in, const int* in_sizes, int n_in, void* d_out, int out_size, void* d_ws, size_t ws_size,
                              hipStream_t stream) {
  const u16* x = (const u16*)d_in[0];
  const u16* wsr = (const u16*)d_in[1];
  const u16* wsi = (const u16*)d_in[2];
  const u16* whr = (const u16*)d_in[3];
  const u16* whi = (const u16*)d_in[4];
  const u16* w_r = (const u16*)d_in[6];
  const u16* w_gate = (const u16*)d_in[7];
  const u16* w_up = (const u16*)d_in[8];
  const u16* w_down = (const u16*)d_in[9];
  const u16* c = (const u16*)d_in[10];
  const u16* w_mod = (const u16*)d_in[11];
  u16* out = (u16*)d_out;
  k_main<<<256, 512, 0, stream>>>(x, (char*)d_ws, wsr, wsi, c, w_mod, whr, whi, w_r, w_gate, w_up, w_down, out);
}
```

```cpp
#include <hip/hip_runtime.h>
#include <hip/hip_bf16.h>
#include <stdint.h>
#include <utility>

#define NEWLAY 1
typedef unsigned short u16;
typedef unsigned long long u64;
using bf16x8 = __attribute__((ext_vector_type(8))) short;
using bf16v8 = __attribute__((ext_vector_type(8))) __bf16;
using f32x4 = __attribute__((ext_vector_type(4))) float;
using f32x2 = __attribute__((ext_vector_type(2))) float;
using u32x4 = __attribute__((ext_vector_type(4))) unsigned;
using u32x2 = __attribute__((ext_vector_type(2))) unsigned;

constexpr int S = 2048, D = 1024, F = 3584;

__device__ __forceinline__ float bf2f(unsigned v) { return __uint_as_float(v << 16); }
__device__ __forceinline__ float bflo(unsigned v) { return __uint_as_float(v << 16); }
__device__ __forceinline__ float bfhi(unsigned v) { return __uint_as_float(v & 0xffff0000u); }
__device__ __forceinline__ unsigned f2bf(float f) {
  __hip_bfloat16 b = __float2bfloat16(f);
  return (unsigned)*reinterpret_cast<u16*>(&b);
}
__device__ __forceinline__ float rbf(float f) { return bf2f(f2bf(f)); }
using bf16v2 = __attribute__((ext_vector_type(2))) __bf16;
__device__ __forceinline__ unsigned pack2(float lo, float hi) {
  return __builtin_bit_cast(unsigned, __builtin_convertvector(f32x2{lo, hi}, bf16v2));
}

template <int CTRL>
__device__ __forceinline__ float dpp_f(float v) {
  return __int_as_float(__builtin_amdgcn_mov_dpp(__float_as_int(v), CTRL, 0xf, 0xf, true));
}
template <int CTRL>
__device__ __forceinline__ float dpp_add(float v) { return v + dpp_f<CTRL>(v); }
__device__ __forceinline__ float row_sum(float v) { return dpp_add<0x140>(dpp_add<0x141>(dpp_add<0x4E>(dpp_add<0xB1>(v)))); }
template <int CTRL>
__device__ __forceinline__ unsigned dpp_u(unsigned v) { return (unsigned)__builtin_amdgcn_mov_dpp((int)v, CTRL, 0xf, 0xf, true); }
__device__ __forceinline__ u64 mk64(unsigned hi, unsigned lo) { return ((u64)hi << 32) | lo; }
__device__ __forceinline__ float swap32_add(float a, float b) {
  auto r = __builtin_amdgcn_permlane32_swap(__float_as_uint(a), __float_as_uint(b), false, false);
  return __uint_as_float(r[0]) + __uint_as_float(r[1]);
}
__device__ __forceinline__ float swap16_add(float a, float b) {
  auto r = __builtin_amdgcn_permlane16_swap(__float_as_uint(a), __float_as_uint(b), false, false);
  return __uint_as_float(r[0]) + __uint_as_float(r[1]);
}
__device__ __forceinline__ float ar4(float v) {
  const float t = swap32_add(v, v);
  return swap16_add(t, t);
}
__device__ __forceinline__ float wave_sum_s(float v) {
  v = row_sum(v);
  v += __int_as_float(__builtin_amdgcn_update_dpp(0, __float_as_int(v), 0x142, 0xa, 0xf, false));
  v += __int_as_float(__builtin_amdgcn_update_dpp(0, __float_as_int(v), 0x143, 0xc, 0xf, false));
  return __int_as_float(__builtin_amdgcn_readlane(__float_as_int(v), 63));
}
__device__ __forceinline__ float silu_bf(float c) { return rbf(c * __builtin_amdgcn_rcpf(1.0f + __expf(-c))); }
__device__ __forceinline__ float dsum(unsigned v, float a) {
  return __builtin_amdgcn_fdot2_f32_bf16(__builtin_bit_cast(bf16v2, v), __builtin_bit_cast(bf16v2, 0x3f803f80u), a, false);
}
template <class Fn, int... I>
__device__ __forceinline__ void sfor_(Fn&& f, std::integer_sequence<int, I...>) { (f(std::integral_constant<int, I>{}), ...); }
template <int N, class Fn>
__device__ __forceinline__ void sfor(Fn&& f) { sfor_(f, std::make_integer_sequence<int, N>{}); }
constexpr unsigned SENT = 0xFFFFFFFFu;
constexpr int NPART = 224;
__device__ __forceinline__ f32x2 unpk(unsigned v) { return f32x2{bflo(v), bfhi(v)}; }

constexpr size_t WS_EPO = 0;
constexpr size_t WS_XFL = 4096;
constexpr size_t WS_MODP = 40960;
constexpr size_t WS_KEYG = 320u << 10;
constexpr size_t WS_UFL = WS_KEYG + 16384;
constexpr size_t WS_MOD = WS_KEYG + (80u << 10);
constexpr size_t WS_RS = WS_KEYG + (92u << 10);
constexpr size_t WS_PART = WS_KEYG + (316u << 10);
constexpr unsigned O_MODP = WS_MODP, O_XFL = WS_XFL, O_KEY = WS_KEYG, O_MOD = WS_MOD, O_HDR = WS_MOD + 8192, O_RS = WS_RS, O_M2 = WS_RS + 80 * 1024;
constexpr unsigned O_PART = WS_PART, O_GU = O_PART + 56 * 8192, O_STAT = O_GU + 224 * 1024, O_X1C = O_STAT + 262144;
#define D_R 1
constexpr unsigned O_ST2 = O_X1C + 20480, X_CS = 22528, O_VD = O_X1C + D_R * X_CS, O_P2 = 1536u << 10, O_F2 = 3584u << 10;
constexpr unsigned O_GC = O_M2 + 8704, O_VP = O_M2 + 12288, O_NP = O_VP + 65536, O_K2 = O_NP + 65536, O_G2 = 3u << 20, O_G1 = O_G2 + 262144;
static_assert(O_K2 + 4096 == O_PART && O_VD + 4096 <= O_KEY + (512 + 896) * 1024 && O_PART + O_P2 >= O_VD + 4096 && O_STAT + O_P2 <= O_G2, "armed area");
constexpr size_t WS_H = 4u << 20;
constexpr size_t WS_T = WS_H + (size_t)S * D * 2;
constexpr size_t WS_P = WS_T + (size_t)2 * D * S * 2;

#define TSG(base, i)
#define TSD(i)
#define PCAP (1 << 20)
#define PDBG(it)
#define TS(i)
#define TSE()
#define BT(k)
#define BTE()
#define BAR(k) do { BT(k); BARRIER(); } while (0)
__device__ __forceinline__ void st16_wt(void* p, u32x4 v) {
  asm volatile("global_store_dwordx4 %0, %1, off sc1\n\ts_nop 1" ::"v"(p), "v"(v) : "memory");
}
#define WAIT_V(n) asm volatile("s_waitcnt vmcnt(%0)" ::"n"(n) : "memory")
#define WAIT_L(n) asm volatile("s_waitcnt lgkmcnt(%0)" ::"n"(n) : "memory")
#define BARRIER() asm volatile("s_barrier" ::: "memory")
#define LBAR() asm volatile("s_waitcnt lgkmcnt(0)\n\ts_barrier" ::: "memory")
#define DSR(dst, addr, off) asm volatile("ds_read_b128 %0, %1 offset:%2" : "=v"(dst) : "v"(addr), "n"(off) : "memory")
#define MFMA(c, a, b) asm volatile("v_mfma_f32_16x16x32_bf16 %0, %1, %2, %0" : "+v"(c) : "v"(a), "v"(b))
#define MFMA0(c, a, b) asm volatile("v_mfma_f32_16x16x32_bf16 %0, %1, %2, 0" : "=&v"(c) : "v"(a), "v"(b))
#define GLDS(m0v, voff, gbase) \
  asm volatile("s_mov_b32 m0, %0\n\ts_nop 0\n\tglobal_load_lds_dwordx4 %1, %2" ::"s"(m0v), "v"(voff), "s"(gbase) : "memory")
#define GLDS4(m0v, v, gbase)                                                                                                        \
  asm volatile("s_mov_b32 m0, %0\n\ts_nop 0\n\tglobal_load_lds_dwordx4 %1, %5\n\tglobal_load_lds_dwordx4 %2, %5 offset:1024\n\t"    \
               "global_load_lds_dwordx4 %3, %5 offset:2048\n\tglobal_load_lds_dwordx4 %4, %5 offset:3072" ::"s"(m0v),               \
               "v"((v)[0]), "v"((v)[1]), "v"((v)[2]), "v"((v)[3]), "s"(gbase) : "memory")
#define LDG(dst, voff, base) asm volatile("global_load_dwordx4 %0, %1, %2" : "=v"(dst) : "v"(voff), "s"(base) : "memory")
#define WPOL " nt"
#define LDM(dst, voff, base) asm volatile("global_load_dwordx4 %0, %1, %2" WPOL : "=v"(dst) : "v"(voff), "s"(base) : "memory")
#define LDX(dst, voff, base, off) asm volatile("global_load_dwordx4 %0, %1, %2 offset:%3" : "=v"(dst) : "v"(voff), "s"(base), "n"(off) : "memory")
#define LDXS(dst, voff, base, off) asm volatile("global_load_dwordx4 %0, %1, %2 offset:%3 sc1" : "=v"(dst) : "v"(voff), "s"(base), "n"(off) : "memory")
#define BF8(v) __builtin_bit_cast(bf16v8, v)
#define LSPIN(v, p, cond)                                   \
  _Pragma("unroll 1") for (int it_ = 0; it_ < (1 << 26); ++it_) { \
    v = __atomic_load_n(p, __ATOMIC_RELAXED);               \
    if (cond) break;                                        \
    __builtin_amdgcn_s_sleep(1);                            \
  }
#define POLL(ok, sl, reload)                                                  \
  if (__builtin_expect(!(ok), 0)) {                                           \
    _Pragma("unroll 1") for (int it = 0; it < PCAP; ++it) {                   \
      PDBG(it);                                                               \
      __builtin_amdgcn_s_sleep(sl);                                           \
      reload;                                                                 \
      if (ok) break;                                                          \
    }                                                                         \
  }
#define POLL2(bad, sl, cap, load)                                             \
  {                                                                           \
    int it = (cap);                                                           \
    u64 live;                                                                 \
    _Pragma("unroll 1") do {                                                  \
      if (sl) __builtin_amdgcn_s_sleep(sl);                                   \
      asm volatile("s_sub_u32 %0, %0, 1\n\ts_cselect_b64 %1, 0, -1" : "+s"(it), "=s"(live)::"scc"); \
      load;                                                                   \
    } while (__builtin_amdgcn_ballot_w64(bad) & live);                        \
    PDBG(PCAP - 2 - it);                                                      \
  }
__device__ __forceinline__ unsigned tick() {
  u64 t;
  asm volatile("s_memrealtime %0\n\ts_waitcnt lgkmcnt(0)" : "=s"(t)::"memory");
  return (unsigned)t;
}
__device__ __forceinline__ void wait_tick(unsigned t) {
#pragma unroll 1
  for (int i = 0; i < 256; ++i)
    if ((int)(tick() - t) >= 0) break;
}
__device__ __forceinline__ void nap_tick(unsigned t) {
#pragma unroll 1
  for (int i = 0; i < 1024; ++i) {
    if ((int)(tick() - t) >= 0) break;
    __builtin_amdgcn_s_sleep(2);
  }
}
#define T_C1 245
#define T_A 40
#define T_N 2500
#define T_D1 78
#define T_C2 322
#define VW0 "\n\ts_waitcnt vmcnt(0)"
#define W8(a) "+v"(a[0]), "+v"(a[1]), "+v"(a[2]), "+v"(a[3]), "+v"(a[4]), "+v"(a[5]), "+v"(a[6]), "+v"(a[7])
#define PFL(ptr) asm volatile("s_mov_b32 m0, %0\n\ts_nop 0\n\tglobal_load_lds_dword %1, off" ::"s"(pfl), "v"(ptr) : "memory")

#define HHOOK(j)
#define XPV 1557
#define XPW 1557
#define XPM (MODE ? XPW : XPV)
#define XPA0 (XPM / 1000)
#define XPA1 (XPM / 100 % 10)
#define XPB0 (XPM / 10 % 10)
#define XPB1 (XPM % 10)
#define U_X0 6
#define XLD(dst, XP, XH, i)                                                                                   \
  do {                                                                                                        \
    if (MODE) LDXS(dst, (i) ? xo1 : xo0, XP, ((XH)&3) * 2048 - 4096);                                         \
    else LDXS(dst, ((XH)&4) ? xo1 : xo0, XP, ((XH)&3) * 2048 + (i)*1024 - 4096);                              \
  } while (0)
#define HALF(XC, YA, RD, XN, XP, XH, LD, YP)                                                 \
  do {                                                                                       \
    _Pragma("unroll") for (int j = 0; j < 8; ++j) {                                          \
      if ((j & 1) == 0) { if (RD) WAIT_L(5); else WAIT_L(j == 0 ? 5 : 6 - j); }              \
      if (MODE == 2) MFMA0(acc[0][j], Y[j], XC[0]); else if (MODE) MFMA(acc[0][j], Y[j], XC[0]); else MFMA(acc[0][j], XC[0], Y[j]); \
      if (j == 0) DSR(Y[7], YP, 7 * 2048);                                                   \
      else if (RD) DSR(Y[j - 1], YA, (j - 1) * 2048);                                        \
      if (MODE == 2) MFMA0(acc[1][j], Y[j], XC[1]); else if (MODE) MFMA(acc[1][j], Y[j], XC[1]); else MFMA(acc[1][j], XC[1], Y[j]); \
      if ((LD) && j == (((XH)&1) ? XPA0 : XPB0)) XLD(XN[0], XP, XH, 0);                                             \
      if ((LD) && j == (((XH)&1) ? XPA1 : XPB1)) XLD(XN[1], XP, XH, 1);                                             \
      HHOOK(j);                                                                              \
    }                                                                                        \
  } while (0)

__device__ __forceinline__ int key_row(unsigned k) { return (int)((~k >> 1) & 2047u); }
__device__ __forceinline__ unsigned mkkey(float v) {
  const unsigned rb = pack2(0.f, v);
  return rb ^ ((unsigned)((int)rb >> 31) & 0x7fff0000u) ^ 0x80000000u;
}
__device__ __forceinline__ float key_val(unsigned k) {
  const unsigned ord = k >> 16;
  return bf2f(ord ^ ((ord & 0x8000u) ? 0x8000u : 0xffffu));
}
#define OPT_RD 22
#define OPT_RE 20
#define OPT_GD 2
__device__ __forceinline__ unsigned nsent(float v) { return min(__float_as_uint(v), SENT - 1u); }
__device__ __forceinline__ f32x4 lo4(u32x4 q) { return f32x4{bflo(q[0]), bfhi(q[0]), bflo(q[1]), bfhi(q[1])}; }
__device__ __forceinline__ f32x4 hi4(u32x4 q) { return f32x4{bflo(q[2]), bfhi(q[2]), bflo(q[3]), bfhi(q[3])}; }
__device__ __forceinline__ u32x4 pk8(f32x4 a, f32x4 b) { return u32x4{pack2(a[0], a[1]), pack2(a[2], a[3]), pack2(b[0], b[1]), pack2(b[2], b[3])}; }
__device__ __forceinline__ u32x4 nsent4(u32x4 v) { return u32x4{min(v[0], SENT - 1u), min(v[1], SENT - 1u), min(v[2], SENT - 1u), min(v[3], SENT - 1u)}; }
__device__ __forceinline__ unsigned max4(u32x4 v) { return max(max(v[0], v[1]), max(v[2], v[3])); }
__device__ __forceinline__ float amin4(u32x4 v, float m) {
  asm("v_min3_f32 %0, |%1|, |%2|, %0\n\tv_min3_f32 %0, |%3|, |%4|, %0" : "+v"(m) : "v"(v[0]), "v"(v[1]), "v"(v[2]), "v"(v[3]));
  return m;
}
__device__ __forceinline__ u32x4 selu(unsigned mk, u32x4 a, u32x4 b) { return (a & mk) | (b & ~mk); }
#define KSTR 16
#define TOP2(CTRL)                                                  \
  do {                                                              \
    const unsigned o1 = dpp_u<CTRL>(t1), o2 = dpp_u<CTRL>(t2);      \
    t2 = max(min(t1, o1), max(t2, o2));                             \
    t1 = max(t1, o1);                                               \
  } while (0)
#define TOP2S(SW)                                                   \
  do {                                                              \
    auto a_ = SW(t1, t1, false, false), b_ = SW(t2, t2, false, false); \
    t2 = max(min(a_[0], a_[1]), max(b_[0], b_[1]));                 \
    t1 = max(a_[0], a_[1]);                                         \
  } while (0)
#define T_C3 253
#define T_C4 340
#define T_G 9
#define T_U 10
#define E_D 6
#define O_DW 6
#define O_DS 4
#define O_TR 1
#define OPOL " sc1"
#define L_PR 1
#define D_W 1
#define X_PA 2
#define G1_C 2
#define R_AT 24
#define B_TK 8
#define W_T 4
#define V_K 3
#define V_T 5
#define N_K 10
#define N_D 2
#define T_K 0
#define H_AT 23
#define G1_S 1
#define V_F 0
#define J_L 0
#define F_T 142
#define K_S 1
#define K_R 1
#define T_C5 470
#define VD_E 1
#define K_E 1
#define Q_L 0
#define N_C (V_T + 1)
#define N_P 2
#define R_S 0
#define N_S 0
#define T_O 252
#define L_H 0
#define A_W 0
#define A_R 1
#define PF23 0
#define P_D5 2
#define S_N 5
#define S_D 2
#define J_D 0
#define W_L 0
#define W_D 2
#define D_S2 1
#define D_G 1
#define R_N 0
#define R_D 8
#define R_B 0
#define R_M 0
#define W_N 0
#define W_G 0
#define T_H 60
#define H_P 2
#define T_X 80
#define D_S 1
#define A_PW 8
#define A_PS 2
#define U_S 4
#define C_LD 2
#define C_DP 8
#define F_D 2
#define P_AT 2
#define F_AT 29
#define XQ_N 6
#define RD_E 12
#define PW0 2
#define PW1 1
#define T_W 70
#define P_D2 1
#define P_D4 1
#define XSTR 16
#define P_D3 20
#ifndef M_NS
#define M_NS 6
#endif
#define A_Q1 27
#define B_RAT 7
#define B_MAT 13
#define U_D 4
#define FSTR 16
#define B_J 4
#define RETIRE()                                                                                                                  \
  do {                                                                                                                            \
    if (hw == 3 && lane == 0) {                                                                                                   \
      __hip_atomic_store(reinterpret_cast<u64*>(xfl + b * XSTR), ~tag, __ATOMIC_RELAXED, __HIP_MEMORY_SCOPE_AGENT);               \
      if (b == 255) __hip_atomic_store(reinterpret_cast<u64*>(epo), tag, __ATOMIC_RELAXED, __HIP_MEMORY_SCOPE_AGENT);             \
    }                                                                                                                             \
  } while (0)
#define U_N1 3
#define OPT_RR 2
#define B_QAT 8
#define B_QD 4
#define TP_PS 5
#define TP_XC (4 * TP_PS)
#define TP_P0 2
#define TP_X0 24
#define TP_XL 58
#define W_UO 3
#define CPF_K 24
#define CPF_B 8
#define B_WN 4
#define B_WK 15
struct BSched {
  int cnt[16], rcnt, mcnt, qcnt, vcnt, ncnt;
  constexpr BSched() : cnt{}, rcnt(0), mcnt(0), qcnt(0), vcnt(0), ncnt(0) {
    int pos[16] = {}, n = 0, rpos = 0, mpos = 0, qpos = 0, npos = 0;
    for (int k = 0; k <= B_J - 2 + M_NS; ++k) pos[k] = -8 * (B_J - 2 + M_NS - k);
    for (int k = B_J + 1; k < 16; ++k) {
      cnt[k] = n - pos[k] > 63 ? 63 : n - pos[k];
      if (k - 2 + M_NS < 16) { n += 4; pos[k - 2 + M_NS] = n; }
      if (k == B_RAT + 2) rcnt = n - rpos;
      if (k == B_QAT + B_QD) qcnt = n - qpos;
      if (k == V_T) vcnt = n;
      if (k == N_K + N_D) ncnt = n - npos;
      if (k == N_K) npos = n;
      if (k == B_RAT) { n += 2; rpos = n; }
      if (k == B_QAT) { n += 3; qpos = n; }
      if (k == B_MAT) { n += 3; mpos = n; }
      if (k == B_WK) n += B_WN;
    }
    mcnt = n - mpos > 63 ? 63 : n - mpos;
  }
};
__global__ __launch_bounds__(512, 2) void k_main(const u16* __restrict__ x, char* ws, const u16* __restrict__ Y0, const u16* __restrict__ Y1,
                                              const u16* __restrict__ cvec, const u16* __restrict__ wmod, const u16* __restrict__ X0,
                                              const u16* __restrict__ X1, const u16* __restrict__ wr_, const u16* __restrict__ wg,
                                              const u16* __restrict__ wu, const u16* __restrict__ wd, u16* __restrict__ out) {
  constexpr int DX = 4, NS = M_NS, STG = NS * 16384, SPARE = STG + 32768;
  constexpr int LST = SPARE + 17664, SLT = LST + 256, XC = SLT + 512, VT = XC + 4096;
  __shared__ __attribute__((aligned(1024))) char shm[VT + 256];
  u16(*svb)[1024] = reinterpret_cast<u16(*)[1024]>(shm + SPARE);
  float* cwj = reinterpret_cast<float*>(shm + SPARE + 10240);
  unsigned* ssel = reinterpret_cast<unsigned*>(shm + SPARE + 10368);
  u16(*acts)[64] = reinterpret_cast<u16(*)[64]>(shm + SPARE + 15872);
  float* smod = reinterpret_cast<float*>(shm + SPARE + 15872);
  u16* ssa = reinterpret_cast<u16*>(smod);
  constexpr int TAB = SPARE + 10752, RKA = TAB + 1536, RKB = TAB + 3584;
  u16(*scw)[128] = reinterpret_cast<u16(*)[128]>(shm + TAB + 512);
  static_assert(42 % NS == 0, "slots 0 and 1 are free behind GEMM_B barrier 13");
  const int b = blockIdx.x, xcd = b & 7, j8 = b >> 3;
  const int cc = xcd >> 2, nt = (xcd & 3) * 4 + (j8 & 3), mt = j8 >> 2, et = mt;
  const int tid = threadIdx.x, lane = tid & 63;
  const int wid = __builtin_amdgcn_readfirstlane(tid >> 6);
  const unsigned lds0 = (unsigned)(size_t)(__attribute__((address_space(3))) char*)shm;
  const char* cpf;
  {
    unsigned long long pc;
    unsigned len;
    asm volatile("s_getpc_b64 %0\n.Lcpf_here:\n\ts_mov_b32 %1, .Lcpf_end-.Lcpf_here-4\n\t.subsection 1\n.Lcpf_end:\n\t.subsection 0" : "=s"(pc), "=s"(len));
    cpf = reinterpret_cast<const char*>(pc) + min((unsigned)((lane * 32 + j8) * 128), len);
  }
  const unsigned pfl = lds0 + SPARE + 17408;
  if (wid == 4 && lane < 12) PFL(cpf);
  u32x4 xv[2];
  {
    const unsigned xo = (unsigned)((b * 8 + wid) * D + lane * 8) * 2;
    asm volatile("global_load_dwordx4 %0, %2, %3\n\tglobal_load_dwordx4 %1, %2, %3 offset:1024" : "=&v"(xv[0]), "=&v"(xv[1]) : "v"(xo), "s"(x) : "memory");
  }
  asm volatile("" ::"s"(ws), "s"(Y0), "s"(Y1), "s"(cvec), "s"(wmod), "s"(wr_));
  u16* xn = reinterpret_cast<u16*>(ws + WS_H);
  u16* U = reinterpret_cast<u16*>(ws + WS_T);
  unsigned* part = reinterpret_cast<unsigned*>(ws + WS_PART);
  const int g = (b & 7) * 28 + (b >> 3);
  unsigned* ufl = reinterpret_cast<unsigned*>(ws + WS_UFL) + (cc * 16 + nt) * 32 * FSTR;
  unsigned* xfl = reinterpret_cast<unsigned*>(ws + WS_XFL);
  unsigned* epo = reinterpret_cast<unsigned*>(ws + WS_EPO);
#define SKEWPT()
  const int g4 = lane >> 4;
  u64 tag0, tag;
#define TAGW(n) asm volatile("s_waitcnt vmcnt(%2)\n\tv_lshl_add_u64 %0, %1, 0, 1" : "=v"(tag) : "v"(tag0), "n"(n) : "memory")
  u32x4 wv[8], cq;
  const char* pfy = reinterpret_cast<const char*>(cc ? Y1 : Y0) + (size_t)((xcd & 3) * 512 + j8 * 16 + (lane & 15)) * 4096 + g4 * 128;
#define PFG(g) (pfy + ((((g) + xcd) & 7) << 9))
  const int u = b * 2 + (wid & 1), cg0 = u & 31, ks0 = u >> 5;
  unsigned ta = lds0 + STG + ((lane >> 2) & 3) * 2368 + (wid * 128 + (lane >> 5) * 32 + (lane & 3) * 8 + (g4 & 1) * 4) * 2;
  unsigned oo = (unsigned)((wid * 4 + (lane >> 5)) * 131072 + (b >> 2) * 2048 + (g4 & 1) * 1024 + ((b & 3) * 16 + (lane & 15)) * 16), oo2 = oo + 262144;
  asm volatile("" : "+v"(ta), "+v"(oo), "+v"(oo2));
  unsigned wa = lds0 + STG + wid * 2368 + lane * 16;
  asm volatile("" : "+v"(wa));
  const unsigned wo = (unsigned)((ks0 * 64 + (lane >> 3) * 8) * 6144 + 2048 + (cg0 >> 4) * 1024 + cg0 * 64 + (lane & 7) * 8) * 2;
  if (wid == 0) TSG(part, 0);
  BT(50);
  if (wid >= 3) {
    asm volatile("global_load_dwordx2 %0, %1, off sc1" : "=v"(tag0) : "v"(epo) : "memory");
    if (wid >= 6) {
      LDG(cq, (ks0 * 64 + g4 * 16 + (lane & 1) * 8) * 2, cvec);
      if (W_L == 2) __builtin_amdgcn_s_sleep(W_D);
      if (W_L != 1) {
#pragma unroll
        for (int i = 0; i < 8; ++i) LDM(wv[i], wo + i * 12288, wmod);
      }
      const unsigned av = u < 80 ? 0u : ~0u;
      asm volatile("global_store_dwordx4 %0, %1, %2 sc1\n\ts_nop 1" ::"v"(O_KEY + u * 1024 + lane * 16), "v"(u32x4{av, av, av, av}), "s"(ws) : "memory");
      WAIT_V(W_L == 1 ? 3 : 11);
    } else if (wid == 4 || (W_L == 1 && wid == 5)) {
      __builtin_amdgcn_s_sleep(C_DP);
      PFL(PFG(wid & 1));
      WAIT_V(2);
    } else {
      WAIT_V(1);
    }
  } else {
    WAIT_V(0);
  }
  {
    asm volatile("" : "+v"(xv[0]), "+v"(xv[1])::"memory");
    if (wid == 1) TSG(part, 2);
    BT(51);
    const float s0 = dsum(xv[0][3], dsum(xv[0][2], dsum(xv[0][1], dsum(xv[0][0], 0.f))));
    const float s1 = dsum(xv[1][3], dsum(xv[1][2], dsum(xv[1][1], dsum(xv[1][0], 0.f))));
    const float mf = wave_sum_s(s0 + s1) * (1.0f / D), m = rbf(mf);
    const f32x4 d0 = lo4(xv[0]) - m, d1 = hi4(xv[0]) - m, d2 = lo4(xv[1]) - m, d3 = hi4(xv[1]) - m;
    const f32x4 q = (d0 * d0 + d1 * d1) + (d2 * d2 + d3 * d3);
    const float vf = wave_sum_s((q[0] + q[1]) + (q[2] + q[3])) * (1.0f / D) - (mf - m) * (mf - m);
    const float rden = __builtin_amdgcn_rcpf(rbf(__builtin_amdgcn_sqrtf(rbf(rbf(vf) + 1e-5f))));
    asm volatile("ds_write_b128 %0, %1" ::"v"(wa), "v"(pk8(d0 * rden, d1 * rden)) : "memory");
    asm volatile("ds_write_b128 %0, %1 offset:1024" ::"v"(wa), "v"(pk8(d2 * rden, d3 * rden)) : "memory");
    if (wid == 1) TSG(part, 12);
    BT(52);
    LBAR();
    if (wid == 1) TSG(part, 10);
    BT(53);
    u32x2 t[4];
    asm volatile(
        "ds_read_b64_tr_b16 %0, %4\n\tds_read_b64_tr_b16 %1, %4 offset:%5\n\t"
        "ds_read_b64_tr_b16 %2, %4 offset:128\n\tds_read_b64_tr_b16 %3, %4 offset:%6\n\ts_waitcnt lgkmcnt(0)"
        : "=&v"(t[0]), "=&v"(t[1]), "=&v"(t[2]), "=&v"(t[3])
        : "v"(ta), "n"(4 * 2368), "n"(4 * 2368 + 128)
        : "memory");
    asm volatile("global_store_dwordx4 %0, %2, %4 sc1\n\tglobal_store_dwordx4 %1, %3, %4 sc1\n\ts_nop 1" ::"v"(oo), "v"(oo2),
                 "v"(u32x4{t[0][0], t[0][1], t[1][0], t[1][1]}), "v"(u32x4{t[2][0], t[2][1], t[3][0], t[3][1]}), "s"(xn)
                 : "memory");
    if (wid == 2) TSG(part, 14);
    BT(54);
  }
  u32x2 vd;
#define VDLD(sl) POLL2(max(vd[0], vd[1]) == SENT, sl, PCAP, asm volatile("global_load_dwordx2 %0, %1, %2 sc1" VW0 : "=&v"(vd) : "v"(O_VD + (b & 31) * 128), "s"(ws) : "memory"))
  if (wid < 4) {
    const unsigned ya0 = lds0 + (lane & 15) * 128 + (g4 ^ (lane & 7)) * 16;
    bf16x8 X[DX][2][2], Y[8];
    f32x4 acc[2][8];
    {
      constexpr int MODE = 0;
      const unsigned xo0 = lane * 16 + 4096, xo1 = xo0 + 8192;
      const char* xb = reinterpret_cast<const char*>(xn) + (size_t)(mt * 4 + wid) * 131072;
#define XB(i) (xb + (((xcd + (i)) & 7) << 14))
#pragma unroll
      for (int i = 0; i < 2; ++i)
#pragma unroll
        for (int j = 0; j < 8; ++j) acc[i][j] = f32x4{0.f, 0.f, 0.f, 0.f};
      asm volatile("" : W8(acc[0]), W8(acc[1]));
      __builtin_amdgcn_s_setprio(1);
      WAIT_V(0);
      if (wid == 0) TSG(part, 1);
      const char* xpb = XB(0);
      BAR(55);
      if (wid == 3) {
        TAGW(0);
        const unsigned* fp = xfl + ((xcd * 32 + lane) & 255) * XSTR;
        if (P_D3) __builtin_amdgcn_s_sleep(P_D3);
#pragma unroll 1
        for (int it = 0; it < PCAP; ++it) { PDBG(it);
          u64 f;
          if (it < 2) BT(58 + 2 * it);
          asm volatile("global_load_dwordx2 %0, %1, off sc1\n\ts_waitcnt vmcnt(0)" : "=&v"(f) : "v"(fp) : "memory");
          if (it < 2) BT(59 + 2 * it);
          if (__all(f == tag)) break;
          __builtin_amdgcn_s_sleep(P_D4);
        }
      }
      BAR(0);
      if (wid == 1) TSG(part, 3);
#pragma unroll
      for (int h = 0; h < 7; ++h) {
        XLD(X[h >> 1][h & 1][0], xpb, h, 0);
        XLD(X[h >> 1][h & 1][1], xpb, h, 1);
      }
#pragma unroll
      for (int j = 0; j < 7; ++j) DSR(Y[j], ya0, j * 2048);
      if (W_N) {
        f32x4 w0, w1;
        WAIT_L(0);
        MFMA0(w0, Y[0], Y[1]);
        MFMA0(w1, Y[2], Y[3]);
#pragma unroll
        for (int j = 0; j < W_N; ++j) {
          MFMA(w0, Y[(2 * j) % 7], Y[(2 * j + 3) % 7]);
          MFMA(w1, Y[(2 * j + 1) % 7], Y[(2 * j + 5) % 7]);
          if (W_G) asm volatile("s_nop %0" ::"n"(W_G));
        }
        asm volatile("s_nop 7\n\ts_nop 7" : "+v"(w0), "+v"(w1));
      }
      unsigned so = 0, a0 = ya0;
      f32x4 c0, c1;
#pragma unroll 1
      for (int it = 0; it < 7; ++it) {
        const char* xpa = xpb;
        xpb = XB(it + 1);
#pragma unroll
        for (int s = 0; s < 4; ++s) {
          const unsigned a1 = (ya0 ^ 64) + so;
          so = so == (NS - 1) * 16384 ? 0 : so + 16384;
          WAIT_V(12);
          HALF(X[s][0], a1, true, X[(s + 3) & 3][1], (s == 0 ? xpa : xpb), (2 * s + 7) & 7, true, a0);
          a0 = ya0 + so;
          BAR(1 + it * 4 + s);
          WAIT_V(12);
          HALF(X[s][1], a0, true, X[s][0], xpb, 2 * s, true, a1);
        }
      }
#pragma unroll
      for (int s = 0; s < 4; ++s) {
        const unsigned a1 = (ya0 ^ 64) + so;
        so = so == (NS - 1) * 16384 ? 0 : so + 16384;
        WAIT_V(s == 0 ? 12 : 14 - 4 * s);
        HALF(X[s][0], a1, true, X[3][1], xpb, 7, s == 0, a0);
        a0 = ya0 + so;
        if (s < 3) BAR(29 + s);
        WAIT_V(12 - 4 * s);
        if (s == 2) {
          DSR(c0, lds0 + SPARE + 15872 + 512 + wid * 128 + g4 * 32, 0);
          DSR(c1, lds0 + SPARE + 15872 + 512 + wid * 128 + g4 * 32, 16);
        }
        if (s < 3) HALF(X[s][1], a0, true, X[s][0], xpb, 0, false, a1);
      }
      const char* up = reinterpret_cast<const char*>(U) + (size_t)cc * 4194304 + (size_t)(nt * 4) * 65536 + (mt * 4 + wid) * 2048;
      const unsigned ul = lds0 + STG + wid * 1024 + lane * 16;
#define UEP(j)                                                                                                                             \
  do {                                                                                                                                     \
    u32x4 v_;                                                                                                                              \
    asm volatile("v_pk_mul_f32 %0, %0, %4\n\tv_pk_mul_f32 %1, %1, %5\n\tv_pk_mul_f32 %2, %2, %6\n\tv_pk_mul_f32 %3, %3, %7"            \
                 : "+v"(acc[0][j].lo), "+v"(acc[0][j].hi), "+v"(acc[1][j].lo), "+v"(acc[1][j].hi)                                          \
                 : "v"(c0.lo), "v"(c0.hi), "v"(c1.lo), "v"(c1.hi));                                                                        \
    asm volatile("v_cvt_pk_bf16_f32 %0, %4, %5\n\tv_cvt_pk_bf16_f32 %1, %6, %7\n\tv_cvt_pk_bf16_f32 %2, %8, %9\n\tv_cvt_pk_bf16_f32 %3, %10, %11" \
                 : "=&v"(v_[0]), "=&v"(v_[1]), "=&v"(v_[2]), "=&v"(v_[3])                                                                  \
                 : "v"(acc[0][j][0]), "v"(acc[0][j][1]), "v"(acc[0][j][2]), "v"(acc[0][j][3]), "v"(acc[1][j][0]), "v"(acc[1][j][1]),       \
                   "v"(acc[1][j][2]), "v"(acc[1][j][3]));                                                                                  \
    asm volatile("global_store_dwordx4 %0, %2, %3 offset:%4 sc1\n\tds_write_b128 %1, %2 offset:%5\n\ts_nop 0"                          \
                 ::"v"(lane * 16), "v"(ul), "v"(v_), "s"(up + ((j) >> 1) * 65536), "n"(((j)&1) * 1024), "n"((j)*4096) : "memory");         \
  } while (0)
      {
        const unsigned a1 = (ya0 ^ 64) + (31 % NS) * 16384;
        DSR(Y[7], a1, 7 * 2048);
        WAIT_L(6);
#pragma unroll
        for (int j = 0; j < 8; ++j) {
          if (j == 2) WAIT_L(0);
          MFMA(acc[0][j], X[3][1][0], Y[j]);
          MFMA(acc[1][j], X[3][1][1], Y[j]);
          if (j >= 2) UEP(j - 2);
        }
      }
      if (wid == 0) TSG(part, 4);
      TS(0);
      SKEWPT();
      asm volatile("s_nop 3");
#pragma unroll
      for (int j = 6; j < 8; ++j) UEP(j);
    }
    const int rg = nt * 8 + wid * 2 + cc, row0 = rg * 16 + (lane & 15), col0 = et * 128 + g4 * 8;
    u32x4 xq[4], pv[4], sa4[4];
    f32x4 ga[8];
    f32x2 cj;
    float sw, ce;
    u32x4 dk;
    unsigned spo, soo, th, slt, slf, sla, tha;
    u32x2 se;
    const char* xr = reinterpret_cast<const char*>(xn) + mt * 524288 + (cc * 16 + nt) * 16384 + wid * 4096;
#define XCHLD()                                                                                                     \
  asm volatile(                                                                                                     \
      "global_load_dwordx4 %0, %4, %5 sc1\n\tglobal_load_dwordx4 %1, %4, %5 offset:1024 sc1\n\t"                   \
      "global_load_dwordx4 %2, %4, %5 offset:2048 sc1\n\tglobal_load_dwordx4 %3, %4, %5 offset:3072 sc1"            \
      : "=&v"(pv[0]), "=&v"(pv[1]), "=&v"(pv[2]), "=&v"(pv[3])                                                      \
      : "v"(lane * 16), "s"(xr)                                                                                     \
      : "memory")
    {
      constexpr int MODE = 1;
      const char* ub = reinterpret_cast<const char*>(U) + (size_t)cc * 4194304 + (size_t)(nt * 4 + wid) * 65536;
      const unsigned xo0 = lane * 16 + cc * 1024 + 4096, xo1 = xo0 ^ 1024;
      BT(32);
      LBAR();
      if (wid == 1) TSG(part, 6);
      {
        const unsigned sa = lds0 + STG + wid * 8192 + lane * 16 + cc * 4096;
#pragma unroll
        for (int q = 0; q < 8; ++q) DSR(X[(q >> 1) & 1][q & 1][q >> 2], (q >> 2) ? sa ^ 4096 : sa, (q & 3) * 1024);
        const unsigned a0 = ya0 + (32 % NS) * 16384;
#pragma unroll
        for (int j = 0; j < 7; ++j) DSR(Y[j], a0, j * 2048);
      }
#define UFLAG()                                                                                                         \
  do {                                                                                                                  \
    WAIT_V(0);                                                                                                          \
    if (lane == 0) __hip_atomic_store(ufl + (mt * 4 + wid) * FSTR, 1u, __ATOMIC_RELAXED, __HIP_MEMORY_SCOPE_AGENT);     \
    if (wid == 0) TSG(part, 5);                                                                                         \
  } while (0)
#define XP(h) (ub + (((mt + ((h) >> 2)) & 7) << 13))
#define XS(h) X[((h) >> 1) & 3][(h)&1]
#define XW(h) do { if ((h) >= 4) WAIT_V(2 * (((h) + 6 > 31 ? 31 : (h) + 6) - (h)) + ((h) >= 25 + XQ_N / 8 ? 4 : 0)); } while (0)
      constexpr int H0 = 24, NP = 64, NE = 2 * NP + 8;
#pragma unroll
      for (int s = 0; s < H0 / 2; ++s) {
        const unsigned a0 = ya0 + ((32 + s) % NS) * 16384, a1 = a0 ^ 64, an = ya0 + ((33 + s) % NS) * 16384;
        XW(2 * s);
        if (s == 0) {
          constexpr int MODE = 2;
          HALF(XS(0), a1, true, XS(7), XP(7), 7, false, a0);
          UFLAG();
        } else {
          HALF(XS(2 * s), a1, true, XS(2 * s + 7), XP(2 * s + 7), 2 * s + 7, 2 * s >= 4, a0);
        }
        if (s == 1 && wid == 2) TSD(12);
        BAR(33 + s);
        if (s == 1) {
          if (wid == 1) TSG(part, 7);
          TS(4);
#pragma unroll
          for (int h = 4; h < U_X0; ++h) {
            XLD(XS(h)[0], XP(h), h, 0);
            XLD(XS(h)[1], XP(h), h, 1);
          }
        }
        XW(2 * s + 1);
#undef HHOOK
#define HHOOK(j) if (s == 1 && j + 3 >= U_X0) { XLD(XS(j + 3)[0], XP(j + 3), j + 3, 0); XLD(XS(j + 3)[1], XP(j + 3), j + 3, 1); }
        HALF(XS(2 * s + 1), an, true, XS(2 * s + 8), XP(2 * s + 8), 2 * s + 8, 2 * s + 1 >= 5, a1);
#undef HHOOK
#define HHOOK(j)
      }
      const char* xw = reinterpret_cast<const char*>(xn) + mt * 524288 + ((cc ^ 1) * 16 + nt) * 16384 + wid * 4096;
      unsigned c128 = 0x43000000u;
      asm volatile("" : "+v"(c128));
      const unsigned xoff = (unsigned)(row0 * D + col0) * 2;
      const unsigned rka = lds0 + RKA + (lane & 15) * 16, rkb = lds0 + RKB + (g4 ? 1024 : wid * 256 + lane * 16);
      const unsigned ea = lds0 + TAB + g4 * 32, eb = lds0 + TAB + 512 + (lane & 3) * 256 + g4 * 16;
      u32x4 ov;
      bf16x8 xk;
#pragma unroll
      for (int q = 0; q < 4; ++q) dk[q] = (lane & 3) == q ? ~0u : 0u;
      spo = O_STAT + rg * 2048 + g4 * 512 + (lane & 15) * 16, soo = O_STAT + rg * 2048 + et * 256 + (lane & 15) * 16;
      unsigned cwa = lds0 + SPARE + 10240 + g4 * 8, cwu = lds0 + SPARE + 10240 + et * 4;
      {
        const unsigned one = g4 == ((lane >> 2) & 3) ? ((lane & 1) ? 0x1f800000u : 0x3f80u) : 0u;
        se = u32x2{(lane & 2) ? 0u : one, (lane & 2) ? one : 0u};
        asm volatile("" : "+v"(se));
      }
      tha = lds0 + SPARE + 10368 + 24;
      sla = lds0 + SLT + (row0 & 127) * 4;
      asm volatile("" : "+v"(dk), "+v"(spo), "+v"(soo), "+v"(cwa), "+v"(cwu), "+v"(tha), "+v"(sla));
#define EH(e) (H0 + (((e) < NP ? (e) : (e) - NP - 8) >> 3))
#define EA(e) ((ya0 ^ ((EH(e) & 1) * 64)) + ((32 + (EH(e) >> 1)) % NS) * 16384)
      sfor<NE>([&](auto nn) __attribute__((always_inline)) {
        constexpr int n = decltype(nn)::value, h = EH(n), j = n & 7, m = n - NP - TP_P0, e = n + 7, z = n - (NE - 8);
        (void)sa4, (void)eb, (void)ga, (void)ea, (void)xk, (void)rka, (void)rkb, (void)xq, (void)xoff, (void)ov, (void)xw, (void)x;
        (void)cj, (void)sw, (void)ce, (void)cwa, (void)cwu, (void)th, (void)tha, (void)slt, (void)sla, (void)slf, (void)c128;
        if (n == 0) TS(2);
        if (n == NP) { TS(3); SKEWPT(); }
        if (n < NP && j == 0) {
          if ((h & 1) && h < 31) BAR(33 + (h >> 1));
          XW(h);
        }
        if (n == NP + TP_X0) { TS(14); BAR(48); TS(11); }
        if (n == NP + TP_XL) XCHLD();
        if constexpr ((j & 1) == 0) WAIT_L(z <= 0 ? 5 : 6 - z + 2 * z);
        if constexpr (n < NP) MFMA(acc[1][j], Y[j], XS(h)[1]);
        else if constexpr (n < NP + 8) MFMA(acc[0][j], Y[j], xk);
        else MFMA(acc[0][j], Y[j], XS(h)[0]);
        if constexpr (n == NP - 16) DSR(xk, rkb, 0);
        if constexpr (e >= NP && e < NP + 8) DSR(Y[e & 7], rka, (e & 7) * 256);
        else if constexpr (e < NE) DSR(Y[e & 7], EA(e), (e & 7) * 2048);
        if constexpr (n == 2 || n == 4) XLD(XS(31)[(j >> 1) - 1], XP(31), 31, (j >> 1) - 1);
        if constexpr (n == XQ_N) {
#pragma unroll
          for (int q = 0; q < 4; ++q) LDX(xq[q], xoff, x, q * 64);
        }
        if constexpr (m >= 0 && m < 4 * TP_PS) {
          constexpr int q = m / TP_PS, r = m % TP_PS;
#pragma unroll
          for (int u = (r * 13 + TP_PS - 1) / TP_PS; u < ((r + 1) * 13 + TP_PS - 1) / TP_PS; ++u) {
#define AO(i) acc[1][2 * q + ((u >> 1) & 1)][(u & 1) * 2 + (i)]
            if (u < 4) asm volatile("v_and_or_b32 %0, %1, %2, %3" : "=v"(ov[u & 3]) : "v"(AO(1)), "s"(0x80000000u), "v"(c128));
            else if (u < 8) asm volatile("v_fmamk_f32 %0, %1, 0x5f800000, %0" : "+v"(ov[u & 3]) : "v"(AO(1)));
            else if (u < 12) asm volatile("v_cvt_pk_bf16_f32 %0, %1, %0" : "+v"(ov[u & 3]) : "v"(AO(0)));
            else asm volatile("global_store_dwordx4 %0, %1, %2 offset:%3 sc1\n\ts_nop 1" ::"v"(lane * 16), "v"(ov), "s"(xw), "n"(q * 1024) : "memory");
          }
          if (m == 4 * TP_PS - 1) TS(10);
        }
        if constexpr (m == TP_XC) asm volatile("s_waitcnt vmcnt(%4)" : "+v"(xq[0]), "+v"(xq[1]), "+v"(xq[2]), "+v"(xq[3]) : "n"(n >= NP + TP_XL ? 8 : 4) : "memory");
        if constexpr (m >= TP_XC && m < TP_XC + 32) {
          constexpr int c = m - TP_XC;
          if constexpr (c & 1) asm volatile("v_and_b32 %0, 0xffff0000, %1" : "=v"(acc[1][c >> 2][c & 3]) : "v"(xq[c >> 3][(c >> 1) & 3]));
          else asm volatile("v_lshlrev_b32 %0, 16, %1" : "=v"(acc[1][c >> 2][c & 3]) : "v"(xq[c >> 3][(c >> 1) & 3]));
        }
        if constexpr (z >= 0 && z < 4) {
          DSR(ga[2 * z], ea, z * 128);
          DSR(ga[2 * z + 1], ea, z * 128 + 16);
        } else if constexpr (z >= 4 && z < 6) {
          DSR(sa4[2 * z - 8], eb, (2 * z - 8) * 64);
          DSR(sa4[2 * z - 7], eb, (2 * z - 7) * 64);
        } else if constexpr (z == 6) {
          asm volatile("ds_read_b64 %0, %2\n\tds_read_b32 %1, %3" : "=&v"(cj), "=&v"(ce) : "v"(cwa), "v"(cwu) : "memory");
        } else if constexpr (z == 7) {
          asm volatile("ds_read_b32 %0, %4 offset:32\n\tds_read_b32 %1, %5 offset:4\n\tds_read_b32 %3, %5 offset:56\n\tds_read_b32 %2, %6"
                       : "=&v"(sw), "=&v"(th), "=&v"(slt), "=&v"(slf) : "v"(cwa), "v"(tha), "v"(sla) : "memory");
        }
      });
    }
    if (wid == 0) TSG(part, 8);
    TS(13);
    BT(49);
    BTE();
    SKEWPT();
    u16* op = out + (size_t)row0 * D + col0;
    {
      u32x4 e0 = {se[0], se[1], 0u, 0u}, e1 = {0u, 0u, se[0], se[1]};
      asm volatile("" : "+v"(e0), "+v"(e1));
      TS(26);
#define XCHW() asm volatile("s_waitcnt vmcnt(0)" : "+v"(pv[0]), "+v"(pv[1]), "+v"(pv[2]), "+v"(pv[3])::"memory")
      XCHW();
#define XBAD (amin4(pv[3], amin4(pv[2], 128.0f)) < 128.0f || amin4(pv[1], amin4(pv[0], 128.0f)) < 128.0f)
      if (__builtin_expect(__any(XBAD), 1)) POLL2(XBAD, 1, PCAP, XCHLD(); XCHW());
      TS(27);
      asm volatile("s_waitcnt lgkmcnt(0)" : W8(acc[0]), W8(ga), "+v"(sa4[0]), "+v"(sa4[1]), "+v"(sa4[2]), "+v"(sa4[3]), "+v"(cj), "+v"(sw), "+v"(ce), "+v"(th), "+v"(slt), "+v"(slf)::"memory");
#pragma unroll
      for (int q = 0; q < 8; ++q) acc[0][q] = __builtin_amdgcn_mfma_f32_16x16x32_bf16(BF8((q & 1) ? e1 : e0), BF8(pv[q >> 1]), acc[0][q], 0, 0, 0);
      f32x4 s4 = {0.f, 0.f, 0.f, 0.f}, gr = {0.f, 0.f, 0.f, 0.f};
#pragma unroll
      for (int q = 0; q < 4; ++q) {
        xq[q] = pk8(acc[1][2 * q] + ga[2 * q] * acc[0][2 * q], acc[1][2 * q + 1] + ga[2 * q + 1] * acc[0][2 * q + 1]);
        gr = __builtin_amdgcn_mfma_f32_16x16x32_bf16(BF8(xq[q]), BF8(xq[q]), gr, 0, 0, 0);
        s4 = __builtin_amdgcn_mfma_f32_16x16x32_bf16(BF8(sa4[q]), BF8(xq[q]), s4, 0, 0, 0);
      }
#define XCST()                                                                                                                         \
  do {                                                                                                                                 \
    const u32x4 n0 = nsent4(xq[0]), n1 = nsent4(xq[1]), n2 = nsent4(xq[2]), n3 = nsent4(xq[3]);                                        \
    _Pragma("unroll") for (int r = D_R - 1; r >= 0; --r) asm volatile(                                                                      \
        "global_store_dwordx4 %0, %1, %5 sc1\n\tglobal_store_dwordx4 %0, %2, %5 offset:64 sc1\n\t"                                    \
        "global_store_dwordx4 %0, %3, %5 offset:128 sc1\n\tglobal_store_dwordx4 %0, %4, %5 offset:192 sc1\n\ts_nop 1" ::"v"(O_X1C - 2048 + r * X_CS + slt * 2048 + col0 * 2), \
        "v"(n0), "v"(n1), "v"(n2), "v"(n3), "s"(ws) : "memory");                                                                       \
  } while (0)
#define SCST()                                                                                                                         \
  do {                                                                                                                                 \
    _Pragma("unroll") for (int r = D_R - 1; r >= 0; --r)                                                                                    \
        asm volatile("global_store_dwordx4 %0, %1, %2 sc1\n\ts_nop 1" ::"v"(O_ST2 - 128 + r * X_CS + slt * 128 + et * 16), "v"(sg), "s"(ws) : "memory"); \
  } while (0)
      if (slt) XCST();
      const bool dl = ((lane >> 2) & 3) == g4;
      const u32x4 gd = __builtin_bit_cast(u32x4, gr) & dk;
      const float m = s4[2] * (1.0f / 128);
      float q2 = __uint_as_float((gd[0] | gd[1]) | (gd[2] | gd[3])) - s4[2] * m, lg = (s4[0] + s4[1]) - m * ce;
      if (__builtin_expect(__any(dl && !(s4[2] * m <= 256.0f * q2)), 0)) {
        asm volatile("" : "+v"(xq[0]), "+v"(xq[1]), "+v"(xq[2]), "+v"(xq[3]));
        f32x4 a = {0.f, 0.f, 0.f, 0.f}, l = {0.f, 0.f, 0.f, 0.f};
#pragma unroll
        for (int q = 0; q < 4; ++q) {
          const u32x4 ch = *reinterpret_cast<const u32x4*>(&scw[0][g4 * 8 + q * 32]), cl = *reinterpret_cast<const u32x4*>(&scw[1][g4 * 8 + q * 32]);
          const f32x4 d0 = lo4(xq[q]) - m, d1 = hi4(xq[q]) - m;
          a += d0 * d0 + d1 * d1;
          l += d0 * (lo4(ch) + lo4(cl)) + d1 * (hi4(ch) + hi4(cl));
        }
        q2 = ar4((a[0] + a[1]) + (a[2] + a[3]));
        lg = ar4((l[0] + l[1]) + (l[2] + l[3]));
      }
      const u32x4 sg = {nsent(m), nsent(q2), nsent(lg), 0u};
      if (dl) {
        if (slt) SCST();
        asm volatile("global_store_dwordx4 %0, %1, %2 sc1\n\ts_nop 1" ::"v"(soo), "v"(sg), "s"(ws) : "memory");
      }
      if (__any(slf == 0u)) {
        POLL2(slf == 0u, 1, 1 << 26, asm volatile("ds_read_b32 %1, %2 offset:56\n\tds_read_b32 %0, %3\n\ts_waitcnt lgkmcnt(0)" : "=&v"(slt), "=&v"(slf) : "v"(tha), "v"(sla) : "memory"));
        if (slt) {
          XCST();
          if (dl) SCST();
        }
      }
      if (D_G) asm volatile("ds_write_b32 %0, %1 offset:72" ::"v"(tha + wid * 4), "v"(1u) : "memory");
      TS(7);
      SKEWPT();
      const unsigned tp = tick() + T_D1, tq = (unsigned)__builtin_amdgcn_readfirstlane((int)th) + T_C1;
      u32x4 sr[2];
#define STLD(W) asm volatile("global_load_dwordx4 %0, %2, %3 sc1\n\tglobal_load_dwordx4 %1, %2, %3 offset:256 sc1" W : "=&v"(sr[0]), "=&v"(sr[1]) : "v"(spo), "s"(ws) : "memory")
#define SBAD (max(max(max(sr[0][0], sr[0][1]), sr[0][2]), max(max(sr[1][0], sr[1][1]), sr[1][2])) == SENT)
      asm volatile("" : "+v"(xq[0]), "+v"(xq[1]), "+v"(xq[2]), "+v"(xq[3])::"memory");
      f32x4 xf[8], xs[8], gc[8];
      auto PREW = [&](int q0, int q1) __attribute__((always_inline)) {
#pragma unroll
        for (int q = q0; q < q1; ++q) {
          const u32x4 sbq = *reinterpret_cast<const u32x4*>(&svb[1][col0 + q * 32]), gbq = *reinterpret_cast<const u32x4*>(&svb[3][col0 + q * 32]);
          const u32x4 cq = *reinterpret_cast<const u32x4*>(&svb[2][col0 + q * 32]);
          xf[2 * q] = lo4(xq[q]), xf[2 * q + 1] = hi4(xq[q]);
          xs[2 * q] = xf[2 * q] + lo4(gbq) * lo4(sbq), xs[2 * q + 1] = xf[2 * q + 1] + hi4(gbq) * hi4(sbq);
          gc[2 * q] = lo4(gbq) * lo4(cq), gc[2 * q + 1] = hi4(gbq) * hi4(cq);
          asm volatile("" : "+v"(xf[2 * q]), "+v"(xf[2 * q + 1]), "+v"(xs[2 * q]), "+v"(xs[2 * q + 1]), "+v"(gc[2 * q]), "+v"(gc[2 * q + 1]));
        }
      };
      PREW(0, PW0);
      wait_tick((int)(tp - tq) < 0 ? tp : tq);
      STLD("");
      unsigned rk = 0x8ffeu - (unsigned)row0 * 2;
      unsigned twa = lds0 + STG + wid * 8192 + (lane & 15) * 256 + ((g4 ^ lane) & 15) * 16, tra = lds0 + STG + wid * 8192 + g4 * 256 + ((g4 ^ lane) & 15) * 16;
      unsigned oo_ = (unsigned)((rg * 16 + g4) * D + et * 128 + (lane & 15) * 8) * 2;
      asm volatile("" : "+v"(rk), "+v"(op), "+v"(twa), "+v"(tra), "+v"(oo_));
      PREW(PW0, 4);
      asm volatile("s_waitcnt vmcnt(0)" : "+v"(sr[0]), "+v"(sr[1])::"memory");
      if (__builtin_expect(__any(SBAD), 1)) POLL2(SBAD, 1, PCAP, STLD(VW0));
      TS(9);
      const f32x2 mk = {__uint_as_float(sr[0][0]), __uint_as_float(sr[1][0])};
      const f32x2 qk = {__uint_as_float(sr[0][1]), __uint_as_float(sr[1][1])};
      const f32x2 lk = {__uint_as_float(sr[0][2]), __uint_as_float(sr[1][2])};
      const float mean = ar4(mk[0] + mk[1]) * (1.0f / 8);
      const f32x2 dm = mk - mean;
      const f32x2 a = qk + 128.0f * (dm * dm), l = lk + dm * cj;
      const float al = swap32_add(a[0] + a[1], l[0] + l[1]), am = swap16_add(al, al);
      auto r = __builtin_amdgcn_permlane32_swap(__float_as_uint(am), __float_as_uint(am), false, false);
      const float rstd = __builtin_amdgcn_rsqf(__uint_as_float(r[0]) * (1.0f / D) + 1e-5f);
      const unsigned key = mkkey(rstd * __uint_as_float(r[1]) + sw) | rk;
      unsigned t1 = slt ? 0u : key, t2 = 0u;
      TOP2(0xB1); TOP2(0x4E); TOP2(0x141); TOP2(0x140);
      if (lane == 0 && et == (rg & 7)) asm volatile("global_store_dwordx2 %0, %1, %2 sc1" ::"v"(O_KEY + rg * KSTR * 8), "v"(u32x2{t1, t2}), "s"(ws) : "memory");
      u32x4 o[4];
#pragma unroll
      for (int q = 0; q < 4; ++q) o[q] = pk8(xs[2 * q] + (gc[2 * q] * (xf[2 * q] - mean)) * rstd, xs[2 * q + 1] + (gc[2 * q + 1] * (xf[2 * q + 1] - mean)) * rstd);
      {
        asm volatile("ds_write_b128 %0, %4\n\tds_write_b128 %1, %5\n\tds_write_b128 %2, %6\n\tds_write_b128 %3, %7" ::"v"(twa), "v"(twa ^ 64), "v"(twa ^ 128), "v"(twa ^ 192),
                     "v"(o[0]), "v"(o[1]), "v"(o[2]), "v"(o[3]) : "memory");
        asm volatile("ds_read_b128 %0, %4\n\tds_read_b128 %1, %5 offset:1024\n\tds_read_b128 %2, %6 offset:2048\n\tds_read_b128 %3, %7 offset:3072\n\ts_waitcnt lgkmcnt(0)"
                     : "=&v"(o[0]), "=&v"(o[1]), "=&v"(o[2]), "=&v"(o[3]) : "v"(tra), "v"(tra ^ 64), "v"(tra ^ 128), "v"(tra ^ 192) : "memory");
      }
      auto OST = [&](bool on) __attribute__((always_inline)) {
        const unsigned mk_ = (unsigned)__builtin_amdgcn_ballot_w64(on) >> g4;
#pragma unroll
        for (int q = 0; q < 4; ++q)
          if (mk_ & (1u << (4 * q))) asm volatile("global_store_dwordx4 %0, %1, %2" OPOL "\n\ts_nop 1" ::"v"(oo_), "v"(o[q]), "s"(out + q * 4096) : "memory");
      };
      bool df = slt;
      {
        const int tn = (int)(tick() - (tq + (T_C3 - T_C1)));
        if (tn > T_X) df = true;
        else if (tn > -T_W) { unsigned sv; LSPIN(sv, ssel + 16 + wid, sv != 0u); }
      }
      OST(!df);
      TS(1);
      u32x2 ks;
      POLL2(ks[1] == 0u, 1, 1 << 26, asm volatile("ds_read_b64 %0, %1\n\ts_waitcnt lgkmcnt(0)" : "=v"(ks) : "v"(lds0 + SPARE + 10368) : "memory"));
      df = df && (ks[0] & 0xffffu) != rk && (ks[1] & 0xffffu) != rk;
      SKEWPT();
      if (__any(df)) {
        if (b < NPART) {
          unsigned sv;
          LSPIN(sv, ssel + O_DW, sv != 0u);
          if (O_DS) __builtin_amdgcn_s_sleep(O_DS);
        }
        OST(df);
      }
      if (__builtin_expect(__any(t1 > ks[1]), 0)) {
        VDLD(8);
        slt = slt ? 0u : key == vd[0] ? 9u : key == vd[1] ? 10u : 0u;
        if (slt) {
          XCST();
          if (dl) SCST();
        }
      }
    }
    TS(16);
    TSE();
  } else {
    const int hw = wid - 4;
    unsigned ts0 = tick();
    asm volatile("" : "+v"(ts0));
    const char* Hc = reinterpret_cast<const char*>((cc ? X1 : X0) + (size_t)et * 128 * D);
    auto UPOLL = [&](int n) {
      const unsigned fo = (unsigned)WS_UFL + ((cc * 16 + nt) * 32 + ((mt + 1 + (lane >> 2)) & 7) * 4 + (lane & 3)) * FSTR * 4;
      unsigned f;
      POLL2(f == 0u && lane < 4 * n, U_S, PCAP, asm volatile("global_load_dword %0, %1, %2 sc1" VW0 : "=&v"(f) : "v"(fo), "s"(ws) : "memory"));
      return (__builtin_amdgcn_ballot_w64(f == 0u) & 0xFFFFFFFull) == 0;
    };
    bool urdy = true;
    u32x4 gT[8], uT[8], wdv[8];
    unsigned og = (unsigned)(((g & 3) * 256 + hw * 64 + (lane >> 3)) * F + (g >> 2) * 64 + ((lane & 7) ^ ((lane >> 3) & 2) ^ 4) * 8) * 2;
    unsigned wta = lds0 + STG + (g4 * 8 + ((lane >> 2) & 3)) * 128 + (((hw * 2 + ((lane >> 1) & 1)) ^ ((lane >> 2) & 2) ^ ((g4 & 1) * 4)) << 4) + (lane & 1) * 8;
    unsigned od = (unsigned)(((g >> 2) * 64 + (lane >> 3) * 8) * D + (g & 3) * 256 + hw * 64 + (lane & 7) * 8) * 2;
    unsigned ou = og + (unsigned)(((mt + W_UO) % 7 - mt) * 128);
    asm volatile("" : "+v"(og), "+v"(ou), "+v"(od), "+v"(wta));
#define LDWI(dst, voff, base, inc) asm volatile("global_load_dwordx4 %0, %1, %2" WPOL "\n\tv_add_u32 %1, %3, %1" : "=v"(dst), "+v"(voff) : "s"(base), "n"(inc) : "memory")
#define WST(o, w)                                                                                                          \
  if (b < NPART) {                                                                                                         \
    asm volatile("s_mov_b32 m0, %0" ::"s"(lds0 + STG + hw * 8192));                                                        \
    _Pragma("unroll") for (int i = 0; i < 8; ++i) {                                                                        \
      asm volatile("v_xor_b32 %0, 64, %0\n\tglobal_load_lds_dwordx4 %0, %1 offset:%2" WPOL "\n\tv_add_u32 %0, %3, %0"       \
                   : "+v"(o) : "s"(w), "n"((i & 3) * 1024), "n"(F * 16 + (i == 3 ? 3072 : -1024)) : "memory");             \
      if (i == 3) asm volatile("s_add_u32 m0, m0, 4096" ::: "scc");                                                        \
    }                                                                                                                      \
  }
#define WTR2(i) "ds_read_b64_tr_b16 %" #i ", %16 offset:(" #i "/2)*4096+(" #i "&1)*512\n\t"
#define WTK(T)                                                                                                                         \
  if (b < NPART) {                                                                                                                     \
    u32x2 t_[16];                                                                                                                      \
    asm volatile(WTR2(0) WTR2(1) WTR2(2) WTR2(3) WTR2(4) WTR2(5) WTR2(6) WTR2(7) WTR2(8) WTR2(9) WTR2(10) WTR2(11) WTR2(12) WTR2(13)   \
                     WTR2(14) WTR2(15) "s_waitcnt lgkmcnt(0)"                                                                          \
                 : "=&v"(t_[0]), "=&v"(t_[1]), "=&v"(t_[2]), "=&v"(t_[3]), "=&v"(t_[4]), "=&v"(t_[5]), "=&v"(t_[6]), "=&v"(t_[7]),     \
                   "=&v"(t_[8]), "=&v"(t_[9]), "=&v"(t_[10]), "=&v"(t_[11]), "=&v"(t_[12]), "=&v"(t_[13]), "=&v"(t_[14]), "=&v"(t_[15]) \
                 : "v"(wta) : "memory");                                                                                               \
    _Pragma("unroll") for (int f = 0; f < 8; ++f) T[f] = u32x4{t_[2 * f][0], t_[2 * f][1], t_[2 * f + 1][0], t_[2 * f + 1][1]};        \
  }
#define DLD(q0, q1) if (b < NPART) { _Pragma("unroll") for (int q = q0; q < q1; ++q) LDWI(wdv[q], od, wd, D * 2); }
    const int pf0 = A_PS + (j8 & (A_PW - 1)), q1 = A_Q1;
    static_assert(A_PS + 2 * A_PW + 2 <= R_AT && R_AT < A_Q1 && A_PS + 2 * A_PW + W_T <= A_Q1 && H_AT + 3 <= A_Q1 && G1_C + G1_S < W_T && W_T < A_PW && W_T >= M_NS - 2 && A_Q1 < F_AT && F_AT <= 29 && V_K <= B_J && V_T > B_J && B_WK == 15 && N_K > N_C && N_C > V_T && B_RAT > B_J && B_QAT > B_J && B_QAT + B_QD < B_MAT, "barrier counts");
#define HOFF(r) (unsigned)((((r) >> 5) * 32 + (((r) >> 2) & 3) * 8 + (((r) >> 4) & 1) * 4 + ((r)&3)) * 2048 + ((lane & 7) ^ (lane >> 3)) * 16)
#define TR8(T, W)                                                                                                    \
  _Pragma("unroll") for (int c = 0; c < 8; ++c) {                                                                    \
    _Pragma("unroll") for (int p = 0; p < 4; ++p)                                                                    \
        T[c][p] = __builtin_amdgcn_perm(W[2 * p + 1][c >> 1], W[2 * p][c >> 1], (c & 1) ? 0x07060302u : 0x05040100u); \
    asm volatile("" : "+v"(T[c]));                                                                                   \
  }
    u32x4 qm;
#pragma unroll
    for (int i = 0; i < 4; ++i) qm[i] = ((lane >> 2) & 3) == i ? ~0u : 0u;
    auto MVR = [&](f32x4 d) __attribute__((always_inline)) { return ((lane & 8) ? d[3] : d[0]) + dpp_f<0x128>((lane & 8) ? d[1] : d[2]); };
    auto MV64 = [&](const u32x4* T, u32x4 a) __attribute__((always_inline)) {
      f32x4 d[2] = {};
#pragma unroll
      for (int c = 0; c < 8; ++c) d[c & 1] = __builtin_amdgcn_mfma_f32_16x16x32_bf16(BF8(a & qm[c >> 1]), BF8(T[c]), d[c & 1], 0, 0, 0);
      return f32x2{MVR(d[0]), MVR(d[1])};
    };
    u32x4 sel, gp[4];
    unsigned vc;
#define VCLD(W) asm volatile("global_load_dword %0, %1, %2 sc1" W : "=&v"(vc) : "v"(O_VP + ((cc ^ 1) * 8 + mt) * 4096 + (lane & 15) * 256), "s"(ws) : "memory")
#define GP4(o, W)                                                                                                         \
  asm volatile(                                                                                                           \
      "global_load_dwordx4 %0, %4, %5 sc1\n\tglobal_load_dwordx4 %1, %4, %5 offset:1024 sc1\n\t"                         \
      "global_load_dwordx4 %2, %4, %5 offset:2048 sc1\n\tglobal_load_dwordx4 %3, %4, %5 offset:3072 sc1" W                \
      : "=&v"(gp[0]), "=&v"(gp[1]), "=&v"(gp[2]), "=&v"(gp[3])                                                            \
      : "v"(o), "s"(ws)                                                                                                   \
      : "memory")
#define GPBAD (max(max(max4(gp[0]), max4(gp[1])), max(max4(gp[2]), max4(gp[3]))) == SENT)
      auto GPSUM = [&]() __attribute__((always_inline)) {
        f32x4 d = {0.f, 0.f, 0.f, 0.f};
#pragma unroll
        for (int i = 0; i < 4; ++i) d = __builtin_amdgcn_mfma_f32_16x16x32_bf16(BF8(sel), BF8(gp[i]), d, 0, 0, 0);
        return d;
      };
    const bool rdc = b >= NPART && hw == 3, rnp = b >= NPART && hw == 2 && cc == 0;
    const int gbq = (b >> 4) & 1, cj = (b & 15) * 64 + (lane & 31) * 2;
    unsigned voff[4], ro, ra1, wqa, qo, so_, qa, mo, mo1, mo2, ma2;
    auto JSET = [&]() {
#pragma unroll
      for (int i = 0; i < 4; ++i) {
        voff[i] = HOFF((hw * 4 + i) * 8 + (lane >> 3)) - i * 1024;
        sel[i] = ((lane & 15) >> 1) == i ? ((lane & 1) ? 0x3f800000u : 0x3f80u) : 0u;
      }
      ro = rdc ? O_G2 + ((lane >> 5) * 16384 + gbq * 1024 + cj) * 8 : (unsigned)(((nt * 128 + hw * 32 + cc * 16 + ((lane >> 1) & 3) + (lane & 8)) * D + et * 128 + (lane & 1) * 64) * 2);
      ra1 = lane < 32 ? O_MOD + 2048 + gbq * 4096 + cj * 2 : O_M2 + 8192 + 64 - gbq * 64 + (b & 15) * 4;
      wqa = lds0 + SPARE + 8192 + cj * 2;
      mo = hw ? O_MOD + hw * 2048 + lane * 16 : O_MOD + 4096, mo1 = hw ? mo + 1024 : mo;
      qo = O_RS + (4096 + ((hw & 1) * 8 + (lane >> 4) * 2 + 1) * D + et * 128 + (hw >> 1) * 64 + (lane & 15) * 4) * 4;
      so_ = O_RS + ((hw & 1) * S + nt * 128 + (lane >> 4) * 32 + cc * 16 + (lane & 15)) * 4;
      qa = lds0 + RKA + ((hw >> 1) * 64 + ((lane >> 3) & 1) * 32 + (lane & 1) * 16 + ((lane >> 1) & 3) * 4 + (lane >> 4)) * 16 + (hw & 1) * 8;
      mo2 = O_M2 + (hw == 2 ? 8448u + (lane & 15) * 16 : hw ? 8192u + (lane & 7) * 16 : lane < 32 ? et * 512 + lane * 16 : 4096u + (lane & 16) * 128 + et * 256 + (lane & 15) * 16);
      ma2 = lds0 + (hw ? SPARE + 10240 + (lane & 7) * 8 : TAB + lane * 16);
      asm volatile("" : "+v"(voff[0]), "+v"(voff[1]), "+v"(voff[2]), "+v"(voff[3]), "+v"(ro), "+v"(ra1), "+v"(wqa), "+v"(qo), "+v"(so_), "+v"(qa), "+v"(mo), "+v"(mo1), "+v"(mo2), "+v"(ma2), "+v"(sel));
      *reinterpret_cast<unsigned*>((hw & 1) ? shm + RKB + 1024 + lane * 4 : reinterpret_cast<char*>(scw[2]) + lane * 4) = (hw & 1) ? 0u : 0x3f803f80u;
      if (hw == 3) {
        if (lane < 32) ssel[lane] = 0u;
        *reinterpret_cast<u32x2*>(shm + SLT + lane * 8) = u32x2{0u, 0u};
      }
    };
    if (hw < 2) {
      constexpr int P = 8;
      __builtin_amdgcn_s_setprio(2);
      unsigned voy[P];
#pragma unroll
      for (int i = 0; i < P; ++i) voy[i] = (unsigned)(((hw * 8 + i) * 8 + (lane >> 3)) * 4096 + ((lane & 7) ^ (lane >> 3)) * 16 - (i & 3) * 1024);
      const unsigned lbase = lds0 + hw * 8192;
      const char* Yc = reinterpret_cast<const char*>((cc ? Y1 : Y0) + (size_t)nt * 128 * S);
      auto stage = [&](const char* yp, unsigned l, bool pace) {
        GLDS4(l, voy, yp);
        if (L_H && pace) __builtin_amdgcn_s_sleep(L_H);
        GLDS4(l + 4096, voy + 4, yp);
      };
#define YSRC(n) (Yc + ((((n) + xcd * 4) & 31) << 7))
      __builtin_amdgcn_s_sleep(C_LD);
      if (W_L == 1) { if (hw) WAIT_V(0); else PFL(cpf); }
      else PFL(hw ? PFG(1) : cpf);
      if (!J_L) JSET();
      if (J_L && J_D) __builtin_amdgcn_s_sleep(J_D);
      TAGW(1);
      BAR(55);
      BT(56);
      if (hw == 1 && lane == 0) __hip_atomic_store(reinterpret_cast<u64*>(xfl + b * XSTR), tag, __ATOMIC_RELAXED, __HIP_MEMORY_SCOPE_AGENT);
#pragma unroll
      for (int s = 0; s < S_N; ++s) stage(YSRC(s), lbase + s * 16384, false);
      BT(57);
      WAIT_V((S_N - 1) * P);
      BT(62);
      BAR(0);
      if (S_N < NS - 1) {
        if (S_D) __builtin_amdgcn_s_sleep(S_D);
#pragma unroll
        for (int s = S_N; s < NS - 1; ++s) stage(YSRC(s), lbase + s * 16384, false);
      }
      unsigned slot = lbase + (NS - 1) * 16384;
      const u64 evm = ((u64)(1u | 1u << W_T | 1u << A_PW | 1u << (A_PW + W_T)) << pf0) | 1ull << CPF_K | 1ull << (33 - NS) | 3ull << 32 | ((2ull << R_N) - 2) | (R_B ? 7ull << 34 : 0ull);
      const char* yb = Yc;
      unsigned yo = (NS - 1 + xcd * 4) << 7, ym = 31 << 7;
#pragma unroll 1
      for (int k = 1; k <= 32 + B_J; ++k) {
        const char* ys = yb + (yo & ym);
        yo += 128;
        asm volatile("" : "+s"(ys));
        WAIT_V((NS - 3) * P);
        BAR(k);
        stage(ys, slot, true);
        slot = slot == lbase + (NS - 1) * 16384 ? lbase : slot + 16384;
        if (__builtin_expect((evm >> k) & 1, 0)) {
          if (R_N && k <= R_N && (R_M == 0 || (j8 & R_M))) __builtin_amdgcn_s_sleep(R_D);
          if (R_B && k >= 35) __builtin_amdgcn_s_sleep(R_B);
          if (k == pf0) {
            WST(og, wg);
            if (b >= NPART) GLDS(lds0 + SPARE + 8192 + hw * 1024, (unsigned)(hw * 1024 + lane * 16), wr_);
          }
          if (k == pf0 + W_T) { WTK(gT); }
          if (k == pf0 + A_PW) { WST(ou, wu); }
          if (k == pf0 + A_PW + W_T) { WTK(uT); }
          if (k == CPF_K && hw == 1 && lane < 12) PFL(cpf);
          if (k == 33 - NS) {
#pragma unroll
            for (int i = 0; i < P; ++i) voy[i] = HOFF((hw * 8 + i) * 8 + (lane >> 3)) - (i & 3) * 1024;
            yb = Hc, yo = mt << 8, ym = 15 << 7;
          }
          if (k == 32) {
            DLD(0, 8 - B_WN);
            if (J_L) JSET();
          }
          if (k == 33 && hw == 0) {
            if (U_D) __builtin_amdgcn_s_sleep(U_D);
            urdy = UPOLL(U_N1);
          }
        }
      }
    } else {
      const int aw = hw - 2;
      unsigned ow = wo - (2048 + (cg0 >> 4) * 1024) * 2;
      const unsigned oq = (unsigned)((nt * 64 + aw * 32 + (lane >> 4)) * D + mt * 128) * 2;
      u32x4 hv[8], wt[8], sc;
      {
        if (W_L == 1) {
          if (W_D) __builtin_amdgcn_s_sleep(W_D);
#pragma unroll
          for (int i = 0; i < 8; ++i) LDM(wv[i], wo + i * 12288, wmod);
        }
        if (PF23 == 1) {
          if (P_D5) __builtin_amdgcn_s_sleep(P_D5);
          PFL(PFG(2 + aw));
        }
        asm volatile("s_waitcnt vmcnt(%1)" : "+v"(cq) : "n"(11 + (PF23 == 1)) : "memory");
#pragma unroll
        for (int i = 0; i < 4; ++i) sc[i] = pack2(silu_bf(bflo(cq[i])), silu_bf(bfhi(cq[i])));
        asm volatile("" : "+v"(sc));
      }
#define GPUB(o, off)                                                                                   \
  if ((lane & 8) == 0)                                                                                 \
    asm volatile("global_store_dwordx4 %0, %1, %2 sc1 ; ALWAYS\n\ts_nop 1" ::"v"(off),                          \
                 "v"(u32x4{__float_as_uint(o[0]), (unsigned)tag, __float_as_uint(o[1]), (unsigned)tag}), "s"(ws) : "memory")
      const unsigned gpo = ks0 * 2048 + cg0 * 64 + (lane & 7) * 8 + (lane >> 4) * 2;
      {
        unsigned mpo = O_G1 + gpo * 8;
        if (W_L == 1) {
          TAGW(8);
          BAR(55);
          if (P_D2) __builtin_amdgcn_s_sleep(P_D2);
          PFL(PFG(2 + aw));
        }
        asm volatile("s_waitcnt vmcnt(%9)" : W8(wv), "+v"(mpo) : "n"(W_L == 1 ? 1 : 3 + (PF23 == 1)) : "memory");
        TR8(wt, wv);
        f32x2 o = MV64(wt, sc);
        asm volatile("" : "+v"(o));
        if (W_L != 1) {
          TAGW(PF23 == 1);
          BAR(55);
          if (P_D2) __builtin_amdgcn_s_sleep(P_D2);
          if (PF23 == 0) PFL(PFG(2 + aw));
        }
        GPUB(o, mpo);
      }
      unsigned vta = lds0 + STG + (g4 * 8 + ((lane >> 2) & 3)) * 256 + (((aw * 8 + ((lane >> 1) & 1)) ^ ((g4 & 1) * 8 + ((lane >> 2) & 3))) << 4) + (lane & 1) * 8;
      unsigned vso = O_VP + ((cc * 8 + mt) * 16 + nt) * 256 + aw * 128 + (lane & 3) * 32 + g4 * 8;
      asm volatile("" : "+v"(vta), "+v"(vso), "+v"(ow));
      unsigned f0 = O_XFL + ((xcd * 32 + 64 + aw * 128 + lane) & 255) * XSTR * 4, f1 = O_XFL + ((xcd * 32 + 128 + aw * 64 + lane) & 255) * XSTR * 4;
      unsigned lao = O_KEY + 512 * 1024 + b * 3584 + aw * 1536 + lane * 16;
      unsigned wao = (lane < 32 ? O_MODP : O_G2 - 512) + (ks0 * 2048 + cg0 * 64) * 8 + lane * 16;
      asm volatile("" : "+v"(wao));
      const char* pf4 = PFG(4 + aw);
      const char* pf6 = PFG(6 + aw);
      asm volatile("" : "+v"(f0), "+v"(f1), "+v"(lao), "+v"(pf4), "+v"(pf6));
      BT(57);
      BAR(0);
      float rs = 0.f, rs2 = 0.f;
      const unsigned ra = lds0 + (mt * 16 + aw * 8) * 128 + lane * 16;
      unsigned so = 0;
#define TSX()
#define RSUM()                                                                                                    \
  do {                                                                                                            \
    TSX();                                                                                                        \
    u32x4 R_;                                                                                                     \
    asm volatile("ds_read_b128 %0, %1\n\ts_waitcnt lgkmcnt(0)" : "=&v"(R_) : "v"(ra + so) : "memory");            \
    _Pragma("unroll") for (int j = 0; j < 4; ++j) {                                                               \
      asm volatile("v_add_f32 %0, %0, %1" : "+v"(rs) : "v"(R_[j] << 16));                                         \
      asm volatile("v_add_f32 %0, %0, %1" : "+v"(rs2) : "v"(R_[j] & 0xffff0000u));                                \
    }                                                                                                             \
    so = so == (NS - 1) * 16384 ? 0 : so + 16384;                                                                 \
  } while (0)
      {
        u64 f[2];
#define FLD() asm volatile("global_load_dwordx2 %0, %2, %4 sc1\n\tglobal_load_dwordx2 %1, %3, %4 sc1" : "=&v"(f[0]), "=&v"(f[1]) : "v"(f0), "v"(f1), "s"(ws) : "memory")
        __builtin_amdgcn_s_sleep(F_D);
        FLD();
        asm volatile("global_store_dwordx4 %0, %1, %2 offset:1024 sc1" ::"v"(lao), "v"(u32x4{~0u, ~0u, ~0u, ~0u}), "s"(ws) : "memory");
        if (!aw || lane >= 32) asm volatile("global_store_dwordx4 %0, %1, %2 sc1 ; ALWAYS" ::"v"(lao), "v"(u32x4{~0u, ~0u, ~0u, ~0u}), "s"(ws) : "memory");
        if (A_W) asm volatile("global_store_dwordx4 %0, %1, %2 sc1" ::"v"(wao), "v"(u32x4{0u, ~(unsigned)tag, 0u, ~(unsigned)tag}), "s"(ws) : "memory");
        asm volatile("s_nop 1");
        if (PF23 == 2) PFL(PFG(2 + aw));
        RSUM();
        BAR(1);
        RSUM();
        asm volatile("s_waitcnt vmcnt(%2)" : "+v"(f[0]), "+v"(f[1]) : "n"(2 + A_W + (PF23 == 2)) : "memory");
        POLL(__all(f[0] == tag && f[1] == tag), 1, FLD(); asm volatile("s_waitcnt vmcnt(0)" : "+v"(f[0]), "+v"(f[1])::"memory"));
      }
      BAR(2);
      if (X_PA) __builtin_amdgcn_s_setprio(X_PA);
#define ARUN(b0, b1) _Pragma("unroll 1") for (int bi = (b0); bi < (b1); ++bi) { RSUM(); BAR(bi + 1); }
#define GLD() _Pragma("unroll") for (int i = 0; i < 8; ++i) LDWI(wv[i], ow, wmod, 12288)
      ARUN(2, pf0);
      PFL(pf4);
      GLD();
#define HVLD() _Pragma("unroll") for (int i = 0; i < 8; ++i) GLDS(lds0 + STG + aw * 8192 + i * 1024, oq + i * 8192 + (((lane & 15) ^ ((i * 4 + (lane >> 4)) & 15)) << 4), cc ? X1 : X0)
      WST(og, wg);
      ARUN(pf0, pf0 + G1_C);
      {
        if (b >= NPART) WAIT_V(0);
        asm volatile("s_waitcnt vmcnt(8)" : W8(wv)::"memory");
        TR8(wt, wv);
        if (G1_S) { RSUM(); BAR(pf0 + G1_C + 1); }
        const f32x2 o = MV64(wt, sc);
        GPUB(o, O_MODP + gpo * 8);
        ow = wo + 2048;
      }
      ARUN(pf0 + G1_C + G1_S, pf0 + W_T - 1);
      WAIT_V(1);
      RSUM();
      BAR(pf0 + W_T);
      WTK(gT);
      ARUN(pf0 + W_T, pf0 + A_PW);
      PFL(pf6);
      WST(ou, wu);
      const int rb_ = b < NPART ? pf0 + A_PW + W_T : R_AT, hb_ = max(rb_ + 1, H_AT + (j8 & 3));
      const unsigned c0 = (b - NPART) * 64 + aw * 32 + (lane & 15) * 2, c1 = (b - NPART) * 32 + aw * 16 + (lane & 7) * 2;
#define ERLD(W)                                                                                                                     \
  asm volatile("global_load_dwordx4 %0, %8, %10 sc1\n\tglobal_load_dwordx4 %1, %8, %11 sc1\n\t"                                    \
               "global_load_dwordx4 %2, %8, %12 sc1\n\tglobal_load_dwordx4 %3, %8, %13 sc1\n\t"                                    \
               "global_load_dwordx4 %4, %9, %10 sc1\n\tglobal_load_dwordx4 %5, %9, %11 sc1\n\t"                                    \
               "global_load_dwordx4 %6, %9, %12 sc1\n\tglobal_load_dwordx4 %7, %9, %13 sc1" W                                      \
               : "=&v"(wt[0]), "=&v"(wt[1]), "=&v"(wt[2]), "=&v"(wt[3]), "=&v"(wt[4]), "=&v"(wt[5]), "=&v"(wt[6]), "=&v"(wt[7])     \
               : "v"(O_MODP + (g4 * 8192 + c0) * 8), "v"(O_G1 + (g4 * 8192 + (lane & 8) * 128 + c1) * 8), "s"(ws), "s"(ws + 16384), \
                 "s"(ws + 32768), "s"(ws + 49152)                                                                                   \
               : "memory")
      ARUN(pf0 + A_PW, rb_ - 3);
      if (b >= NPART) ERLD("");
      ARUN(rb_ - 3, rb_ - 1);
      WAIT_V(0);
      RSUM();
      BAR(rb_);
      WTK(uT);
      if (b >= NPART) {
        const unsigned wq = *reinterpret_cast<const unsigned*>(&svb[4][c1]);
#define TG2(r, i) ((r[i][1] ^ (unsigned)tag) | (r[i][3] ^ (unsigned)tag) | (r[i + 1][1] ^ (unsigned)tag) | (r[i + 1][3] ^ (unsigned)tag))
        asm volatile("" : W8(wt));
        POLL(__all((TG2(wt, 0) | TG2(wt, 2) | TG2(wt, 4) | TG2(wt, 6)) == 0u), 2, ERLD(VW0));
        auto FIN = [&](int q, int i, bool inc) __attribute__((always_inline)) {
          return rbf((inc ? 1.0f : 0.0f) + rbf(ar4((__uint_as_float(wt[q][i]) + __uint_as_float(wt[q + 1][i])) + (__uint_as_float(wt[q + 2][i]) + __uint_as_float(wt[q + 3][i])))));
        };
        const float s0 = FIN(0, 0, gbq), s1 = FIN(0, 2, gbq);
        if (lane < 16)
          asm volatile("global_store_dwordx4 %0, %1, %2 sc1\n\ts_nop 1" ::"v"(O_MODP + (16 * 2048 + c0) * 8),
                       "v"(u32x4{__float_as_uint(s0), (unsigned)tag, __float_as_uint(s1), (unsigned)tag}), "s"(ws) : "memory");
        const bool hi = lane & 8;
        const float t0 = FIN(4, 0, hi), t1 = FIN(4, 2, hi), o0 = dpp_f<0x128>(t0), o1 = dpp_f<0x128>(t1);
        const float a0 = hi ? o0 : t0, a1 = hi ? o1 : t1, e0 = hi ? t0 : o0, e1 = hi ? t1 : o1, p0 = e0 * bflo(wq), p1 = e1 * bfhi(wq);
        const unsigned h = pack2(p0, p1);
        const unsigned v = g4 == 0 ? __float_as_uint(hi ? a1 : a0) : g4 == 1 ? (hi ? h : pack2(e0, e1)) : hi ? pack2(a0 * p0, a1 * p1) : pack2(p0 - bflo(h), p1 - bfhi(h));
        const unsigned po = g4 == 0 ? O_M2 + c1 * 4 + (hi ? 4 : 0) : (g4 == 1 ? (hi ? O_M2 + 4096 : O_MOD + 4096) : hi ? O_GC : O_M2 + 6144) + c1 * 2;
        if (lane < 48) asm volatile("global_store_dword %0, %1, %2 sc1" ::"v"(po), "v"(min(v, SENT - 1u)), "s"(ws) : "memory");
      }
      ARUN(rb_, hb_);
      HVLD();
      ARUN(hb_, q1);
      u32x4 fm, g0, g1;
#define FMLD(W)                                                                                                                                     \
  asm volatile("global_load_dwordx4 %0, %3, %5 sc1\n\tglobal_load_dwordx4 %1, %4, %5 sc1\n\tglobal_load_dwordx4 %2, %4, %5 offset:64 sc1" W        \
               : "=&v"(fm), "=&v"(g0), "=&v"(g1) : "v"(O_MODP + (16 * 2048 + aw * 1024 + mt * 128) * 8 + lane * 16), "v"(O_GC + nt * 128 + g4 * 16), "s"(ws) : "memory")
      FMLD("");
      RSUM();
      BAR(q1 + 1);
      unsigned ha[4], sma = lds0 + SPARE + 15872 + (aw ? 512 + lane * 8 : lane * 4);
#pragma unroll
      for (int i = 0; i < 4; ++i) ha[i] = lds0 + STG + aw * 8192 + (lane & 15) * 256 + (((i * 4 + (lane >> 4)) ^ (lane & 15)) << 4);
      asm volatile("" : "+v"(ha[0]), "+v"(ha[1]), "+v"(ha[2]), "+v"(ha[3]), "+v"(sma), "+v"(ow));
      ARUN(q1 + 1, F_AT);
      asm volatile("s_waitcnt vmcnt(0)" : "+v"(fm), "+v"(g0), "+v"(g1)::"memory");
#pragma unroll
      for (int i = 0; i < 8; ++i) DSR(hv[i], ha[i & 3], (i >> 2) * 4096);
      POLL(__all(fm[1] == (unsigned)tag && fm[3] == (unsigned)tag && max(max4(g0), max4(g1)) != SENT), 1, FMLD(VW0));
      {
        const unsigned sg = cc ? 0x80000000u : 0u;
        asm volatile("ds_write_b32 %0, %2\n\tds_write_b32 %1, %3" ::"v"(sma), "v"(sma + (aw ? 4 : 256)), "v"(aw ? fm[0] ^ sg : (fm[0] >> 16) | (fm[2] & 0xffff0000u)), "v"(fm[2] ^ sg) : "memory");
      }
      ARUN(F_AT, 29);
      GLD();
      RSUM();
      BAR(30);
        u32x2 tv[16];
#pragma unroll
        for (int q = 0; q < 16; ++q)
          asm volatile("ds_read_b64_tr_b16 %0, %1 offset:%2" : "=v"(tv[q]) : "v"(vta ^ ((((q >> 2) * 2) ^ ((q & 1) * 4)) << 4)), "n"((q & 1) * 1024 + ((q >> 1) & 1) * 8192) : "memory");
      RSUM();
      BAR(31);
      RSUM();
      {
        const float v = dpp_add<0x141>(dpp_add<0x4E>(dpp_add<0xB1>(rs + rs2)));
        if ((lane & 7) == 0)
          asm volatile("global_store_dword %0, %1, %2 sc1 ; ALWAYS" ::"v"(O_RS + (cc * S + nt * 128 + mt * 16 + aw * 8 + (lane >> 3)) * 4), "v"(nsent(v)), "s"(ws) : "memory");
      }
      BAR(32);
      if (!V_F) {
        __builtin_amdgcn_s_sleep(2);
        DLD(0, 8 - B_WN);
      }
      __builtin_amdgcn_s_setprio(2);
      {
        asm volatile("" : W8(tv), W8((tv + 8))::"memory");
        f32x4 d[4];
#pragma unroll
        for (int j = 0; j < 4; ++j) {
          d[j] = __builtin_amdgcn_mfma_f32_16x16x32_bf16(BF8((u32x4{tv[4 * j][0], tv[4 * j][1], tv[4 * j + 1][0], tv[4 * j + 1][1]})), BF8(g0), f32x4{0.f, 0.f, 0.f, 0.f}, 0, 0, 0);
          d[j] = __builtin_amdgcn_mfma_f32_16x16x32_bf16(BF8((u32x4{tv[4 * j + 2][0], tv[4 * j + 2][1], tv[4 * j + 3][0], tv[4 * j + 3][1]})), BF8(g1), d[j], 0, 0, 0);
        }
        u32x2 vo;
#pragma unroll
        for (int i = 0; i < 2; ++i) {
          const unsigned e0 = pack2(d[0][2 * i], d[0][2 * i + 1]), e1 = pack2(d[1][2 * i], d[1][2 * i + 1]), e2 = pack2(d[2][2 * i], d[2][2 * i + 1]), e3 = pack2(d[3][2 * i], d[3][2 * i + 1]);
          vo[i] = min((lane & 2) ? ((lane & 1) ? e3 : e2) : ((lane & 1) ? e1 : e0), SENT - 1u);
        }
        if ((lane & 12) == 0) asm volatile("global_store_dwordx2 %0, %1, %2 sc1 ; ALWAYS" ::"v"(vso), "v"(vo), "s"(ws) : "memory");
      }
      if (V_F) { DLD(0, 8 - B_WN); }
      if (Q_L) BAR(33);
      asm volatile("" : W8(hv)::"memory");
      {
        f32x4 qa[2] = {};
#pragma unroll
        for (int kk = 0; kk < 4; ++kk) {
          const bf16x8 sb8 = *reinterpret_cast<const bf16x8*>(ssa + kk * 32 + (lane >> 4) * 8);
#pragma unroll
          for (int r = 0; r < 2; ++r)
            qa[r] = __builtin_amdgcn_mfma_f32_16x16x32_bf16(__builtin_bit_cast(bf16v8, hv[r * 4 + kk]), __builtin_bit_cast(bf16v8, sb8), qa[r], 0, 0, 0);
        }
        if ((lane & 15) < 2)
          asm volatile("global_store_dwordx4 %0, %1, %2 sc1 ; ALWAYS\n\ts_nop 1" ::"v"(O_RS + (4096 + (cc * 8 + mt) * D + nt * 64 + aw * 32 + (lane & 1) * 16 + (lane >> 4) * 4) * 4),
                       "v"(nsent4(selu((lane & 1) ? ~0u : 0u, __builtin_bit_cast(u32x4, qa[1]), __builtin_bit_cast(u32x4, qa[0])))), "s"(ws) : "memory");
      }
      if (!Q_L) BAR(33);
      {
        if (b >= NPART) WAIT_V(3);
        asm volatile("s_waitcnt vmcnt(%8)" : W8(wv) : "n"(8 - B_WN + 3) : "memory");
        TR8(wt, wv);
        const f32x2 o = MV64(wt, sc);
        GPUB(o, O_G2 + gpo * 8);
      }
      JSET();
      BAR(34);
#pragma unroll
      for (int k = 3; k <= B_J; ++k) {
        BAR(32 + k);
        if (k == V_K) {
          if (!aw) GP4(O_VP + (cc * 8 + mt) * 4096 + lane * 16, "");
          else VCLD("");
        }
      }
    }
    u32x4 dT[8];
    u32x4 m[3];
    u32x2 kk = {SENT, SENT};
#define K2LD(W) asm volatile("global_load_dwordx2 %0, %1, %2 sc1" W : "+v"(kk) : "v"(O_K2 + (lane & 15) * 64 + (xcd & (K_R - 1)) * 1024), "s"(ws) : "memory")
    {
      constexpr BSched sch;

      const unsigned lbase = lds0 + hw * 4096;
      u32x4 rq[2];
      u32x4 rr[8];
      unsigned cbv, t1, t2;
      float t[2];
#define RDLD() asm volatile("global_load_dwordx4 %0, %2, %3 sc1\n\tglobal_load_dwordx4 %1, %2, %4 sc1" : "=&v"(rq[0]), "=&v"(rq[1]) : "v"(ro), "s"(x), "s"(x + 4096) : "memory")
#define RRLD(W)                                                                                                                               \
  asm volatile("global_load_dwordx4 %0, %9, %10 sc1\n\tglobal_load_dwordx4 %1, %9, %11 sc1\n\t"                                              \
               "global_load_dwordx4 %2, %9, %12 sc1\n\tglobal_load_dwordx4 %3, %9, %13 sc1\n\t"                                              \
               "global_load_dwordx4 %4, %14, %10 sc1\n\tglobal_load_dwordx4 %5, %14, %11 sc1\n\t"                                            \
               "global_load_dwordx4 %6, %14, %12 sc1\n\tglobal_load_dwordx4 %7, %14, %13 sc1\n\tglobal_load_dword %8, %15, %10 sc1" W         \
               : "=&v"(rr[0]), "=&v"(rr[1]), "=&v"(rr[2]), "=&v"(rr[3]), "=&v"(rr[4]), "=&v"(rr[5]), "=&v"(rr[6]), "=&v"(rr[7]), "=&v"(cbv)  \
               : "v"(ro), "s"(ws), "s"(ws + 16384), "s"(ws + 32768), "s"(ws + 49152), "v"(ro + 65536), "v"(O_MOD + 4096 + cj * 2)            \
               : "memory")
      unsigned wq;
      unsigned mr;
#define QLD(W)                                                                                                           \
  asm volatile("global_load_dwordx4 %0, %3, %5 offset:-4096 sc1\n\tglobal_load_dwordx4 %1, %3, %5 sc1\n\tglobal_load_dword %2, %4, %5 sc1" W \
               : "=&v"(m[0]), "=&v"(m[1]), "=&v"(mr)                                                                     \
               : "v"(qo), "v"(so_), "s"(ws)                                                                              \
               : "memory")
#define MODLD()                                                                                                                \
  asm volatile("global_load_dwordx4 %0, %3, %6 sc1\n\tglobal_load_dwordx4 %1, %4, %6 sc1\n\tglobal_load_dwordx4 %2, %5, %6 sc1" \
               : "=&v"(m[0]), "=&v"(m[1]), "=&v"(m[2])                                                                         \
               : "v"(mo), "v"(mo1), "v"(mo2), "s"(ws)                                                                          \
               : "memory")
#define MODW(n) asm volatile("s_waitcnt vmcnt(%3)" : "+v"(m[0]), "+v"(m[1]), "+v"(m[2]) : "n"(n) : "memory")
#pragma unroll
      for (int k = B_J; k < 16; ++k) {
        if (k > B_J) {
          if (k == 2 * U_N1 - 1 && !urdy) UPOLL(7);
          WAIT_V(sch.cnt[k]);
          if (k == B_QAT + B_QD + 1) WAIT_L(0);
          BAR(32 + k);
        }
        if (k > B_J && k - 2 + NS < 16) {
          const char* yp = Hc + ((mt * 2 + k - 2 + NS) & 15) * 128;
          const unsigned l = lbase + ((30 + k + NS) % NS) * 16384;
          GLDS4(l, voff, yp);
        }
        if (k == B_TK && hw == 1 && b >= 192) {
          const unsigned t = tick();
          if (lane == 0) asm volatile("global_store_dword %0, %1, %2 sc1" ::"v"(O_M2 + 8448 + (b - 192) * 4), "v"(min(t, SENT - 1u)), "s"(ws) : "memory");
        }
        if (k == B_RAT + 2) {
          WAIT_V(sch.rcnt);
          if (rdc) {
            asm volatile("" : W8(rr), "+v"(cbv));
            POLL(__all((TG2(rr, 0) | TG2(rr, 2) | TG2(rr, 4) | TG2(rr, 6)) == 0u && cbv != SENT), 2, RRLD(VW0));
#pragma unroll
            for (int j = 0; j < 2; ++j) {
#define RF(q) __uint_as_float(rr[q][2 * j])
              t[j] = ((RF(0) + RF(1)) + (RF(2) + RF(3))) + ((RF(4) + RF(5)) + (RF(6) + RF(7)));
            }
            asm volatile("" : "+v"(t[0]), "+v"(t[1]));
          } else {
            asm volatile("" ::"v"(rq[0]), "v"(rq[1]));
          }
        }
        if (k == B_RAT + 2 + R_S && rdc) {
          const unsigned o = pack2(swap32_add(t[0], t[0]), swap32_add(t[1], t[1])), f = gbq ? cbv : o;
          const float pr = row_sum(bflo(f) * bflo(wq) + bfhi(f) * bfhi(wq)), ps = swap16_add(pr, pr);
          if (lane <= 32) asm volatile("global_store_dword %0, %1, %2 sc1" ::"v"(ra1), "v"(min(lane < 32 ? o : __float_as_uint(ps), SENT - 1u)), "s"(ws) : "memory");
          TS(12);
        }
        if (k == V_T && hw == 3) {
          WAIT_V(sch.vcnt);
          asm volatile("" : "+v"(vc));
          POLL(__all(vc != SENT), 2, VCLD(VW0));
        }
        if (k == V_T && hw == 2) {
          WAIT_V(sch.vcnt);
          asm volatile("" : "+v"(gp[0]), "+v"(gp[1]), "+v"(gp[2]), "+v"(gp[3]));
          POLL(!__any(GPBAD), 2, GP4(O_VP + (cc * 8 + mt) * 4096 + lane * 16, VW0));
          const f32x4 d = GPSUM();
          if (lane < 32) *reinterpret_cast<u32x2*>(shm + VT + (lane & 15) * 16 + g4 * 8) = u32x2{pack2(d[0], d[1]), pack2(d[2], d[3])};
          WAIT_L(0);
        }
        if (k == N_C) {
          if (N_P != 2) __builtin_amdgcn_s_setprio(N_P);
          f32x4 n[2] = {};
#pragma unroll
          for (int q = 0; q < 8; ++q)
            n[q >> 2] = __builtin_amdgcn_mfma_f32_16x16x32_bf16(BF8(*reinterpret_cast<const u32x4*>(shm + VT + (q & 3) * 64 + g4 * 16)),
                                                                BF8(*reinterpret_cast<const u32x4*>(shm + STG + hw * 8192 + q * 1024 + lane * 16)), n[q >> 2], 0, 0, 0);
          if (lane < 16)
            asm volatile("global_store_dword %0, %1, %2 sc1" ::"v"(O_NP + nt * 4096 + (cc * 8 + mt) * 256 + hw * 64 + lane * 4), "v"(min(pack2(n[0][0], n[1][0]), SENT - 1u)), "s"(ws) : "memory");
          if (N_P != 2) __builtin_amdgcn_s_setprio(2);
          TS(33);
        }
        if (k == N_K + N_D && rnp) {
            WAIT_V(sch.ncnt);
            asm volatile("" : "+v"(gp[0]), "+v"(gp[1]), "+v"(gp[2]), "+v"(gp[3]));
            POLL(!__any(GPBAD), 2, GP4(O_NP + nt * 4096 + lane * 16, VW0));
            const unsigned r0 = nt * 128 + ((lane >> 2) & 3) * 32 + (lane & 3) * 4 + (g4 & 1) * 2;
        const f32x4 d = GPSUM();
        unsigned ky[4];
#pragma unroll
        for (int i = 0; i < 4; ++i) {
          const unsigned f = __float_as_uint(d[i]);
          ky[i] = (((f ^ ((unsigned)((int)f >> 31) | 0x80000000u)) >> 1) & ~2047u) | (r0 + (i & 1) * 16 + (i >> 1));
        }
        t1 = max(max(ky[0], ky[1]), max(ky[2], ky[3])), t2 = max(min(max(ky[0], ky[1]), max(ky[2], ky[3])), max(min(ky[0], ky[1]), min(ky[2], ky[3])));
        asm volatile("" : "+v"(t1), "+v"(t2));
        }
        if (k == N_K + N_D + N_S && rnp) {
        TOP2(0xB1); TOP2(0x4E); TOP2(0x141); TOP2(0x140);
        TOP2S(__builtin_amdgcn_permlane16_swap);
            if (lane < K_R) asm volatile("global_store_dwordx2 %0, %1, %2 sc1" ::"v"(O_K2 + nt * 64 + lane * 1024), "v"(u32x2{t1, t2}), "s"(ws) : "memory");
            TS(34);
        }
        if (k == N_K && rnp) GP4(O_NP + nt * 4096 + lane * 16, "");
        if (k == B_QAT + B_QD) {
          asm volatile("s_waitcnt vmcnt(%3)" : "+v"(m[0]), "+v"(m[1]), "+v"(mr) : "n"(sch.qcnt) : "memory");
          POLL(__all(max(max(max4(m[0]), max4(m[1])), mr) != SENT), 2, QLD(VW0));
          const f32x4 t = __builtin_bit_cast(f32x4, m[0]) + __builtin_bit_cast(f32x4, m[1]);
          const float r = swap16_add(swap32_add(t[0], t[2]), swap32_add(t[1], t[3]));
          const unsigned a0 = pack2(r, r);
          asm volatile("ds_write_b64 %0, %1" ::"v"(qa), "v"(u32x2{a0, pack2(r - bflo(a0), r - bflo(a0))}) : "memory");
          const float rs = __uint_as_float(mr ^ ((hw & 1) ? 0x80000000u : 0u));
          const unsigned bb = pack2(rs, rs - rbf(rs));
          *reinterpret_cast<u32x2*>(shm + RKB + lane * 16 + (hw & 1) * 8) = u32x2{bb, bb};
        }
        if (k == CPF_B && hw == 2 && lane < 12) PFL(cpf);
        if (k == B_RAT) {
          if (rdc) RRLD(""); else RDLD();
          asm volatile("ds_read_b32 %0, %1\n\ts_waitcnt lgkmcnt(0)" : "=v"(wq) : "v"(wqa) : "memory");
        }
        if (k == B_QAT) QLD("");
        if (k == B_MAT) MODLD();
        if (k == B_WK) { DLD(8 - B_WN, 8); }
      }
      if (b >= NPART) WAIT_V(0);
      MODW(sch.mcnt);
      TS(30);
      SKEWPT();
      POLL(__all(max(max(max4(m[0]), max4(m[1])), max4(m[2])) != SENT), 4, MODLD(); MODW(0));
      *reinterpret_cast<u32x4*>(&svb[hw][lane * 8]) = m[0];
      *reinterpret_cast<u32x4*>(&svb[hw][512 + lane * 8]) = m[1];
      if (hw == 0) {
        asm volatile("ds_write_b128 %0, %1" ::"v"(ma2), "v"(m[2]) : "memory");
      } else if (hw == 2) {
        int d = max(max((int)(m[2][0] - ts0), (int)(m[2][1] - ts0)), max((int)(m[2][2] - ts0), (int)(m[2][3] - ts0)));
        d = max(d, (int)dpp_u<0xB1>(d));
        d = max(d, (int)dpp_u<0x4E>(d));
        d = max(d, (int)dpp_u<0x141>(d));
        d = max(d, (int)dpp_u<0x140>(d));
        const unsigned hh = ts0 + d + T_O + ((d - T_N) * T_A >> 8);
        ssel[7] = hh;
        if (K_E && (int)(tick() - (unsigned)__builtin_amdgcn_readfirstlane((int)hh) - T_K) >= 0) K2LD("");
      } else {
        const f32x4 v = __builtin_bit_cast(f32x4, m[2]);
        const f32x2 c = {v[0] + v[1], v[2] + v[3]};
        const float s4 = dpp_add<0x4E>(dpp_add<0xB1>(c[0] + c[1]));
        if (lane < 8) asm volatile("ds_write_b64 %0, %1" ::"v"(ma2), "v"(lane < 4 ? c : f32x2{s4, s4}) : "memory");
      }
    }
    if (hw != 2 || !L_PR) __builtin_amdgcn_s_setprio(0);
    TS(17);
    BT(48);
    LBAR();
    BTE();
    TS(18);
    if (hw == 2) {
      if (K_E) WAIT_V(0);
      asm volatile("" : "+v"(kk));
      if (__any(max(kk[0], kk[1]) == SENT)) {
        nap_tick((unsigned)__builtin_amdgcn_readfirstlane((int)ssel[7]) + T_K);
        POLL2(max(kk[0], kk[1]) == SENT, K_S, PCAP, K2LD(VW0));
      }
      TS(35);
      const unsigned me = lane < 32 ? kk[0] : kk[1], ot = (lane & 16) ? kk[1] : kk[0];
      unsigned rk = ot > me;
      sfor<15>([&](auto r) __attribute__((always_inline)) { rk += dpp_u<0x121 + decltype(r)::value>(ot) > me; });
      auto rs_ = __builtin_amdgcn_permlane16_swap(rk, rk, false, false);
      rk = rs_[0] + rs_[1];
      if (rk < 8u) {
        *reinterpret_cast<unsigned*>(shm + LST + rk * 4) = 0x8ffeu - (me & 2047u) * 2;
        if ((lane & 15) == nt) *reinterpret_cast<unsigned*>(shm + SLT + (me & 127u) * 4) = rk + 1;
      }
      asm volatile("" ::: "memory");
      ssel[20] = 1u;
      if (L_PR == 2) __builtin_amdgcn_s_setprio(0);
    }
    TS(19);
    unsigned sfa = lds0 + SPARE + 10368, sv, one = 1u;
    RETIRE();
    const int kc = (g & 3) * 256 + hw * 64 + (lane & 7) * 8;
    const u32x4 cbq = *reinterpret_cast<const u32x4*>(&svb[2][kc]), sbq = *reinterpret_cast<const u32x4*>(&svb[1][kc]);
    f32x4 cb2[2] = {lo4(cbq), hi4(cbq)}, sb2[2] = {lo4(sbq), hi4(sbq)};
    float sw = cwj[8], cj8 = cwj[lane & 7];
    unsigned rk2 = lds0 + LST + (lane >> 3) * 4;
    unsigned xo_ = O_X1C + (lane >> 3) * 2048 + kc * 2, hwa = lds0 + XC + hw * 1024 + (lane & 7) * 128 + (((lane >> 3) ^ lane) & 7) * 16;
    unsigned hra = lds0 + XC + g4 * 128 + ((lane ^ g4) & 7) * 16, hrb = hra ^ 64, ara = lds0 + SPARE + 15872 + (lane & 2) * 64 + g4 * 32 + (lane & 1) * 16;
    unsigned pso = O_PART + ((g >> 2) * 2048 + (lane & 8) * 128 + (g & 3) * 256 + hw * 64 + (lane & 7) * 8 + g4 * 2) * 4;
    unsigned gso = O_GU + g * 1024 + (lane >> 5) * 256 + hw * 64 + (lane & 15) * 4, awa = lds0 + SPARE + 15872 + lane * 4, sfw = sfa + hw * 4;
    unsigned gl0 = O_GU + (g >> 2) * 4096 + lane * 8, gl1 = O_GU + (xcd * 7 + (mt + 7 - W_UO) % 7) * 4096 + 512 + lane * 8;
    asm volatile("" : "+v"(sfa), "+v"(one), "+v"(hra), "+v"(hrb), "+v"(ara), "+v"(pso), "+v"(gso), "+v"(awa), "+v"(gl0), "+v"(gl1), "+v"(sfw), "+v"(qm));
    asm volatile("" : "+v"(cb2[0]), "+v"(cb2[1]), "+v"(sb2[0]), "+v"(sb2[1]), "+v"(hwa), "+v"(sw), "+v"(cj8), "+v"(rk2), "+v"(xo_));
    unsigned sto = O_ST2 + lane * 16, kmk = ~0u;
    asm volatile("" : "+v"(sto), "+v"(kmk));
    const unsigned th = (unsigned)__builtin_amdgcn_readfirstlane((int)ssel[7]);
    const char* wb = ws;
#define P2PUT(i) if (__builtin_expect(wb != ws, 0)) { WAIT_V(0); if (lane == 0) asm volatile("global_store_dwordx2 %0, %1, %2 sc1" ::"v"(O_F2 + (i)*8192 + (g * 4 + hw) * 8), "v"(tag), "s"(ws) : "memory"); }
#define P2GET(o) if (__builtin_expect(wb != ws, 0)) { u64 f; POLL2(f != tag, 4, PCAP, asm volatile("global_load_dwordx2 %0, %1, %2 sc1" VW0 : "=&v"(f) : "v"(O_F2 + (o)), "s"(ws) : "memory")); }
#pragma unroll 1
    for (;;) {
    TS(20);
    if (wb == ws) {
      unsigned r_;
      POLL2(sv == 0u, 2, 1 << 26, asm volatile("ds_read_b32 %0, %2 offset:80\n\tds_read_b32 %1, %3\n\ts_waitcnt lgkmcnt(0)" : "=&v"(sv), "=&v"(r_) : "v"(sfa), "v"(rk2) : "memory"));
      rk2 = r_;
    }
    TS(21);
    nap_tick(th + T_C3 - 10);
    if (D_G) {
      u32x4 dn;
      POLL2((dn[0] & dn[1] & dn[2] & dn[3]) == 0u, 1, 1 << 26, asm volatile("ds_read_b128 %0, %1 offset:96\n\ts_waitcnt lgkmcnt(0)" : "=v"(dn) : "v"(sfa) : "memory"));
      if (D_G > 1) __builtin_amdgcn_s_sleep(D_G);
    }
    __builtin_amdgcn_s_setprio(H_P);
    wait_tick(th + T_C3);
    TS(36);
    u32x4 st, xv_;
    unsigned cpy = 0;
    POLL2(max(max4(xv_), max4(st)) == SENT, 0, PCAP,
          if (cpy == D_R * X_CS) { __builtin_amdgcn_s_sleep(D_S2); cpy -= X_CS; } else if (D_S && cpy) __builtin_amdgcn_s_sleep(D_S);
          asm volatile("global_load_dwordx4 %0, %2, %4 sc1\n\tglobal_load_dwordx4 %1, %3, %4 sc1" VW0 : "=&v"(st), "=&v"(xv_) : "v"(sto), "v"(xo_), "s"(ws + cpy) : "memory");
          cpy += X_CS);
    TS(22);
    SKEWPT();
#define SUM8(v) dpp_add<0x141>(dpp_add<0x4E>(dpp_add<0xB1>(v)))
    const f32x4 sf = __builtin_bit_cast(f32x4, st);
    const float mean = SUM8(sf[0]) * 0.125f, dm = sf[0] - mean;
    const float rstd = __builtin_amdgcn_rsqf(SUM8(sf[1] + 128.0f * (dm * dm)) * (1.0f / D) + 1e-5f);
    asm volatile("ds_write_b128 %0, %1\n\tds_write_b32 %2, %3 offset:64" ::"v"(hwa),
                 "v"(pk8(((lo4(xv_) - mean) * rstd) * cb2[0] + sb2[0], ((hi4(xv_) - mean) * rstd) * cb2[1] + sb2[1])), "v"(sfw), "v"(one) : "memory");
    TS(3);
    u32x4 ha[8], fg;
#define HSLD(W)                                                                                                                             \
  asm volatile(                                                                                                                             \
      "ds_read_b128 %8, %11 offset:64\n\tds_read_b128 %0, %9\n\tds_read_b128 %1, %10 offset:512\n\tds_read_b128 %2, %9 offset:1024\n\t"       \
      "ds_read_b128 %3, %10 offset:1536\n\tds_read_b128 %4, %9 offset:2048\n\tds_read_b128 %5, %10 offset:2560\n\t"                          \
      "ds_read_b128 %6, %9 offset:3072\n\tds_read_b128 %7, %10 offset:3584" W                                                               \
      : "=&v"(ha[0]), "=&v"(ha[1]), "=&v"(ha[2]), "=&v"(ha[3]), "=&v"(ha[4]), "=&v"(ha[5]), "=&v"(ha[6]), "=&v"(ha[7]), "=&v"(fg)           \
      : "v"(hra), "v"(hrb), "v"(sfa)                                                                                                        \
      : "memory")
    unsigned key = (mkkey(rstd * SUM8(sf[2] + dm * cj8) + sw) | rk2) & kmk;
    asm volatile("" : "+v"(key));
    HSLD("");
    unsigned t1 = key, t2 = 0u;
    TOP2(0x140);
    TOP2S(__builtin_amdgcn_permlane16_swap);
    TOP2S(__builtin_amdgcn_permlane32_swap);
    const int l1 = __builtin_ctzll(__builtin_amdgcn_ballot_w64(key == t1)), l2 = __builtin_ctzll(__builtin_amdgcn_ballot_w64(key == t2));
    unsigned mq[6];
    asm volatile("s_bfe_i32 %0, %6, 0x10003\n\ts_bfe_i32 %1, %7, 0x10003\n\ts_bfe_i32 %2, %6, 0x10004\n\ts_bfe_i32 %3, %7, 0x10004\n\t"
                 "s_bfe_i32 %4, %6, 0x10005\n\ts_bfe_i32 %5, %7, 0x10005\n\ts_xor_b32 %4, %4, 0xffff\n\ts_xor_b32 %5, %5, 0xffff"
                 : "=&s"(mq[0]), "=&s"(mq[1]), "=&s"(mq[2]), "=&s"(mq[3]), "=&s"(mq[4]), "=&s"(mq[5]) : "s"(l1), "s"(l2) : "scc");
    const u64 m0 = mk64(mq[1], mq[0]), m1 = mk64(mq[3], mq[2]), m2 = mk64(mq[5], mq[4]);
    asm volatile("s_waitcnt lgkmcnt(0)" : W8(ha), "+v"(fg)::"memory");
    TS(2);
    if (b < NPART) {
#define HBAD ((fg[0] & fg[1] & fg[2] & fg[3]) != one)
      if (__any(HBAD)) POLL2(HBAD, 0, 1 << 26, HSLD("\n\ts_waitcnt lgkmcnt(0)"));
      TS(23);
      f32x4 dg = {0.f, 0.f, 0.f, 0.f}, du = {0.f, 0.f, 0.f, 0.f};
#pragma unroll
      for (int f = 0; f < 8; ++f) {
        dg = __builtin_amdgcn_mfma_f32_16x16x32_bf16(BF8(ha[f]), BF8(gT[f]), dg, 0, 0, 0);
        du = __builtin_amdgcn_mfma_f32_16x16x32_bf16(BF8(ha[f]), BF8(uT[f]), du, 0, 0, 0);
      }
      const bool s0 = __builtin_amdgcn_inverse_ballot_w64(m0), s1 = __builtin_amdgcn_inverse_ballot_w64(m1);
      if (__builtin_amdgcn_inverse_ballot_w64(m2))
        asm volatile("global_store_dword %0, %1, %3 sc1 ; ALWAYS\n\tglobal_store_dword %0, %2, %3 offset:512 sc1" ::"v"(gso),
                     "v"(nsent(s1 ? (s0 ? dg[3] : dg[2]) : (s0 ? dg[1] : dg[0]))), "v"(nsent(s1 ? (s0 ? du[3] : du[2]) : (s0 ? du[1] : du[0]))), "s"(wb) : "memory");
    }
    if (hw == 0 && wb == ws) asm volatile("ds_write_b64 %0, %1" ::"v"(sfa), "v"(u32x2{t1, t2}) : "memory");
    if (b < NPART) {
      TS(24);
      asm volatile("" : W8(wdv)::"memory");
      TR8(dT, wdv);
      P2PUT(0);
      if (hw == 0) {
        u32x2 rr[8];
        if (E_D) __builtin_amdgcn_s_sleep(E_D);
        TS(39);
        P2GET(((((lane & 16) ? gl1 : gl0) - O_GU) >> 12) * 128 + (lane & 15) * 8);
#define MX2(i) max(max(rr[i][0], rr[i][1]), max(rr[i + 1][0], rr[i + 1][1]))
        POLL2(max(max(MX2(0), MX2(2)), max(MX2(4), MX2(6))) == SENT, 1, PCAP,
              TS(40);
              asm volatile("global_load_dwordx2 %0, %8, %10 sc1\n\tglobal_load_dwordx2 %1, %8, %10 offset:1024 sc1\n\t"
                           "global_load_dwordx2 %2, %8, %10 offset:2048 sc1\n\tglobal_load_dwordx2 %3, %8, %10 offset:3072 sc1\n\t"
                           "global_load_dwordx2 %4, %9, %10 sc1\n\tglobal_load_dwordx2 %5, %9, %10 offset:1024 sc1\n\t"
                           "global_load_dwordx2 %6, %9, %10 offset:2048 sc1\n\tglobal_load_dwordx2 %7, %9, %10 offset:3072 sc1" VW0
                           : "=&v"(rr[0]), "=&v"(rr[1]), "=&v"(rr[2]), "=&v"(rr[3]), "=&v"(rr[4]), "=&v"(rr[5]), "=&v"(rr[6]), "=&v"(rr[7])
                           : "v"(gl0), "v"(gl1), "s"(wb)
                           : "memory"));
        TS(8);
#define R2(i) __builtin_bit_cast(f32x2, rr[i])
        const f32x2 gs = (R2(0) + R2(1)) + (R2(2) + R2(3)), us = (R2(4) + R2(5)) + (R2(6) + R2(7));
        const unsigned gp = pack2(gs[0], gs[1]), up = pack2(us[0], us[1]);
        const unsigned sp = pack2(bflo(gp) * __builtin_amdgcn_rcpf(1.0f + __expf(-bflo(gp))), bfhi(gp) * __builtin_amdgcn_rcpf(1.0f + __expf(-bfhi(gp))));
        asm volatile("ds_write_b32 %0, %1\n\tds_write_b32 %2, %3 offset:24" ::"v"(awa), "v"(pack2(bflo(sp) * bflo(up), bfhi(sp) * bfhi(up))), "v"(sfa), "v"(one) : "memory");
      }
      TS(25);
      SKEWPT();
      POLL2(sv != one, 0, 1 << 26, asm volatile("ds_read_b32 %1, %3 offset:24\n\tds_read_b128 %0, %2\n\ts_waitcnt lgkmcnt(0)" : "=&v"(fg), "=&v"(sv) : "v"(ara), "v"(sfa) : "memory"));
      if (VD_E == 1) asm volatile("global_load_dwordx2 %0, %1, %2 sc1" : "=&v"(vd) : "v"(O_VD + (b & 31) * 128), "s"(ws) : "memory");
      const f32x2 dd = MV64(dT, fg);
      asm volatile("global_store_dwordx2 %0, %1, %2 sc1" ::"v"(pso), "v"(u32x2{nsent(dd[0]), nsent(dd[1])}), "s"(wb) : "memory");
      TS(6);
      P2PUT(1);
      if (VD_E == 1) {
        asm volatile("s_waitcnt vmcnt(1)" : "+v"(vd)::"memory");
        if (__any(max(vd[0], vd[1]) == SENT)) VDLD(2);
      } else if (VD_E == 2) {
        if (hw == 1) {
          nap_tick(th + T_C5);
          VDLD(2);
          asm volatile("ds_write_b64 %0, %1 offset:8" ::"v"(sfa), "v"(vd) : "memory");
        } else {
          POLL2(vd[1] == 0u, 2, 1 << 26, asm volatile("ds_read_b64 %0, %1 offset:8\n\ts_waitcnt lgkmcnt(0)" : "=v"(vd) : "v"(sfa) : "memory"));
        }
      } else if (VD_E == 3) {
        break;
      } else {
        nap_tick(th + T_C5);
        VDLD(2);
      }
      TS(5);
    } else if (hw == 3) {
      u32x2 g0, g1;
      nap_tick(th + T_C4);
      POLL2(min(min(g0[0], g0[1]), min(g1[0], g1[1])) == 0u, 2, PCAP,
            asm volatile("global_load_dwordx2 %0, %2, %4 sc1\n\tglobal_load_dwordx2 %1, %3, %4 sc1" VW0
                         : "=&v"(g0), "=&v"(g1) : "v"(O_KEY + lane * 8 * KSTR), "v"(O_KEY + (lane + 64) * 8 * KSTR), "s"(ws) : "memory"));
      const unsigned k1 = t1, k2 = t2;
      t1 = max(g0[0], g1[0]), t2 = max(min(g0[0], g1[0]), max(g0[1], g1[1]));
      TOP2(0xB1); TOP2(0x4E); TOP2(0x141); TOP2(0x140);
      TOP2S(__builtin_amdgcn_permlane16_swap);
      TOP2S(__builtin_amdgcn_permlane32_swap);
      vd = u32x2{max(t1, k1), max(min(t1, k1), max(t2, k2))};
      if (lane == 0) asm volatile("global_store_dwordx2 %0, %1, %2 sc1\n\tds_write_b64 %3, %1 offset:8" ::"v"(O_VD + (b & 31) * 128), "v"(vd), "s"(ws), "v"(sfa) : "memory");
      TS(29);
      break;
    } else if (hw < 2) {
      const int tok = b & 1, c0 = ((b - NPART) >> 1) * 64 + hw * 32 + (lane & 7) * 4, col = c0 + g4;
      const unsigned td = tick(), kk = tok ? t2 : t1;
      const int ln = tok ? l2 : l1;
      unsigned v;
      POLL2(v == SENT, 1, PCAP, asm volatile("global_load_dword %0, %1, %2 sc1" VW0 : "=&v"(v) : "v"(O_X1C + ((__builtin_amdgcn_readlane(sto, ln) - O_ST2) >> 7) * 2048 + (col >> 1) * 4), "s"(ws) : "memory"));
      float x1 = (col & 1) ? bfhi(v) : bflo(v);
      float h2 = rbf(((x1 - __int_as_float(__builtin_amdgcn_readlane(__float_as_int(mean), ln))) * __int_as_float(__builtin_amdgcn_readlane(__float_as_int(rstd), ln))) * bf2f(svb[2][col]) + bf2f(svb[1][col]));
      float gb = bf2f(svb[3][col]), rsg = rbf(1.0f / (1.0f + __expf(-key_val(kk))));
      unsigned oo_ = (key_row(kk) * D + col) * 2, po = O_PART + ((lane >> 3) * 2048 + tok * 1024 + c0) * 4, p1 = po + 4 * 65536;
      asm volatile("" : "+v"(x1), "+v"(h2), "+v"(gb), "+v"(rsg), "+v"(oo_), "+v"(po), "+v"(p1));
      u32x4 y[7];
      nap_tick(td + F_T - 10);
      wait_tick(td + F_T);
      TS(37);
      P2GET(8192 + (min(lane, 55) * 16 + ((b - NPART) >> 1)) * 8);
      POLL2(max(max(max(max4(y[0]), max4(y[1])), max(max4(y[2]), max4(y[3]))), max(max(max4(y[4]), max4(y[5])), max4(y[6]))) == SENT || vd[1] == 0u, 0, PCAP,
            TS(38);
            asm volatile("global_load_dwordx4 %0, %7, %9 sc1\n\tglobal_load_dwordx4 %1, %7, %10 sc1\n\tglobal_load_dwordx4 %2, %7, %11 sc1\n\t"
                         "global_load_dwordx4 %3, %7, %12 sc1\n\tglobal_load_dwordx4 %4, %8, %9 sc1\n\tglobal_load_dwordx4 %5, %8, %10 sc1\n\t"
                         "global_load_dwordx4 %6, %8, %11 sc1"
                         : "=&v"(y[0]), "=&v"(y[1]), "=&v"(y[2]), "=&v"(y[3]), "=&v"(y[4]), "=&v"(y[5]), "=&v"(y[6])
                         : "v"(po), "v"(p1), "s"(wb), "s"(wb + 65536), "s"(wb + 131072), "s"(wb + 196608)
                         : "memory");
            asm volatile("s_waitcnt vmcnt(2)\n\tds_read_b64 %7, %8 offset:8\n\ts_waitcnt vmcnt(0) lgkmcnt(0)"
                         : "+v"(y[0]), "+v"(y[1]), "+v"(y[2]), "+v"(y[3]), "+v"(y[4]), "+v"(y[5]), "+v"(y[6]), "=&v"(vd) : "v"(sfa) : "memory"));
      TS(28);
#define YF(i) __builtin_bit_cast(f32x4, y[i])
      const f32x4 t = ((YF(0) + YF(1)) + (YF(2) + YF(3))) + ((YF(4) + YF(5)) + YF(6));
      const float sum = dpp_add<0x128>(swap16_add(swap32_add(t[0], t[2]), swap32_add(t[1], t[3])));
      asm volatile("global_store_short %0, %1, %2" ::"v"(oo_), "v"(pack2(x1 + gb * (wb == ws && kk < vd[1] ? h2 : rbf(h2 + rbf(rbf(sum) * rsg))), 0.f)), "s"(out) : "memory");
      TS(15);
    } else {
      break;
    }
    if (__builtin_expect(wb != ws || __all(vd[1] == t2), 1)) break;
    const unsigned tk = (lane & 8) ? vd[1] : vd[0], ns = tk == t1 ? l1 >> 3 : 8 + ((lane >> 3) & 1);
    rk2 = tk & 0xffffu, kmk = lane < 16 ? ~0u : 0u, one = 3u;
    xo_ = O_X1C + ns * 2048 + kc * 2, sto = O_ST2 + ns * 128 + (lane & 7) * 16;
    wb = ws + O_P2;
    }
    TSE();
  }
}

extern "C" void kernel_launch(void* const* d_in, const int* in_sizes, int n_in, void* d_out, int out_size, void* d_ws, size_t ws_size,
                              hipStream_t stream) {
  const u16* x = (const u16*)d_in[0];
  const u16* wsr = (const u16*)d_in[1];
  const u16* wsi = (const u16*)d_in[2];
  const u16* whr = (const u16*)d_in[3];
  const u16* whi = (const u16*)d_in[4];
  const u16* w_r = (const u16*)d_in[6];
  const u16* w_gate = (const u16*)d_in[7];
  const u16* w_up = (const u16*)d_in[8];
  const u16* w_down = (const u16*)d_in[9];
  const u16* c = (const u16*)d_in[10];
  const u16* w_mod = (const u16*)d_in[11];
  u16* out = (u16*)d_out;
  k_main<<<256, 512, 0, stream>>>(x, (char*)d_ws, wsr, wsi, c, w_mod, whr, whi, w_r, w_gate, w_up, w_down, out);
}
```

```cpp
#include <hip/hip_runtime.h>
#include <hip/hip_bf16.h>
#include <stdint.h>
#include <utility>

#define NEWLAY 1
typedef unsigned short u16;
typedef unsigned long long u64;
using bf16x8 = __attribute__((ext_vector_type(8))) short;
using bf16v8 = __attribute__((ext_vector_type(8))) __bf16;
using f32x4 = __attribute__((ext_vector_type(4))) float;
using f32x2 = __attribute__((ext_vector_type(2))) float;
using u32x4 = __attribute__((ext_vector_type(4))) unsigned;
using u32x2 = __attribute__((ext_vector_type(2))) unsigned;

constexpr int S = 2048, D = 1024, F = 3584;

__device__ __forceinline__ float bf2f(unsigned v) { return __uint_as_float(v << 16); }
__device__ __forceinline__ float bflo(unsigned v) { return __uint_as_float(v << 16); }
__device__ __forceinline__ float bfhi(unsigned v) { return __uint_as_float(v & 0xffff0000u); }
__device__ __forceinline__ unsigned f2bf(float f) {
  __hip_bfloat16 b = __float2bfloat16(f);
  return (unsigned)*reinterpret_cast<u16*>(&b);
}
__device__ __forceinline__ float rbf(float f) { return bf2f(f2bf(f)); }
using bf16v2 = __attribute__((ext_vector_type(2))) __bf16;
__device__ __forceinline__ unsigned pack2(float lo, float hi) {
  return __builtin_bit_cast(unsigned, __builtin_convertvector(f32x2{lo, hi}, bf16v2));
}

template <int CTRL>
__device__ __forceinline__ float dpp_f(float v) {
  return __int_as_float(__builtin_amdgcn_mov_dpp(__float_as_int(v), CTRL, 0xf, 0xf, true));
}
template <int CTRL>
__device__ __forceinline__ float dpp_add(float v) { return v + dpp_f<CTRL>(v); }
__device__ __forceinline__ float row_sum(float v) { return dpp_add<0x140>(dpp_add<0x141>(dpp_add<0x4E>(dpp_add<0xB1>(v)))); }
template <int CTRL>
__device__ __forceinline__ unsigned dpp_u(unsigned v) { return (unsigned)__builtin_amdgcn_mov_dpp((int)v, CTRL, 0xf, 0xf, true); }
__device__ __forceinline__ u64 mk64(unsigned hi, unsigned lo) { return ((u64)hi << 32) | lo; }
__device__ __forceinline__ float swap32_add(float a, float b) {
  auto r = __builtin_amdgcn_permlane32_swap(__float_as_uint(a), __float_as_uint(b), false, false);
  return __uint_as_float(r[0]) + __uint_as_float(r[1]);
}
__device__ __forceinline__ float swap16_add(float a, float b) {
  auto r = __builtin_amdgcn_permlane16_swap(__float_as_uint(a), __float_as_uint(b), false, false);
  return __uint_as_float(r[0]) + __uint_as_float(r[1]);
}
__device__ __forceinline__ float ar4(float v) {
  const float t = swap32_add(v, v);
  return swap16_add(t, t);
}
__device__ __forceinline__ float wave_sum_s(float v) {
  v = row_sum(v);
  v += __int_as_float(__builtin_amdgcn_update_dpp(0, __float_as_int(v), 0x142, 0xa, 0xf, false));
  v += __int_as_float(__builtin_amdgcn_update_dpp(0, __float_as_int(v), 0x143, 0xc, 0xf, false));
  return __int_as_float(__builtin_amdgcn_readlane(__float_as_int(v), 63));
}
__device__ __forceinline__ float silu_bf(float c) { return rbf(c * __builtin_amdgcn_rcpf(1.0f + __expf(-c))); }
__device__ __forceinline__ float dsum(unsigned v, float a) {
  return __builtin_amdgcn_fdot2_f32_bf16(__builtin_bit_cast(bf16v2, v), __builtin_bit_cast(bf16v2, 0x3f803f80u), a, false);
}
template <class Fn, int... I>
__device__ __forceinline__ void sfor_(Fn&& f, std::integer_sequence<int, I...>) { (f(std::integral_constant<int, I>{}), ...); }
template <int N, class Fn>
__device__ __forceinline__ void sfor(Fn&& f) { sfor_(f, std::make_integer_sequence<int, N>{}); }
constexpr unsigned SENT = 0xFFFFFFFFu;
constexpr int NPART = 224;

constexpr size_t WS_EPO = 0;
constexpr size_t WS_XFL = 4096;
constexpr size_t WS_MODP = 40960;
constexpr size_t WS_KEYG = 320u << 10;
constexpr size_t WS_UFL = WS_KEYG + 16384;
constexpr size_t WS_MOD = WS_KEYG + (80u << 10);
constexpr size_t WS_RS = WS_KEYG + (92u << 10);
constexpr size_t WS_PART = WS_KEYG + (316u << 10);
constexpr unsigned O_MODP = WS_MODP, O_XFL = WS_XFL, O_KEY = WS_KEYG, O_MOD = WS_MOD, O_RS = WS_RS, O_M2 = WS_RS + 80 * 1024;
constexpr unsigned O_PART = WS_PART, O_GU = O_PART + 56 * 8192, O_STAT = O_GU + 224 * 1024, O_X1C = O_STAT + 262144;
constexpr unsigned O_ST2 = O_X1C + 20480, O_VD = O_ST2 + 2048, O_P2 = 1536u << 10, O_F2 = 3584u << 10;
constexpr unsigned O_GC = O_M2 + 8704, O_VP = O_M2 + 12288, O_NP = O_VP + 65536, O_K2 = O_NP + 65536, O_G2 = 3u << 20, O_G1 = O_G2 + 262144;
static_assert(O_K2 + 4096 == O_PART && O_VD + 4096 <= O_KEY + (512 + 768) * 1024 && O_PART + O_P2 >= O_VD + 4096 && O_STAT + O_P2 <= O_G2, "armed area");
constexpr size_t WS_H = 4u << 20;
constexpr size_t WS_T = WS_H + (size_t)S * D * 2;
constexpr size_t WS_P = WS_T + (size_t)2 * D * S * 2;

#define TSG(base, i)
#define TSD(i)
#define PCAP (1 << 20)
#define PDBG(it)
#define TS(i)
#define TSE()
#define BT(k)
#define BTE()
#define BAR(k) do { BT(k); BARRIER(); } while (0)
#define WAIT_V(n) asm volatile("s_waitcnt vmcnt(%0)" ::"n"(n) : "memory")
#define WAIT_L(n) asm volatile("s_waitcnt lgkmcnt(%0)" ::"n"(n) : "memory")
#define BARRIER() asm volatile("s_barrier" ::: "memory")
#define LBAR() asm volatile("s_waitcnt lgkmcnt(0)\n\ts_barrier" ::: "memory")
#define DSR(dst, addr, off) asm volatile("ds_read_b128 %0, %1 offset:%2" : "=v"(dst) : "v"(addr), "n"(off) : "memory")
#define MFMA(c, a, b) asm volatile("v_mfma_f32_16x16x32_bf16 %0, %1, %2, %0" : "+v"(c) : "v"(a), "v"(b))
#define MFMA0(c, a, b) asm volatile("v_mfma_f32_16x16x32_bf16 %0, %1, %2, 0" : "=&v"(c) : "v"(a), "v"(b))
#define GLDS(m0v, voff, gbase) \
  asm volatile("s_mov_b32 m0, %0\n\ts_nop 0\n\tglobal_load_lds_dwordx4 %1, %2" ::"s"(m0v), "v"(voff), "s"(gbase) : "memory")
#define GLDS4(m0v, v, gbase)                                                                                                        \
  asm volatile("s_mov_b32 m0, %0\n\ts_nop 0\n\tglobal_load_lds_dwordx4 %1, %5\n\tglobal_load_lds_dwordx4 %2, %5 offset:1024\n\t"    \
               "global_load_lds_dwordx4 %3, %5 offset:2048\n\tglobal_load_lds_dwordx4 %4, %5 offset:3072" ::"s"(m0v),               \
               "v"((v)[0]), "v"((v)[1]), "v"((v)[2]), "v"((v)[3]), "s"(gbase) : "memory")
#define LDG(dst, voff, base) asm volatile("global_load_dwordx4 %0, %1, %2" : "=v"(dst) : "v"(voff), "s"(base) : "memory")
#define WPOL " nt"
#define LDM(dst, voff, base) asm volatile("global_load_dwordx4 %0, %1, %2" WPOL : "=v"(dst) : "v"(voff), "s"(base) : "memory")
#define LDX(dst, voff, base, off) asm volatile("global_load_dwordx4 %0, %1, %2 offset:%3" : "=v"(dst) : "v"(voff), "s"(base), "n"(off) : "memory")
#define LDXS(dst, voff, base, off) asm volatile("global_load_dwordx4 %0, %1, %2 offset:%3 sc1" : "=v"(dst) : "v"(voff), "s"(base), "n"(off) : "memory")
#define BF8(v) __builtin_bit_cast(bf16v8, v)
#define LSPIN(v, p, cond)                                   \
  _Pragma("unroll 1") for (int it_ = 0; it_ < (1 << 26); ++it_) { \
    v = __atomic_load_n(p, __ATOMIC_RELAXED);               \
    if (cond) break;                                        \
    __builtin_amdgcn_s_sleep(1);                            \
  }
#define POLL(ok, sl, reload)                                                  \
  if (__builtin_expect(!(ok), 0)) {                                           \
    _Pragma("unroll 1") for (int it = 0; it < PCAP; ++it) {                   \
      PDBG(it);                                                               \
      __builtin_amdgcn_s_sleep(sl);                                           \
      reload;                                                                 \
      if (ok) break;                                                          \
    }                                                                         \
  }
#define POLL2(bad, sl, cap, load)                                             \
  {                                                                           \
    int it = (cap);                                                           \
    u64 live;                                                                 \
    _Pragma("unroll 1") do {                                                  \
      if (sl) __builtin_amdgcn_s_sleep(sl);                                   \
      asm volatile("s_sub_u32 %0, %0, 1\n\ts_cselect_b64 %1, 0, -1" : "+s"(it), "=s"(live)::"scc"); \
      load;                                                                   \
    } while (__builtin_amdgcn_ballot_w64(bad) & live);                        \
    PDBG(PCAP - 2 - it);                                                      \
  }
__device__ __forceinline__ unsigned tick() {
  u64 t;
  asm volatile("s_memrealtime %0\n\ts_waitcnt lgkmcnt(0)" : "=s"(t)::"memory");
  return (unsigned)t;
}
__device__ __forceinline__ void wait_tick(unsigned t) {
#pragma unroll 1
  for (int i = 0; i < 256; ++i)
    if ((int)(tick() - t) >= 0) break;
}
__device__ __forceinline__ void nap_tick(unsigned t) {
#pragma unroll 1
  for (int i = 0; i < 1024; ++i) {
    if ((int)(tick() - t) >= 0) break;
    __builtin_amdgcn_s_sleep(2);
  }
}
#define T_C1 245
#define T_A 40
#define T_N 2500
#define T_D1 78
#define VW0 "\n\ts_waitcnt vmcnt(0)"
#define W8(a) "+v"(a[0]), "+v"(a[1]), "+v"(a[2]), "+v"(a[3]), "+v"(a[4]), "+v"(a[5]), "+v"(a[6]), "+v"(a[7])
#define PFL(ptr) asm volatile("s_mov_b32 m0, %0\n\ts_nop 0\n\tglobal_load_lds_dword %1, off" ::"s"(pfl), "v"(ptr) : "memory")

#define CPFL() asm volatile("s_mov_b32 m0, %0\n\ts_mov_b64 exec, 0xfff\n\tglobal_load_lds_dword %1, off\n\ts_mov_b64 exec, -1" ::"s"(pfl), "v"(cpf) : "memory")
#define HHOOK(j)
#define XPV 1557
#define XPW 1557
#define XPM (MODE ? XPW : XPV)
#define XPA0 (XPM / 1000)
#define XPA1 (XPM / 100 % 10)
#define XPB0 (XPM / 10 % 10)
#define XPB1 (XPM % 10)
#define U_X0 6
#define XLD(dst, XP, XH, i)                                                                                   \
  do {                                                                                                        \
    if (MODE) LDXS(dst, (i) ? xo1 : xo0, XP, ((XH)&3) * 2048 - 4096);                                         \
    else LDXS(dst, ((XH)&4) ? xo1 : xo0, XP, ((XH)&3) * 2048 + (i)*1024 - 4096);                              \
  } while (0)
#define HALF(XC, YA, RD, XN, XP, XH, LD, YP)                                                 \
  do {                                                                                       \
    _Pragma("unroll") for (int j = 0; j < 8; ++j) {                                          \
      if ((j & 1) == 0) { if (RD) WAIT_L(5); else WAIT_L(j == 0 ? 5 : 6 - j); }              \
      if (MODE == 2) MFMA0(acc[0][j], Y[j], XC[0]); else if (MODE) MFMA(acc[0][j], Y[j], XC[0]); else MFMA(acc[0][j], XC[0], Y[j]); \
      if (j == 0) DSR(Y[7], YP, 7 * 2048);                                                   \
      else if (RD) DSR(Y[j - 1], YA, (j - 1) * 2048);                                        \
      if (MODE == 2) MFMA0(acc[1][j], Y[j], XC[1]); else if (MODE) MFMA(acc[1][j], Y[j], XC[1]); else MFMA(acc[1][j], XC[1], Y[j]); \
      if ((LD) && j == (((XH)&1) ? XPA0 : XPB0)) XLD(XN[0], XP, XH, 0);                                             \
      if ((LD) && j == (((XH)&1) ? XPA1 : XPB1)) XLD(XN[1], XP, XH, 1);                                             \
      HHOOK(j);                                                                              \
    }                                                                                        \
  } while (0)

__device__ __forceinline__ int key_row(unsigned k) { return (int)((~k >> 1) & 2047u); }
__device__ __forceinline__ unsigned mkkey(float v) {
  const unsigned rb = pack2(0.f, v);
  return rb ^ ((unsigned)((int)rb >> 31) & 0x7fff0000u) ^ 0x80000000u;
}
__device__ __forceinline__ float key_val(unsigned k) {
  const unsigned ord = k >> 16;
  return bf2f(ord ^ ((ord & 0x8000u) ? 0x8000u : 0xffffu));
}
__device__ __forceinline__ unsigned nsent(float v) { return min(__float_as_uint(v), SENT - 1u); }
__device__ __forceinline__ f32x4 lo4(u32x4 q) { return f32x4{bflo(q[0]), bfhi(q[0]), bflo(q[1]), bfhi(q[1])}; }
__device__ __forceinline__ f32x4 hi4(u32x4 q) { return f32x4{bflo(q[2]), bfhi(q[2]), bflo(q[3]), bfhi(q[3])}; }
__device__ __forceinline__ u32x4 pk8(f32x4 a, f32x4 b) { return u32x4{pack2(a[0], a[1]), pack2(a[2], a[3]), pack2(b[0], b[1]), pack2(b[2], b[3])}; }
__device__ __forceinline__ u32x4 nsent4(u32x4 v) { return u32x4{min(v[0], SENT - 1u), min(v[1], SENT - 1u), min(v[2], SENT - 1u), min(v[3], SENT - 1u)}; }
__device__ __forceinline__ unsigned max4(u32x4 v) { return max(max(v[0], v[1]), max(v[2], v[3])); }
__device__ __forceinline__ float amin4(u32x4 v, float m) {
  asm("v_min3_f32 %0, |%1|, |%2|, %0\n\tv_min3_f32 %0, |%3|, |%4|, %0" : "+v"(m) : "v"(v[0]), "v"(v[1]), "v"(v[2]), "v"(v[3]));
  return m;
}
__device__ __forceinline__ u32x4 selu(unsigned mk, u32x4 a, u32x4 b) { return (a & mk) | (b & ~mk); }
#define KSTR 16
#define TOP2(CTRL)                                                  \
  do {                                                              \
    const unsigned o1 = dpp_u<CTRL>(t1), o2 = dpp_u<CTRL>(t2);      \
    t2 = max(min(t1, o1), max(t2, o2));                             \
    t1 = max(t1, o1);                                               \
  } while (0)
#define TOP2S(SW)                                                   \
  do {                                                              \
    auto a_ = SW(t1, t1, false, false), b_ = SW(t2, t2, false, false); \
    t2 = max(min(a_[0], a_[1]), max(b_[0], b_[1]));                 \
    t1 = max(a_[0], a_[1]);                                         \
  } while (0)
#define T_C3 253
#define T_C4 340
#define E_D 6
#define O_DW 6
#define O_DS 4
#define OPOL " sc1"
#define X_PA 2
#define G1_C 2
#define R_AT 24
#define B_TK 8
#define W_T 4
#define V_K 3
#define V_T 5
#define N_K 10
#define N_D 2
#define T_K 0
#define H_AT 23
#define G1_S 1
#define F_T 142
#define K_S 1
#define N_C (V_T + 1)
#define T_O 246
#define A_S1 8
#define A_S2 8
#define A_P2 9
#define X_K 13
#define H_P 2
#define T_X 80
#define A_PS 1
#define U_S 4
#define C_LD 2
#define C_DP 8
#define F_D 2
#define F_AT 29
#define PW0 2
#define T_W 70
#define P_D2 1
#define P_D4 1
#define XSTR 16
#define P_D3 20
#ifndef M_NS
#define M_NS 6
#endif
#define A_Q1 27
#define B_RAT 7
#define B_MAT 13
#define U_D 4
#define FSTR 16
#define B_J 4
#define RETIRE()                                                                                                              \
  do {                                                                                                                        \
    if (hw == 3 && lane < (b == 255 ? 2 : 1))                                                                                 \
      asm volatile("global_store_dwordx2 %0, %1, %2 sc1" ::"v"(lane ? (unsigned)WS_EPO : O_XFL + b * XSTR * 4), "v"(lane ? tag : ~tag), "s"(ws) : "memory"); \
  } while (0)
#define U_N1 3
#define B_QAT 8
#define B_QD 4
#define TP_PS 5
#define TP_XC 34
#define TP_P0 2
#define TP_X0 24
#define TP_XL 58
#define W_UO 3
#define CPF_K 24
#define CPF_B 8
#define B_WN 4
#define B_WK 15
struct BSched {
  int cnt[16], rcnt, mcnt, qcnt, vcnt, ncnt;
  constexpr BSched() : cnt{}, rcnt(0), mcnt(0), qcnt(0), vcnt(0), ncnt(0) {
    int pos[16] = {}, n = 0, rpos = 0, mpos = 0, qpos = 0, npos = 0;
    for (int k = 0; k <= B_J - 2 + M_NS; ++k) pos[k] = -8 * (B_J - 2 + M_NS - k);
    for (int k = B_J + 1; k < 16; ++k) {
      cnt[k] = n - pos[k] > 63 ? 63 : n - pos[k];
      if (k - 2 + M_NS < 16) { n += 4; pos[k - 2 + M_NS] = n; }
      if (k == B_RAT + 2) rcnt = n - rpos;
      if (k == B_QAT + B_QD) qcnt = n - qpos;
      if (k == V_T) vcnt = n;
      if (k == N_K + N_D) ncnt = n - npos;
      if (k == N_K) npos = n;
      if (k == B_RAT) rpos = n;
      if (k == X_K) n += 4;
      if (k == B_QAT) { n += 3; qpos = n; }
      if (k == B_MAT) { n += 3; mpos = n; }
      if (k == B_WK) n += B_WN;
    }
    mcnt = n - mpos > 63 ? 63 : n - mpos;
  }
};
__global__ __launch_bounds__(512, 2) void k_main(const u16* __restrict__ x, char* ws, const u16* __restrict__ Y0, const u16* __restrict__ Y1,
                                              const u16* __restrict__ cvec, const u16* __restrict__ wmod, const u16* __restrict__ X0,
                                              const u16* __restrict__ X1, const u16* __restrict__ wr_, const u16* __restrict__ wg,
                                              const u16* __restrict__ wu, const u16* __restrict__ wd, u16* __restrict__ out) {
  constexpr int DX = 4, NS = M_NS, STG = NS * 16384, SPARE = STG + 32768;
  constexpr int LST = SPARE + 17664, SLT = LST + 256, XC = SLT + 512, VT = XC + 4096;
  __shared__ __attribute__((aligned(1024))) char shm[VT + 256];
  u16(*svb)[1024] = reinterpret_cast<u16(*)[1024]>(shm + SPARE);
  float* cwj = reinterpret_cast<float*>(shm + SPARE + 10240);
  unsigned* ssel = reinterpret_cast<unsigned*>(shm + SPARE + 10368);
  float* smod = reinterpret_cast<float*>(shm + SPARE + 15872);
  u16* ssa = reinterpret_cast<u16*>(smod);
  constexpr int TAB = SPARE + 10752, RKA = TAB + 1536, RKB = TAB + 3584;
  u16(*scw)[128] = reinterpret_cast<u16(*)[128]>(shm + TAB + 512);
  const int b = blockIdx.x, xcd = b & 7, j8 = b >> 3;
  const int cc = xcd >> 2, nt = (xcd & 3) * 4 + (j8 & 3), mt = j8 >> 2, et = mt;
  const int tid = threadIdx.x, lane = tid & 63;
  const int wid = __builtin_amdgcn_readfirstlane(tid >> 6);
  const unsigned lds0 = (unsigned)(size_t)(__attribute__((address_space(3))) char*)shm;
  const char* cpf;
  {
    unsigned long long pc;
    unsigned len;
    asm volatile("s_getpc_b64 %0\n.Lcpf_here:\n\ts_mov_b32 %1, .Lcpf_end-.Lcpf_here-4\n\t.subsection 1\n.Lcpf_end:\n\t.subsection 0" : "=s"(pc), "=s"(len));
    cpf = reinterpret_cast<const char*>(pc) + min((unsigned)((lane * 32 + j8) * 128), len);
  }
  const unsigned pfl = lds0 + SPARE + 17408;
  if (wid == 4) CPFL();
  u32x4 xv[2];
  {
    const unsigned xo = (unsigned)((b * 8 + wid) * D + lane * 8) * 2;
    asm volatile("global_load_dwordx4 %0, %2, %3\n\tglobal_load_dwordx4 %1, %2, %3 offset:1024" : "=&v"(xv[0]), "=&v"(xv[1]) : "v"(xo), "s"(x) : "memory");
  }
  asm volatile("" ::"s"(ws), "s"(Y0), "s"(Y1), "s"(cvec), "s"(wmod), "s"(wr_));
  u16* xn = reinterpret_cast<u16*>(ws + WS_H);
  u16* U = reinterpret_cast<u16*>(ws + WS_T);
  unsigned* part = reinterpret_cast<unsigned*>(ws + WS_PART);
  const int g = (b & 7) * 28 + (b >> 3);
  unsigned* ufl = reinterpret_cast<unsigned*>(ws + WS_UFL) + (cc * 16 + nt) * 32 * FSTR;
  unsigned* epo = reinterpret_cast<unsigned*>(ws + WS_EPO);
#define SKEWPT()
  const int g4 = lane >> 4;
  u64 tag0, tag;
#define TAGW(n) asm volatile("s_waitcnt vmcnt(%2)\n\tv_lshl_add_u64 %0, %1, 0, 1" : "=v"(tag) : "v"(tag0), "n"(n) : "memory")
  u32x4 wv[8], cq;
  const char* pfy = reinterpret_cast<const char*>(cc ? Y1 : Y0) + (size_t)((xcd & 3) * 512 + j8 * 16 + (lane & 15)) * 4096 + g4 * 128;
#define PFG(g) (pfy + ((((g) + xcd) & 7) << 9))
  const int u = b * 2 + (wid & 1), cg0 = u & 31, ks0 = u >> 5;
  unsigned ta = lds0 + STG + ((lane >> 2) & 3) * 2368 + (wid * 128 + (lane >> 5) * 32 + (lane & 3) * 8 + (g4 & 1) * 4) * 2;
  unsigned oo = (unsigned)((wid * 4 + (lane >> 5)) * 131072 + (b >> 2) * 2048 + (g4 & 1) * 1024 + ((b & 3) * 16 + (lane & 15)) * 16), oo2 = oo + 262144;
  asm volatile("" : "+v"(ta), "+v"(oo), "+v"(oo2));
  unsigned wa = lds0 + STG + wid * 2368 + lane * 16;
  asm volatile("" : "+v"(wa));
  const unsigned wo = (unsigned)((ks0 * 64 + (lane >> 3) * 8) * 6144 + 2048 + (cg0 >> 4) * 1024 + cg0 * 64 + (lane & 7) * 8) * 2;
  if (wid == 0) TSG(part, 0);
  BT(50);
  if (wid >= 3) {
    asm volatile("global_load_dwordx2 %0, %1, off sc1" : "=v"(tag0) : "v"(epo) : "memory");
    if (wid >= 6) {
      LDG(cq, (ks0 * 64 + g4 * 16 + (lane & 1) * 8) * 2, cvec);
#pragma unroll
      for (int i = 0; i < 8; ++i) LDM(wv[i], wo + i * 12288, wmod);
      const unsigned av = u < 80 ? 0u : ~0u;
      asm volatile("global_store_dwordx4 %0, %1, %2 sc1\n\ts_nop 1" ::"v"(O_KEY + u * 1024 + lane * 16), "v"(u32x4{av, av, av, av}), "s"(ws) : "memory");
      WAIT_V(11);
    } else if (wid == 4) {
      __builtin_amdgcn_s_sleep(C_DP);
      PFL(PFG(0));
      WAIT_V(2);
    } else {
      WAIT_V(1);
    }
  } else {
    WAIT_V(0);
  }
  {
    asm volatile("" : "+v"(xv[0]), "+v"(xv[1])::"memory");
    if (wid == 1) TSG(part, 2);
    BT(51);
    const float s0 = dsum(xv[0][3], dsum(xv[0][2], dsum(xv[0][1], dsum(xv[0][0], 0.f))));
    const float s1 = dsum(xv[1][3], dsum(xv[1][2], dsum(xv[1][1], dsum(xv[1][0], 0.f))));
    const float mf = wave_sum_s(s0 + s1) * (1.0f / D), m = rbf(mf);
    const f32x4 d0 = lo4(xv[0]) - m, d1 = hi4(xv[0]) - m, d2 = lo4(xv[1]) - m, d3 = hi4(xv[1]) - m;
    const f32x4 q = (d0 * d0 + d1 * d1) + (d2 * d2 + d3 * d3);
    const float vf = wave_sum_s((q[0] + q[1]) + (q[2] + q[3])) * (1.0f / D) - (mf - m) * (mf - m);
    const float rden = __builtin_amdgcn_rcpf(rbf(__builtin_amdgcn_sqrtf(rbf(rbf(vf) + 1e-5f))));
    asm volatile("ds_write_b128 %0, %1" ::"v"(wa), "v"(pk8(d0 * rden, d1 * rden)) : "memory");
    asm volatile("ds_write_b128 %0, %1 offset:1024" ::"v"(wa), "v"(pk8(d2 * rden, d3 * rden)) : "memory");
    if (wid == 1) TSG(part, 12);
    BT(52);
    LBAR();
    if (wid == 1) TSG(part, 10);
    BT(53);
    u32x2 t[4];
    asm volatile(
        "ds_read_b64_tr_b16 %0, %4\n\tds_read_b64_tr_b16 %1, %4 offset:%5\n\t"
        "ds_read_b64_tr_b16 %2, %4 offset:128\n\tds_read_b64_tr_b16 %3, %4 offset:%6\n\ts_waitcnt lgkmcnt(0)"
        : "=&v"(t[0]), "=&v"(t[1]), "=&v"(t[2]), "=&v"(t[3])
        : "v"(ta), "n"(4 * 2368), "n"(4 * 2368 + 128)
        : "memory");
    asm volatile("global_store_dwordx4 %0, %2, %4 sc1\n\tglobal_store_dwordx4 %1, %3, %4 sc1\n\ts_nop 1" ::"v"(oo), "v"(oo2),
                 "v"(u32x4{t[0][0], t[0][1], t[1][0], t[1][1]}), "v"(u32x4{t[2][0], t[2][1], t[3][0], t[3][1]}), "s"(xn)
                 : "memory");
    if (wid == 2) TSG(part, 14);
    BT(54);
  }
  u32x2 vd;
#define VDLD(sl) POLL2(max(vd[0], vd[1]) == SENT, sl, PCAP, asm volatile("global_load_dwordx2 %0, %1, %2 sc1" VW0 : "=&v"(vd) : "v"(O_VD + (b & 31) * 128), "s"(ws) : "memory"))
  if (wid < 4) {
    const unsigned ya0 = lds0 + (lane & 15) * 128 + (g4 ^ (lane & 7)) * 16;
    bf16x8 X[DX][2][2], Y[8];
    f32x4 acc[2][8];
    {
      constexpr int MODE = 0;
      const unsigned xo0 = lane * 16 + 4096, xo1 = xo0 + 8192;
      const char* xb = reinterpret_cast<const char*>(xn) + (size_t)(mt * 4 + wid) * 131072;
#define XB(i) (xb + (((xcd + (i)) & 7) << 14))
#pragma unroll
      for (int i = 0; i < 2; ++i)
#pragma unroll
        for (int j = 0; j < 8; ++j) acc[i][j] = f32x4{0.f, 0.f, 0.f, 0.f};
      asm volatile("" : W8(acc[0]), W8(acc[1]));
      __builtin_amdgcn_s_setprio(1);
      WAIT_V(0);
      if (wid == 0) TSG(part, 1);
      const char* xpb = XB(0);
      BAR(55);
      if (wid == 3) {
        TAGW(0);
        const unsigned fp = O_XFL + ((xcd * 32 + lane) & 255) * XSTR * 4;
        if (P_D3) __builtin_amdgcn_s_sleep(P_D3);
#pragma unroll 1
        for (int it = 0; it < PCAP; ++it) { PDBG(it);
          u64 f;
          if (it < 2) BT(58 + 2 * it);
          asm volatile("global_load_dwordx2 %0, %1, %2 sc1\n\ts_waitcnt vmcnt(0)" : "=&v"(f) : "v"(fp), "s"(ws) : "memory");
          if (it < 2) BT(59 + 2 * it);
          if (__all(f == tag)) break;
          __builtin_amdgcn_s_sleep(P_D4);
        }
      }
      BAR(0);
      if (wid == 1) TSG(part, 3);
#pragma unroll
      for (int h = 0; h < 7; ++h) {
        XLD(X[h >> 1][h & 1][0], xpb, h, 0);
        XLD(X[h >> 1][h & 1][1], xpb, h, 1);
      }
#pragma unroll
      for (int j = 0; j < 7; ++j) DSR(Y[j], ya0, j * 2048);
      unsigned so = 0, a0 = ya0;
      f32x4 c0, c1;
#pragma unroll 1
      for (int it = 0; it < 7; ++it) {
        const char* xpa = xpb;
        xpb = XB(it + 1);
#pragma unroll
        for (int s = 0; s < 4; ++s) {
          const unsigned a1 = (ya0 ^ 64) + so;
          so = so == (NS - 1) * 16384 ? 0 : so + 16384;
          WAIT_V(12);
          HALF(X[s][0], a1, true, X[(s + 3) & 3][1], (s == 0 ? xpa : xpb), (2 * s + 7) & 7, true, a0);
          a0 = ya0 + so;
          BAR(1 + it * 4 + s);
          WAIT_V(12);
          HALF(X[s][1], a0, true, X[s][0], xpb, 2 * s, true, a1);
        }
      }
#pragma unroll
      for (int s = 0; s < 4; ++s) {
        const unsigned a1 = (ya0 ^ 64) + so;
        so = so == (NS - 1) * 16384 ? 0 : so + 16384;
        WAIT_V(s == 0 ? 12 : 14 - 4 * s);
        HALF(X[s][0], a1, true, X[3][1], xpb, 7, s == 0, a0);
        a0 = ya0 + so;
        if (s < 3) BAR(29 + s);
        WAIT_V(12 - 4 * s);
        if (s == 2) {
          DSR(c0, lds0 + SPARE + 15872 + 512 + wid * 128 + g4 * 32, 0);
          DSR(c1, lds0 + SPARE + 15872 + 512 + wid * 128 + g4 * 32, 16);
        }
        if (s < 3) HALF(X[s][1], a0, true, X[s][0], xpb, 0, false, a1);
      }
      const char* up = reinterpret_cast<const char*>(U) + (size_t)cc * 4194304 + (size_t)(nt * 4) * 65536 + (mt * 4 + wid) * 2048;
      const unsigned ul = lds0 + STG + wid * 1024 + lane * 16;
#define UEP(j)                                                                                                                             \
  do {                                                                                                                                     \
    u32x4 v_;                                                                                                                              \
    asm volatile("v_pk_mul_f32 %0, %0, %4\n\tv_pk_mul_f32 %1, %1, %5\n\tv_pk_mul_f32 %2, %2, %6\n\tv_pk_mul_f32 %3, %3, %7"            \
                 : "+v"(acc[0][j].lo), "+v"(acc[0][j].hi), "+v"(acc[1][j].lo), "+v"(acc[1][j].hi)                                          \
                 : "v"(c0.lo), "v"(c0.hi), "v"(c1.lo), "v"(c1.hi));                                                                        \
    asm volatile("v_cvt_pk_bf16_f32 %0, %4, %5\n\tv_cvt_pk_bf16_f32 %1, %6, %7\n\tv_cvt_pk_bf16_f32 %2, %8, %9\n\tv_cvt_pk_bf16_f32 %3, %10, %11" \
                 : "=&v"(v_[0]), "=&v"(v_[1]), "=&v"(v_[2]), "=&v"(v_[3])                                                                  \
                 : "v"(acc[0][j][0]), "v"(acc[0][j][1]), "v"(acc[0][j][2]), "v"(acc[0][j][3]), "v"(acc[1][j][0]), "v"(acc[1][j][1]),       \
                   "v"(acc[1][j][2]), "v"(acc[1][j][3]));                                                                                  \
    asm volatile("global_store_dwordx4 %0, %2, %3 offset:%4 sc1\n\tds_write_b128 %1, %2 offset:%5\n\ts_nop 0"                          \
                 ::"v"(lane * 16), "v"(ul), "v"(v_), "s"(up + ((j) >> 1) * 65536), "n"(((j)&1) * 1024), "n"((j)*4096) : "memory");         \
  } while (0)
      {
        const unsigned a1 = (ya0 ^ 64) + (31 % NS) * 16384;
        DSR(Y[7], a1, 7 * 2048);
        WAIT_L(6);
#pragma unroll
        for (int j = 0; j < 8; ++j) {
          if (j == 2) WAIT_L(0);
          MFMA(acc[0][j], X[3][1][0], Y[j]);
          MFMA(acc[1][j], X[3][1][1], Y[j]);
          if (j >= 2) UEP(j - 2);
        }
      }
      if (wid == 0) TSG(part, 4);
      TS(0);
      SKEWPT();
      asm volatile("s_nop 3");
#pragma unroll
      for (int j = 6; j < 8; ++j) UEP(j);
    }
    const int rg = nt * 8 + wid * 2 + cc, row0 = rg * 16 + (lane & 15), col0 = et * 128 + g4 * 8;
    u32x4 xq[4], pv[4], sa4[4];
    f32x4 ga[8];
    f32x2 cj;
    float sw, ce;
    u32x4 dk;
    unsigned spo, soo, th, slt, slf, sla, tha;
    u32x2 se;
    const char* xr = reinterpret_cast<const char*>(xn) + mt * 524288 + (cc * 16 + nt) * 16384 + wid * 4096;
#define XCHLD()                                                                                                     \
  asm volatile(                                                                                                     \
      "global_load_dwordx4 %0, %4, %5 sc1\n\tglobal_load_dwordx4 %1, %4, %5 offset:1024 sc1\n\t"                   \
      "global_load_dwordx4 %2, %4, %5 offset:2048 sc1\n\tglobal_load_dwordx4 %3, %4, %5 offset:3072 sc1"            \
      : "=&v"(pv[0]), "=&v"(pv[1]), "=&v"(pv[2]), "=&v"(pv[3])                                                      \
      : "v"(lane * 16), "s"(xr)                                                                                     \
      : "memory")
    {
      constexpr int MODE = 1;
      const char* ub = reinterpret_cast<const char*>(U) + (size_t)cc * 4194304 + (size_t)(nt * 4 + wid) * 65536;
      const unsigned xo0 = lane * 16 + cc * 1024 + 4096, xo1 = xo0 ^ 1024;
      BT(32);
      LBAR();
      if (wid == 1) TSG(part, 6);
      {
        const unsigned sa = lds0 + STG + wid * 8192 + lane * 16 + cc * 4096;
#pragma unroll
        for (int q = 0; q < 8; ++q) DSR(X[(q >> 1) & 1][q & 1][q >> 2], (q >> 2) ? sa ^ 4096 : sa, (q & 3) * 1024);
        const unsigned a0 = ya0 + (32 % NS) * 16384;
#pragma unroll
        for (int j = 0; j < 7; ++j) DSR(Y[j], a0, j * 2048);
      }
#define UFLAG()                                                                                                         \
  do {                                                                                                                  \
    WAIT_V(0);                                                                                                          \
    if (lane == 0) __hip_atomic_store(ufl + (mt * 4 + wid) * FSTR, 1u, __ATOMIC_RELAXED, __HIP_MEMORY_SCOPE_AGENT);     \
    if (wid == 0) TSG(part, 5);                                                                                         \
  } while (0)
#define XP(h) (ub + (((mt + ((h) >> 2)) & 7) << 13))
#define XS(h) X[((h) >> 1) & 3][(h)&1]
#define XW(h) do { if ((h) >= 4) WAIT_V(2 * (((h) + 6 > 31 ? 31 : (h) + 6) - (h)) ); } while (0)
      constexpr int H0 = 24, NP = 64, NE = 2 * NP + 8;
#pragma unroll
      for (int s = 0; s < H0 / 2; ++s) {
        const unsigned a0 = ya0 + ((32 + s) % NS) * 16384, a1 = a0 ^ 64, an = ya0 + ((33 + s) % NS) * 16384;
        XW(2 * s);
        if (s == 0) {
          constexpr int MODE = 2;
          HALF(XS(0), a1, true, XS(7), XP(7), 7, false, a0);
          UFLAG();
        } else {
          HALF(XS(2 * s), a1, true, XS(2 * s + 7), XP(2 * s + 7), 2 * s + 7, 2 * s >= 4, a0);
        }
        if (s == 1 && wid == 2) TSD(12);
        BAR(33 + s);
        if (s == 1) {
          if (wid == 1) TSG(part, 7);
          TS(4);
#pragma unroll
          for (int h = 4; h < U_X0; ++h) {
            XLD(XS(h)[0], XP(h), h, 0);
            XLD(XS(h)[1], XP(h), h, 1);
          }
        }
        XW(2 * s + 1);
#undef HHOOK
#define HHOOK(j) if (s == 1 && j + 3 >= U_X0) { XLD(XS(j + 3)[0], XP(j + 3), j + 3, 0); XLD(XS(j + 3)[1], XP(j + 3), j + 3, 1); }
        HALF(XS(2 * s + 1), an, true, XS(2 * s + 8), XP(2 * s + 8), 2 * s + 8, 2 * s + 1 >= 5, a1);
#undef HHOOK
#define HHOOK(j)
      }
      const char* xw = reinterpret_cast<const char*>(xn) + mt * 524288 + ((cc ^ 1) * 16 + nt) * 16384 + wid * 4096;
      unsigned c128 = 0x43000000u;
      asm volatile("" : "+v"(c128));
      const unsigned xoff = lds0 + STG + wid * 8192 + (lane & 15) * 256 + ((g4 ^ lane) & 15) * 16;
      const unsigned rka = lds0 + RKA + (lane & 15) * 16, rkb = lds0 + RKB + (g4 ? 1024 : wid * 256 + lane * 16);
      const unsigned ea = lds0 + TAB + g4 * 32, eb = lds0 + TAB + 512 + (lane & 3) * 256 + g4 * 16;
      u32x4 ov;
      bf16x8 xk;
#pragma unroll
      for (int q = 0; q < 4; ++q) dk[q] = (lane & 3) == q ? ~0u : 0u;
      spo = O_STAT + rg * 2048 + g4 * 512 + (lane & 15) * 16, soo = O_STAT + rg * 2048 + et * 256 + (lane & 15) * 16;
      unsigned cwa = lds0 + SPARE + 10240 + g4 * 8, cwu = lds0 + SPARE + 10240 + et * 4;
      {
        const unsigned one = g4 == ((lane >> 2) & 3) ? ((lane & 1) ? 0x1f800000u : 0x3f80u) : 0u;
        se = u32x2{(lane & 2) ? 0u : one, (lane & 2) ? one : 0u};
        asm volatile("" : "+v"(se));
      }
      tha = lds0 + SPARE + 10368 + 24;
      sla = lds0 + SLT + (row0 & 127) * 4;
      asm volatile("" : "+v"(dk), "+v"(spo), "+v"(soo), "+v"(cwa), "+v"(cwu), "+v"(tha), "+v"(sla));
#define EH(e) (H0 + (((e) < NP ? (e) : (e) - NP - 8) >> 3))
#define EA(e) ((ya0 ^ ((EH(e) & 1) * 64)) + ((32 + (EH(e) >> 1)) % NS) * 16384)
      sfor<NE>([&](auto nn) __attribute__((always_inline)) {
        constexpr int n = decltype(nn)::value, h = EH(n), j = n & 7, m = n - NP - TP_P0, e = n + 7, z = n - (NE - 8);
        (void)sa4, (void)eb, (void)ga, (void)ea, (void)xk, (void)rka, (void)rkb, (void)xq, (void)xoff, (void)ov, (void)xw, (void)x;
        (void)cj, (void)sw, (void)ce, (void)cwa, (void)cwu, (void)th, (void)tha, (void)slt, (void)sla, (void)slf, (void)c128;
        if (n == 0) TS(2);
        if (n == NP) { TS(3); SKEWPT(); }
        if (n < NP && j == 0) {
          if ((h & 1) && h < 31) BAR(33 + (h >> 1));
          XW(h);
        }
        if (n == NP + TP_X0) { TS(14); BAR(48); TS(11); }
        if (n == NP + TP_XL) XCHLD();
        if constexpr ((j & 1) == 0) WAIT_L(z <= 0 ? 5 : 6 - z + 2 * z);
        if constexpr (n < NP) MFMA(acc[1][j], Y[j], XS(h)[1]);
        else if constexpr (n < NP + 8) MFMA(acc[0][j], Y[j], xk);
        else MFMA(acc[0][j], Y[j], XS(h)[0]);
        if constexpr (n == NP - 16) DSR(xk, rkb, 0);
        if constexpr (e >= NP && e < NP + 8) DSR(Y[e & 7], rka, (e & 7) * 256);
        else if constexpr (e < NE) DSR(Y[e & 7], EA(e), (e & 7) * 2048);
        if constexpr (n == 2 || n == 4) XLD(XS(31)[(j >> 1) - 1], XP(31), 31, (j >> 1) - 1);
        if constexpr (m >= TP_XC - 11 && m < TP_XC - 7) DSR(xq[m - TP_XC + 11], xoff ^ ((m - TP_XC + 11) * 64), 4096);
        if constexpr (m >= 0 && m < 4 * TP_PS) {
          constexpr int q = m / TP_PS, r = m % TP_PS;
#pragma unroll
          for (int u = (r * 13 + TP_PS - 1) / TP_PS; u < ((r + 1) * 13 + TP_PS - 1) / TP_PS; ++u) {
#define AO(i) acc[1][2 * q + ((u >> 1) & 1)][(u & 1) * 2 + (i)]
            if (u < 4) asm volatile("v_and_or_b32 %0, %1, %2, %3" : "=v"(ov[u & 3]) : "v"(AO(1)), "s"(0x80000000u), "v"(c128));
            else if (u < 8) asm volatile("v_fmamk_f32 %0, %1, 0x5f800000, %0" : "+v"(ov[u & 3]) : "v"(AO(1)));
            else if (u < 12) asm volatile("v_cvt_pk_bf16_f32 %0, %1, %0" : "+v"(ov[u & 3]) : "v"(AO(0)));
            else asm volatile("global_store_dwordx4 %0, %1, %2 offset:%3 sc1\n\ts_nop 1" ::"v"(lane * 16), "v"(ov), "s"(xw), "n"(q * 1024) : "memory");
          }
          if (m == 4 * TP_PS - 1) TS(10);
        }
        if constexpr (m == TP_XC) asm volatile("" : "+v"(xq[0]), "+v"(xq[1]), "+v"(xq[2]), "+v"(xq[3]));
        if constexpr (m >= TP_XC && m < TP_XC + 32) {
          constexpr int c = m - TP_XC;
          if constexpr (c & 1) asm volatile("v_and_b32 %0, 0xffff0000, %1" : "=v"(acc[1][c >> 2][c & 3]) : "v"(xq[c >> 3][(c >> 1) & 3]));
          else asm volatile("v_lshlrev_b32 %0, 16, %1" : "=v"(acc[1][c >> 2][c & 3]) : "v"(xq[c >> 3][(c >> 1) & 3]));
        }
        if constexpr (z >= 0 && z < 4) {
          DSR(ga[2 * z], ea, z * 128);
          DSR(ga[2 * z + 1], ea, z * 128 + 16);
        } else if constexpr (z >= 4 && z < 6) {
          DSR(sa4[2 * z - 8], eb, (2 * z - 8) * 64);
          DSR(sa4[2 * z - 7], eb, (2 * z - 7) * 64);
        } else if constexpr (z == 6) {
          asm volatile("ds_read_b64 %0, %2\n\tds_read_b32 %1, %3" : "=&v"(cj), "=&v"(ce) : "v"(cwa), "v"(cwu) : "memory");
        } else if constexpr (z == 7) {
          asm volatile("ds_read_b32 %0, %4 offset:32\n\tds_read_b32 %1, %5 offset:4\n\tds_read_b32 %3, %5 offset:56\n\tds_read_b32 %2, %6"
                       : "=&v"(sw), "=&v"(th), "=&v"(slt), "=&v"(slf) : "v"(cwa), "v"(tha), "v"(sla) : "memory");
        }
      });
    }
    if (wid == 0) TSG(part, 8);
    TS(13);
    BT(49);
    BTE();
    SKEWPT();
    {
      u32x4 e0 = {se[0], se[1], 0u, 0u}, e1 = {0u, 0u, se[0], se[1]};
      asm volatile("" : "+v"(e0), "+v"(e1));
      TS(26);
#define XCHW() asm volatile("s_waitcnt vmcnt(0)" : "+v"(pv[0]), "+v"(pv[1]), "+v"(pv[2]), "+v"(pv[3])::"memory")
      XCHW();
#define XBAD (amin4(pv[3], amin4(pv[2], 128.0f)) < 128.0f || amin4(pv[1], amin4(pv[0], 128.0f)) < 128.0f)
      if (__builtin_expect(__any(XBAD), 1)) POLL2(XBAD, 1, PCAP, XCHLD(); XCHW());
      TS(27);
      asm volatile("s_waitcnt lgkmcnt(0)" : W8(acc[0]), W8(ga), "+v"(sa4[0]), "+v"(sa4[1]), "+v"(sa4[2]), "+v"(sa4[3]), "+v"(cj), "+v"(sw), "+v"(ce), "+v"(th), "+v"(slt), "+v"(slf)::"memory");
#pragma unroll
      for (int q = 0; q < 8; ++q) acc[0][q] = __builtin_amdgcn_mfma_f32_16x16x32_bf16(BF8((q & 1) ? e1 : e0), BF8(pv[q >> 1]), acc[0][q], 0, 0, 0);
      f32x4 s4 = {0.f, 0.f, 0.f, 0.f}, gr = {0.f, 0.f, 0.f, 0.f};
#pragma unroll
      for (int q = 0; q < 4; ++q) {
        xq[q] = pk8(acc[1][2 * q] + ga[2 * q] * acc[0][2 * q], acc[1][2 * q + 1] + ga[2 * q + 1] * acc[0][2 * q + 1]);
        gr = __builtin_amdgcn_mfma_f32_16x16x32_bf16(BF8(xq[q]), BF8(xq[q]), gr, 0, 0, 0);
        s4 = __builtin_amdgcn_mfma_f32_16x16x32_bf16(BF8(sa4[q]), BF8(xq[q]), s4, 0, 0, 0);
      }
#define XCST()                                                                                                                         \
  asm volatile(                                                                                                                        \
      "global_store_dwordx4 %0, %1, %5 sc1\n\tglobal_store_dwordx4 %0, %2, %5 offset:64 sc1\n\t"                                      \
      "global_store_dwordx4 %0, %3, %5 offset:128 sc1\n\tglobal_store_dwordx4 %0, %4, %5 offset:192 sc1\n\ts_nop 1" ::"v"(O_X1C - 2048 + slt * 2048 + col0 * 2), \
      "v"(nsent4(xq[0])), "v"(nsent4(xq[1])), "v"(nsent4(xq[2])), "v"(nsent4(xq[3])), "s"(ws)                                          \
      : "memory")
#define SCST() asm volatile("global_store_dwordx4 %0, %1, %2 sc1\n\ts_nop 1" ::"v"(O_ST2 - 128 + slt * 128 + et * 16), "v"(sg), "s"(ws) : "memory")
      if (slt) XCST();
      const bool dl = ((lane >> 2) & 3) == g4;
      const u32x4 gd = __builtin_bit_cast(u32x4, gr) & dk;
      const float m = s4[2] * (1.0f / 128);
      float q2 = __uint_as_float((gd[0] | gd[1]) | (gd[2] | gd[3])) - s4[2] * m, lg = (s4[0] + s4[1]) - m * ce;
      if (__builtin_expect(__any(dl && !(s4[2] * m <= 256.0f * q2)), 0)) {
        asm volatile("" : "+v"(xq[0]), "+v"(xq[1]), "+v"(xq[2]), "+v"(xq[3]));
        f32x4 a = {0.f, 0.f, 0.f, 0.f}, l = {0.f, 0.f, 0.f, 0.f};
#pragma unroll
        for (int q = 0; q < 4; ++q) {
          const u32x4 ch = *reinterpret_cast<const u32x4*>(&scw[0][g4 * 8 + q * 32]), cl = *reinterpret_cast<const u32x4*>(&scw[1][g4 * 8 + q * 32]);
          const f32x4 d0 = lo4(xq[q]) - m, d1 = hi4(xq[q]) - m;
          a += d0 * d0 + d1 * d1;
          l += d0 * (lo4(ch) + lo4(cl)) + d1 * (hi4(ch) + hi4(cl));
        }
        q2 = ar4((a[0] + a[1]) + (a[2] + a[3]));
        lg = ar4((l[0] + l[1]) + (l[2] + l[3]));
      }
      const u32x4 sg = {nsent(m), nsent(q2), nsent(lg), 0u};
      if (dl) {
        if (slt) SCST();
        asm volatile("global_store_dwordx4 %0, %1, %2 sc1\n\ts_nop 1" ::"v"(soo), "v"(sg), "s"(ws) : "memory");
      }
      if (__any(slf == 0u)) {
        POLL2(slf == 0u, 1, 1 << 26, asm volatile("ds_read_b32 %1, %2 offset:56\n\tds_read_b32 %0, %3\n\ts_waitcnt lgkmcnt(0)" : "=&v"(slt), "=&v"(slf) : "v"(tha), "v"(sla) : "memory"));
        if (slt) {
          XCST();
          if (dl) SCST();
        }
      }
      asm volatile("ds_write_b32 %0, %1 offset:72" ::"v"(tha + wid * 4), "v"(1u) : "memory");
      TS(7);
      SKEWPT();
      const unsigned tp = tick() + T_D1, tq = (unsigned)__builtin_amdgcn_readfirstlane((int)th) + T_C1;
      u32x4 sr[2];
#define STLD(W) asm volatile("global_load_dwordx4 %0, %2, %3 sc1\n\tglobal_load_dwordx4 %1, %2, %3 offset:256 sc1" W : "=&v"(sr[0]), "=&v"(sr[1]) : "v"(spo), "s"(ws) : "memory")
#define SBAD (max(max(max(sr[0][0], sr[0][1]), sr[0][2]), max(max(sr[1][0], sr[1][1]), sr[1][2])) == SENT)
      asm volatile("" : "+v"(xq[0]), "+v"(xq[1]), "+v"(xq[2]), "+v"(xq[3])::"memory");
      f32x4 xf[8], xs[8], gc[8];
      auto PREW = [&](int q0, int q1) __attribute__((always_inline)) {
#pragma unroll
        for (int q = q0; q < q1; ++q) {
          const u32x4 sbq = *reinterpret_cast<const u32x4*>(&svb[1][col0 + q * 32]), gbq = *reinterpret_cast<const u32x4*>(&svb[3][col0 + q * 32]);
          const u32x4 cq = *reinterpret_cast<const u32x4*>(&svb[2][col0 + q * 32]);
          xf[2 * q] = lo4(xq[q]), xf[2 * q + 1] = hi4(xq[q]);
          xs[2 * q] = xf[2 * q] + lo4(gbq) * lo4(sbq), xs[2 * q + 1] = xf[2 * q + 1] + hi4(gbq) * hi4(sbq);
          gc[2 * q] = lo4(gbq) * lo4(cq), gc[2 * q + 1] = hi4(gbq) * hi4(cq);
          asm volatile("" : "+v"(xf[2 * q]), "+v"(xf[2 * q + 1]), "+v"(xs[2 * q]), "+v"(xs[2 * q + 1]), "+v"(gc[2 * q]), "+v"(gc[2 * q + 1]));
        }
      };
      PREW(0, PW0);
      wait_tick((int)(tp - tq) < 0 ? tp : tq);
      STLD("");
      unsigned rk = 0x8ffeu - (unsigned)row0 * 2;
      unsigned twa = lds0 + STG + wid * 8192 + (lane & 15) * 256 + ((g4 ^ lane) & 15) * 16, tra = lds0 + STG + wid * 8192 + g4 * 256 + ((g4 ^ lane) & 15) * 16;
      unsigned oo_ = (unsigned)((rg * 16 + g4) * D + et * 128 + (lane & 15) * 8) * 2;
      asm volatile("" : "+v"(rk), "+v"(twa), "+v"(tra), "+v"(oo_));
      PREW(PW0, 4);
      asm volatile("s_waitcnt vmcnt(0)" : "+v"(sr[0]), "+v"(sr[1])::"memory");
      if (__builtin_expect(__any(SBAD), 1)) POLL2(SBAD, 1, PCAP, STLD(VW0));
      TS(9);
      const f32x2 mk = {__uint_as_float(sr[0][0]), __uint_as_float(sr[1][0])};
      const f32x2 qk = {__uint_as_float(sr[0][1]), __uint_as_float(sr[1][1])};
      const f32x2 lk = {__uint_as_float(sr[0][2]), __uint_as_float(sr[1][2])};
      const float mean = ar4(mk[0] + mk[1]) * (1.0f / 8);
      const f32x2 dm = mk - mean;
      const f32x2 a = qk + 128.0f * (dm * dm), l = lk + dm * cj;
      const float al = swap32_add(a[0] + a[1], l[0] + l[1]), am = swap16_add(al, al);
      auto r = __builtin_amdgcn_permlane32_swap(__float_as_uint(am), __float_as_uint(am), false, false);
      const float rstd = __builtin_amdgcn_rsqf(__uint_as_float(r[0]) * (1.0f / D) + 1e-5f);
      const unsigned key = mkkey(rstd * __uint_as_float(r[1]) + sw) | rk;
      unsigned t1 = slt ? 0u : key, t2 = 0u;
      TOP2(0xB1); TOP2(0x4E); TOP2(0x141); TOP2(0x140);
      if (lane == 0 && et == (rg & 7)) asm volatile("global_store_dwordx2 %0, %1, %2 sc1" ::"v"(O_KEY + rg * KSTR * 8), "v"(u32x2{t1, t2}), "s"(ws) : "memory");
      u32x4 o[4];
#pragma unroll
      for (int q = 0; q < 4; ++q) o[q] = pk8(xs[2 * q] + (gc[2 * q] * (xf[2 * q] - mean)) * rstd, xs[2 * q + 1] + (gc[2 * q + 1] * (xf[2 * q + 1] - mean)) * rstd);
      {
        asm volatile("ds_write_b128 %0, %4\n\tds_write_b128 %1, %5\n\tds_write_b128 %2, %6\n\tds_write_b128 %3, %7" ::"v"(twa), "v"(twa ^ 64), "v"(twa ^ 128), "v"(twa ^ 192),
                     "v"(o[0]), "v"(o[1]), "v"(o[2]), "v"(o[3]) : "memory");
        asm volatile("ds_read_b128 %0, %4\n\tds_read_b128 %1, %5 offset:1024\n\tds_read_b128 %2, %6 offset:2048\n\tds_read_b128 %3, %7 offset:3072\n\ts_waitcnt lgkmcnt(0)"
                     : "=&v"(o[0]), "=&v"(o[1]), "=&v"(o[2]), "=&v"(o[3]) : "v"(tra), "v"(tra ^ 64), "v"(tra ^ 128), "v"(tra ^ 192) : "memory");
      }
      auto OST = [&](bool on) __attribute__((always_inline)) {
        const unsigned mk_ = (unsigned)__builtin_amdgcn_ballot_w64(on) >> g4;
#pragma unroll
        for (int q = 0; q < 4; ++q)
          if (mk_ & (1u << (4 * q))) asm volatile("global_store_dwordx4 %0, %1, %2" OPOL "\n\ts_nop 1" ::"v"(oo_), "v"(o[q]), "s"(out + q * 4096) : "memory");
      };
      bool df = slt;
      {
        const int tn = (int)(tick() - (tq + (T_C3 - T_C1)));
        if (tn > T_X) df = true;
        else if (tn > -T_W) { unsigned sv; LSPIN(sv, ssel + 16 + wid, sv != 0u); }
      }
      OST(!df);
      TS(1);
      u32x2 ks;
      POLL2(ks[1] == 0u, 1, 1 << 26, asm volatile("ds_read_b64 %0, %1\n\ts_waitcnt lgkmcnt(0)" : "=v"(ks) : "v"(lds0 + SPARE + 10368) : "memory"));
      df = df && (ks[0] & 0xffffu) != rk && (ks[1] & 0xffffu) != rk;
      SKEWPT();
      if (__any(df)) {
        if (b < NPART) {
          unsigned sv;
          LSPIN(sv, ssel + O_DW, sv != 0u);
          if (O_DS) __builtin_amdgcn_s_sleep(O_DS);
        }
        OST(df);
      }
      if (__builtin_expect(__any(t1 > ks[1]), 0)) {
        VDLD(8);
        slt = slt ? 0u : key == vd[0] ? 9u : key == vd[1] ? 10u : 0u;
        if (slt) {
          XCST();
          if (dl) SCST();
        }
      }
    }
    TS(16);
    TSE();
  } else {
    const int hw = wid - 4;
    unsigned ts0 = tick();
    asm volatile("" : "+v"(ts0));
    const char* Hc = reinterpret_cast<const char*>((cc ? X1 : X0) + (size_t)et * 128 * D);
    auto UPOLL = [&](int n) {
      const unsigned fo = (unsigned)WS_UFL + ((cc * 16 + nt) * 32 + ((mt + 1 + (lane >> 2)) & 7) * 4 + (lane & 3)) * FSTR * 4;
      unsigned f;
      POLL2(f == 0u && lane < 4 * n, U_S, PCAP, asm volatile("global_load_dword %0, %1, %2 sc1" VW0 : "=&v"(f) : "v"(fo), "s"(ws) : "memory"));
      return (__builtin_amdgcn_ballot_w64(f == 0u) & 0xFFFFFFFull) == 0;
    };
    bool urdy = true;
    u32x4 gT[8], uT[8], wdv[8];
    unsigned og = (unsigned)(((g & 3) * 256 + hw * 64 + (lane >> 3)) * F + (g >> 2) * 64 + ((lane & 7) ^ ((lane >> 3) & 2) ^ 4) * 8) * 2;
    unsigned wta = lds0 + STG + (g4 * 8 + ((lane >> 2) & 3)) * 128 + (((hw * 2 + ((lane >> 1) & 1)) ^ ((lane >> 2) & 2) ^ ((g4 & 1) * 4)) << 4) + (lane & 1) * 8;
    unsigned od = (unsigned)(((g >> 2) * 64 + (lane >> 3) * 8) * D + (g & 3) * 256 + hw * 64 + (lane & 7) * 8) * 2;
    unsigned ou = og + (unsigned)(((mt + W_UO) % 7 - mt) * 128);
    asm volatile("" : "+v"(og), "+v"(ou), "+v"(od), "+v"(wta));
#define LDWI(dst, voff, base, inc) asm volatile("global_load_dwordx4 %0, %1, %2" WPOL "\n\tv_add_u32 %1, %3, %1" : "=v"(dst), "+v"(voff) : "s"(base), "n"(inc) : "memory")
#define WST(o, w)                                                                                                          \
  if (b < NPART) {                                                                                                         \
    asm volatile("s_mov_b32 m0, %0" ::"s"(lds0 + STG + hw * 8192));                                                        \
    _Pragma("unroll") for (int i = 0; i < 8; ++i) {                                                                        \
      asm volatile("v_xor_b32 %0, 64, %0\n\tglobal_load_lds_dwordx4 %0, %1 offset:%2" WPOL "\n\tv_add_u32 %0, %3, %0"       \
                   : "+v"(o) : "s"(w), "n"((i & 3) * 1024), "n"(F * 16 + (i == 3 ? 3072 : -1024)) : "memory");             \
      if (i == 3) asm volatile("s_add_u32 m0, m0, 4096" ::: "scc");                                                        \
    }                                                                                                                      \
  }
#define WTR2(i) "ds_read_b64_tr_b16 %" #i ", %16 offset:(" #i "/2)*4096+(" #i "&1)*512\n\t"
#define WTK(T)                                                                                                                         \
  if (b < NPART) {                                                                                                                     \
    u32x2 t_[16];                                                                                                                      \
    asm volatile(WTR2(0) WTR2(1) WTR2(2) WTR2(3) WTR2(4) WTR2(5) WTR2(6) WTR2(7) WTR2(8) WTR2(9) WTR2(10) WTR2(11) WTR2(12) WTR2(13)   \
                     WTR2(14) WTR2(15) "s_waitcnt lgkmcnt(0)"                                                                          \
                 : "=&v"(t_[0]), "=&v"(t_[1]), "=&v"(t_[2]), "=&v"(t_[3]), "=&v"(t_[4]), "=&v"(t_[5]), "=&v"(t_[6]), "=&v"(t_[7]),     \
                   "=&v"(t_[8]), "=&v"(t_[9]), "=&v"(t_[10]), "=&v"(t_[11]), "=&v"(t_[12]), "=&v"(t_[13]), "=&v"(t_[14]), "=&v"(t_[15]) \
                 : "v"(wta) : "memory");                                                                                               \
    _Pragma("unroll") for (int f = 0; f < 8; ++f) T[f] = u32x4{t_[2 * f][0], t_[2 * f][1], t_[2 * f + 1][0], t_[2 * f + 1][1]};        \
  }
#define DLD(q0, q1) if (b < NPART) { _Pragma("unroll") for (int q = q0; q < q1; ++q) LDWI(wdv[q], od, wd, D * 2); }
    const int pf0 = A_PS + (((j8 & 7) * A_S1) >> 3), pf1 = A_P2 + (((j8 & 7) * A_S2) >> 3), q1 = A_Q1;
    static_assert(A_PS >= 1 && A_P2 + A_S2 * 7 / 8 + 3 <= R_AT && R_AT < A_Q1 && A_P2 + A_S2 * 7 / 8 + 1 + W_T <= A_Q1 && H_AT + 3 <= A_Q1 && G1_C + G1_S < W_T && A_PS + W_T < A_P2 && A_PS + A_S1 * 7 / 8 + W_T + 1 < A_P2 + A_S2 * 7 / 8 && W_T >= M_NS - 2 && A_Q1 < F_AT && F_AT <= 29 && V_K <= B_J && V_T > B_J && B_WK == 15 && X_K >= N_C && (X_K + M_NS - 1 < 16 || (X_K <= B_MAT && TP_XC - 11 >= TP_X0 - TP_P0)) && TP_XC >= 4 * TP_PS && TP_XC + TP_P0 + 32 <= 72 && N_K > N_C && N_C > V_T && B_RAT > B_J && B_QAT > B_J && B_QAT + B_QD < B_MAT, "barrier counts");
#define HOFF(r) (unsigned)((((r) >> 5) * 32 + (((r) >> 2) & 3) * 8 + (((r) >> 4) & 1) * 4 + ((r)&3)) * 2048 + ((lane & 7) ^ (lane >> 3)) * 16)
#define TR8(T, W)                                                                                                    \
  _Pragma("unroll") for (int c = 0; c < 8; ++c) {                                                                    \
    _Pragma("unroll") for (int p = 0; p < 4; ++p)                                                                    \
        T[c][p] = __builtin_amdgcn_perm(W[2 * p + 1][c >> 1], W[2 * p][c >> 1], (c & 1) ? 0x07060302u : 0x05040100u); \
    asm volatile("" : "+v"(T[c]));                                                                                   \
  }
    u32x4 qm;
#pragma unroll
    for (int i = 0; i < 4; ++i) qm[i] = ((lane >> 2) & 3) == i ? ~0u : 0u;
    auto MVR = [&](f32x4 d) __attribute__((always_inline)) { return ((lane & 8) ? d[3] : d[0]) + dpp_f<0x128>((lane & 8) ? d[1] : d[2]); };
    auto MV64 = [&](const u32x4* T, u32x4 a) __attribute__((always_inline)) {
      f32x4 d[2] = {};
#pragma unroll
      for (int c = 0; c < 8; ++c) d[c & 1] = __builtin_amdgcn_mfma_f32_16x16x32_bf16(BF8(a & qm[c >> 1]), BF8(T[c]), d[c & 1], 0, 0, 0);
      return f32x2{MVR(d[0]), MVR(d[1])};
    };
#define LD1(i, a) "global_load_dwordx4 %" #i ", %" #a ", %10 sc1\n\tv_add_u32 %" #a ", 0x4000, %" #a "\n\t"
#define LD8(r, o0, o1, W)                                                                                                          \
  do {                                                                                                                             \
    unsigned a0_ = (o0), a1_ = (o1);                                                                                               \
    asm volatile(LD1(0, 8) LD1(4, 9) LD1(1, 8) LD1(5, 9) LD1(2, 8) LD1(6, 9) LD1(3, 8) LD1(7, 9) "s_nop 0" W                        \
                 : "=&v"(r[0]), "=&v"(r[1]), "=&v"(r[2]), "=&v"(r[3]), "=&v"(r[4]), "=&v"(r[5]), "=&v"(r[6]), "=&v"(r[7]), "+v"(a0_), "+v"(a1_) \
                 : "s"(ws) : "memory");                                                                                            \
  } while (0)
    u32x4 sel, gp[4];
    unsigned vc;
#define VCLD(W) asm volatile("global_load_dword %0, %1, %2 sc1" W : "=&v"(vc) : "v"(O_VP + ((cc ^ 1) * 8 + mt) * 4096 + (lane & 15) * 256), "s"(ws) : "memory")
#define GP4(o, W)                                                                                                         \
  asm volatile(                                                                                                           \
      "global_load_dwordx4 %0, %4, %5 sc1\n\tglobal_load_dwordx4 %1, %4, %5 offset:1024 sc1\n\t"                         \
      "global_load_dwordx4 %2, %4, %5 offset:2048 sc1\n\tglobal_load_dwordx4 %3, %4, %5 offset:3072 sc1" W                \
      : "=&v"(gp[0]), "=&v"(gp[1]), "=&v"(gp[2]), "=&v"(gp[3])                                                            \
      : "v"(o), "s"(ws)                                                                                                   \
      : "memory")
#define GPBAD (max(max(max4(gp[0]), max4(gp[1])), max(max4(gp[2]), max4(gp[3]))) == SENT)
      auto GPSUM = [&]() __attribute__((always_inline)) {
        f32x4 d = {0.f, 0.f, 0.f, 0.f};
#pragma unroll
        for (int i = 0; i < 4; ++i) d = __builtin_amdgcn_mfma_f32_16x16x32_bf16(BF8(sel), BF8(gp[i]), d, 0, 0, 0);
        return d;
      };
    const bool rdc = b >= NPART && hw == 3, rnp = b >= NPART && hw == 2 && cc == 0;
    const int gbq = (b >> 4) & 1, cj = (b & 15) * 64 + (lane & 31) * 2;
    unsigned voff[4], ro, xo, ra1, wqa, qo, so_, qa, mo, mo1, mo2, ma2;
    auto JSET = [&]() {
#pragma unroll
      for (int i = 0; i < 4; ++i) {
        voff[i] = HOFF((hw * 4 + i) * 8 + (lane >> 3)) - i * 1024;
        sel[i] = ((lane & 15) >> 1) == i ? ((lane & 1) ? 0x3f800000u : 0x3f80u) : 0u;
      }
      ro = O_G2 + ((lane >> 5) * 16384 + gbq * 1024 + cj) * 8;
      xo = (unsigned)((nt * 128 + hw * 32 + cc * 16 + g4) * D + et * 128 + ((g4 ^ lane) & 15) * 8) * 2;
      ra1 = lane < 32 ? O_MOD + 2048 + gbq * 4096 + cj * 2 : O_M2 + 8192 + 64 - gbq * 64 + (b & 15) * 4;
      wqa = lds0 + SPARE + 8192 + cj * 2;
      mo = hw ? O_MOD + hw * 2048 + lane * 16 : O_MOD + 4096, mo1 = hw ? mo + 1024 : mo;
      qo = O_RS + (4096 + ((hw & 1) * 8 + (lane >> 4) * 2 + 1) * D + et * 128 + (hw >> 1) * 64 + (lane & 15) * 4) * 4;
      so_ = O_RS + ((hw & 1) * S + nt * 128 + (lane >> 4) * 32 + cc * 16 + (lane & 15)) * 4;
      qa = lds0 + RKA + ((hw >> 1) * 64 + ((lane >> 3) & 1) * 32 + (lane & 1) * 16 + ((lane >> 1) & 3) * 4 + (lane >> 4)) * 16 + (hw & 1) * 8;
      mo2 = O_M2 + (hw == 2 ? 8448u + (lane & 15) * 16 : hw ? 8192u + (lane & 7) * 16 : lane < 32 ? et * 512 + lane * 16 : 4096u + (lane & 16) * 128 + et * 256 + (lane & 15) * 16);
      ma2 = lds0 + (hw ? SPARE + 10240 + (lane & 7) * 8 : TAB + lane * 16);
      asm volatile("" : "+v"(voff[0]), "+v"(voff[1]), "+v"(voff[2]), "+v"(voff[3]), "+v"(ro), "+v"(xo), "+v"(ra1), "+v"(wqa), "+v"(qo), "+v"(so_), "+v"(qa), "+v"(mo), "+v"(mo1), "+v"(mo2), "+v"(ma2), "+v"(sel));
      *reinterpret_cast<unsigned*>((hw & 1) ? shm + RKB + 1024 + lane * 4 : reinterpret_cast<char*>(scw[2]) + lane * 4) = (hw & 1) ? 0u : 0x3f803f80u;
      if (hw == 3) {
        if (lane < 32) ssel[lane] = 0u;
        *reinterpret_cast<u32x2*>(shm + SLT + lane * 8) = u32x2{0u, 0u};
      }
    };
    if (hw < 2) {
      constexpr int P = 8;
      __builtin_amdgcn_s_setprio(2);
      unsigned voy[P];
#pragma unroll
      for (int i = 0; i < P; ++i) voy[i] = (unsigned)(((hw * 8 + i) * 8 + (lane >> 3)) * 4096 + ((lane & 7) ^ (lane >> 3)) * 16 - (i & 3) * 1024);
      const unsigned lbase = lds0 + hw * 8192;
      const char* Yc = reinterpret_cast<const char*>((cc ? Y1 : Y0) + (size_t)nt * 128 * S);
      auto stage = [&](const char* yp, unsigned l) {
        GLDS4(l, voy, yp);
        GLDS4(l + 4096, voy + 4, yp);
      };
#define YSRC(n) (Yc + ((((n) + xcd * 4) & 31) << 7))
      __builtin_amdgcn_s_sleep(C_LD);
      PFL(hw ? PFG(1) : cpf);
      JSET();
      TAGW(1);
      BAR(55);
      BT(56);
      if (hw == 1 && lane == 0) asm volatile("global_store_dwordx2 %0, %1, %2 sc1" ::"v"(O_XFL + b * XSTR * 4), "v"(tag), "s"(ws) : "memory");
#pragma unroll
      for (int s = 0; s < NS - 1; ++s) stage(YSRC(s), lbase + s * 16384);
      BT(57);
      WAIT_V((NS - 2) * P);
      BAR(0);
      unsigned slot = lbase + (NS - 1) * 16384;
      const u64 evm = (u64)((1u | 1u << W_T) << pf0 | (1u | 1u << W_T) << pf1) | 1ull << CPF_K | 1ull << (33 - NS) | 3ull << 32;
      const char* yb = Yc;
      unsigned yo = (NS - 1 + xcd * 4) << 7, ym = 31 << 7;
#pragma unroll 1
      for (int k = 1; k <= 32 + B_J; ++k) {
        const char* ys = yb + (yo & ym);
        yo += 128;
        asm volatile("" : "+s"(ys));
        WAIT_V((NS - 3) * P);
        BAR(k);
        stage(ys, slot);
        slot = slot == lbase + (NS - 1) * 16384 ? lbase : slot + 16384;
        if (__builtin_expect((evm >> k) & 1, 0)) {
          if (k == pf0) {
            WST(og, wg);
            if (b >= NPART) GLDS(lds0 + SPARE + 8192 + hw * 1024, (unsigned)(hw * 1024 + lane * 16), wr_);
          }
          if (k == pf0 + W_T) { WTK(gT); }
          if (k == pf1) { WST(ou, wu); }
          if (k == pf1 + W_T) { WTK(uT); }
          if (k == CPF_K && hw == 1) CPFL();
          if (k == 33 - NS) {
#pragma unroll
            for (int i = 0; i < P; ++i) voy[i] = HOFF((hw * 8 + i) * 8 + (lane >> 3)) - (i & 3) * 1024;
            yb = Hc, yo = mt << 8, ym = 15 << 7;
          }
          if (k == 32) { DLD(0, 8 - B_WN); }
          if (k == 33 && hw == 0) {
            if (U_D) __builtin_amdgcn_s_sleep(U_D);
            urdy = UPOLL(U_N1);
          }
        }
      }
    } else {
      const int aw = hw - 2;
      unsigned ow = wo - (2048 + (cg0 >> 4) * 1024) * 2;
      const unsigned oq = (unsigned)((nt * 64 + aw * 32 + (lane >> 4)) * D + mt * 128) * 2;
      u32x4 hv[8], wt[8], sc;
      {
        asm volatile("s_waitcnt vmcnt(11)" : "+v"(cq)::"memory");
#pragma unroll
        for (int i = 0; i < 4; ++i) sc[i] = pack2(silu_bf(bflo(cq[i])), silu_bf(bfhi(cq[i])));
        asm volatile("" : "+v"(sc));
      }
#define GPUB(o, off)                                                                                   \
  if ((lane & 8) == 0)                                                                                 \
    asm volatile("global_store_dwordx4 %0, %1, %2 sc1 ; ALWAYS\n\ts_nop 1" ::"v"(off),                          \
                 "v"(u32x4{__float_as_uint(o[0]), (unsigned)tag, __float_as_uint(o[1]), (unsigned)tag}), "s"(ws) : "memory")
      const unsigned gpo = ks0 * 2048 + cg0 * 64 + (lane & 7) * 8 + (lane >> 4) * 2;
      {
        unsigned mpo = O_G1 + gpo * 8;
        asm volatile("s_waitcnt vmcnt(3)" : W8(wv), "+v"(mpo)::"memory");
        TR8(wt, wv);
        f32x2 o = MV64(wt, sc);
        asm volatile("" : "+v"(o));
        TAGW(0);
        BAR(55);
        if (P_D2) __builtin_amdgcn_s_sleep(P_D2);
        PFL(PFG(2 + aw));
        GPUB(o, mpo);
      }
      unsigned vta = lds0 + STG + (g4 * 8 + ((lane >> 2) & 3)) * 256 + (((aw * 8 + ((lane >> 1) & 1)) ^ ((g4 & 1) * 8 + ((lane >> 2) & 3))) << 4) + (lane & 1) * 8;
      unsigned vso = O_VP + ((cc * 8 + mt) * 16 + nt) * 256 + aw * 128 + (lane & 3) * 32 + g4 * 8;
      asm volatile("" : "+v"(vta), "+v"(vso), "+v"(ow));
      unsigned f0 = O_XFL + ((xcd * 32 + 64 + aw * 128 + lane) & 255) * XSTR * 4, f1 = O_XFL + ((xcd * 32 + 128 + aw * 64 + lane) & 255) * XSTR * 4;
      unsigned lao = O_KEY + 512 * 1024 + b * 3072 + aw * 1536 + lane * 16;
      const char* pf4 = PFG(4 + aw);
      const char* pf6 = PFG(6 + aw);
      asm volatile("" : "+v"(f0), "+v"(f1), "+v"(lao), "+v"(pf4), "+v"(pf6));
      BT(57);
      BAR(0);
      float rs = 0.f, rs2 = 0.f;
      const unsigned ra = lds0 + (mt * 16 + aw * 8) * 128 + lane * 16;
      unsigned so = 0;
#define TSX()
#define RSUM()                                                                                                    \
  do {                                                                                                            \
    TSX();                                                                                                        \
    u32x4 R_;                                                                                                     \
    asm volatile("ds_read_b128 %0, %1\n\ts_waitcnt lgkmcnt(0)" : "=&v"(R_) : "v"(ra + so) : "memory");            \
    _Pragma("unroll") for (int j = 0; j < 4; ++j) {                                                               \
      asm volatile("v_add_f32 %0, %0, %1" : "+v"(rs) : "v"(R_[j] << 16));                                         \
      asm volatile("v_add_f32 %0, %0, %1" : "+v"(rs2) : "v"(R_[j] & 0xffff0000u));                                \
    }                                                                                                             \
    so = so == (NS - 1) * 16384 ? 0 : so + 16384;                                                                 \
  } while (0)
      {
        u64 f[2];
#define FLD() asm volatile("global_load_dwordx2 %0, %2, %4 sc1\n\tglobal_load_dwordx2 %1, %3, %4 sc1" : "=&v"(f[0]), "=&v"(f[1]) : "v"(f0), "v"(f1), "s"(ws) : "memory")
        __builtin_amdgcn_s_sleep(F_D);
        FLD();
        asm volatile("global_store_dwordx4 %0, %1, %2 sc1" ::"v"(lao), "v"(u32x4{~0u, ~0u, ~0u, ~0u}), "s"(ws) : "memory");
        if (lane < 32) asm volatile("global_store_dwordx4 %0, %1, %2 offset:1024 sc1 ; ALWAYS" ::"v"(lao), "v"(u32x4{~0u, ~0u, ~0u, ~0u}), "s"(ws) : "memory");
        asm volatile("s_nop 1");
        RSUM();
        BAR(1);
        asm volatile("s_waitcnt vmcnt(%2)" : "+v"(f[0]), "+v"(f[1]) : "n"(2) : "memory");
        POLL(__all(f[0] == tag && f[1] == tag), 1, FLD(); asm volatile("s_waitcnt vmcnt(0)" : "+v"(f[0]), "+v"(f[1])::"memory"));
      }
      if (X_PA) __builtin_amdgcn_s_setprio(X_PA);
#define ARUN(b0, b1) _Pragma("unroll 1") for (int bi = (b0); bi < (b1); ++bi) { RSUM(); BAR(bi + 1); }
#define GLD() _Pragma("unroll") for (int i = 0; i < 8; ++i) LDWI(wv[i], ow, wmod, 12288)
      ARUN(1, pf0);
      PFL(pf4);
      GLD();
#define HVLD() _Pragma("unroll") for (int i = 0; i < 8; ++i) GLDS(lds0 + STG + aw * 8192 + i * 1024, oq + i * 8192 + (((lane & 15) ^ ((i * 4 + (lane >> 4)) & 15)) << 4), cc ? X1 : X0)
      WST(og, wg);
      ARUN(pf0, pf0 + G1_C);
      {
        if (b >= NPART) WAIT_V(0);
        asm volatile("s_waitcnt vmcnt(8)" : W8(wv)::"memory");
        TR8(wt, wv);
        if (G1_S) { RSUM(); BAR(pf0 + G1_C + 1); }
        const f32x2 o = MV64(wt, sc);
        GPUB(o, O_MODP + gpo * 8);
        ow = wo + 2048;
      }
      ARUN(pf0 + G1_C + G1_S, pf0 + W_T - 1);
      WAIT_V(1);
      RSUM();
      BAR(pf0 + W_T);
      WTK(gT);
      ARUN(pf0 + W_T, pf1);
      PFL(pf6);
      WST(ou, wu);
      const int rb_ = b < NPART ? pf1 + W_T : R_AT, hb_ = max(rb_ + 1, H_AT + (j8 & 3));
      const unsigned c0 = (b - NPART) * 64 + aw * 32 + (lane & 15) * 2, c1 = (b - NPART) * 32 + aw * 16 + (lane & 7) * 2;
#define ERLD(W) LD8(wt, O_MODP + (g4 * 8192 + c0) * 8, O_G1 + (g4 * 8192 + (lane & 8) * 128 + c1) * 8, W)
      ARUN(pf1, rb_ - 3);
      if (b >= NPART) ERLD("");
      ARUN(rb_ - 3, rb_ - 1);
      WAIT_V(0);
      RSUM();
      BAR(rb_);
      WTK(uT);
      if (b >= NPART) {
        const unsigned wq = *reinterpret_cast<const unsigned*>(&svb[4][c1]);
#define TG2(r, i) ((r[i][1] ^ (unsigned)tag) | (r[i][3] ^ (unsigned)tag) | (r[i + 1][1] ^ (unsigned)tag) | (r[i + 1][3] ^ (unsigned)tag))
        asm volatile("" : W8(wt));
        POLL(__all((TG2(wt, 0) | TG2(wt, 2) | TG2(wt, 4) | TG2(wt, 6)) == 0u), 2, ERLD(VW0));
        auto FIN = [&](int q, int i, bool inc) __attribute__((always_inline)) {
          return rbf((inc ? 1.0f : 0.0f) + rbf(ar4((__uint_as_float(wt[q][i]) + __uint_as_float(wt[q + 1][i])) + (__uint_as_float(wt[q + 2][i]) + __uint_as_float(wt[q + 3][i])))));
        };
        const float s0 = FIN(0, 0, gbq), s1 = FIN(0, 2, gbq);
        if (lane < 16)
          asm volatile("global_store_dwordx4 %0, %1, %2 sc1\n\ts_nop 1" ::"v"(O_MODP + (16 * 2048 + c0) * 8),
                       "v"(u32x4{__float_as_uint(s0), (unsigned)tag, __float_as_uint(s1), (unsigned)tag}), "s"(ws) : "memory");
        const bool hi = lane & 8;
        const float t0 = FIN(4, 0, hi), t1 = FIN(4, 2, hi), o0 = dpp_f<0x128>(t0), o1 = dpp_f<0x128>(t1);
        const float a0 = hi ? o0 : t0, a1 = hi ? o1 : t1, e0 = hi ? t0 : o0, e1 = hi ? t1 : o1, p0 = e0 * bflo(wq), p1 = e1 * bfhi(wq);
        const unsigned h = pack2(p0, p1);
        const unsigned v = g4 == 0 ? __float_as_uint(hi ? a1 : a0) : g4 == 1 ? (hi ? h : pack2(e0, e1)) : hi ? pack2(a0 * p0, a1 * p1) : pack2(p0 - bflo(h), p1 - bfhi(h));
        const unsigned po = g4 == 0 ? O_M2 + c1 * 4 + (hi ? 4 : 0) : (g4 == 1 ? (hi ? O_M2 + 4096 : O_MOD + 4096) : hi ? O_GC : O_M2 + 6144) + c1 * 2;
        if (lane < 48) asm volatile("global_store_dword %0, %1, %2 sc1" ::"v"(po), "v"(min(v, SENT - 1u)), "s"(ws) : "memory");
      }
      ARUN(rb_, hb_);
      HVLD();
      ARUN(hb_, q1);
      u32x4 fm, g0, g1;
#define FMLD(W)                                                                                                                                     \
  asm volatile("global_load_dwordx4 %0, %3, %5 sc1\n\tglobal_load_dwordx4 %1, %4, %5 sc1\n\tglobal_load_dwordx4 %2, %4, %5 offset:64 sc1" W        \
               : "=&v"(fm), "=&v"(g0), "=&v"(g1) : "v"(O_MODP + (16 * 2048 + aw * 1024 + mt * 128) * 8 + lane * 16), "v"(O_GC + nt * 128 + g4 * 16), "s"(ws) : "memory")
      FMLD("");
      RSUM();
      BAR(q1 + 1);
      unsigned ha[4], sma = lds0 + SPARE + 15872 + (aw ? 512 + lane * 8 : lane * 4);
#pragma unroll
      for (int i = 0; i < 4; ++i) ha[i] = lds0 + STG + aw * 8192 + (lane & 15) * 256 + (((i * 4 + (lane >> 4)) ^ (lane & 15)) << 4);
      asm volatile("" : "+v"(ha[0]), "+v"(ha[1]), "+v"(ha[2]), "+v"(ha[3]), "+v"(sma), "+v"(ow));
      ARUN(q1 + 1, F_AT);
      asm volatile("s_waitcnt vmcnt(0)" : "+v"(fm), "+v"(g0), "+v"(g1)::"memory");
#pragma unroll
      for (int i = 0; i < 8; ++i) DSR(hv[i], ha[i & 3], (i >> 2) * 4096);
      POLL(__all(fm[1] == (unsigned)tag && fm[3] == (unsigned)tag && max(max4(g0), max4(g1)) != SENT), 1, FMLD(VW0));
      {
        const unsigned sg = cc ? 0x80000000u : 0u;
        asm volatile("ds_write_b32 %0, %2\n\tds_write_b32 %1, %3" ::"v"(sma), "v"(sma + (aw ? 4 : 256)), "v"(aw ? fm[0] ^ sg : (fm[0] >> 16) | (fm[2] & 0xffff0000u)), "v"(fm[2] ^ sg) : "memory");
      }
      ARUN(F_AT, 29);
      GLD();
      RSUM();
      BAR(30);
        u32x2 tv[16];
#pragma unroll
        for (int q = 0; q < 16; ++q)
          asm volatile("ds_read_b64_tr_b16 %0, %1 offset:%2" : "=v"(tv[q]) : "v"(vta ^ ((((q >> 2) * 2) ^ ((q & 1) * 4)) << 4)), "n"((q & 1) * 1024 + ((q >> 1) & 1) * 8192) : "memory");
      RSUM();
      BAR(31);
      RSUM();
      {
        const float v = dpp_add<0x141>(dpp_add<0x4E>(dpp_add<0xB1>(rs + rs2)));
        if ((lane & 7) == 0)
          asm volatile("global_store_dword %0, %1, %2 sc1 ; ALWAYS" ::"v"(O_RS + (cc * S + nt * 128 + mt * 16 + aw * 8 + (lane >> 3)) * 4), "v"(nsent(v)), "s"(ws) : "memory");
      }
      BAR(32);
      __builtin_amdgcn_s_sleep(2);
      DLD(0, 8 - B_WN);
      __builtin_amdgcn_s_setprio(2);
      {
        asm volatile("" : W8(tv), W8((tv + 8))::"memory");
        f32x4 d[4];
#pragma unroll
        for (int j = 0; j < 4; ++j) {
          d[j] = __builtin_amdgcn_mfma_f32_16x16x32_bf16(BF8((u32x4{tv[4 * j][0], tv[4 * j][1], tv[4 * j + 1][0], tv[4 * j + 1][1]})), BF8(g0), f32x4{0.f, 0.f, 0.f, 0.f}, 0, 0, 0);
          d[j] = __builtin_amdgcn_mfma_f32_16x16x32_bf16(BF8((u32x4{tv[4 * j + 2][0], tv[4 * j + 2][1], tv[4 * j + 3][0], tv[4 * j + 3][1]})), BF8(g1), d[j], 0, 0, 0);
        }
        u32x2 vo;
#pragma unroll
        for (int i = 0; i < 2; ++i) {
          const unsigned e0 = pack2(d[0][2 * i], d[0][2 * i + 1]), e1 = pack2(d[1][2 * i], d[1][2 * i + 1]), e2 = pack2(d[2][2 * i], d[2][2 * i + 1]), e3 = pack2(d[3][2 * i], d[3][2 * i + 1]);
          vo[i] = min((lane & 2) ? ((lane & 1) ? e3 : e2) : ((lane & 1) ? e1 : e0), SENT - 1u);
        }
        if ((lane & 12) == 0) asm volatile("global_store_dwordx2 %0, %1, %2 sc1 ; ALWAYS" ::"v"(vso), "v"(vo), "s"(ws) : "memory");
      }
      asm volatile("" : W8(hv)::"memory");
      {
        f32x4 qa[2] = {};
#pragma unroll
        for (int kk = 0; kk < 4; ++kk) {
          const bf16x8 sb8 = *reinterpret_cast<const bf16x8*>(ssa + kk * 32 + (lane >> 4) * 8);
#pragma unroll
          for (int r = 0; r < 2; ++r)
            qa[r] = __builtin_amdgcn_mfma_f32_16x16x32_bf16(__builtin_bit_cast(bf16v8, hv[r * 4 + kk]), __builtin_bit_cast(bf16v8, sb8), qa[r], 0, 0, 0);
        }
        if ((lane & 15) < 2)
          asm volatile("global_store_dwordx4 %0, %1, %2 sc1 ; ALWAYS\n\ts_nop 1" ::"v"(O_RS + (4096 + (cc * 8 + mt) * D + nt * 64 + aw * 32 + (lane & 1) * 16 + (lane >> 4) * 4) * 4),
                       "v"(nsent4(selu((lane & 1) ? ~0u : 0u, __builtin_bit_cast(u32x4, qa[1]), __builtin_bit_cast(u32x4, qa[0])))), "s"(ws) : "memory");
      }
      BAR(33);
      {
        if (b >= NPART) WAIT_V(3);
        asm volatile("s_waitcnt vmcnt(%8)" : W8(wv) : "n"(8 - B_WN + 3) : "memory");
        TR8(wt, wv);
        const f32x2 o = MV64(wt, sc);
        GPUB(o, O_G2 + gpo * 8);
      }
      JSET();
      BAR(34);
#pragma unroll
      for (int k = 3; k <= B_J; ++k) {
        BAR(32 + k);
        if (k == V_K) {
          if (!aw) GP4(O_VP + (cc * 8 + mt) * 4096 + lane * 16, "");
          else VCLD("");
        }
      }
    }
    u32x4 dT[8];
    u32x4 m[3];
    u32x2 kk = {SENT, SENT};
#define K2LD(W) asm volatile("global_load_dwordx2 %0, %1, %2 sc1" W : "+v"(kk) : "v"(O_K2 + (lane & 15) * 64), "s"(ws) : "memory")
    {
      constexpr BSched sch;

      const unsigned lbase = lds0 + hw * 4096;
      u32x4 rr[8];
      unsigned cbv, t1, t2;
      float t[2];
#define RRLD(W)                                                                                                          \
  do {                                                                                                                   \
    asm volatile("global_load_dword %0, %1, %2 sc1" : "=&v"(cbv) : "v"(O_MOD + 4096 + cj * 2), "s"(ws) : "memory");      \
    LD8(rr, ro, ro + 65536, W);                                                                                          \
  } while (0)
      unsigned wq;
      unsigned mr;
#define QLD(W)                                                                                                           \
  asm volatile("global_load_dwordx4 %0, %3, %5 offset:-4096 sc1\n\tglobal_load_dwordx4 %1, %3, %5 sc1\n\tglobal_load_dword %2, %4, %5 sc1" W \
               : "=&v"(m[0]), "=&v"(m[1]), "=&v"(mr)                                                                     \
               : "v"(qo), "v"(so_), "s"(ws)                                                                              \
               : "memory")
#define MODLD()                                                                                                                \
  asm volatile("global_load_dwordx4 %0, %3, %6 sc1\n\tglobal_load_dwordx4 %1, %4, %6 sc1\n\tglobal_load_dwordx4 %2, %5, %6 sc1" \
               : "=&v"(m[0]), "=&v"(m[1]), "=&v"(m[2])                                                                         \
               : "v"(mo), "v"(mo1), "v"(mo2), "s"(ws)                                                                          \
               : "memory")
#define MODW(n) asm volatile("s_waitcnt vmcnt(%3)" : "+v"(m[0]), "+v"(m[1]), "+v"(m[2]) : "n"(n) : "memory")
#pragma unroll
      for (int k = B_J; k < 16; ++k) {
        if (k > B_J) {
          if (k == 2 * U_N1 - 1 && !urdy) UPOLL(7);
          WAIT_V(sch.cnt[k]);
          if (k == B_QAT + B_QD + 1) WAIT_L(0);
          BAR(32 + k);
        }
        if (k > B_J && k - 2 + NS < 16) {
          const char* yp = Hc + ((mt * 2 + k - 2 + NS) & 15) * 128;
          const unsigned l = lbase + ((30 + k + NS) % NS) * 16384;
          GLDS4(l, voff, yp);
        }
        if (k == B_TK && hw == 1 && b >= 192) {
          const unsigned t = tick(), h = t + T_O + (((int)(t - ts0) - T_N) * T_A >> 8);
          if (lane == 0) asm volatile("global_store_dword %0, %1, %2 sc1" ::"v"(O_M2 + 8448 + (b - 192) * 4), "v"(min(h, SENT - 1u)), "s"(ws) : "memory");
        }
        if (k == B_RAT + 2) {
          if (rdc) {
            WAIT_V(sch.rcnt);
            asm volatile("" : W8(rr), "+v"(cbv));
            POLL(__all((TG2(rr, 0) | TG2(rr, 2) | TG2(rr, 4) | TG2(rr, 6)) == 0u && cbv != SENT), 2, RRLD(VW0); asm volatile("" : "+v"(cbv)));
#pragma unroll
            for (int j = 0; j < 2; ++j) {
#define RF(q) __uint_as_float(rr[q][2 * j])
              t[j] = ((RF(0) + RF(1)) + (RF(2) + RF(3))) + ((RF(4) + RF(5)) + (RF(6) + RF(7)));
            }
            asm volatile("" : "+v"(t[0]), "+v"(t[1]));
          }
        }
        if (k == B_RAT + 2 && rdc) {
          const unsigned o = pack2(swap32_add(t[0], t[0]), swap32_add(t[1], t[1])), f = gbq ? cbv : o;
          const float pr = row_sum(bflo(f) * bflo(wq) + bfhi(f) * bfhi(wq)), ps = swap16_add(pr, pr);
          if (lane <= 32) asm volatile("global_store_dword %0, %1, %2 sc1" ::"v"(ra1), "v"(min(lane < 32 ? o : __float_as_uint(ps), SENT - 1u)), "s"(ws) : "memory");
          TS(12);
        }
        if (k == V_T && hw == 3) {
          WAIT_V(sch.vcnt);
          asm volatile("" : "+v"(vc));
          POLL(__all(vc != SENT), 2, VCLD(VW0));
        }
        if (k == V_T && hw == 2) {
          WAIT_V(sch.vcnt);
          asm volatile("" : "+v"(gp[0]), "+v"(gp[1]), "+v"(gp[2]), "+v"(gp[3]));
          POLL(!__any(GPBAD), 2, GP4(O_VP + (cc * 8 + mt) * 4096 + lane * 16, VW0));
          const f32x4 d = GPSUM();
          if (lane < 32) *reinterpret_cast<u32x2*>(shm + VT + (lane & 15) * 16 + g4 * 8) = u32x2{pack2(d[0], d[1]), pack2(d[2], d[3])};
          WAIT_L(0);
        }
        if (k == N_C) {
          f32x4 n[2] = {};
#pragma unroll
          for (int q = 0; q < 8; ++q)
            n[q >> 2] = __builtin_amdgcn_mfma_f32_16x16x32_bf16(BF8(*reinterpret_cast<const u32x4*>(shm + VT + (q & 3) * 64 + g4 * 16)),
                                                                BF8(*reinterpret_cast<const u32x4*>(shm + STG + hw * 8192 + q * 1024 + lane * 16)), n[q >> 2], 0, 0, 0);
          if (lane < 16)
            asm volatile("global_store_dword %0, %1, %2 sc1" ::"v"(O_NP + nt * 4096 + (cc * 8 + mt) * 256 + hw * 64 + lane * 4), "v"(min(pack2(n[0][0], n[1][0]), SENT - 1u)), "s"(ws) : "memory");
          TS(33);
        }
        if (k == N_K + N_D && rnp) {
            WAIT_V(sch.ncnt);
            asm volatile("" : "+v"(gp[0]), "+v"(gp[1]), "+v"(gp[2]), "+v"(gp[3]));
            POLL(!__any(GPBAD), 2, GP4(O_NP + nt * 4096 + lane * 16, VW0));
            const unsigned r0 = nt * 128 + ((lane >> 2) & 3) * 32 + (lane & 3) * 4 + (g4 & 1) * 2;
        const f32x4 d = GPSUM();
        unsigned ky[4];
#pragma unroll
        for (int i = 0; i < 4; ++i) {
          const unsigned f = __float_as_uint(d[i]);
          ky[i] = (((f ^ ((unsigned)((int)f >> 31) | 0x80000000u)) >> 1) & ~2047u) | (r0 + (i & 1) * 16 + (i >> 1));
        }
        t1 = max(max(ky[0], ky[1]), max(ky[2], ky[3])), t2 = max(min(max(ky[0], ky[1]), max(ky[2], ky[3])), max(min(ky[0], ky[1]), min(ky[2], ky[3])));
        asm volatile("" : "+v"(t1), "+v"(t2));
        }
        if (k == N_K + N_D && rnp) {
        TOP2(0xB1); TOP2(0x4E); TOP2(0x141); TOP2(0x140);
        TOP2S(__builtin_amdgcn_permlane16_swap);
            if (lane == 0) asm volatile("global_store_dwordx2 %0, %1, %2 sc1" ::"v"(O_K2 + nt * 64), "v"(u32x2{t1, t2}), "s"(ws) : "memory");
            TS(34);
        }
        if (k == N_K && rnp) GP4(O_NP + nt * 4096 + lane * 16, "");
        if (k == B_QAT + B_QD) {
          asm volatile("s_waitcnt vmcnt(%3)" : "+v"(m[0]), "+v"(m[1]), "+v"(mr) : "n"(sch.qcnt) : "memory");
          POLL(__all(max(max(max4(m[0]), max4(m[1])), mr) != SENT), 2, QLD(VW0));
          const f32x4 t = __builtin_bit_cast(f32x4, m[0]) + __builtin_bit_cast(f32x4, m[1]);
          const float r = swap16_add(swap32_add(t[0], t[2]), swap32_add(t[1], t[3]));
          const unsigned a0 = pack2(r, r);
          asm volatile("ds_write_b64 %0, %1" ::"v"(qa), "v"(u32x2{a0, pack2(r - bflo(a0), r - bflo(a0))}) : "memory");
          const float rs = __uint_as_float(mr ^ ((hw & 1) ? 0x80000000u : 0u));
          const unsigned bb = pack2(rs, rs - rbf(rs));
          *reinterpret_cast<u32x2*>(shm + RKB + lane * 16 + (hw & 1) * 8) = u32x2{bb, bb};
        }
        if (k == CPF_B && hw == 2) CPFL();
        if (k == B_RAT) {
          if (rdc) RRLD("");
          asm volatile("ds_read_b32 %0, %1\n\ts_waitcnt lgkmcnt(0)" : "=v"(wq) : "v"(wqa) : "memory");
        }
        if (k == X_K) {
          const unsigned v4[4] = {xo, (xo ^ 64) + 8192 - 1024, (xo ^ 128) + 16384 - 2048, (xo ^ 192) + 24576 - 3072};
          unsigned l2 = lbase;
          asm volatile("" : "+s"(l2));
          GLDS4(l2 * 2 - lds0 + STG + 4096, v4, x);
        }
        if (k == V_T + 1 || k == N_K + 1) SKEWPT();
        if (k == B_QAT) QLD("");
        if (k == B_MAT) MODLD();
        if (k == B_WK) { DLD(8 - B_WN, 8); }
      }
      if (b >= NPART) WAIT_V(0);
      MODW(sch.mcnt);
      TS(30);
      SKEWPT();
      POLL(__all(max(max(max4(m[0]), max4(m[1])), max4(m[2])) != SENT), 4, MODLD(); MODW(0));
      *reinterpret_cast<u32x4*>(&svb[hw][lane * 8]) = m[0];
      *reinterpret_cast<u32x4*>(&svb[hw][512 + lane * 8]) = m[1];
      if (hw == 0) {
        asm volatile("ds_write_b128 %0, %1" ::"v"(ma2), "v"(m[2]) : "memory");
      } else if (hw == 2) {
        int d = max(max((int)(m[2][0] - ts0), (int)(m[2][1] - ts0)), max((int)(m[2][2] - ts0), (int)(m[2][3] - ts0)));
        d = max(d, (int)dpp_u<0xB1>(d));
        d = max(d, (int)dpp_u<0x4E>(d));
        d = max(d, (int)dpp_u<0x141>(d));
        d = max(d, (int)dpp_u<0x140>(d));
        const unsigned hh = ts0 + d;
        ssel[7] = hh;
        if ((int)(tick() - (unsigned)__builtin_amdgcn_readfirstlane((int)hh) - T_K) >= 0) K2LD("");
      } else {
        const f32x4 v = __builtin_bit_cast(f32x4, m[2]);
        const f32x2 c = {v[0] + v[1], v[2] + v[3]};
        const float s4 = dpp_add<0x4E>(dpp_add<0xB1>(c[0] + c[1]));
        if (lane < 8) asm volatile("ds_write_b64 %0, %1" ::"v"(ma2), "v"(lane < 4 ? c : f32x2{s4, s4}) : "memory");
      }
    }
    if (hw != 2) __builtin_amdgcn_s_setprio(0);
    TS(17);
    BT(48);
    LBAR();
    BTE();
    TS(18);
    if (hw == 2) {
      WAIT_V(0);
      asm volatile("" : "+v"(kk));
      if (__any(max(kk[0], kk[1]) == SENT)) {
        nap_tick((unsigned)__builtin_amdgcn_readfirstlane((int)ssel[7]) + T_K);
        POLL2(max(kk[0], kk[1]) == SENT, K_S, PCAP, K2LD(VW0));
      }
      TS(35);
      const unsigned me = lane < 32 ? kk[0] : kk[1], ot = (lane & 16) ? kk[1] : kk[0];
      unsigned rk = ot > me;
      sfor<15>([&](auto r) __attribute__((always_inline)) { rk += dpp_u<0x121 + decltype(r)::value>(ot) > me; });
      auto rs_ = __builtin_amdgcn_permlane16_swap(rk, rk, false, false);
      rk = rs_[0] + rs_[1];
      if (rk < 8u) {
        *reinterpret_cast<unsigned*>(shm + LST + rk * 4) = 0x8ffeu - (me & 2047u) * 2;
        if ((lane & 15) == nt) *reinterpret_cast<unsigned*>(shm + SLT + (me & 127u) * 4) = rk + 1;
      }
      asm volatile("" ::: "memory");
      ssel[20] = 1u;
    }
    TS(19);
    unsigned sfa = lds0 + SPARE + 10368, sv, one = 1u;
    RETIRE();
    const int kc = (g & 3) * 256 + hw * 64 + (lane & 7) * 8;
    const u32x4 cbq = *reinterpret_cast<const u32x4*>(&svb[2][kc]), sbq = *reinterpret_cast<const u32x4*>(&svb[1][kc]);
    f32x4 cb2[2] = {lo4(cbq), hi4(cbq)}, sb2[2] = {lo4(sbq), hi4(sbq)};
    float sw = cwj[8], cj8 = cwj[lane & 7];
    unsigned rk2 = lds0 + LST + (lane >> 3) * 4;
    unsigned xo_ = O_X1C + (lane >> 3) * 2048 + kc * 2, hwa = lds0 + XC + hw * 1024 + (lane & 7) * 128 + (((lane >> 3) ^ lane) & 7) * 16;
    unsigned hra = lds0 + XC + g4 * 128 + ((lane ^ g4) & 7) * 16, hrb = hra ^ 64, ara = lds0 + SPARE + 15872 + (lane & 2) * 64 + g4 * 32 + (lane & 1) * 16;
    unsigned pso = O_PART + ((g >> 2) * 2048 + (lane & 8) * 128 + (g & 3) * 256 + hw * 64 + (lane & 7) * 8 + g4 * 2) * 4;
    unsigned gso = O_GU + g * 1024 + (lane >> 5) * 256 + hw * 64 + (lane & 15) * 4, awa = lds0 + SPARE + 15872 + lane * 4, sfw = sfa + hw * 4;
    unsigned gl0 = O_GU + (g >> 2) * 4096 + lane * 8, gl1 = O_GU + (xcd * 7 + (mt + 7 - W_UO) % 7) * 4096 + 512 + lane * 8;
    asm volatile("" : "+v"(sfa), "+v"(one), "+v"(hra), "+v"(hrb), "+v"(ara), "+v"(pso), "+v"(gso), "+v"(awa), "+v"(gl0), "+v"(gl1), "+v"(sfw), "+v"(qm));
    asm volatile("" : "+v"(cb2[0]), "+v"(cb2[1]), "+v"(sb2[0]), "+v"(sb2[1]), "+v"(hwa), "+v"(sw), "+v"(cj8), "+v"(rk2), "+v"(xo_));
    unsigned sto = O_ST2 + lane * 16, kmk = ~0u;
    asm volatile("" : "+v"(sto), "+v"(kmk));
    const unsigned th = (unsigned)__builtin_amdgcn_readfirstlane((int)ssel[7]);
    const char* wb = ws;
#define P2PUT(i) if (__builtin_expect(wb != ws, 0)) { WAIT_V(0); if (lane == 0) asm volatile("global_store_dwordx2 %0, %1, %2 sc1" ::"v"(O_F2 + (i)*8192 + (g * 4 + hw) * 8), "v"(tag), "s"(ws) : "memory"); }
#define P2GET(o) if (__builtin_expect(wb != ws, 0)) { u64 f; POLL2(f != tag, 4, PCAP, asm volatile("global_load_dwordx2 %0, %1, %2 sc1" VW0 : "=&v"(f) : "v"(O_F2 + (o)), "s"(ws) : "memory")); }
#pragma unroll 1
    for (;;) {
    TS(20);
    if (wb == ws) {
      unsigned r_;
      POLL2(sv == 0u, 2, 1 << 26, asm volatile("ds_read_b32 %0, %2 offset:80\n\tds_read_b32 %1, %3\n\ts_waitcnt lgkmcnt(0)" : "=&v"(sv), "=&v"(r_) : "v"(sfa), "v"(rk2) : "memory"));
      rk2 = r_;
    }
    TS(21);
    nap_tick(th + T_C3 - 10);
    {
      u32x4 dn;
      POLL2((dn[0] & dn[1] & dn[2] & dn[3]) == 0u, 1, 1 << 26, asm volatile("ds_read_b128 %0, %1 offset:96\n\ts_waitcnt lgkmcnt(0)" : "=v"(dn) : "v"(sfa) : "memory"));
    }
    __builtin_amdgcn_s_setprio(H_P);
    wait_tick(th + T_C3);
    TS(36);
    u32x4 st, xv_;
    POLL2(max(max4(xv_), max4(st)) == SENT, 0, PCAP,
          asm volatile("global_load_dwordx4 %0, %2, %4 sc1\n\tglobal_load_dwordx4 %1, %3, %4 sc1" VW0 : "=&v"(st), "=&v"(xv_) : "v"(sto), "v"(xo_), "s"(ws) : "memory"));
    TS(22);
    SKEWPT();
#define SUM8(v) dpp_add<0x141>(dpp_add<0x4E>(dpp_add<0xB1>(v)))
    const f32x4 sf = __builtin_bit_cast(f32x4, st);
    const float mean = SUM8(sf[0]) * 0.125f, dm = sf[0] - mean;
    const float rstd = __builtin_amdgcn_rsqf(SUM8(sf[1] + 128.0f * (dm * dm)) * (1.0f / D) + 1e-5f);
    asm volatile("ds_write_b128 %0, %1\n\tds_write_b32 %2, %3 offset:64" ::"v"(hwa),
                 "v"(pk8(((lo4(xv_) - mean) * rstd) * cb2[0] + sb2[0], ((hi4(xv_) - mean) * rstd) * cb2[1] + sb2[1])), "v"(sfw), "v"(one) : "memory");
    TS(3);
    u32x4 ha[8], fg;
#define HSLD(W)                                                                                                                             \
  asm volatile(                                                                                                                             \
      "ds_read_b128 %8, %11 offset:64\n\tds_read_b128 %0, %9\n\tds_read_b128 %1, %10 offset:512\n\tds_read_b128 %2, %9 offset:1024\n\t"       \
      "ds_read_b128 %3, %10 offset:1536\n\tds_read_b128 %4, %9 offset:2048\n\tds_read_b128 %5, %10 offset:2560\n\t"                          \
      "ds_read_b128 %6, %9 offset:3072\n\tds_read_b128 %7, %10 offset:3584" W                                                               \
      : "=&v"(ha[0]), "=&v"(ha[1]), "=&v"(ha[2]), "=&v"(ha[3]), "=&v"(ha[4]), "=&v"(ha[5]), "=&v"(ha[6]), "=&v"(ha[7]), "=&v"(fg)           \
      : "v"(hra), "v"(hrb), "v"(sfa)                                                                                                        \
      : "memory")
    unsigned key = (mkkey(rstd * SUM8(sf[2] + dm * cj8) + sw) | rk2) & kmk;
    asm volatile("" : "+v"(key));
    HSLD("");
    unsigned t1 = key, t2 = 0u;
    TOP2(0x140);
    TOP2S(__builtin_amdgcn_permlane16_swap);
    TOP2S(__builtin_amdgcn_permlane32_swap);
    const int l1 = __builtin_ctzll(__builtin_amdgcn_ballot_w64(key == t1)), l2 = __builtin_ctzll(__builtin_amdgcn_ballot_w64(key == t2));
    unsigned mq[6];
    asm volatile("s_bfe_i32 %0, %6, 0x10003\n\ts_bfe_i32 %1, %7, 0x10003\n\ts_bfe_i32 %2, %6, 0x10004\n\ts_bfe_i32 %3, %7, 0x10004\n\t"
                 "s_bfe_i32 %4, %6, 0x10005\n\ts_bfe_i32 %5, %7, 0x10005\n\ts_xor_b32 %4, %4, 0xffff\n\ts_xor_b32 %5, %5, 0xffff"
                 : "=&s"(mq[0]), "=&s"(mq[1]), "=&s"(mq[2]), "=&s"(mq[3]), "=&s"(mq[4]), "=&s"(mq[5]) : "s"(l1), "s"(l2) : "scc");
    const u64 m0 = mk64(mq[1], mq[0]), m1 = mk64(mq[3], mq[2]), m2 = mk64(mq[5], mq[4]);
    asm volatile("s_waitcnt lgkmcnt(0)" : W8(ha), "+v"(fg)::"memory");
    TS(2);
    if (b < NPART) {
#define HBAD ((fg[0] & fg[1] & fg[2] & fg[3]) != one)
      if (__any(HBAD)) POLL2(HBAD, 0, 1 << 26, HSLD("\n\ts_waitcnt lgkmcnt(0)"));
      TS(23);
      f32x4 dg = {0.f, 0.f, 0.f, 0.f}, du = {0.f, 0.f, 0.f, 0.f};
#pragma unroll
      for (int f = 0; f < 8; ++f) {
        dg = __builtin_amdgcn_mfma_f32_16x16x32_bf16(BF8(ha[f]), BF8(gT[f]), dg, 0, 0, 0);
        du = __builtin_amdgcn_mfma_f32_16x16x32_bf16(BF8(ha[f]), BF8(uT[f]), du, 0, 0, 0);
      }
      const bool s0 = __builtin_amdgcn_inverse_ballot_w64(m0), s1 = __builtin_amdgcn_inverse_ballot_w64(m1);
      if (__builtin_amdgcn_inverse_ballot_w64(m2))
        asm volatile("global_store_dword %0, %1, %3 sc1 ; ALWAYS\n\tglobal_store_dword %0, %2, %3 offset:512 sc1" ::"v"(gso),
                     "v"(nsent(s1 ? (s0 ? dg[3] : dg[2]) : (s0 ? dg[1] : dg[0]))), "v"(nsent(s1 ? (s0 ? du[3] : du[2]) : (s0 ? du[1] : du[0]))), "s"(wb) : "memory");
    }
    if (hw == 0 && wb == ws) asm volatile("ds_write_b64 %0, %1" ::"v"(sfa), "v"(u32x2{t1, t2}) : "memory");
    if (b < NPART) {
      TS(24);
      asm volatile("" : W8(wdv)::"memory");
      TR8(dT, wdv);
      P2PUT(0);
      if (hw == 0) {
        u32x2 rr[8];
        if (E_D) __builtin_amdgcn_s_sleep(E_D);
        TS(39);
        P2GET(((((lane & 16) ? gl1 : gl0) - O_GU) >> 12) * 128 + (lane & 15) * 8);
#define MX2(i) max(max(rr[i][0], rr[i][1]), max(rr[i + 1][0], rr[i + 1][1]))
        POLL2(max(max(MX2(0), MX2(2)), max(MX2(4), MX2(6))) == SENT, 1, PCAP,
              TS(40);
              asm volatile("global_load_dwordx2 %0, %8, %10 sc1\n\tglobal_load_dwordx2 %1, %8, %10 offset:1024 sc1\n\t"
                           "global_load_dwordx2 %2, %8, %10 offset:2048 sc1\n\tglobal_load_dwordx2 %3, %8, %10 offset:3072 sc1\n\t"
                           "global_load_dwordx2 %4, %9, %10 sc1\n\tglobal_load_dwordx2 %5, %9, %10 offset:1024 sc1\n\t"
                           "global_load_dwordx2 %6, %9, %10 offset:2048 sc1\n\tglobal_load_dwordx2 %7, %9, %10 offset:3072 sc1" VW0
                           : "=&v"(rr[0]), "=&v"(rr[1]), "=&v"(rr[2]), "=&v"(rr[3]), "=&v"(rr[4]), "=&v"(rr[5]), "=&v"(rr[6]), "=&v"(rr[7])
                           : "v"(gl0), "v"(gl1), "s"(wb)
                           : "memory"));
        TS(8);
#define R2(i) __builtin_bit_cast(f32x2, rr[i])
        const f32x2 gs = (R2(0) + R2(1)) + (R2(2) + R2(3)), us = (R2(4) + R2(5)) + (R2(6) + R2(7));
        const unsigned gp = pack2(gs[0], gs[1]), up = pack2(us[0], us[1]);
        const unsigned sp = pack2(bflo(gp) * __builtin_amdgcn_rcpf(1.0f + __expf(-bflo(gp))), bfhi(gp) * __builtin_amdgcn_rcpf(1.0f + __expf(-bfhi(gp))));
        asm volatile("ds_write_b32 %0, %1\n\tds_write_b32 %2, %3 offset:24" ::"v"(awa), "v"(pack2(bflo(sp) * bflo(up), bfhi(sp) * bfhi(up))), "v"(sfa), "v"(one) : "memory");
      }
      TS(25);
      SKEWPT();
      POLL2(sv != one, 0, 1 << 26, asm volatile("ds_read_b32 %1, %3 offset:24\n\tds_read_b128 %0, %2\n\ts_waitcnt lgkmcnt(0)" : "=&v"(fg), "=&v"(sv) : "v"(ara), "v"(sfa) : "memory"));
      asm volatile("global_load_dwordx2 %0, %1, %2 sc1" : "=&v"(vd) : "v"(O_VD + (b & 31) * 128), "s"(ws) : "memory");
      const f32x2 dd = MV64(dT, fg);
      asm volatile("global_store_dwordx2 %0, %1, %2 sc1" ::"v"(pso), "v"(u32x2{nsent(dd[0]), nsent(dd[1])}), "s"(wb) : "memory");
      TS(6);
      P2PUT(1);
      asm volatile("s_waitcnt vmcnt(1)" : "+v"(vd)::"memory");
      if (__any(max(vd[0], vd[1]) == SENT)) VDLD(2);
      TS(5);
    } else if (hw == 3) {
      u32x2 g0, g1;
      nap_tick(th + T_C4);
      POLL2(min(min(g0[0], g0[1]), min(g1[0], g1[1])) == 0u, 2, PCAP,
            asm volatile("global_load_dwordx2 %0, %2, %4 sc1\n\tglobal_load_dwordx2 %1, %3, %4 sc1" VW0
                         : "=&v"(g0), "=&v"(g1) : "v"(O_KEY + lane * 8 * KSTR), "v"(O_KEY + (lane + 64) * 8 * KSTR), "s"(ws) : "memory"));
      const unsigned k1 = t1, k2 = t2;
      t1 = max(g0[0], g1[0]), t2 = max(min(g0[0], g1[0]), max(g0[1], g1[1]));
      TOP2(0xB1); TOP2(0x4E); TOP2(0x141); TOP2(0x140);
      TOP2S(__builtin_amdgcn_permlane16_swap);
      TOP2S(__builtin_amdgcn_permlane32_swap);
      vd = u32x2{max(t1, k1), max(min(t1, k1), max(t2, k2))};
      if (lane == 0) asm volatile("global_store_dwordx2 %0, %1, %2 sc1\n\tds_write_b64 %3, %1 offset:8" ::"v"(O_VD + (b & 31) * 128), "v"(vd), "s"(ws), "v"(sfa) : "memory");
      TS(29);
      break;
    } else if (hw < 2) {
      const int tok = b & 1, c0 = ((b - NPART) >> 1) * 64 + hw * 32 + (lane & 7) * 4, col = c0 + g4;
      const unsigned td = tick(), kk = tok ? t2 : t1;
      const int ln = tok ? l2 : l1;
      unsigned v;
      POLL2(v == SENT, 1, PCAP, asm volatile("global_load_dword %0, %1, %2 sc1" VW0 : "=&v"(v) : "v"(O_X1C + ((__builtin_amdgcn_readlane(sto, ln) - O_ST2) >> 7) * 2048 + (col >> 1) * 4), "s"(ws) : "memory"));
      float x1 = (col & 1) ? bfhi(v) : bflo(v);
      float h2 = rbf(((x1 - __int_as_float(__builtin_amdgcn_readlane(__float_as_int(mean), ln))) * __int_as_float(__builtin_amdgcn_readlane(__float_as_int(rstd), ln))) * bf2f(svb[2][col]) + bf2f(svb[1][col]));
      float gb = bf2f(svb[3][col]), rsg = rbf(1.0f / (1.0f + __expf(-key_val(kk))));
      unsigned oo_ = (key_row(kk) * D + col) * 2, po = O_PART + ((lane >> 3) * 2048 + tok * 1024 + c0) * 4, p1 = po + 4 * 65536;
      asm volatile("" : "+v"(x1), "+v"(h2), "+v"(gb), "+v"(rsg), "+v"(oo_), "+v"(po), "+v"(p1));
      u32x4 y[7];
      nap_tick(td + F_T - 10);
      wait_tick(td + F_T);
      TS(37);
      P2GET(8192 + (min(lane, 55) * 16 + ((b - NPART) >> 1)) * 8);
      POLL2(max(max(max(max4(y[0]), max4(y[1])), max(max4(y[2]), max4(y[3]))), max(max(max4(y[4]), max4(y[5])), max4(y[6]))) == SENT || vd[1] == 0u, 0, PCAP,
            TS(38);
            asm volatile("global_load_dwordx4 %0, %7, %9 sc1\n\tglobal_load_dwordx4 %1, %7, %10 sc1\n\tglobal_load_dwordx4 %2, %7, %11 sc1\n\t"
                         "global_load_dwordx4 %3, %7, %12 sc1\n\tglobal_load_dwordx4 %4, %8, %9 sc1\n\tglobal_load_dwordx4 %5, %8, %10 sc1\n\t"
                         "global_load_dwordx4 %6, %8, %11 sc1"
                         : "=&v"(y[0]), "=&v"(y[1]), "=&v"(y[2]), "=&v"(y[3]), "=&v"(y[4]), "=&v"(y[5]), "=&v"(y[6])
                         : "v"(po), "v"(p1), "s"(wb), "s"(wb + 65536), "s"(wb + 131072), "s"(wb + 196608)
                         : "memory");
            asm volatile("s_waitcnt vmcnt(2)\n\tds_read_b64 %7, %8 offset:8\n\ts_waitcnt vmcnt(0) lgkmcnt(0)"
                         : "+v"(y[0]), "+v"(y[1]), "+v"(y[2]), "+v"(y[3]), "+v"(y[4]), "+v"(y[5]), "+v"(y[6]), "=&v"(vd) : "v"(sfa) : "memory"));
      TS(28);
#define YF(i) __builtin_bit_cast(f32x4, y[i])
      const f32x4 t = ((YF(0) + YF(1)) + (YF(2) + YF(3))) + ((YF(4) + YF(5)) + YF(6));
      const float sum = dpp_add<0x128>(swap16_add(swap32_add(t[0], t[2]), swap32_add(t[1], t[3])));
      asm volatile("global_store_short %0, %1, %2" ::"v"(oo_), "v"(pack2(x1 + gb * (wb == ws && kk < vd[1] ? h2 : rbf(h2 + rbf(rbf(sum) * rsg))), 0.f)), "s"(out) : "memory");
      TS(15);
    } else {
      break;
    }
    if (__builtin_expect(wb != ws || __all(vd[1] == t2), 1)) break;
    const unsigned tk = (lane & 8) ? vd[1] : vd[0], ns = tk == t1 ? l1 >> 3 : 8 + ((lane >> 3) & 1);
    rk2 = tk & 0xffffu, kmk = lane < 16 ? ~0u : 0u, one = 3u;
    xo_ = O_X1C + ns * 2048 + kc * 2, sto = O_ST2 + ns * 128 + (lane & 7) * 16;
    wb = ws + O_P2;
    }
    TSE();
  }
}

extern "C" void kernel_launch(void* const* d_in, const int* in_sizes, int n_in, void* d_out, int out_size, void* d_ws, size_t ws_size,
                              hipStream_t stream) {
  const u16* x = (const u16*)d_in[0];
  const u16* wsr = (const u16*)d_in[1];
  const u16* wsi = (const u16*)d_in[2];
  const u16* whr = (const u16*)d_in[3];
  const u16* whi = (const u16*)d_in[4];
  const u16* w_r = (const u16*)d_in[6];
  const u16* w_gate = (const u16*)d_in[7];
  const u16* w_up = (const u16*)d_in[8];
  const u16* w_down = (const u16*)d_in[9];
  const u16* c = (const u16*)d_in[10];
  const u16* w_mod = (const u16*)d_in[11];
  u16* out = (u16*)d_out;
  k_main<<<256, 512, 0, stream>>>(x, (char*)d_ws, wsr, wsi, c, w_mod, whr, whi, w_r, w_gate, w_up, w_down, out);
}
```

```cpp
#include <hip/hip_runtime.h>
#include <hip/hip_bf16.h>
#include <stdint.h>
#include <utility>

#define NEWLAY 1
typedef unsigned short u16;
typedef unsigned long long u64;
using bf16x8 = __attribute__((ext_vector_type(8))) short;
using bf16v8 = __attribute__((ext_vector_type(8))) __bf16;
using f32x4 = __attribute__((ext_vector_type(4))) float;
using f32x2 = __attribute__((ext_vector_type(2))) float;
using u32x4 = __attribute__((ext_vector_type(4))) unsigned;
using u32x2 = __attribute__((ext_vector_type(2))) unsigned;

constexpr int S = 2048, D = 1024, F = 3584;

__device__ __forceinline__ float bf2f(unsigned v) { return __uint_as_float(v << 16); }
__device__ __forceinline__ float bflo(unsigned v) { return __uint_as_float(v << 16); }
__device__ __forceinline__ float bfhi(unsigned v) { return __uint_as_float(v & 0xffff0000u); }
__device__ __forceinline__ unsigned f2bf(float f) {
  __hip_bfloat16 b = __float2bfloat16(f);
  return (unsigned)*reinterpret_cast<u16*>(&b);
}
__device__ __forceinline__ float rbf(float f) { return bf2f(f2bf(f)); }
using bf16v2 = __attribute__((ext_vector_type(2))) __bf16;
__device__ __forceinline__ unsigned pack2(float lo, float hi) {
  return __builtin_bit_cast(unsigned, __builtin_convertvector(f32x2{lo, hi}, bf16v2));
}

template <int CTRL>
__device__ __forceinline__ float dpp_f(float v) {
  return __int_as_float(__builtin_amdgcn_mov_dpp(__float_as_int(v), CTRL, 0xf, 0xf, true));
}
template <int CTRL>
__device__ __forceinline__ float dpp_add(float v) { return v + dpp_f<CTRL>(v); }
__device__ __forceinline__ float row_sum(float v) { return dpp_add<0x140>(dpp_add<0x141>(dpp_add<0x4E>(dpp_add<0xB1>(v)))); }
template <int CTRL>
__device__ __forceinline__ unsigned dpp_u(unsigned v) { return (unsigned)__builtin_amdgcn_mov_dpp((int)v, CTRL, 0xf, 0xf, true); }
__device__ __forceinline__ u64 mk64(unsigned hi, unsigned lo) { return ((u64)hi << 32) | lo; }
__device__ __forceinline__ float swap32_add(float a, float b) {
  auto r = __builtin_amdgcn_permlane32_swap(__float_as_uint(a), __float_as_uint(b), false, false);
  return __uint_as_float(r[0]) + __uint_as_float(r[1]);
}
__device__ __forceinline__ float swap16_add(float a, float b) {
  auto r = __builtin_amdgcn_permlane16_swap(__float_as_uint(a), __float_as_uint(b), false, false);
  return __uint_as_float(r[0]) + __uint_as_float(r[1]);
}
__device__ __forceinline__ float ar4(float v) {
  const float t = swap32_add(v, v);
  return swap16_add(t, t);
}
__device__ __forceinline__ float wave_sum_s(float v) {
  v = row_sum(v);
  v += __int_as_float(__builtin_amdgcn_update_dpp(0, __float_as_int(v), 0x142, 0xa, 0xf, false));
  v += __int_as_float(__builtin_amdgcn_update_dpp(0, __float_as_int(v), 0x143, 0xc, 0xf, false));
  return __int_as_float(__builtin_amdgcn_readlane(__float_as_int(v), 63));
}
__device__ __forceinline__ float silu_bf(float c) { return rbf(c * __builtin_amdgcn_rcpf(1.0f + __expf(-c))); }
__device__ __forceinline__ float dsum(unsigned v, float a) {
  return __builtin_amdgcn_fdot2_f32_bf16(__builtin_bit_cast(bf16v2, v), __builtin_bit_cast(bf16v2, 0x3f803f80u), a, false);
}
template <class Fn, int... I>
__device__ __forceinline__ void sfor_(Fn&& f, std::integer_sequence<int, I...>) { (f(std::integral_constant<int, I>{}), ...); }
template <int N, class Fn>
__device__ __forceinline__ void sfor(Fn&& f) { sfor_(f, std::make_integer_sequence<int, N>{}); }
constexpr unsigned SENT = 0xFFFFFFFFu;
constexpr int NPART = 224;

constexpr size_t WS_EPO = 0;
constexpr size_t WS_XFL = 4096;
constexpr size_t WS_MODP = 40960;
constexpr size_t WS_KEYG = 320u << 10;
constexpr size_t WS_UFL = WS_KEYG + 16384;
constexpr size_t WS_MOD = WS_KEYG + (80u << 10);
constexpr size_t WS_RS = WS_KEYG + (92u << 10);
constexpr size_t WS_PART = WS_KEYG + (316u << 10);
constexpr unsigned O_MODP = WS_MODP, O_XFL = WS_XFL, O_KEY = WS_KEYG, O_MOD = WS_MOD, O_RS = WS_RS, O_M2 = WS_RS + 80 * 1024;
constexpr unsigned O_PART = WS_PART, O_GU = O_PART + 56 * 8192, O_STAT = O_GU + 224 * 1024, O_X1C = O_STAT + 262144;
constexpr unsigned O_ST2 = O_X1C + 20480, O_VD = O_ST2 + 2048, O_P2 = 1536u << 10, O_F2 = 3584u << 10;
constexpr unsigned O_GC = O_M2 + 8704, O_VP = O_M2 + 12288, O_NP = O_VP + 65536, O_K2 = O_NP + 65536, O_G2 = 3u << 20, O_G1 = O_G2 + 262144;
static_assert(O_K2 + 4096 == O_PART && O_VD + 4096 <= O_KEY + (512 + 768) * 1024 && O_PART + O_P2 >= O_VD + 4096 && O_STAT + O_P2 <= O_G2, "armed area");
constexpr size_t WS_H = 4u << 20;
constexpr size_t WS_T = WS_H + (size_t)S * D * 2;
constexpr size_t WS_P = WS_T + (size_t)2 * D * S * 2;

#define TSG(base, i)
#define TSD(i)
#define PCAP (1 << 20)
#define PDBG(it)
#define TS(i)
#define TSE()
#define BT(k)
#define BTE()
#define BAR(k) do { BT(k); BARRIER(); } while (0)
#define WAIT_V(n) asm volatile("s_waitcnt vmcnt(%0)" ::"n"(n) : "memory")
#define WAIT_L(n) asm volatile("s_waitcnt lgkmcnt(%0)" ::"n"(n) : "memory")
#define BARRIER() asm volatile("s_barrier" ::: "memory")
#define LBAR() asm volatile("s_waitcnt lgkmcnt(0)\n\ts_barrier" ::: "memory")
#define DSR(dst, addr, off) asm volatile("ds_read_b128 %0, %1 offset:%2" : "=v"(dst) : "v"(addr), "n"(off) : "memory")
#define MFMA(c, a, b) asm volatile("v_mfma_f32_16x16x32_bf16 %0, %1, %2, %0" : "+v"(c) : "v"(a), "v"(b))
#define MFMA0(c, a, b) asm volatile("v_mfma_f32_16x16x32_bf16 %0, %1, %2, 0" : "=&v"(c) : "v"(a), "v"(b))
#define GLDS(m0v, voff, gbase) \
  asm volatile("s_mov_b32 m0, %0\n\ts_nop 0\n\tglobal_load_lds_dwordx4 %1, %2" ::"s"(m0v), "v"(voff), "s"(gbase) : "memory")
#define GLDS4(m0v, v, gbase)                                                                                                        \
  asm volatile("s_mov_b32 m0, %0\n\ts_nop 0\n\tglobal_load_lds_dwordx4 %1, %5\n\tglobal_load_lds_dwordx4 %2, %5 offset:1024\n\t"    \
               "global_load_lds_dwordx4 %3, %5 offset:2048\n\tglobal_load_lds_dwordx4 %4, %5 offset:3072" ::"s"(m0v),               \
               "v"((v)[0]), "v"((v)[1]), "v"((v)[2]), "v"((v)[3]), "s"(gbase) : "memory")
#define LDG(dst, voff, base) asm volatile("global_load_dwordx4 %0, %1, %2" : "=v"(dst) : "v"(voff), "s"(base) : "memory")
#define WPOL " nt"
#define LDM(dst, voff, base) asm volatile("global_load_dwordx4 %0, %1, %2" WPOL : "=v"(dst) : "v"(voff), "s"(base) : "memory")
#define LDX(dst, voff, base, off) asm volatile("global_load_dwordx4 %0, %1, %2 offset:%3" : "=v"(dst) : "v"(voff), "s"(base), "n"(off) : "memory")
#define LDXS(dst, voff, base, off) asm volatile("global_load_dwordx4 %0, %1, %2 offset:%3 sc1" : "=v"(dst) : "v"(voff), "s"(base), "n"(off) : "memory")
#define BF8(v) __builtin_bit_cast(bf16v8, v)
#define LSPIN(v, p, cond)                                   \
  _Pragma("unroll 1") for (int it_ = 0; it_ < (1 << 26); ++it_) { \
    v = __atomic_load_n(p, __ATOMIC_RELAXED);               \
    if (cond) break;                                        \
    __builtin_amdgcn_s_sleep(1);                            \
  }
#define POLL(ok, sl, reload)                                                  \
  if (__builtin_expect(!(ok), 0)) {                                           \
    _Pragma("unroll 1") for (int it = 0; it < PCAP; ++it) {                   \
      PDBG(it);                                                               \
      __builtin_amdgcn_s_sleep(sl);                                           \
      reload;                                                                 \
      if (ok) break;                                                          \
    }                                                                         \
  }
#define POLL2(bad, sl, cap, load)                                             \
  {                                                                           \
    int it = (cap);                                                           \
    u64 live;                                                                 \
    _Pragma("unroll 1") do {                                                  \
      if (sl) __builtin_amdgcn_s_sleep(sl);                                   \
      asm volatile("s_sub_u32 %0, %0, 1\n\ts_cselect_b64 %1, 0, -1" : "+s"(it), "=s"(live)::"scc"); \
      load;                                                                   \
    } while (__builtin_amdgcn_ballot_w64(bad) & live);                        \
    PDBG(PCAP - 2 - it);                                                      \
  }
__device__ __forceinline__ unsigned tick() {
  u64 t;
  asm volatile("s_memrealtime %0\n\ts_waitcnt lgkmcnt(0)" : "=s"(t)::"memory");
  return (unsigned)t;
}
__device__ __forceinline__ void wait_tick(unsigned t) {
#pragma unroll 1
  for (int i = 0; i < 256; ++i)
    if ((int)(tick() - t) >= 0) break;
}
__device__ __forceinline__ void nap_tick(unsigned t) {
#pragma unroll 1
  for (int i = 0; i < 1024; ++i) {
    if ((int)(tick() - t) >= 0) break;
    __builtin_amdgcn_s_sleep(2);
  }
}
#define T_C1 245
#define T_A 44
#define T_N 2500
#define T_D1 78
#define VW0 "\n\ts_waitcnt vmcnt(0)"
#define W8(a) "+v"(a[0]), "+v"(a[1]), "+v"(a[2]), "+v"(a[3]), "+v"(a[4]), "+v"(a[5]), "+v"(a[6]), "+v"(a[7])
#define PFL(ptr) asm volatile("s_mov_b32 m0, %0\n\ts_nop 0\n\tglobal_load_lds_dword %1, off" ::"s"(pfl), "v"(ptr) : "memory")

#define CPFL() asm volatile("s_mov_b32 m0, %0\n\ts_mov_b64 exec, 0xfff\n\tglobal_load_lds_dword %1, off\n\ts_mov_b64 exec, -1" ::"s"(pfl), "v"(cpf) : "memory")
#define HHOOK(j)
#define XPV 1557
#define XPW 1557
#define XPM (MODE ? XPW : XPV)
#define XPA0 (XPM / 1000)
#define XPA1 (XPM / 100 % 10)
#define XPB0 (XPM / 10 % 10)
#define XPB1 (XPM % 10)
#define U_X0 6
#define XLD(dst, XP, XH, i)                                                                                   \
  do {                                                                                                        \
    if (MODE) LDXS(dst, (i) ? xo1 : xo0, XP, ((XH)&3) * 2048 - 4096);                                         \
    else LDXS(dst, ((XH)&4) ? xo1 : xo0, XP, ((XH)&3) * 2048 + (i)*1024 - 4096);                              \
  } while (0)
#define HALF(XC, YA, RD, XN, XP, XH, LD, YP)                                                 \
  do {                                                                                       \
    _Pragma("unroll") for (int j = 0; j < 8; ++j) {                                          \
      if ((j & 1) == 0) { if (RD) WAIT_L(5); else WAIT_L(j == 0 ? 5 : 6 - j); }              \
      if (MODE == 2) MFMA0(acc[0][j], Y[j], XC[0]); else if (MODE) MFMA(acc[0][j], Y[j], XC[0]); else MFMA(acc[0][j], XC[0], Y[j]); \
      if (j == 0) DSR(Y[7], YP, 7 * 2048);                                                   \
      else if (RD) DSR(Y[j - 1], YA, (j - 1) * 2048);                                        \
      if (MODE == 2) MFMA0(acc[1][j], Y[j], XC[1]); else if (MODE) MFMA(acc[1][j], Y[j], XC[1]); else MFMA(acc[1][j], XC[1], Y[j]); \
      if ((LD) && j == (((XH)&1) ? XPA0 : XPB0)) XLD(XN[0], XP, XH, 0);                                             \
      if ((LD) && j == (((XH)&1) ? XPA1 : XPB1)) XLD(XN[1], XP, XH, 1);                                             \
      HHOOK(j);                                                                              \
    }                                                                                        \
  } while (0)

__device__ __forceinline__ int key_row(unsigned k) { return (int)((~k >> 1) & 2047u); }
__device__ __forceinline__ unsigned mkkey(float v) {
  const unsigned rb = pack2(0.f, v);
  return rb ^ ((unsigned)((int)rb >> 31) & 0x7fff0000u) ^ 0x80000000u;
}
__device__ __forceinline__ float key_val(unsigned k) {
  const unsigned ord = k >> 16;
  return bf2f(ord ^ ((ord & 0x8000u) ? 0x8000u : 0xffffu));
}
__device__ __forceinline__ unsigned nsent(float v) { return min(__float_as_uint(v), SENT - 1u); }
__device__ __forceinline__ f32x4 lo4(u32x4 q) { return f32x4{bflo(q[0]), bfhi(q[0]), bflo(q[1]), bfhi(q[1])}; }
__device__ __forceinline__ f32x4 hi4(u32x4 q) { return f32x4{bflo(q[2]), bfhi(q[2]), bflo(q[3]), bfhi(q[3])}; }
__device__ __forceinline__ u32x4 pk8(f32x4 a, f32x4 b) { return u32x4{pack2(a[0], a[1]), pack2(a[2], a[3]), pack2(b[0], b[1]), pack2(b[2], b[3])}; }
__device__ __forceinline__ u32x4 nsent4(u32x4 v) { return u32x4{min(v[0], SENT - 1u), min(v[1], SENT - 1u), min(v[2], SENT - 1u), min(v[3], SENT - 1u)}; }
__device__ __forceinline__ unsigned max4(u32x4 v) { return max(max(v[0], v[1]), max(v[2], v[3])); }
__device__ __forceinline__ float amin16(const u32x4* v) {
  float a, b;
  asm("v_min3_f32 %0, |%2|, |%3|, |%4|\n\tv_min3_f32 %1, |%5|, |%6|, |%7|\n\tv_min3_f32 %0, |%8|, |%9|, %0\n\tv_min3_f32 %1, |%10|, |%11|, %1\n\t"
      "v_min3_f32 %0, |%12|, |%13|, %0\n\tv_min3_f32 %1, |%14|, |%15|, %1\n\tv_min3_f32 %0, |%16|, |%17|, %0\n\tv_min_f32 %0, %0, %1"
      : "=&v"(a), "=&v"(b)
      : "v"(v[0][0]), "v"(v[0][1]), "v"(v[0][2]), "v"(v[0][3]), "v"(v[1][0]), "v"(v[1][1]), "v"(v[1][2]), "v"(v[1][3]), "v"(v[2][0]), "v"(v[2][1]),
        "v"(v[2][2]), "v"(v[2][3]), "v"(v[3][0]), "v"(v[3][1]), "v"(v[3][2]), "v"(v[3][3]));
  return a;
}
__device__ __forceinline__ u32x4 selu(unsigned mk, u32x4 a, u32x4 b) { return (a & mk) | (b & ~mk); }
#define KSTR 16
#define TOP2(CTRL)                                                  \
  do {                                                              \
    const unsigned o1 = dpp_u<CTRL>(t1), o2 = dpp_u<CTRL>(t2);      \
    t2 = max(min(t1, o1), max(t2, o2));                             \
    t1 = max(t1, o1);                                               \
  } while (0)
#define TOP2S(SW)                                                   \
  do {                                                              \
    auto a_ = SW(t1, t1, false, false), b_ = SW(t2, t2, false, false); \
    t2 = max(min(a_[0], a_[1]), max(b_[0], b_[1]));                 \
    t1 = max(a_[0], a_[1]);                                         \
  } while (0)
#define T_C3 253
#define T_C4 340
#define E_D 6
#define O_DW 6
#define O_DS 4
#define OPOL " sc1"
#define X_PA 2
#define G1_C 2
#define R_AT 24
#define B_TK 7
#define W_T 4
#define V_K 3
#define V_T 5
#define N_K 9
#define N_D 2
#define T_K 0
#define H_AT 23
#define G1_S 1
#define F_T 142
#define K_S 1
#define N_C (V_T + 1)
#define T_O 262
#define A_S1 9
#define A_S2 9
#define A_P2 12
#define X_K 13
#define H_P 2
#define Q_S 1
#define Q_S2 2
#define T_X 80
#define A_PS 3
#define U_S 4
#define C_LD 2
#define C_DP 8
#define F_D 2
#define F_AT 29
#define PW0 2
#define T_W 70
#define P_D2 1
#define P_D4 1
#define XSTR 16
#define P_D3 20
#ifndef M_NS
#define M_NS 6
#endif
#define A_Q1 27
#define B_RAT 6
#define B_MAT 13
#define U_D 4
#define FSTR 16
#define B_J 4
#define RETIRE()                                                                                                              \
  do {                                                                                                                        \
    if (hw == 3 && lane < (b == 255 ? 2 : 1))                                                                                 \
      asm volatile("global_store_dwordx2 %0, %1, %2 sc1" ::"v"(lane ? (unsigned)WS_EPO : O_XFL + b * XSTR * 4), "v"(lane ? tag : ~tag), "s"(ws) : "memory"); \
  } while (0)
#define U_N1 3
#define B_QAT 7
#define B_QD 3
#define TP_PS 5
#define TP_XC 34
#define TP_P0 2
#define TP_X0 24
#define TP_XL 54
#define W_UO 3
#define CPF_K 24
#define CPF_B 8
#define B_WN 4
#define B_WK 15
struct BSched {
  int cnt[16], rcnt, mcnt, qcnt, vcnt, ncnt;
  constexpr BSched() : cnt{}, rcnt(0), mcnt(0), qcnt(0), vcnt(0), ncnt(0) {
    int pos[16] = {}, n = 0, rpos = 0, mpos = 0, qpos = 0, npos = 0;
    for (int k = 0; k <= B_J - 2 + M_NS; ++k) pos[k] = -8 * (B_J - 2 + M_NS - k);
    for (int k = B_J + 1; k < 16; ++k) {
      cnt[k] = n - pos[k] > 63 ? 63 : n - pos[k];
      if (k - 2 + M_NS < 16) { n += 4; pos[k - 2 + M_NS] = n; }
      if (k == B_RAT + 2) rcnt = n - rpos;
      if (k == B_QAT + B_QD) qcnt = n - qpos;
      if (k == V_T) vcnt = n;
      if (k == N_K + N_D) ncnt = n - npos;
      if (k == N_K) npos = n;
      if (k == B_RAT) rpos = n;
      if (k == X_K) n += 4;
      if (k == B_QAT) { n += 3; qpos = n; }
      if (k == B_MAT) { n += 3; mpos = n; }
      if (k == B_WK) n += B_WN;
    }
    mcnt = n - mpos > 63 ? 63 : n - mpos;
  }
};
__global__ __launch_bounds__(512, 2) void k_main(const u16* __restrict__ x, char* ws, const u16* __restrict__ Y0, const u16* __restrict__ Y1,
                                              const u16* __restrict__ cvec, const u16* __restrict__ wmod, const u16* __restrict__ X0,
                                              const u16* __restrict__ X1, const u16* __restrict__ wr_, const u16* __restrict__ wg,
                                              const u16* __restrict__ wu, const u16* __restrict__ wd, u16* __restrict__ out) {
  constexpr int DX = 4, NS = M_NS, STG = NS * 16384, SPARE = STG + 32768;
  constexpr int LST = SPARE + 17664, SLT = LST + 256, XC = SLT + 512, VT = XC + 4096;
  __shared__ __attribute__((aligned(1024))) char shm[VT + 256];
  u16(*svb)[1024] = reinterpret_cast<u16(*)[1024]>(shm + SPARE);
  float* cwj = reinterpret_cast<float*>(shm + SPARE + 10240);
  unsigned* ssel = reinterpret_cast<unsigned*>(shm + SPARE + 10368);
  float* smod = reinterpret_cast<float*>(shm + SPARE + 15872);
  u16* ssa = reinterpret_cast<u16*>(smod);
  constexpr int TAB = SPARE + 10752, RKA = TAB + 1536, RKB = TAB + 3584;
  u16(*scw)[128] = reinterpret_cast<u16(*)[128]>(shm + TAB + 512);
  const int b = blockIdx.x, xcd = b & 7, j8 = b >> 3;
  const int cc = xcd >> 2, nt = (xcd & 3) * 4 + (j8 & 3), mt = j8 >> 2, et = mt;
  const int tid = threadIdx.x, lane = tid & 63;
  const int wid = __builtin_amdgcn_readfirstlane(tid >> 6);
  const unsigned lds0 = (unsigned)(size_t)(__attribute__((address_space(3))) char*)shm;
  const char* cpf;
  {
    unsigned long long pc;
    unsigned len;
    asm volatile("s_getpc_b64 %0\n.Lcpf_here:\n\ts_mov_b32 %1, .Lcpf_end-.Lcpf_here-4\n\t.subsection 1\n.Lcpf_end:\n\t.subsection 0" : "=s"(pc), "=s"(len));
    cpf = reinterpret_cast<const char*>(pc) + min((unsigned)((lane * 32 + j8) * 128), len);
  }
  const unsigned pfl = lds0 + SPARE + 17408;
  if (wid == 4) CPFL();
  u32x4 xv[2];
  {
    const unsigned xo = (unsigned)((b * 8 + wid) * D + lane * 8) * 2;
    asm volatile("global_load_dwordx4 %0, %2, %3\n\tglobal_load_dwordx4 %1, %2, %3 offset:1024" : "=&v"(xv[0]), "=&v"(xv[1]) : "v"(xo), "s"(x) : "memory");
  }
  asm volatile("" ::"s"(ws), "s"(Y0), "s"(Y1), "s"(cvec), "s"(wmod), "s"(wr_));
  u16* xn = reinterpret_cast<u16*>(ws + WS_H);
  u16* U = reinterpret_cast<u16*>(ws + WS_T);
  unsigned* part = reinterpret_cast<unsigned*>(ws + WS_PART);
  const int g = (b & 7) * 28 + (b >> 3);
  unsigned* ufl = reinterpret_cast<unsigned*>(ws + WS_UFL) + (cc * 16 + nt) * 32 * FSTR;
  unsigned* epo = reinterpret_cast<unsigned*>(ws + WS_EPO);
#define SKEWPT()
  const int g4 = lane >> 4;
  u64 tag0, tag;
#define TAGW(n) asm volatile("s_waitcnt vmcnt(%2)\n\tv_lshl_add_u64 %0, %1, 0, 1" : "=v"(tag) : "v"(tag0), "n"(n) : "memory")
  u32x4 wv[8], cq;
  const char* pfy = reinterpret_cast<const char*>(cc ? Y1 : Y0) + (size_t)((xcd & 3) * 512 + j8 * 16 + (lane & 15)) * 4096 + g4 * 128;
#define PFG(g) (pfy + ((((g) + xcd) & 7) << 9))
  const int u = b * 2 + (wid & 1), cg0 = u & 31, ks0 = u >> 5;
  unsigned ta = lds0 + STG + ((lane >> 2) & 3) * 2368 + (wid * 128 + (lane >> 5) * 32 + (lane & 3) * 8 + (g4 & 1) * 4) * 2;
  unsigned oo = (unsigned)((wid * 4 + (lane >> 5)) * 131072 + (b >> 2) * 2048 + (g4 & 1) * 1024 + ((b & 3) * 16 + (lane & 15)) * 16), oo2 = oo + 262144;
  asm volatile("" : "+v"(ta), "+v"(oo), "+v"(oo2));
  unsigned wa = lds0 + STG + wid * 2368 + lane * 16;
  asm volatile("" : "+v"(wa));
  const unsigned wo = (unsigned)((ks0 * 64 + (lane >> 3) * 8) * 6144 + 2048 + (cg0 >> 4) * 1024 + cg0 * 64 + (lane & 7) * 8) * 2;
  if (wid == 0) TSG(part, 0);
  BT(50);
  if (wid >= 3) {
    asm volatile("global_load_dwordx2 %0, %1, off sc1" : "=v"(tag0) : "v"(epo) : "memory");
    if (wid >= 6) {
      LDG(cq, (ks0 * 64 + g4 * 16 + (lane & 1) * 8) * 2, cvec);
#pragma unroll
      for (int i = 0; i < 8; ++i) LDM(wv[i], wo + i * 12288, wmod);
      const unsigned av = u < 80 ? 0u : ~0u;
      asm volatile("global_store_dwordx4 %0, %1, %2 sc1\n\ts_nop 1" ::"v"(O_KEY + u * 1024 + lane * 16), "v"(u32x4{av, av, av, av}), "s"(ws) : "memory");
      WAIT_V(11);
    } else if (wid == 4) {
      __builtin_amdgcn_s_sleep(C_DP);
      PFL(PFG(0));
      WAIT_V(2);
    } else {
      WAIT_V(1);
    }
  } else {
    WAIT_V(0);
  }
  {
    asm volatile("" : "+v"(xv[0]), "+v"(xv[1])::"memory");
    if (wid == 1) TSG(part, 2);
    BT(51);
    const float s0 = dsum(xv[0][3], dsum(xv[0][2], dsum(xv[0][1], dsum(xv[0][0], 0.f))));
    const float s1 = dsum(xv[1][3], dsum(xv[1][2], dsum(xv[1][1], dsum(xv[1][0], 0.f))));
    const float mf = wave_sum_s(s0 + s1) * (1.0f / D), m = rbf(mf);
    const f32x4 d0 = lo4(xv[0]) - m, d1 = hi4(xv[0]) - m, d2 = lo4(xv[1]) - m, d3 = hi4(xv[1]) - m;
    const f32x4 q = (d0 * d0 + d1 * d1) + (d2 * d2 + d3 * d3);
    const float vf = wave_sum_s((q[0] + q[1]) + (q[2] + q[3])) * (1.0f / D) - (mf - m) * (mf - m);
    const float rden = __builtin_amdgcn_rcpf(rbf(__builtin_amdgcn_sqrtf(rbf(rbf(vf) + 1e-5f))));
    asm volatile("ds_write_b128 %0, %1" ::"v"(wa), "v"(pk8(d0 * rden, d1 * rden)) : "memory");
    asm volatile("ds_write_b128 %0, %1 offset:1024" ::"v"(wa), "v"(pk8(d2 * rden, d3 * rden)) : "memory");
    if (wid == 1) TSG(part, 12);
    BT(52);
    LBAR();
    if (wid == 1) TSG(part, 10);
    BT(53);
    u32x2 t[4];
    asm volatile(
        "ds_read_b64_tr_b16 %0, %4\n\tds_read_b64_tr_b16 %1, %4 offset:%5\n\t"
        "ds_read_b64_tr_b16 %2, %4 offset:128\n\tds_read_b64_tr_b16 %3, %4 offset:%6\n\ts_waitcnt lgkmcnt(0)"
        : "=&v"(t[0]), "=&v"(t[1]), "=&v"(t[2]), "=&v"(t[3])
        : "v"(ta), "n"(4 * 2368), "n"(4 * 2368 + 128)
        : "memory");
    asm volatile("global_store_dwordx4 %0, %2, %4 sc1\n\tglobal_store_dwordx4 %1, %3, %4 sc1\n\ts_nop 1" ::"v"(oo), "v"(oo2),
                 "v"(u32x4{t[0][0], t[0][1], t[1][0], t[1][1]}), "v"(u32x4{t[2][0], t[2][1], t[3][0], t[3][1]}), "s"(xn)
                 : "memory");
    if (wid == 2) TSG(part, 14);
    BT(54);
  }
  u32x2 vd;
#define VDLD(sl) POLL2(max(vd[0], vd[1]) == SENT, sl, PCAP, asm volatile("global_load_dwordx2 %0, %1, %2 sc1" VW0 : "=&v"(vd) : "v"(O_VD + (b & 31) * 128), "s"(ws) : "memory"))
  if (wid < 4) {
    const unsigned ya0 = lds0 + (lane & 15) * 128 + (g4 ^ (lane & 7)) * 16;
    bf16x8 X[DX][2][2], Y[8];
    f32x4 acc[2][8];
    {
      constexpr int MODE = 0;
      const unsigned xo0 = lane * 16 + 4096, xo1 = xo0 + 8192;
      const char* xb = reinterpret_cast<const char*>(xn) + (size_t)(mt * 4 + wid) * 131072;
#define XB(i) (xb + (((xcd + (i)) & 7) << 14))
#pragma unroll
      for (int i = 0; i < 2; ++i)
#pragma unroll
        for (int j = 0; j < 8; ++j) acc[i][j] = f32x4{0.f, 0.f, 0.f, 0.f};
      asm volatile("" : W8(acc[0]), W8(acc[1]));
      __builtin_amdgcn_s_setprio(1);
      WAIT_V(0);
      if (wid == 0) TSG(part, 1);
      const char* xpb = XB(0);
      BAR(55);
      if (wid == 3) {
        TAGW(0);
        const unsigned fp = O_XFL + ((xcd * 32 + lane) & 255) * XSTR * 4;
        if (P_D3) __builtin_amdgcn_s_sleep(P_D3);
#pragma unroll 1
        for (int it = 0; it < PCAP; ++it) { PDBG(it);
          u64 f;
          if (it < 2) BT(58 + 2 * it);
          asm volatile("global_load_dwordx2 %0, %1, %2 sc1\n\ts_waitcnt vmcnt(0)" : "=&v"(f) : "v"(fp), "s"(ws) : "memory");
          if (it < 2) BT(59 + 2 * it);
          if (__all(f == tag)) break;
          __builtin_amdgcn_s_sleep(P_D4);
        }
      }
      BAR(0);
      if (wid == 1) TSG(part, 3);
#pragma unroll
      for (int h = 0; h < 7; ++h) {
        XLD(X[h >> 1][h & 1][0], xpb, h, 0);
        XLD(X[h >> 1][h & 1][1], xpb, h, 1);
      }
#pragma unroll
      for (int j = 0; j < 7; ++j) DSR(Y[j], ya0, j * 2048);
      unsigned so = 0, a0 = ya0;
      f32x4 c0, c1;
#pragma unroll 1
      for (int it = 0; it < 7; ++it) {
        const char* xpa = xpb;
        xpb = XB(it + 1);
#pragma unroll
        for (int s = 0; s < 4; ++s) {
          const unsigned a1 = (ya0 ^ 64) + so;
          so = so == (NS - 1) * 16384 ? 0 : so + 16384;
          WAIT_V(12);
          HALF(X[s][0], a1, true, X[(s + 3) & 3][1], (s == 0 ? xpa : xpb), (2 * s + 7) & 7, true, a0);
          a0 = ya0 + so;
          BAR(1 + it * 4 + s);
          WAIT_V(12);
          HALF(X[s][1], a0, true, X[s][0], xpb, 2 * s, true, a1);
        }
      }
#pragma unroll
      for (int s = 0; s < 4; ++s) {
        const unsigned a1 = (ya0 ^ 64) + so;
        so = so == (NS - 1) * 16384 ? 0 : so + 16384;
        WAIT_V(s == 0 ? 12 : 14 - 4 * s);
        HALF(X[s][0], a1, true, X[3][1], xpb, 7, s == 0, a0);
        a0 = ya0 + so;
        if (s < 3) BAR(29 + s);
        WAIT_V(12 - 4 * s);
        if (s == 2) {
          DSR(c0, lds0 + SPARE + 15872 + 512 + wid * 128 + g4 * 32, 0);
          DSR(c1, lds0 + SPARE + 15872 + 512 + wid * 128 + g4 * 32, 16);
        }
        if (s < 3) HALF(X[s][1], a0, true, X[s][0], xpb, 0, false, a1);
      }
      const char* up = reinterpret_cast<const char*>(U) + (size_t)cc * 4194304 + (size_t)(nt * 4) * 65536 + (mt * 4 + wid) * 2048;
      const unsigned ul = lds0 + STG + wid * 1024 + lane * 16;
#define UEP(j)                                                                                                                             \
  do {                                                                                                                                     \
    u32x4 v_;                                                                                                                              \
    asm volatile("v_pk_mul_f32 %0, %0, %4\n\tv_pk_mul_f32 %1, %1, %5\n\tv_pk_mul_f32 %2, %2, %6\n\tv_pk_mul_f32 %3, %3, %7"            \
                 : "+v"(acc[0][j].lo), "+v"(acc[0][j].hi), "+v"(acc[1][j].lo), "+v"(acc[1][j].hi)                                          \
                 : "v"(c0.lo), "v"(c0.hi), "v"(c1.lo), "v"(c1.hi));                                                                        \
    asm volatile("v_cvt_pk_bf16_f32 %0, %4, %5\n\tv_cvt_pk_bf16_f32 %1, %6, %7\n\tv_cvt_pk_bf16_f32 %2, %8, %9\n\tv_cvt_pk_bf16_f32 %3, %10, %11" \
                 : "=&v"(v_[0]), "=&v"(v_[1]), "=&v"(v_[2]), "=&v"(v_[3])                                                                  \
                 : "v"(acc[0][j][0]), "v"(acc[0][j][1]), "v"(acc[0][j][2]), "v"(acc[0][j][3]), "v"(acc[1][j][0]), "v"(acc[1][j][1]),       \
                   "v"(acc[1][j][2]), "v"(acc[1][j][3]));                                                                                  \
    asm volatile("global_store_dwordx4 %0, %2, %3 offset:%4 sc1\n\tds_write_b128 %1, %2 offset:%5\n\ts_nop 0"                          \
                 ::"v"(lane * 16), "v"(ul), "v"(v_), "s"(up + ((j) >> 1) * 65536), "n"(((j)&1) * 1024), "n"((j)*4096) : "memory");         \
  } while (0)
      {
        const unsigned a1 = (ya0 ^ 64) + (31 % NS) * 16384;
        DSR(Y[7], a1, 7 * 2048);
        WAIT_L(6);
#pragma unroll
        for (int j = 0; j < 8; ++j) {
          if (j == 2) WAIT_L(0);
          MFMA(acc[0][j], X[3][1][0], Y[j]);
          MFMA(acc[1][j], X[3][1][1], Y[j]);
          if (j >= 2) UEP(j - 2);
        }
      }
      if (wid == 0) TSG(part, 4);
      TS(0);
      SKEWPT();
      asm volatile("s_nop 3");
#pragma unroll
      for (int j = 6; j < 8; ++j) UEP(j);
    }
    const int rg = nt * 8 + wid * 2 + cc, row0 = rg * 16 + (lane & 15), col0 = et * 128 + g4 * 8;
    u32x4 xq[4], pv[4], sa4[4];
    f32x4 ga[8];
    f32x2 cj;
    float sw, ce;
    u32x4 dk;
    unsigned spo, soo, th, slt, slf, sla, tha, xca, sca;
    u32x4 e0, e1;
    const char* xr = reinterpret_cast<const char*>(xn) + mt * 524288 + (cc * 16 + nt) * 16384 + wid * 4096;
#define XCHLD()                                                                                                     \
  asm volatile(                                                                                                     \
      "global_load_dwordx4 %0, %4, %5 sc1\n\tglobal_load_dwordx4 %1, %4, %5 offset:1024 sc1\n\t"                   \
      "global_load_dwordx4 %2, %4, %5 offset:2048 sc1\n\tglobal_load_dwordx4 %3, %4, %5 offset:3072 sc1"            \
      : "=&v"(pv[0]), "=&v"(pv[1]), "=&v"(pv[2]), "=&v"(pv[3])                                                      \
      : "v"(lane * 16), "s"(xr)                                                                                     \
      : "memory")
    {
      constexpr int MODE = 1;
      const char* ub = reinterpret_cast<const char*>(U) + (size_t)cc * 4194304 + (size_t)(nt * 4 + wid) * 65536;
      const unsigned xo0 = lane * 16 + cc * 1024 + 4096, xo1 = xo0 ^ 1024;
      BT(32);
      LBAR();
      if (wid == 1) TSG(part, 6);
      {
        const unsigned sa = lds0 + STG + wid * 8192 + lane * 16 + cc * 4096;
#pragma unroll
        for (int q = 0; q < 8; ++q) DSR(X[(q >> 1) & 1][q & 1][q >> 2], (q >> 2) ? sa ^ 4096 : sa, (q & 3) * 1024);
        const unsigned a0 = ya0 + (32 % NS) * 16384;
#pragma unroll
        for (int j = 0; j < 7; ++j) DSR(Y[j], a0, j * 2048);
      }
#define UFLAG()                                                                                                         \
  do {                                                                                                                  \
    WAIT_V(0);                                                                                                          \
    if (lane == 0) __hip_atomic_store(ufl + (mt * 4 + wid) * FSTR, 1u, __ATOMIC_RELAXED, __HIP_MEMORY_SCOPE_AGENT);     \
    if (wid == 0) TSG(part, 5);                                                                                         \
  } while (0)
#define XP(h) (ub + (((mt + ((h) >> 2)) & 7) << 13))
#define XS(h) X[((h) >> 1) & 3][(h)&1]
#define XW(h) do { if ((h) >= 4) WAIT_V(2 * (((h) + 6 > 31 ? 31 : (h) + 6) - (h)) ); } while (0)
      constexpr int H0 = 24, NP = 64, NE = 2 * NP + 8;
      const char* xw;
      unsigned c128, xoff[4], rka, rkb, ea, eb, cwa, cwu;
      auto ESET = [&]() __attribute__((always_inline)) {
        int ln_ = lane;
        asm volatile("" : "+v"(ln_));
        const int lane = ln_, g4 = lane >> 4, row0 = rg * 16 + (lane & 15), col0 = et * 128 + g4 * 8;
        xw = reinterpret_cast<const char*>(xn) + mt * 524288 + ((cc ^ 1) * 16 + nt) * 16384 + wid * 4096;
        asm volatile("" : "+s"(xw), "+s"(xr));
        c128 = 0x43000000u;
        rka = lds0 + RKA + (lane & 15) * 16, rkb = lds0 + RKB + (g4 ? 1024 : wid * 256 + lane * 16);
        ea = lds0 + TAB + g4 * 32, eb = lds0 + TAB + 512 + (lane & 3) * 256 + g4 * 16;
#pragma unroll
        for (int q = 0; q < 4; ++q) {
          dk[q] = (lane & 3) == q ? ~0u : 0u;
          xoff[q] = (lds0 + STG + wid * 8192 + (lane & 15) * 256 + ((g4 ^ lane) & 15) * 16) ^ (q * 64);
        }
        spo = O_STAT + rg * 2048 + g4 * 512 + (lane & 15) * 16, soo = O_STAT + rg * 2048 + et * 256 + (lane & 15) * 16;
        cwa = lds0 + SPARE + 10240 + g4 * 8, cwu = lds0 + SPARE + 10240 + et * 4;
        const unsigned one = g4 == ((lane >> 2) & 3) ? ((lane & 1) ? 0x1f800000u : 0x3f80u) : 0u;
        e0 = u32x4{(lane & 2) ? 0u : one, (lane & 2) ? one : 0u, 0u, 0u}, e1 = u32x4{0u, 0u, e0[0], e0[1]};
        tha = lds0 + SPARE + 10368 + 24;
        sla = lds0 + SLT + (row0 & 127) * 4;
        xca = O_X1C - 2048 + col0 * 2, sca = O_ST2 - 128 + et * 16;
        asm volatile("" : "+v"(dk), "+v"(spo), "+v"(soo), "+v"(cwa), "+v"(cwu), "+v"(tha), "+v"(sla), "+v"(xca), "+v"(sca), "+v"(e0), "+v"(e1), "+v"(c128));
        asm volatile("" : "+v"(xoff[0]), "+v"(xoff[1]), "+v"(xoff[2]), "+v"(xoff[3]), "+v"(rka), "+v"(rkb), "+v"(ea), "+v"(eb));
      };
#pragma unroll
      for (int s = 0; s < H0 / 2; ++s) {
        const unsigned a0 = ya0 + ((32 + s) % NS) * 16384, a1 = a0 ^ 64, an = ya0 + ((33 + s) % NS) * 16384;
        XW(2 * s);
        if (s == 0) {
          constexpr int MODE = 2;
          HALF(XS(0), a1, true, XS(7), XP(7), 7, false, a0);
          UFLAG();
        } else {
          HALF(XS(2 * s), a1, true, XS(2 * s + 7), XP(2 * s + 7), 2 * s + 7, 2 * s >= 4, a0);
        }
        if (s == 1) ESET();
        BAR(33 + s);
        if (s == 1) {
          if (wid == 1) TSG(part, 7);
          TS(4);
#pragma unroll
          for (int h = 4; h < U_X0; ++h) {
            XLD(XS(h)[0], XP(h), h, 0);
            XLD(XS(h)[1], XP(h), h, 1);
          }
        }
        XW(2 * s + 1);
#undef HHOOK
#define HHOOK(j) if (s == 1 && j + 3 >= U_X0) { XLD(XS(j + 3)[0], XP(j + 3), j + 3, 0); XLD(XS(j + 3)[1], XP(j + 3), j + 3, 1); }
        HALF(XS(2 * s + 1), an, true, XS(2 * s + 8), XP(2 * s + 8), 2 * s + 8, 2 * s + 1 >= 5, a1);
#undef HHOOK
#define HHOOK(j)
      }
      u32x4 ov;
      bf16x8 xk;
#define EH(e) (H0 + (((e) < NP ? (e) : (e) - NP - 8) >> 3))
#define EA(e) ((ya0 ^ ((EH(e) & 1) * 64)) + ((32 + (EH(e) >> 1)) % NS) * 16384)
      sfor<NE>([&](auto nn) __attribute__((always_inline)) {
        constexpr int n = decltype(nn)::value, h = EH(n), j = n & 7, m = n - NP - TP_P0, e = n + 7, z = n - (NE - 8);
        (void)sa4, (void)eb, (void)ga, (void)ea, (void)xk, (void)rka, (void)rkb, (void)xq, (void)xoff, (void)ov, (void)xw, (void)x;
        (void)cj, (void)sw, (void)ce, (void)cwa, (void)cwu, (void)th, (void)tha, (void)slt, (void)sla, (void)slf, (void)c128;
        if (n == 0) TS(2);
        if (n == NP) { TS(3); SKEWPT(); }
        if (n < NP && j == 0) {
          if ((h & 1) && h < 31) BAR(33 + (h >> 1));
          XW(h);
        }
        if (n == NP + TP_X0) { TS(14); BAR(48); TS(11); }
        if (n == NP + TP_XL) XCHLD();
        if constexpr ((j & 1) == 0) WAIT_L(z <= 0 ? 5 : 6 - z + 2 * z);
        if constexpr (n < NP) MFMA(acc[1][j], Y[j], XS(h)[1]);
        else if constexpr (n < NP + 8) MFMA(acc[0][j], Y[j], xk);
        else MFMA(acc[0][j], Y[j], XS(h)[0]);
        if constexpr (n == NP - 16) DSR(xk, rkb, 0);
        if constexpr (e >= NP && e < NP + 8) DSR(Y[e & 7], rka, (e & 7) * 256);
        else if constexpr (e < NE) DSR(Y[e & 7], EA(e), (e & 7) * 2048);
        if constexpr (n == 2 || n == 4) XLD(XS(31)[(j >> 1) - 1], XP(31), 31, (j >> 1) - 1);
        if constexpr (m >= TP_XC - 11 && m < TP_XC - 7) DSR(xq[m - TP_XC + 11], xoff[m - TP_XC + 11], 4096);
        if constexpr (m >= 0 && m < 4 * TP_PS) {
          constexpr int q = m / TP_PS, r = m % TP_PS;
#pragma unroll
          for (int u = (r * 13 + TP_PS - 1) / TP_PS; u < ((r + 1) * 13 + TP_PS - 1) / TP_PS; ++u) {
#define AO(i) acc[1][2 * q + ((u >> 1) & 1)][(u & 1) * 2 + (i)]
            if (u < 4) asm volatile("v_and_or_b32 %0, %1, %2, %3" : "=v"(ov[u & 3]) : "v"(AO(1)), "s"(0x80000000u), "v"(c128));
            else if (u < 8) asm volatile("v_fmamk_f32 %0, %1, 0x5f800000, %0" : "+v"(ov[u & 3]) : "v"(AO(1)));
            else if (u < 12) asm volatile("v_cvt_pk_bf16_f32 %0, %1, %0" : "+v"(ov[u & 3]) : "v"(AO(0)));
            else asm volatile("global_store_dwordx4 %0, %1, %2 offset:%3 sc1\n\ts_nop 1" ::"v"(lane * 16), "v"(ov), "s"(xw), "n"(q * 1024) : "memory");
          }
          if (m == 4 * TP_PS - 1) TS(10);
        }
        if constexpr (m == TP_XC) asm volatile("" : "+v"(xq[0]), "+v"(xq[1]), "+v"(xq[2]), "+v"(xq[3]));
        if constexpr (m >= TP_XC && m < TP_XC + 32) {
          constexpr int c = m - TP_XC;
          if constexpr (c & 1) asm volatile("v_and_b32 %0, 0xffff0000, %1" : "=v"(acc[1][c >> 2][c & 3]) : "v"(xq[c >> 3][(c >> 1) & 3]));
          else asm volatile("v_lshlrev_b32 %0, 16, %1" : "=v"(acc[1][c >> 2][c & 3]) : "v"(xq[c >> 3][(c >> 1) & 3]));
        }
        if constexpr (z >= 0 && z < 4) {
          DSR(ga[2 * z], ea, z * 128);
          DSR(ga[2 * z + 1], ea, z * 128 + 16);
        } else if constexpr (z >= 4 && z < 6) {
          DSR(sa4[2 * z - 8], eb, (2 * z - 8) * 64);
          DSR(sa4[2 * z - 7], eb, (2 * z - 7) * 64);
        } else if constexpr (z == 6) {
          asm volatile("ds_read_b64 %0, %2\n\tds_read_b32 %1, %3" : "=&v"(cj), "=&v"(ce) : "v"(cwa), "v"(cwu) : "memory");
        } else if constexpr (z == 7) {
          asm volatile("ds_read_b32 %0, %4 offset:32\n\tds_read_b32 %1, %5 offset:4\n\tds_read_b32 %3, %5 offset:56\n\tds_read_b32 %2, %6"
                       : "=&v"(sw), "=&v"(th), "=&v"(slt), "=&v"(slf) : "v"(cwa), "v"(tha), "v"(sla) : "memory");
        }
      });
    }
    if (wid == 0) TSG(part, 8);
    TS(13);
    BT(49);
    BTE();
    SKEWPT();
    {
      TS(26);
#define XCHW() asm volatile("s_waitcnt vmcnt(0)" : "+v"(pv[0]), "+v"(pv[1]), "+v"(pv[2]), "+v"(pv[3])::"memory")
      XCHW();
#define XBAD (amin16(pv) < 128.0f)
      if (__builtin_expect(__any(XBAD), 1)) POLL2(XBAD, 1, PCAP, XCHLD(); XCHW());
      TS(27);
      asm volatile("s_waitcnt lgkmcnt(0)" : W8(acc[0]), W8(ga), "+v"(sa4[0]), "+v"(sa4[1]), "+v"(sa4[2]), "+v"(sa4[3]), "+v"(cj), "+v"(sw), "+v"(ce), "+v"(th), "+v"(slt), "+v"(slf)::"memory");
#pragma unroll
      for (int q = 0; q < 8; ++q) acc[0][q] = __builtin_amdgcn_mfma_f32_16x16x32_bf16(BF8((q & 1) ? e1 : e0), BF8(pv[q >> 1]), acc[0][q], 0, 0, 0);
      f32x4 s4 = {0.f, 0.f, 0.f, 0.f}, gr = {0.f, 0.f, 0.f, 0.f};
      const unsigned xa1 = xca + slt * 2048, sa1 = sca + slt * 128;
#pragma unroll
      for (int q = 0; q < 4; ++q) {
        xq[q] = pk8(acc[1][2 * q] + ga[2 * q] * acc[0][2 * q], acc[1][2 * q + 1] + ga[2 * q + 1] * acc[0][2 * q + 1]);
        gr = __builtin_amdgcn_mfma_f32_16x16x32_bf16(BF8(xq[q]), BF8(xq[q]), gr, 0, 0, 0);
        s4 = __builtin_amdgcn_mfma_f32_16x16x32_bf16(BF8(sa4[q]), BF8(xq[q]), s4, 0, 0, 0);
        xq[q] = nsent4(xq[q]);
      }
#define XCST(a)                                                                                                              \
  asm volatile(                                                                                                              \
      "global_store_dwordx4 %0, %1, %5 sc1\n\tglobal_store_dwordx4 %0, %2, %5 offset:64 sc1\n\t"                            \
      "global_store_dwordx4 %0, %3, %5 offset:128 sc1\n\tglobal_store_dwordx4 %0, %4, %5 offset:192 sc1\n\ts_nop 1" ::"v"(a), \
      "v"(xq[0]), "v"(xq[1]), "v"(xq[2]), "v"(xq[3]), "s"(ws)                                                                \
      : "memory")
#define SCST(a) asm volatile("global_store_dwordx4 %0, %1, %2 sc1\n\ts_nop 1" ::"v"(a), "v"(sg), "s"(ws) : "memory")
      if (slt) XCST(xa1);
      const bool dl = ((lane >> 2) & 3) == g4;
      const u32x4 gd = __builtin_bit_cast(u32x4, gr) & dk;
      const float m = s4[2] * (1.0f / 128);
      float q2 = __uint_as_float((gd[0] | gd[1]) | (gd[2] | gd[3])) - s4[2] * m, lg = (s4[0] + s4[1]) - m * ce;
      if (__builtin_expect(__any(dl && !(s4[2] * m <= 256.0f * q2)), 0)) {
        asm volatile("" : "+v"(xq[0]), "+v"(xq[1]), "+v"(xq[2]), "+v"(xq[3]));
        f32x4 a = {0.f, 0.f, 0.f, 0.f}, l = {0.f, 0.f, 0.f, 0.f};
#pragma unroll
        for (int q = 0; q < 4; ++q) {
          const u32x4 ch = *reinterpret_cast<const u32x4*>(&scw[0][g4 * 8 + q * 32]), cl = *reinterpret_cast<const u32x4*>(&scw[1][g4 * 8 + q * 32]);
          const f32x4 d0 = lo4(xq[q]) - m, d1 = hi4(xq[q]) - m;
          a += d0 * d0 + d1 * d1;
          l += d0 * (lo4(ch) + lo4(cl)) + d1 * (hi4(ch) + hi4(cl));
        }
        q2 = ar4((a[0] + a[1]) + (a[2] + a[3]));
        lg = ar4((l[0] + l[1]) + (l[2] + l[3]));
      }
      const u32x4 sg = {nsent(m), nsent(q2), nsent(lg), 0u};
      if (dl) {
        if (slt) SCST(sa1);
        asm volatile("global_store_dwordx4 %0, %1, %2 sc1\n\ts_nop 1" ::"v"(soo), "v"(sg), "s"(ws) : "memory");
      }
      if (__any(slf == 0u)) {
        POLL2(slf == 0u, 1, 1 << 26, asm volatile("ds_read_b32 %1, %2 offset:56\n\tds_read_b32 %0, %3\n\ts_waitcnt lgkmcnt(0)" : "=&v"(slt), "=&v"(slf) : "v"(tha), "v"(sla) : "memory"));
        if (slt) {
          XCST(xca + slt * 2048);
          if (dl) SCST(sca + slt * 128);
        }
      }
      asm volatile("ds_write_b32 %0, %1 offset:72" ::"v"(tha + wid * 4), "v"(1u) : "memory");
      TS(7);
      SKEWPT();
      const unsigned tp = tick() + T_D1, tq = (unsigned)__builtin_amdgcn_readfirstlane((int)th) + T_C1;
      u32x4 sr[2];
#define STLD(W) asm volatile("global_load_dwordx4 %0, %2, %3 sc1\n\tglobal_load_dwordx4 %1, %2, %3 offset:256 sc1" W : "=&v"(sr[0]), "=&v"(sr[1]) : "v"(spo), "s"(ws) : "memory")
#define SBAD (max(max(max(sr[0][0], sr[0][1]), sr[0][2]), max(max(sr[1][0], sr[1][1]), sr[1][2])) == SENT)
      asm volatile("" : "+v"(xq[0]), "+v"(xq[1]), "+v"(xq[2]), "+v"(xq[3])::"memory");
      f32x4 xf[8], xs[8], gc[8];
      auto PREW = [&](int q0, int q1) __attribute__((always_inline)) {
#pragma unroll
        for (int q = q0; q < q1; ++q) {
          const u32x4 sbq = *reinterpret_cast<const u32x4*>(&svb[1][col0 + q * 32]), gbq = *reinterpret_cast<const u32x4*>(&svb[3][col0 + q * 32]);
          const u32x4 cq = *reinterpret_cast<const u32x4*>(&svb[2][col0 + q * 32]);
          xf[2 * q] = lo4(xq[q]), xf[2 * q + 1] = hi4(xq[q]);
          xs[2 * q] = xf[2 * q] + lo4(gbq) * lo4(sbq), xs[2 * q + 1] = xf[2 * q + 1] + hi4(gbq) * hi4(sbq);
          gc[2 * q] = lo4(gbq) * lo4(cq), gc[2 * q + 1] = hi4(gbq) * hi4(cq);
          asm volatile("" : "+v"(xf[2 * q]), "+v"(xf[2 * q + 1]), "+v"(xs[2 * q]), "+v"(xs[2 * q + 1]), "+v"(gc[2 * q]), "+v"(gc[2 * q + 1]));
        }
      };
      PREW(0, PW0);
      wait_tick((int)(tp - tq) < 0 ? tp : tq);
      STLD("");
      unsigned rk = 0x8ffeu - (unsigned)row0 * 2;
      unsigned twa = lds0 + STG + wid * 8192 + (lane & 15) * 256 + ((g4 ^ lane) & 15) * 16, tra = lds0 + STG + wid * 8192 + g4 * 256 + ((g4 ^ lane) & 15) * 16;
      unsigned oo_ = (unsigned)((rg * 16 + g4) * D + et * 128 + (lane & 15) * 8) * 2;
      asm volatile("" : "+v"(rk), "+v"(twa), "+v"(tra), "+v"(oo_));
      PREW(PW0, 4);
      asm volatile("s_waitcnt vmcnt(0)" : "+v"(sr[0]), "+v"(sr[1])::"memory");
      if (__builtin_expect(__any(SBAD), 1)) POLL2(SBAD, 1, PCAP, STLD(VW0));
      TS(9);
      const f32x2 mk = {__uint_as_float(sr[0][0]), __uint_as_float(sr[1][0])};
      const f32x2 qk = {__uint_as_float(sr[0][1]), __uint_as_float(sr[1][1])};
      const f32x2 lk = {__uint_as_float(sr[0][2]), __uint_as_float(sr[1][2])};
      const float mean = ar4(mk[0] + mk[1]) * (1.0f / 8);
      const f32x2 dm = mk - mean;
      const f32x2 a = qk + 128.0f * (dm * dm), l = lk + dm * cj;
      const float al = swap32_add(a[0] + a[1], l[0] + l[1]), am = swap16_add(al, al);
      auto r = __builtin_amdgcn_permlane32_swap(__float_as_uint(am), __float_as_uint(am), false, false);
      const float rstd = __builtin_amdgcn_rsqf(__uint_as_float(r[0]) * (1.0f / D) + 1e-5f);
      const unsigned key = mkkey(rstd * __uint_as_float(r[1]) + sw) | rk;
      unsigned t1 = slt ? 0u : key, t2 = 0u;
      TOP2(0xB1); TOP2(0x4E); TOP2(0x141); TOP2(0x140);
      if (lane == 0 && et == (rg & 7)) asm volatile("global_store_dwordx2 %0, %1, %2 sc1" ::"v"(O_KEY + rg * KSTR * 8), "v"(u32x2{t1, t2}), "s"(ws) : "memory");
      u32x4 o[4];
#pragma unroll
      for (int q = 0; q < 4; ++q) o[q] = pk8(xs[2 * q] + (gc[2 * q] * (xf[2 * q] - mean)) * rstd, xs[2 * q + 1] + (gc[2 * q + 1] * (xf[2 * q + 1] - mean)) * rstd);
      {
        asm volatile("ds_write_b128 %0, %4\n\tds_write_b128 %1, %5\n\tds_write_b128 %2, %6\n\tds_write_b128 %3, %7" ::"v"(twa), "v"(twa ^ 64), "v"(twa ^ 128), "v"(twa ^ 192),
                     "v"(o[0]), "v"(o[1]), "v"(o[2]), "v"(o[3]) : "memory");
        asm volatile("ds_read_b128 %0, %4\n\tds_read_b128 %1, %5 offset:1024\n\tds_read_b128 %2, %6 offset:2048\n\tds_read_b128 %3, %7 offset:3072\n\ts_waitcnt lgkmcnt(0)"
                     : "=&v"(o[0]), "=&v"(o[1]), "=&v"(o[2]), "=&v"(o[3]) : "v"(tra), "v"(tra ^ 64), "v"(tra ^ 128), "v"(tra ^ 192) : "memory");
      }
      auto OST = [&](bool on) __attribute__((always_inline)) {
        const unsigned mk_ = (unsigned)__builtin_amdgcn_ballot_w64(on) >> g4;
#pragma unroll
        for (int q = 0; q < 4; ++q)
          if (mk_ & (1u << (4 * q))) asm volatile("global_store_dwordx4 %0, %1, %2" OPOL "\n\ts_nop 1" ::"v"(oo_), "v"(o[q]), "s"(out + q * 4096) : "memory");
      };
      bool df = slt;
      {
        const int tn = (int)(tick() - (tq + (T_C3 - T_C1)));
        if (tn > T_X) df = true;
        else if (tn > -T_W) { unsigned sv; LSPIN(sv, ssel + 16 + wid, sv != 0u); }
      }
      OST(!df);
      TS(1);
      u32x2 ks;
      POLL2(ks[1] == 0u, 1, 1 << 26, asm volatile("ds_read_b64 %0, %1\n\ts_waitcnt lgkmcnt(0)" : "=v"(ks) : "v"(lds0 + SPARE + 10368) : "memory"));
      df = df && (ks[0] & 0xffffu) != rk && (ks[1] & 0xffffu) != rk;
      SKEWPT();
      if (__any(df)) {
        if (b < NPART) {
          unsigned sv;
          LSPIN(sv, ssel + O_DW, sv != 0u);
          if (O_DS) __builtin_amdgcn_s_sleep(O_DS);
        }
        OST(df);
      }
      if (__builtin_expect(__any(t1 > ks[1]), 0)) {
        VDLD(8);
        slt = slt ? 0u : key == vd[0] ? 9u : key == vd[1] ? 10u : 0u;
        if (slt) {
          XCST(xca + slt * 2048);
          if (dl) SCST(sca + slt * 128);
        }
      }
    }
    TS(16);
    TSE();
  } else {
    const int hw = wid - 4;
    unsigned ts0 = tick();
    asm volatile("" : "+v"(ts0));
    const char* Hc = reinterpret_cast<const char*>((cc ? X1 : X0) + (size_t)et * 128 * D);
    auto UPOLL = [&](int n) {
      const unsigned fo = (unsigned)WS_UFL + ((cc * 16 + nt) * 32 + ((mt + 1 + (lane >> 2)) & 7) * 4 + (lane & 3)) * FSTR * 4;
      unsigned f;
      POLL2(f == 0u && lane < 4 * n, U_S, PCAP, asm volatile("global_load_dword %0, %1, %2 sc1" VW0 : "=&v"(f) : "v"(fo), "s"(ws) : "memory"));
      return (__builtin_amdgcn_ballot_w64(f == 0u) & 0xFFFFFFFull) == 0;
    };
    bool urdy = true;
    u32x4 gT[8], uT[8], wdv[8];
    unsigned og = (unsigned)(((g & 3) * 256 + hw * 64 + (lane >> 3)) * F + (g >> 2) * 64 + ((lane & 7) ^ ((lane >> 3) & 2) ^ 4) * 8) * 2;
    unsigned wta = lds0 + STG + (g4 * 8 + ((lane >> 2) & 3)) * 128 + (((hw * 2 + ((lane >> 1) & 1)) ^ ((lane >> 2) & 2) ^ ((g4 & 1) * 4)) << 4) + (lane & 1) * 8;
    unsigned od = (unsigned)(((g >> 2) * 64 + (lane >> 3) * 8) * D + (g & 3) * 256 + hw * 64 + (lane & 7) * 8) * 2;
    unsigned ou = og + (unsigned)(((mt + W_UO) % 7 - mt) * 128);
    asm volatile("" : "+v"(og), "+v"(ou), "+v"(od), "+v"(wta));
#define LDWI(dst, voff, base, inc) asm volatile("global_load_dwordx4 %0, %1, %2" WPOL "\n\tv_add_u32 %1, %3, %1" : "=v"(dst), "+v"(voff) : "s"(base), "n"(inc) : "memory")
#define WST(o, w)                                                                                                          \
  if (b < NPART) {                                                                                                         \
    asm volatile("s_mov_b32 m0, %0" ::"s"(lds0 + STG + hw * 8192));                                                        \
    _Pragma("unroll") for (int i = 0; i < 8; ++i) {                                                                        \
      asm volatile("v_xor_b32 %0, 64, %0\n\tglobal_load_lds_dwordx4 %0, %1 offset:%2" WPOL "\n\tv_add_u32 %0, %3, %0"       \
                   : "+v"(o) : "s"(w), "n"((i & 3) * 1024), "n"(F * 16 + (i == 3 ? 3072 : -1024)) : "memory");             \
      if (i == 3) asm volatile("s_add_u32 m0, m0, 4096" ::: "scc");                                                        \
    }                                                                                                                      \
  }
#define WTR2(i) "ds_read_b64_tr_b16 %" #i ", %16 offset:(" #i "/2)*4096+(" #i "&1)*512\n\t"
#define WTK(T)                                                                                                                         \
  if (b < NPART) {                                                                                                                     \
    u32x2 t_[16];                                                                                                                      \
    asm volatile(WTR2(0) WTR2(1) WTR2(2) WTR2(3) WTR2(4) WTR2(5) WTR2(6) WTR2(7) WTR2(8) WTR2(9) WTR2(10) WTR2(11) WTR2(12) WTR2(13)   \
                     WTR2(14) WTR2(15) "s_waitcnt lgkmcnt(0)"                                                                          \
                 : "=&v"(t_[0]), "=&v"(t_[1]), "=&v"(t_[2]), "=&v"(t_[3]), "=&v"(t_[4]), "=&v"(t_[5]), "=&v"(t_[6]), "=&v"(t_[7]),     \
                   "=&v"(t_[8]), "=&v"(t_[9]), "=&v"(t_[10]), "=&v"(t_[11]), "=&v"(t_[12]), "=&v"(t_[13]), "=&v"(t_[14]), "=&v"(t_[15]) \
                 : "v"(wta) : "memory");                                                                                               \
    _Pragma("unroll") for (int f = 0; f < 8; ++f) T[f] = u32x4{t_[2 * f][0], t_[2 * f][1], t_[2 * f + 1][0], t_[2 * f + 1][1]};        \
  }
#define DLD(q0, q1) if (b < NPART) { _Pragma("unroll") for (int q = q0; q < q1; ++q) LDWI(wdv[q], od, wd, D * 2); }
    const int pf0 = A_PS + (((j8 & 7) * A_S1) >> 3), pf1 = A_P2 + (((j8 & 7) * A_S2) >> 3), q1 = A_Q1;
    static_assert(A_PS >= 1 && A_P2 + A_S2 * 7 / 8 + 3 <= R_AT && R_AT < A_Q1 && A_P2 + A_S2 * 7 / 8 + 1 + W_T <= A_Q1 && H_AT + 3 <= A_Q1 && G1_C + G1_S < W_T && A_PS + W_T < A_P2 && A_PS + A_S1 * 7 / 8 + W_T + 1 < A_P2 + A_S2 * 7 / 8 && W_T >= M_NS - 2 && A_Q1 < F_AT && F_AT <= 29 && V_K <= B_J && V_T > B_J && B_WK == 15 && X_K >= N_C && (X_K + M_NS - 1 < 16 || (X_K <= B_MAT && TP_XC - 11 >= TP_X0 - TP_P0)) && TP_XC >= 4 * TP_PS && TP_XC + TP_P0 + 32 <= 72 && N_K > N_C && N_C > V_T && B_RAT > B_J && B_QAT > B_J && B_QAT + B_QD < B_MAT && B_QAT + B_QD + Q_S < 15 && B_QAT + B_QD + Q_S2 < 15, "barrier counts");
#define HOFF(r) (unsigned)((((r) >> 5) * 32 + (((r) >> 2) & 3) * 8 + (((r) >> 4) & 1) * 4 + ((r)&3)) * 2048 + ((lane & 7) ^ (lane >> 3)) * 16)
#define TR8(T, W)                                                                                                    \
  _Pragma("unroll") for (int c = 0; c < 8; ++c) {                                                                    \
    _Pragma("unroll") for (int p = 0; p < 4; ++p)                                                                    \
        T[c][p] = __builtin_amdgcn_perm(W[2 * p + 1][c >> 1], W[2 * p][c >> 1], (c & 1) ? 0x07060302u : 0x05040100u); \
    asm volatile("" : "+v"(T[c]));                                                                                   \
  }
    u32x4 qm;
#pragma unroll
    for (int i = 0; i < 4; ++i) qm[i] = ((lane >> 2) & 3) == i ? ~0u : 0u;
    auto MVR = [&](f32x4 d) __attribute__((always_inline)) { return ((lane & 8) ? d[3] : d[0]) + dpp_f<0x128>((lane & 8) ? d[1] : d[2]); };
    auto MV64 = [&](const u32x4* T, u32x4 a) __attribute__((always_inline)) {
      f32x4 d[2] = {};
#pragma unroll
      for (int c = 0; c < 8; ++c) d[c & 1] = __builtin_amdgcn_mfma_f32_16x16x32_bf16(BF8(a & qm[c >> 1]), BF8(T[c]), d[c & 1], 0, 0, 0);
      return f32x2{MVR(d[0]), MVR(d[1])};
    };
#define LD1(i, a) "global_load_dwordx4 %" #i ", %" #a ", %10 sc1\n\tv_add_u32 %" #a ", 0x4000, %" #a "\n\t"
#define LD8(r, o0, o1, W)                                                                                                          \
  do {                                                                                                                             \
    unsigned a0_ = (o0), a1_ = (o1);                                                                                               \
    asm volatile(LD1(0, 8) LD1(4, 9) LD1(1, 8) LD1(5, 9) LD1(2, 8) LD1(6, 9) LD1(3, 8) LD1(7, 9) "s_nop 0" W                        \
                 : "=&v"(r[0]), "=&v"(r[1]), "=&v"(r[2]), "=&v"(r[3]), "=&v"(r[4]), "=&v"(r[5]), "=&v"(r[6]), "=&v"(r[7]), "+v"(a0_), "+v"(a1_) \
                 : "s"(ws) : "memory");                                                                                            \
  } while (0)
    u32x4 sel, gp[4];
    unsigned vc;
#define VCLD(W) asm volatile("global_load_dword %0, %1, %2 sc1" W : "=&v"(vc) : "v"(O_VP + ((cc ^ 1) * 8 + mt) * 4096 + (lane & 15) * 256), "s"(ws) : "memory")
#define GP4(o, W)                                                                                                         \
  asm volatile(                                                                                                           \
      "global_load_dwordx4 %0, %4, %5 sc1\n\tglobal_load_dwordx4 %1, %4, %5 offset:1024 sc1\n\t"                         \
      "global_load_dwordx4 %2, %4, %5 offset:2048 sc1\n\tglobal_load_dwordx4 %3, %4, %5 offset:3072 sc1" W                \
      : "=&v"(gp[0]), "=&v"(gp[1]), "=&v"(gp[2]), "=&v"(gp[3])                                                            \
      : "v"(o), "s"(ws)                                                                                                   \
      : "memory")
#define GPBAD (max(max(max4(gp[0]), max4(gp[1])), max(max4(gp[2]), max4(gp[3]))) == SENT)
      auto GPSUM = [&]() __attribute__((always_inline)) {
        f32x4 d = {0.f, 0.f, 0.f, 0.f};
#pragma unroll
        for (int i = 0; i < 4; ++i) d = __builtin_amdgcn_mfma_f32_16x16x32_bf16(BF8(sel), BF8(gp[i]), d, 0, 0, 0);
        return d;
      };
    const bool rdc = b >= NPART && hw == 3, rnp = b >= NPART && hw == 2 && cc == 0;
    const int gbq = (b >> 4) & 1, cj = (b & 15) * 64 + (lane & 31) * 2;
    unsigned voff[4], ro, xo, ra1, wqa, qo, so_, qa, qb, mo, mo1, mo2, ma2;
    auto JSET = [&]() {
#pragma unroll
      for (int i = 0; i < 4; ++i) {
        voff[i] = HOFF((hw * 4 + i) * 8 + (lane >> 3)) - i * 1024;
        sel[i] = ((lane & 15) >> 1) == i ? ((lane & 1) ? 0x3f800000u : 0x3f80u) : 0u;
      }
      ro = O_G2 + ((lane >> 5) * 16384 + gbq * 1024 + cj) * 8;
      xo = (unsigned)((nt * 128 + hw * 32 + cc * 16 + g4) * D + et * 128 + ((g4 ^ lane) & 15) * 8) * 2;
      ra1 = lane < 32 ? O_MOD + 2048 + gbq * 4096 + cj * 2 : O_M2 + 8192 + 64 - gbq * 64 + (b & 15) * 4;
      wqa = lds0 + SPARE + 8192 + cj * 2;
      mo = hw ? O_MOD + hw * 2048 + lane * 16 : O_MOD + 4096, mo1 = hw ? mo + 1024 : mo;
      qo = O_RS + (4096 + ((hw & 1) * 8 + (lane >> 4) * 2 + 1) * D + et * 128 + (hw >> 1) * 64 + (lane & 15) * 4) * 4;
      so_ = O_RS + ((hw & 1) * S + nt * 128 + (lane >> 4) * 32 + cc * 16 + (lane & 15)) * 4;
      qa = lds0 + RKA + ((hw >> 1) * 64 + ((lane >> 3) & 1) * 32 + (lane & 1) * 16 + ((lane >> 1) & 3) * 4 + (lane >> 4)) * 16 + (hw & 1) * 8;
      qb = lds0 + RKB + lane * 16 + (hw & 1) * 8;
      mo2 = O_M2 + (hw == 2 ? 8448u + (lane & 15) * 16 : hw ? 8192u + (lane & 7) * 16 : lane < 32 ? et * 512 + lane * 16 : 4096u + (lane & 16) * 128 + et * 256 + (lane & 15) * 16);
      ma2 = lds0 + (hw ? SPARE + 10240 + (lane & 7) * 8 : TAB + lane * 16);
      asm volatile("" : "+v"(voff[0]), "+v"(voff[1]), "+v"(voff[2]), "+v"(voff[3]), "+v"(ro), "+v"(xo), "+v"(ra1), "+v"(wqa), "+v"(qo), "+v"(so_), "+v"(qa), "+v"(qb), "+v"(mo), "+v"(mo1), "+v"(mo2), "+v"(ma2), "+v"(sel));
      *reinterpret_cast<unsigned*>((hw & 1) ? shm + RKB + 1024 + lane * 4 : reinterpret_cast<char*>(scw[2]) + lane * 4) = (hw & 1) ? 0u : 0x3f803f80u;
      if (hw == 3) {
        if (lane < 32) ssel[lane] = 0u;
        *reinterpret_cast<u32x2*>(shm + SLT + lane * 8) = u32x2{0u, 0u};
      }
    };
    if (hw < 2) {
      constexpr int P = 8;
      __builtin_amdgcn_s_setprio(2);
      unsigned voy[P];
#pragma unroll
      for (int i = 0; i < P; ++i) voy[i] = (unsigned)(((hw * 8 + i) * 8 + (lane >> 3)) * 4096 + ((lane & 7) ^ (lane >> 3)) * 16 - (i & 3) * 1024);
      const unsigned lbase = lds0 + hw * 8192;
      const char* Yc = reinterpret_cast<const char*>((cc ? Y1 : Y0) + (size_t)nt * 128 * S);
      auto stage = [&](const char* yp, unsigned l) {
        GLDS4(l, voy, yp);
        GLDS4(l + 4096, voy + 4, yp);
      };
#define YSRC(n) (Yc + ((((n) + xcd * 4) & 31) << 7))
      __builtin_amdgcn_s_sleep(C_LD);
      PFL(hw ? PFG(1) : cpf);
      JSET();
      TAGW(1);
      BAR(55);
      BT(56);
      if (hw == 1 && lane == 0) asm volatile("global_store_dwordx2 %0, %1, %2 sc1" ::"v"(O_XFL + b * XSTR * 4), "v"(tag), "s"(ws) : "memory");
#pragma unroll
      for (int s = 0; s < NS - 1; ++s) stage(YSRC(s), lbase + s * 16384);
      BT(57);
      WAIT_V((NS - 2) * P);
      BAR(0);
      unsigned slot = lbase + (NS - 1) * 16384;
      const u64 evm = (u64)((1u | 1u << W_T) << pf0 | (1u | 1u << W_T) << pf1) | 1ull << CPF_K | 1ull << (33 - NS) | 3ull << 32;
      const char* yb = Yc;
      unsigned yo = (NS - 1 + xcd * 4) << 7, ym = 31 << 7;
#pragma unroll 1
      for (int k = 1; k <= 32 + B_J; ++k) {
        const char* ys = yb + (yo & ym);
        yo += 128;
        asm volatile("" : "+s"(ys));
        WAIT_V((NS - 3) * P);
        BAR(k);
        stage(ys, slot);
        slot = slot == lbase + (NS - 1) * 16384 ? lbase : slot + 16384;
        if (__builtin_expect((evm >> k) & 1, 0)) {
          if (k == pf0) {
            WST(og, wg);
            if (b >= NPART) GLDS(lds0 + SPARE + 8192 + hw * 1024, (unsigned)(hw * 1024 + lane * 16), wr_);
          }
          if (k == pf0 + W_T) { WTK(gT); }
          if (k == pf1) { WST(ou, wu); }
          if (k == pf1 + W_T) { WTK(uT); }
          if (k == CPF_K && hw == 1) CPFL();
          if (k == 33 - NS) {
#pragma unroll
            for (int i = 0; i < P; ++i) voy[i] = HOFF((hw * 8 + i) * 8 + (lane >> 3)) - (i & 3) * 1024;
            yb = Hc, yo = mt << 8, ym = 15 << 7;
          }
          if (k == 32) { DLD(0, 8 - B_WN); }
          if (k == 33 && hw == 0) {
            if (U_D) __builtin_amdgcn_s_sleep(U_D);
            urdy = UPOLL(U_N1);
          }
        }
      }
    } else {
      const int aw = hw - 2;
      unsigned ow = wo - (2048 + (cg0 >> 4) * 1024) * 2;
      const unsigned oq = (unsigned)((nt * 64 + aw * 32 + (lane >> 4)) * D + mt * 128) * 2;
      u32x4 hv[8], wt[8], sc;
      {
        asm volatile("s_waitcnt vmcnt(11)" : "+v"(cq)::"memory");
#pragma unroll
        for (int i = 0; i < 4; ++i) sc[i] = pack2(silu_bf(bflo(cq[i])), silu_bf(bfhi(cq[i])));
        asm volatile("" : "+v"(sc));
      }
#define GPUB(o, off)                                                                                   \
  if ((lane & 8) == 0)                                                                                 \
    asm volatile("global_store_dwordx4 %0, %1, %2 sc1 ; ALWAYS\n\ts_nop 1" ::"v"(off),                          \
                 "v"(u32x4{__float_as_uint(o[0]), (unsigned)tag, __float_as_uint(o[1]), (unsigned)tag}), "s"(ws) : "memory")
      const unsigned gpo = ks0 * 2048 + cg0 * 64 + (lane & 7) * 8 + (lane >> 4) * 2;
      {
        unsigned mpo = O_G1 + gpo * 8;
        asm volatile("s_waitcnt vmcnt(3)" : W8(wv), "+v"(mpo)::"memory");
        TR8(wt, wv);
        f32x2 o = MV64(wt, sc);
        asm volatile("" : "+v"(o));
        TAGW(0);
        BAR(55);
        if (P_D2) __builtin_amdgcn_s_sleep(P_D2);
        PFL(PFG(2 + aw));
        GPUB(o, mpo);
      }
      unsigned vta = lds0 + STG + (g4 * 8 + ((lane >> 2) & 3)) * 256 + (((aw * 8 + ((lane >> 1) & 1)) ^ ((g4 & 1) * 8 + ((lane >> 2) & 3))) << 4) + (lane & 1) * 8;
      unsigned vso = O_VP + ((cc * 8 + mt) * 16 + nt) * 256 + aw * 128 + (lane & 3) * 32 + g4 * 8;
      asm volatile("" : "+v"(vta), "+v"(vso), "+v"(ow));
      unsigned f0 = O_XFL + ((xcd * 32 + 64 + aw * 128 + lane) & 255) * XSTR * 4, f1 = O_XFL + ((xcd * 32 + 128 + aw * 64 + lane) & 255) * XSTR * 4;
      unsigned lao = O_KEY + 512 * 1024 + b * 3072 + aw * 1536 + lane * 16;
      const char* pf4 = PFG(4 + aw);
      const char* pf6 = PFG(6 + aw);
      asm volatile("" : "+v"(f0), "+v"(f1), "+v"(lao), "+v"(pf4), "+v"(pf6));
      BT(57);
      BAR(0);
      f32x4 rs = {0.f, 0.f, 0.f, 0.f};
      u32x4 rsa = (lane & 15) == g4 * 4 ? u32x4{0x3f803f80u, 0x3f803f80u, 0x3f803f80u, 0x3f803f80u} : u32x4{0u, 0u, 0u, 0u}, R_;
      asm volatile("" : "+v"(rs), "+v"(rsa), "=v"(R_));
      const unsigned ra = lds0 + (mt * 16 + aw * 8) * 128 + lane * 16;
      unsigned so = 0;
#define TSX()
#define RSUM()                                                                                                    \
  do {                                                                                                            \
    TSX();                                                                                                        \
    asm volatile("ds_read_b128 %1, %2\n\ts_waitcnt lgkmcnt(0)\n\tv_mfma_f32_16x16x32_bf16 %0, %3, %1, %0" : "+v"(rs), "+v"(R_) : "v"(ra + so), "v"(rsa) : "memory"); \
    so = so == (NS - 1) * 16384 ? 0 : so + 16384;                                                                 \
  } while (0)
      {
        u64 f[2];
#define FLD() asm volatile("global_load_dwordx2 %0, %2, %4 sc1\n\tglobal_load_dwordx2 %1, %3, %4 sc1" : "=&v"(f[0]), "=&v"(f[1]) : "v"(f0), "v"(f1), "s"(ws) : "memory")
        __builtin_amdgcn_s_sleep(F_D);
        FLD();
        asm volatile("global_store_dwordx4 %0, %1, %2 sc1" ::"v"(lao), "v"(u32x4{~0u, ~0u, ~0u, ~0u}), "s"(ws) : "memory");
        if (lane < 32) asm volatile("global_store_dwordx4 %0, %1, %2 offset:1024 sc1 ; ALWAYS" ::"v"(lao), "v"(u32x4{~0u, ~0u, ~0u, ~0u}), "s"(ws) : "memory");
        asm volatile("s_nop 1");
#define GLD() _Pragma("unroll") for (int i = 0; i < 8; ++i) LDWI(wv[i], ow, wmod, 12288)
        RSUM();
        BAR(1);
        asm volatile("s_waitcnt vmcnt(%2)" : "+v"(f[0]), "+v"(f[1]) : "n"(2) : "memory");
        POLL(__all(f[0] == tag && f[1] == tag), 1, FLD(); asm volatile("s_waitcnt vmcnt(0)" : "+v"(f[0]), "+v"(f[1])::"memory"));
      }
      if (X_PA) __builtin_amdgcn_s_setprio(X_PA);
#define ARUN(b0, b1) _Pragma("unroll 1") for (int bi = (b0); bi < (b1); ++bi) { RSUM(); BAR(bi + 1); }
      GLD();
      ARUN(1, pf0);
      PFL(pf4);
#define HVLD() _Pragma("unroll") for (int i = 0; i < 8; ++i) GLDS(lds0 + STG + aw * 8192 + i * 1024, oq + i * 8192 + (((lane & 15) ^ ((i * 4 + (lane >> 4)) & 15)) << 4), cc ? X1 : X0)
      WST(og, wg);
      ARUN(pf0, pf0 + G1_C);
      {
        if (b >= NPART) WAIT_V(0);
        asm volatile("s_waitcnt vmcnt(8)" : W8(wv)::"memory");
        TR8(wt, wv);
        if (G1_S) { RSUM(); BAR(pf0 + G1_C + 1); }
        const f32x2 o = MV64(wt, sc);
        GPUB(o, O_MODP + gpo * 8);
        ow = wo + 2048;
      }
      ARUN(pf0 + G1_C + G1_S, pf0 + W_T - 1);
      WAIT_V(1);
      RSUM();
      BAR(pf0 + W_T);
      WTK(gT);
      ARUN(pf0 + W_T, pf1);
      PFL(pf6);
      WST(ou, wu);
      const int rb_ = b < NPART ? pf1 + W_T : R_AT, hb_ = max(rb_ + 1, H_AT + (j8 & 3));
      const unsigned c0 = (b - NPART) * 64 + aw * 32 + (lane & 15) * 2, c1 = (b - NPART) * 32 + aw * 16 + (lane & 7) * 2;
#define ERLD(W) LD8(wt, O_MODP + (g4 * 8192 + c0) * 8, O_G1 + (g4 * 8192 + (lane & 8) * 128 + c1) * 8, W)
      ARUN(pf1, rb_ - 3);
      if (b >= NPART) ERLD("");
      ARUN(rb_ - 3, rb_ - 1);
      WAIT_V(0);
      RSUM();
      BAR(rb_);
      WTK(uT);
      if (b >= NPART) {
        const unsigned wq = *reinterpret_cast<const unsigned*>(&svb[4][c1]);
#define TG2(r, i) ((r[i][1] ^ (unsigned)tag) | (r[i][3] ^ (unsigned)tag) | (r[i + 1][1] ^ (unsigned)tag) | (r[i + 1][3] ^ (unsigned)tag))
        asm volatile("" : W8(wt));
        POLL(__all((TG2(wt, 0) | TG2(wt, 2) | TG2(wt, 4) | TG2(wt, 6)) == 0u), 2, ERLD(VW0));
        auto FIN = [&](int q, int i, bool inc) __attribute__((always_inline)) {
          return rbf((inc ? 1.0f : 0.0f) + rbf(ar4((__uint_as_float(wt[q][i]) + __uint_as_float(wt[q + 1][i])) + (__uint_as_float(wt[q + 2][i]) + __uint_as_float(wt[q + 3][i])))));
        };
        const float s0 = FIN(0, 0, gbq), s1 = FIN(0, 2, gbq);
        if (lane < 16)
          asm volatile("global_store_dwordx4 %0, %1, %2 sc1\n\ts_nop 1" ::"v"(O_MODP + (16 * 2048 + c0) * 8),
                       "v"(u32x4{__float_as_uint(s0), (unsigned)tag, __float_as_uint(s1), (unsigned)tag}), "s"(ws) : "memory");
        const bool hi = lane & 8;
        const float t0 = FIN(4, 0, hi), t1 = FIN(4, 2, hi), o0 = dpp_f<0x128>(t0), o1 = dpp_f<0x128>(t1);
        const float a0 = hi ? o0 : t0, a1 = hi ? o1 : t1, e0 = hi ? t0 : o0, e1 = hi ? t1 : o1, p0 = e0 * bflo(wq), p1 = e1 * bfhi(wq);
        const unsigned h = pack2(p0, p1);
        const unsigned v = g4 == 0 ? __float_as_uint(hi ? a1 : a0) : g4 == 1 ? (hi ? h : pack2(e0, e1)) : hi ? pack2(a0 * p0, a1 * p1) : pack2(p0 - bflo(h), p1 - bfhi(h));
        const unsigned po = g4 == 0 ? O_M2 + c1 * 4 + (hi ? 4 : 0) : (g4 == 1 ? (hi ? O_M2 + 4096 : O_MOD + 4096) : hi ? O_GC : O_M2 + 6144) + c1 * 2;
        if (lane < 48) asm volatile("global_store_dword %0, %1, %2 sc1" ::"v"(po), "v"(min(v, SENT - 1u)), "s"(ws) : "memory");
      }
      ARUN(rb_, hb_);
      HVLD();
      ARUN(hb_, q1);
      u32x4 fm, g0, g1;
#define FMLD(W)                                                                                                                                     \
  asm volatile("global_load_dwordx4 %0, %3, %5 sc1\n\tglobal_load_dwordx4 %1, %4, %5 sc1\n\tglobal_load_dwordx4 %2, %4, %5 offset:64 sc1" W        \
               : "=&v"(fm), "=&v"(g0), "=&v"(g1) : "v"(O_MODP + (16 * 2048 + aw * 1024 + mt * 128) * 8 + lane * 16), "v"(O_GC + nt * 128 + g4 * 16), "s"(ws) : "memory")
      FMLD("");
      RSUM();
      BAR(q1 + 1);
      unsigned ha[4], sma = lds0 + SPARE + 15872 + (aw ? 512 + lane * 8 : lane * 4);
#pragma unroll
      for (int i = 0; i < 4; ++i) ha[i] = lds0 + STG + aw * 8192 + (lane & 15) * 256 + (((i * 4 + (lane >> 4)) ^ (lane & 15)) << 4);
      asm volatile("" : "+v"(ha[0]), "+v"(ha[1]), "+v"(ha[2]), "+v"(ha[3]), "+v"(sma), "+v"(ow));
      ARUN(q1 + 1, F_AT);
      asm volatile("s_waitcnt vmcnt(0)" : "+v"(fm), "+v"(g0), "+v"(g1)::"memory");
#pragma unroll
      for (int i = 0; i < 8; ++i) DSR(hv[i], ha[i & 3], (i >> 2) * 4096);
      POLL(__all(fm[1] == (unsigned)tag && fm[3] == (unsigned)tag && max(max4(g0), max4(g1)) != SENT), 1, FMLD(VW0));
      {
        const unsigned sg = cc ? 0x80000000u : 0u;
        asm volatile("ds_write_b32 %0, %2\n\tds_write_b32 %1, %3" ::"v"(sma), "v"(sma + (aw ? 4 : 256)), "v"(aw ? fm[0] ^ sg : (fm[0] >> 16) | (fm[2] & 0xffff0000u)), "v"(fm[2] ^ sg) : "memory");
      }
      ARUN(F_AT, 29);
      GLD();
      RSUM();
      BAR(30);
        u32x2 tv[16];
#pragma unroll
        for (int q = 0; q < 16; ++q)
          asm volatile("ds_read_b64_tr_b16 %0, %1 offset:%2" : "=v"(tv[q]) : "v"(vta ^ ((((q >> 2) * 2) ^ ((q & 1) * 4)) << 4)), "n"((q & 1) * 1024 + ((q >> 1) & 1) * 8192) : "memory");
      RSUM();
      BAR(31);
      RSUM();
      {
        asm volatile("s_nop 7\n\ts_nop 7" : "+v"(rs));
        const float v = dpp_add<0x141>(dpp_add<0x4E>(dpp_add<0xB1>(rs[0])));
        if ((lane & 7) == 0)
          asm volatile("global_store_dword %0, %1, %2 sc1 ; ALWAYS" ::"v"(O_RS + (cc * S + nt * 128 + mt * 16 + aw * 8 + (lane >> 3)) * 4), "v"(nsent(v)), "s"(ws) : "memory");
      }
      BAR(32);
      __builtin_amdgcn_s_sleep(2);
      DLD(0, 8 - B_WN);
      __builtin_amdgcn_s_setprio(2);
      {
        asm volatile("" : W8(tv), W8((tv + 8))::"memory");
        f32x4 d[4];
#pragma unroll
        for (int j = 0; j < 4; ++j) {
          d[j] = __builtin_amdgcn_mfma_f32_16x16x32_bf16(BF8((u32x4{tv[4 * j][0], tv[4 * j][1], tv[4 * j + 1][0], tv[4 * j + 1][1]})), BF8(g0), f32x4{0.f, 0.f, 0.f, 0.f}, 0, 0, 0);
          d[j] = __builtin_amdgcn_mfma_f32_16x16x32_bf16(BF8((u32x4{tv[4 * j + 2][0], tv[4 * j + 2][1], tv[4 * j + 3][0], tv[4 * j + 3][1]})), BF8(g1), d[j], 0, 0, 0);
        }
        u32x2 vo;
#pragma unroll
        for (int i = 0; i < 2; ++i) {
          const unsigned e0 = pack2(d[0][2 * i], d[0][2 * i + 1]), e1 = pack2(d[1][2 * i], d[1][2 * i + 1]), e2 = pack2(d[2][2 * i], d[2][2 * i + 1]), e3 = pack2(d[3][2 * i], d[3][2 * i + 1]);
          vo[i] = min((lane & 2) ? ((lane & 1) ? e3 : e2) : ((lane & 1) ? e1 : e0), SENT - 1u);
        }
        if ((lane & 12) == 0) asm volatile("global_store_dwordx2 %0, %1, %2 sc1 ; ALWAYS" ::"v"(vso), "v"(vo), "s"(ws) : "memory");
      }
      asm volatile("" : W8(hv)::"memory");
      {
        f32x4 qa[2] = {};
#pragma unroll
        for (int kk = 0; kk < 4; ++kk) {
          const bf16x8 sb8 = *reinterpret_cast<const bf16x8*>(ssa + kk * 32 + (lane >> 4) * 8);
#pragma unroll
          for (int r = 0; r < 2; ++r)
            qa[r] = __builtin_amdgcn_mfma_f32_16x16x32_bf16(__builtin_bit_cast(bf16v8, hv[r * 4 + kk]), __builtin_bit_cast(bf16v8, sb8), qa[r], 0, 0, 0);
        }
        if ((lane & 15) < 2)
          asm volatile("global_store_dwordx4 %0, %1, %2 sc1 ; ALWAYS\n\ts_nop 1" ::"v"(O_RS + (4096 + (cc * 8 + mt) * D + nt * 64 + aw * 32 + (lane & 1) * 16 + (lane >> 4) * 4) * 4),
                       "v"(nsent4(selu((lane & 1) ? ~0u : 0u, __builtin_bit_cast(u32x4, qa[1]), __builtin_bit_cast(u32x4, qa[0])))), "s"(ws) : "memory");
      }
      BAR(33);
      {
        if (b >= NPART) WAIT_V(3);
        asm volatile("s_waitcnt vmcnt(%8)" : W8(wv) : "n"(8 - B_WN + 3) : "memory");
        TR8(wt, wv);
        const f32x2 o = MV64(wt, sc);
        GPUB(o, O_G2 + gpo * 8);
      }
      JSET();
      BAR(34);
#pragma unroll
      for (int k = 3; k <= B_J; ++k) {
        BAR(32 + k);
        if (k == V_K) {
          if (!aw) GP4(O_VP + (cc * 8 + mt) * 4096 + lane * 16, "");
          else VCLD("");
        }
      }
    }
    u32x4 dT[8];
    u32x4 m[3];
    u32x2 kk = {SENT, SENT};
#define K2LD(W) asm volatile("global_load_dwordx2 %0, %1, %2 sc1" W : "+v"(kk) : "v"(O_K2 + (lane & 15) * 64), "s"(ws) : "memory")
    {
      constexpr BSched sch;

      const unsigned lbase = lds0 + hw * 4096;
      u32x4 rr[8];
      unsigned cbv, t1, t2;
      float t[2];
#define RRLD(W)                                                                                                          \
  do {                                                                                                                   \
    asm volatile("global_load_dword %0, %1, %2 sc1" : "=&v"(cbv) : "v"(O_MOD + 4096 + cj * 2), "s"(ws) : "memory");      \
    LD8(rr, ro, ro + 65536, W);                                                                                          \
  } while (0)
      unsigned wq;
      unsigned mr;
      f32x2 qt;
#define QLD(W)                                                                                                           \
  asm volatile("global_load_dwordx4 %0, %3, %5 offset:-4096 sc1\n\tglobal_load_dwordx4 %1, %3, %5 sc1\n\tglobal_load_dword %2, %4, %5 sc1" W \
               : "=&v"(m[0]), "=&v"(m[1]), "=&v"(mr)                                                                     \
               : "v"(qo), "v"(so_), "s"(ws)                                                                              \
               : "memory")
#define MODLD()                                                                                                                \
  asm volatile("global_load_dwordx4 %0, %3, %6 sc1\n\tglobal_load_dwordx4 %1, %4, %6 sc1\n\tglobal_load_dwordx4 %2, %5, %6 sc1" \
               : "=&v"(m[0]), "=&v"(m[1]), "=&v"(m[2])                                                                         \
               : "v"(mo), "v"(mo1), "v"(mo2), "s"(ws)                                                                          \
               : "memory")
#define MODW(n) asm volatile("s_waitcnt vmcnt(%3)" : "+v"(m[0]), "+v"(m[1]), "+v"(m[2]) : "n"(n) : "memory")
#pragma unroll
      for (int k = B_J; k < 16; ++k) {
        if (k > B_J) {
          if (k == 2 * U_N1 - 1 && !urdy) UPOLL(7);
          WAIT_V(sch.cnt[k]);
          if (k == 15) WAIT_L(0);
          BAR(32 + k);
        }
        if (k > B_J && k - 2 + NS < 16) {
          const char* yp = Hc + ((mt * 2 + k - 2 + NS) & 15) * 128;
          const unsigned l = lbase + ((30 + k + NS) % NS) * 16384;
          GLDS4(l, voff, yp);
        }
        if (k == B_TK && hw == 1 && b >= 192) {
          const unsigned t = tick(), h = t + T_O + (((int)(t - ts0) - T_N) * T_A >> 8);
          if (lane == 0) asm volatile("global_store_dword %0, %1, %2 sc1" ::"v"(O_M2 + 8448 + (b - 192) * 4), "v"(min(h, SENT - 1u)), "s"(ws) : "memory");
        }
        if (k == B_RAT + 2) {
          if (rdc) {
            WAIT_V(sch.rcnt);
            asm volatile("" : W8(rr), "+v"(cbv));
            POLL(__all((TG2(rr, 0) | TG2(rr, 2) | TG2(rr, 4) | TG2(rr, 6)) == 0u && cbv != SENT), 2, RRLD(VW0); asm volatile("" : "+v"(cbv)));
#pragma unroll
            for (int j = 0; j < 2; ++j) {
#define RF(q) __uint_as_float(rr[q][2 * j])
              t[j] = ((RF(0) + RF(1)) + (RF(2) + RF(3))) + ((RF(4) + RF(5)) + (RF(6) + RF(7)));
            }
            asm volatile("" : "+v"(t[0]), "+v"(t[1]));
          }
        }
        if (k == B_RAT + 2 && rdc) {
          const unsigned o = pack2(swap32_add(t[0], t[0]), swap32_add(t[1], t[1])), f = gbq ? cbv : o;
          const float pr = row_sum(bflo(f) * bflo(wq) + bfhi(f) * bfhi(wq)), ps = swap16_add(pr, pr);
          if (lane <= 32) asm volatile("global_store_dword %0, %1, %2 sc1" ::"v"(ra1), "v"(min(lane < 32 ? o : __float_as_uint(ps), SENT - 1u)), "s"(ws) : "memory");
          TS(12);
        }
        if (k == V_T && hw == 3) {
          WAIT_V(sch.vcnt);
          asm volatile("" : "+v"(vc));
          POLL(__all(vc != SENT), 2, VCLD(VW0));
        }
        if (k == V_T && hw == 2) {
          WAIT_V(sch.vcnt);
          asm volatile("" : "+v"(gp[0]), "+v"(gp[1]), "+v"(gp[2]), "+v"(gp[3]));
          POLL(!__any(GPBAD), 2, GP4(O_VP + (cc * 8 + mt) * 4096 + lane * 16, VW0));
          const f32x4 d = GPSUM();
          if (lane < 32) *reinterpret_cast<u32x2*>(shm + VT + (lane & 15) * 16 + g4 * 8) = u32x2{pack2(d[0], d[1]), pack2(d[2], d[3])};
          WAIT_L(0);
        }
        if (k == N_C) {
          f32x4 n[2] = {};
#pragma unroll
          for (int q = 0; q < 8; ++q)
            n[q >> 2] = __builtin_amdgcn_mfma_f32_16x16x32_bf16(BF8(*reinterpret_cast<const u32x4*>(shm + VT + (q & 3) * 64 + g4 * 16)),
                                                                BF8(*reinterpret_cast<const u32x4*>(shm + STG + hw * 8192 + q * 1024 + lane * 16)), n[q >> 2], 0, 0, 0);
          if (lane < 16)
            asm volatile("global_store_dword %0, %1, %2 sc1" ::"v"(O_NP + nt * 4096 + (cc * 8 + mt) * 256 + hw * 64 + lane * 4), "v"(min(pack2(n[0][0], n[1][0]), SENT - 1u)), "s"(ws) : "memory");
          TS(33);
        }
        if (k == N_K + N_D && rnp) {
            WAIT_V(sch.ncnt);
            asm volatile("" : "+v"(gp[0]), "+v"(gp[1]), "+v"(gp[2]), "+v"(gp[3]));
            POLL(!__any(GPBAD), 2, GP4(O_NP + nt * 4096 + lane * 16, VW0));
            const unsigned r0 = nt * 128 + ((lane >> 2) & 3) * 32 + (lane & 3) * 4 + (g4 & 1) * 2;
        const f32x4 d = GPSUM();
        unsigned ky[4];
#pragma unroll
        for (int i = 0; i < 4; ++i) {
          const unsigned f = __float_as_uint(d[i]);
          ky[i] = (((f ^ ((unsigned)((int)f >> 31) | 0x80000000u)) >> 1) & ~2047u) | (r0 + (i & 1) * 16 + (i >> 1));
        }
        t1 = max(max(ky[0], ky[1]), max(ky[2], ky[3])), t2 = max(min(max(ky[0], ky[1]), max(ky[2], ky[3])), max(min(ky[0], ky[1]), min(ky[2], ky[3])));
        asm volatile("" : "+v"(t1), "+v"(t2));
        }
        if (k == N_K + N_D && rnp) {
        TOP2(0xB1); TOP2(0x4E); TOP2(0x141); TOP2(0x140);
        TOP2S(__builtin_amdgcn_permlane16_swap);
            if (lane == 0) asm volatile("global_store_dwordx2 %0, %1, %2 sc1" ::"v"(O_K2 + nt * 64), "v"(u32x2{t1, t2}), "s"(ws) : "memory");
            TS(34);
        }
        if (k == N_K && rnp) GP4(O_NP + nt * 4096 + lane * 16, "");
        if (k == B_QAT + B_QD) {
          asm volatile("s_waitcnt vmcnt(%3)" : "+v"(m[0]), "+v"(m[1]), "+v"(mr) : "n"(sch.qcnt) : "memory");
          POLL(__all(max(max(max4(m[0]), max4(m[1])), mr) != SENT), 2, QLD(VW0));
          const f32x4 t = __builtin_bit_cast(f32x4, m[0]) + __builtin_bit_cast(f32x4, m[1]);
          qt = f32x2{swap32_add(t[0], t[2]), swap32_add(t[1], t[3])};
          asm volatile("" : "+v"(qt), "+v"(mr));
        }
        if (k == B_QAT + B_QD + Q_S) {
          const float r = swap16_add(qt[0], qt[1]);
          const unsigned a0 = pack2(r, r);
          asm volatile("ds_write_b64 %0, %1" ::"v"(qa), "v"(u32x2{a0, pack2(r - bflo(a0), r - bflo(a0))}) : "memory");
        }
        if (k == B_QAT + B_QD + Q_S2) {
          asm volatile("" : "+v"(mr));
          const float rs = __uint_as_float(mr ^ ((hw & 1) ? 0x80000000u : 0u));
          const unsigned bb = pack2(rs, rs - rbf(rs));
          asm volatile("ds_write_b64 %0, %1" ::"v"(qb), "v"(u32x2{bb, bb}) : "memory");
        }
        if (k == CPF_B && hw == 2) CPFL();
        if (k == B_RAT) {
          if (rdc) RRLD("");
          asm volatile("ds_read_b32 %0, %1\n\ts_waitcnt lgkmcnt(0)" : "=v"(wq) : "v"(wqa) : "memory");
        }
        if (k == X_K) {
          const unsigned v4[4] = {xo, (xo ^ 64) + 8192 - 1024, (xo ^ 128) + 16384 - 2048, (xo ^ 192) + 24576 - 3072};
          unsigned l2 = lbase;
          asm volatile("" : "+s"(l2));
          GLDS4(l2 * 2 - lds0 + STG + 4096, v4, x);
        }
        if (k == V_T + 1 || k == N_K + 1) SKEWPT();
        if (k == B_QAT) QLD("");
        if (k == B_MAT) MODLD();
        if (k == B_WK) { DLD(8 - B_WN, 8); }
      }
      if (b >= NPART) WAIT_V(0);
      MODW(sch.mcnt);
      TS(30);
      SKEWPT();
      POLL(__all(max(max(max4(m[0]), max4(m[1])), max4(m[2])) != SENT), 4, MODLD(); MODW(0));
      *reinterpret_cast<u32x4*>(&svb[hw][lane * 8]) = m[0];
      *reinterpret_cast<u32x4*>(&svb[hw][512 + lane * 8]) = m[1];
      if (hw == 0) {
        asm volatile("ds_write_b128 %0, %1" ::"v"(ma2), "v"(m[2]) : "memory");
      } else if (hw == 2) {
        int d = max(max((int)(m[2][0] - ts0), (int)(m[2][1] - ts0)), max((int)(m[2][2] - ts0), (int)(m[2][3] - ts0)));
        d = max(d, (int)dpp_u<0xB1>(d));
        d = max(d, (int)dpp_u<0x4E>(d));
        d = max(d, (int)dpp_u<0x141>(d));
        d = max(d, (int)dpp_u<0x140>(d));
        const unsigned hh = ts0 + d;
        ssel[7] = hh;
        if ((int)(tick() - (unsigned)__builtin_amdgcn_readfirstlane((int)hh) - T_K) >= 0) K2LD("");
      } else {
        const f32x4 v = __builtin_bit_cast(f32x4, m[2]);
        const f32x2 c = {v[0] + v[1], v[2] + v[3]};
        const float s4 = dpp_add<0x4E>(dpp_add<0xB1>(c[0] + c[1]));
        if (lane < 8) asm volatile("ds_write_b64 %0, %1" ::"v"(ma2), "v"(lane < 4 ? c : f32x2{s4, s4}) : "memory");
      }
    }
    if (hw != 2) __builtin_amdgcn_s_setprio(0);
    TS(17);
    BT(48);
    LBAR();
    BTE();
    TS(18);
    if (hw == 2) {
      WAIT_V(0);
      asm volatile("" : "+v"(kk));
      if (__any(max(kk[0], kk[1]) == SENT)) {
        nap_tick((unsigned)__builtin_amdgcn_readfirstlane((int)ssel[7]) + T_K);
        POLL2(max(kk[0], kk[1]) == SENT, K_S, PCAP, K2LD(VW0));
      }
      TS(35);
      const unsigned me = lane < 32 ? kk[0] : kk[1], ot = (lane & 16) ? kk[1] : kk[0];
      unsigned rk = ot > me;
      sfor<15>([&](auto r) __attribute__((always_inline)) { rk += dpp_u<0x121 + decltype(r)::value>(ot) > me; });
      auto rs_ = __builtin_amdgcn_permlane16_swap(rk, rk, false, false);
      rk = rs_[0] + rs_[1];
      if (rk < 8u) {
        *reinterpret_cast<unsigned*>(shm + LST + rk * 4) = 0x8ffeu - (me & 2047u) * 2;
        if ((lane & 15) == nt) *reinterpret_cast<unsigned*>(shm + SLT + (me & 127u) * 4) = rk + 1;
      }
      asm volatile("" ::: "memory");
      ssel[20] = 1u;
    }
    TS(19);
    unsigned sfa = lds0 + SPARE + 10368, sv, one = 1u;
    RETIRE();
    const int kc = (g & 3) * 256 + hw * 64 + (lane & 7) * 8;
    const u32x4 cbq = *reinterpret_cast<const u32x4*>(&svb[2][kc]), sbq = *reinterpret_cast<const u32x4*>(&svb[1][kc]);
    f32x4 cb2[2] = {lo4(cbq), hi4(cbq)}, sb2[2] = {lo4(sbq), hi4(sbq)};
    float sw = cwj[8], cj8 = cwj[lane & 7];
    unsigned rk2 = lds0 + LST + (lane >> 3) * 4;
    unsigned xo_ = O_X1C + (lane >> 3) * 2048 + kc * 2, hwa = lds0 + XC + hw * 1024 + (lane & 7) * 128 + (((lane >> 3) ^ lane) & 7) * 16;
    unsigned hra = lds0 + XC + g4 * 128 + ((lane ^ g4) & 7) * 16, hrb = hra ^ 64, ara = lds0 + SPARE + 15872 + (lane & 2) * 64 + g4 * 32 + (lane & 1) * 16;
    unsigned pso = O_PART + ((g >> 2) * 2048 + (lane & 8) * 128 + (g & 3) * 256 + hw * 64 + (lane & 7) * 8 + g4 * 2) * 4;
    unsigned gso = O_GU + g * 1024 + (lane >> 5) * 256 + hw * 64 + (lane & 15) * 4, awa = lds0 + SPARE + 15872 + lane * 4, sfw = sfa + hw * 4;
    unsigned gl0 = O_GU + (g >> 2) * 4096 + lane * 8, gl1 = O_GU + (xcd * 7 + (mt + 7 - W_UO) % 7) * 4096 + 512 + lane * 8;
    asm volatile("" : "+v"(sfa), "+v"(one), "+v"(hra), "+v"(hrb), "+v"(ara), "+v"(pso), "+v"(gso), "+v"(awa), "+v"(gl0), "+v"(gl1), "+v"(sfw), "+v"(qm));
    asm volatile("" : "+v"(cb2[0]), "+v"(cb2[1]), "+v"(sb2[0]), "+v"(sb2[1]), "+v"(hwa), "+v"(sw), "+v"(cj8), "+v"(rk2), "+v"(xo_));
    unsigned sto = O_ST2 + lane * 16, kmk = ~0u;
    asm volatile("" : "+v"(sto), "+v"(kmk));
    const unsigned th = (unsigned)__builtin_amdgcn_readfirstlane((int)ssel[7]);
    const char* wb = ws;
#define P2PUT(i) if (__builtin_expect(wb != ws, 0)) { WAIT_V(0); if (lane == 0) asm volatile("global_store_dwordx2 %0, %1, %2 sc1" ::"v"(O_F2 + (i)*8192 + (g * 4 + hw) * 8), "v"(tag), "s"(ws) : "memory"); }
#define P2GET(o) if (__builtin_expect(wb != ws, 0)) { u64 f; POLL2(f != tag, 4, PCAP, asm volatile("global_load_dwordx2 %0, %1, %2 sc1" VW0 : "=&v"(f) : "v"(O_F2 + (o)), "s"(ws) : "memory")); }
#pragma unroll 1
    for (;;) {
    TS(20);
    if (wb == ws) {
      unsigned r_;
      POLL2(sv == 0u, 2, 1 << 26, asm volatile("ds_read_b32 %0, %2 offset:80\n\tds_read_b32 %1, %3\n\ts_waitcnt lgkmcnt(0)" : "=&v"(sv), "=&v"(r_) : "v"(sfa), "v"(rk2) : "memory"));
      rk2 = r_;
    }
    TS(21);
    nap_tick(th + T_C3 - 10);
    {
      u32x4 dn;
      POLL2((dn[0] & dn[1] & dn[2] & dn[3]) == 0u, 1, 1 << 26, asm volatile("ds_read_b128 %0, %1 offset:96\n\ts_waitcnt lgkmcnt(0)" : "=v"(dn) : "v"(sfa) : "memory"));
    }
    __builtin_amdgcn_s_setprio(H_P);
    wait_tick(th + T_C3);
    TS(36);
    u32x4 st, xv_;
    POLL2(max(max4(xv_), max4(st)) == SENT, 0, PCAP,
          asm volatile("global_load_dwordx4 %0, %2, %4 sc1\n\tglobal_load_dwordx4 %1, %3, %4 sc1" VW0 : "=&v"(st), "=&v"(xv_) : "v"(sto), "v"(xo_), "s"(ws) : "memory"));
    TS(22);
    SKEWPT();
#define SUM8(v) dpp_add<0x141>(dpp_add<0x4E>(dpp_add<0xB1>(v)))
    const f32x4 sf = __builtin_bit_cast(f32x4, st);
    const float mean = SUM8(sf[0]) * 0.125f, dm = sf[0] - mean;
    const float rstd = __builtin_amdgcn_rsqf(SUM8(sf[1] + 128.0f * (dm * dm)) * (1.0f / D) + 1e-5f);
    asm volatile("ds_write_b128 %0, %1\n\tds_write_b32 %2, %3 offset:64" ::"v"(hwa),
                 "v"(pk8(((lo4(xv_) - mean) * rstd) * cb2[0] + sb2[0], ((hi4(xv_) - mean) * rstd) * cb2[1] + sb2[1])), "v"(sfw), "v"(one) : "memory");
    TS(3);
    u32x4 ha[8], fg;
#define HSLD(W)                                                                                                                             \
  asm volatile(                                                                                                                             \
      "ds_read_b128 %8, %11 offset:64\n\tds_read_b128 %0, %9\n\tds_read_b128 %1, %10 offset:512\n\tds_read_b128 %2, %9 offset:1024\n\t"       \
      "ds_read_b128 %3, %10 offset:1536\n\tds_read_b128 %4, %9 offset:2048\n\tds_read_b128 %5, %10 offset:2560\n\t"                          \
      "ds_read_b128 %6, %9 offset:3072\n\tds_read_b128 %7, %10 offset:3584" W                                                               \
      : "=&v"(ha[0]), "=&v"(ha[1]), "=&v"(ha[2]), "=&v"(ha[3]), "=&v"(ha[4]), "=&v"(ha[5]), "=&v"(ha[6]), "=&v"(ha[7]), "=&v"(fg)           \
      : "v"(hra), "v"(hrb), "v"(sfa)                                                                                                        \
      : "memory")
    unsigned key = (mkkey(rstd * SUM8(sf[2] + dm * cj8) + sw) | rk2) & kmk;
    asm volatile("" : "+v"(key));
    HSLD("");
    unsigned t1 = key, t2 = 0u;
    TOP2(0x140);
    TOP2S(__builtin_amdgcn_permlane16_swap);
    TOP2S(__builtin_amdgcn_permlane32_swap);
    const int l1 = __builtin_ctzll(__builtin_amdgcn_ballot_w64(key == t1)), l2 = __builtin_ctzll(__builtin_amdgcn_ballot_w64(key == t2));
    unsigned mq[6];
    asm volatile("s_bfe_i32 %0, %6, 0x10003\n\ts_bfe_i32 %1, %7, 0x10003\n\ts_bfe_i32 %2, %6, 0x10004\n\ts_bfe_i32 %3, %7, 0x10004\n\t"
                 "s_bfe_i32 %4, %6, 0x10005\n\ts_bfe_i32 %5, %7, 0x10005\n\ts_xor_b32 %4, %4, 0xffff\n\ts_xor_b32 %5, %5, 0xffff"
                 : "=&s"(mq[0]), "=&s"(mq[1]), "=&s"(mq[2]), "=&s"(mq[3]), "=&s"(mq[4]), "=&s"(mq[5]) : "s"(l1), "s"(l2) : "scc");
    const u64 m0 = mk64(mq[1], mq[0]), m1 = mk64(mq[3], mq[2]), m2 = mk64(mq[5], mq[4]);
    asm volatile("s_waitcnt lgkmcnt(0)" : W8(ha), "+v"(fg)::"memory");
    TS(2);
    if (b < NPART) {
#define HBAD ((fg[0] & fg[1] & fg[2] & fg[3]) != one)
      if (__any(HBAD)) POLL2(HBAD, 0, 1 << 26, HSLD("\n\ts_waitcnt lgkmcnt(0)"));
      TS(23);
      f32x4 dg = {0.f, 0.f, 0.f, 0.f}, du = {0.f, 0.f, 0.f, 0.f};
#pragma unroll
      for (int f = 0; f < 8; ++f) {
        dg = __builtin_amdgcn_mfma_f32_16x16x32_bf16(BF8(ha[f]), BF8(gT[f]), dg, 0, 0, 0);
        du = __builtin_amdgcn_mfma_f32_16x16x32_bf16(BF8(ha[f]), BF8(uT[f]), du, 0, 0, 0);
      }
      const bool s0 = __builtin_amdgcn_inverse_ballot_w64(m0), s1 = __builtin_amdgcn_inverse_ballot_w64(m1);
      if (__builtin_amdgcn_inverse_ballot_w64(m2))
        asm volatile("global_store_dword %0, %1, %3 sc1 ; ALWAYS\n\tglobal_store_dword %0, %2, %3 offset:512 sc1" ::"v"(gso),
                     "v"(nsent(s1 ? (s0 ? dg[3] : dg[2]) : (s0 ? dg[1] : dg[0]))), "v"(nsent(s1 ? (s0 ? du[3] : du[2]) : (s0 ? du[1] : du[0]))), "s"(wb) : "memory");
    }
    if (hw == 0 && wb == ws) asm volatile("ds_write_b64 %0, %1" ::"v"(sfa), "v"(u32x2{t1, t2}) : "memory");
    if (b < NPART) {
      TS(24);
      asm volatile("" : W8(wdv)::"memory");
      TR8(dT, wdv);
      P2PUT(0);
      if (hw == 0) {
        u32x2 rr[8];
        if (E_D) __builtin_amdgcn_s_sleep(E_D);
        TS(39);
        P2GET(((((lane & 16) ? gl1 : gl0) - O_GU) >> 12) * 128 + (lane & 15) * 8);
#define MX2(i) max(max(rr[i][0], rr[i][1]), max(rr[i + 1][0], rr[i + 1][1]))
        POLL2(max(max(MX2(0), MX2(2)), max(MX2(4), MX2(6))) == SENT, 1, PCAP,
              TS(40);
              asm volatile("global_load_dwordx2 %0, %8, %10 sc1\n\tglobal_load_dwordx2 %1, %8, %10 offset:1024 sc1\n\t"
                           "global_load_dwordx2 %2, %8, %10 offset:2048 sc1\n\tglobal_load_dwordx2 %3, %8, %10 offset:3072 sc1\n\t"
                           "global_load_dwordx2 %4, %9, %10 sc1\n\tglobal_load_dwordx2 %5, %9, %10 offset:1024 sc1\n\t"
                           "global_load_dwordx2 %6, %9, %10 offset:2048 sc1\n\tglobal_load_dwordx2 %7, %9, %10 offset:3072 sc1" VW0
                           : "=&v"(rr[0]), "=&v"(rr[1]), "=&v"(rr[2]), "=&v"(rr[3]), "=&v"(rr[4]), "=&v"(rr[5]), "=&v"(rr[6]), "=&v"(rr[7])
                           : "v"(gl0), "v"(gl1), "s"(wb)
                           : "memory"));
        TS(8);
#define R2(i) __builtin_bit_cast(f32x2, rr[i])
        const f32x2 gs = (R2(0) + R2(1)) + (R2(2) + R2(3)), us = (R2(4) + R2(5)) + (R2(6) + R2(7));
        const unsigned gp = pack2(gs[0], gs[1]), up = pack2(us[0], us[1]);
        const unsigned sp = pack2(bflo(gp) * __builtin_amdgcn_rcpf(1.0f + __expf(-bflo(gp))), bfhi(gp) * __builtin_amdgcn_rcpf(1.0f + __expf(-bfhi(gp))));
        asm volatile("ds_write_b32 %0, %1\n\tds_write_b32 %2, %3 offset:24" ::"v"(awa), "v"(pack2(bflo(sp) * bflo(up), bfhi(sp) * bfhi(up))), "v"(sfa), "v"(one) : "memory");
      }
      TS(25);
      SKEWPT();
      POLL2(sv != one, 0, 1 << 26, asm volatile("ds_read_b32 %1, %3 offset:24\n\tds_read_b128 %0, %2\n\ts_waitcnt lgkmcnt(0)" : "=&v"(fg), "=&v"(sv) : "v"(ara), "v"(sfa) : "memory"));
      asm volatile("global_load_dwordx2 %0, %1, %2 sc1" : "=&v"(vd) : "v"(O_VD + (b & 31) * 128), "s"(ws) : "memory");
      const f32x2 dd = MV64(dT, fg);
      asm volatile("global_store_dwordx2 %0, %1, %2 sc1" ::"v"(pso), "v"(u32x2{nsent(dd[0]), nsent(dd[1])}), "s"(wb) : "memory");
      TS(6);
      P2PUT(1);
      asm volatile("s_waitcnt vmcnt(1)" : "+v"(vd)::"memory");
      if (__any(max(vd[0], vd[1]) == SENT)) VDLD(2);
      TS(5);
    } else if (hw == 3) {
      u32x2 g0, g1;
      nap_tick(th + T_C4);
      POLL2(min(min(g0[0], g0[1]), min(g1[0], g1[1])) == 0u, 2, PCAP,
            asm volatile("global_load_dwordx2 %0, %2, %4 sc1\n\tglobal_load_dwordx2 %1, %3, %4 sc1" VW0
                         : "=&v"(g0), "=&v"(g1) : "v"(O_KEY + lane * 8 * KSTR), "v"(O_KEY + (lane + 64) * 8 * KSTR), "s"(ws) : "memory"));
      const unsigned k1 = t1, k2 = t2;
      t1 = max(g0[0], g1[0]), t2 = max(min(g0[0], g1[0]), max(g0[1], g1[1]));
      TOP2(0xB1); TOP2(0x4E); TOP2(0x141); TOP2(0x140);
      TOP2S(__builtin_amdgcn_permlane16_swap);
      TOP2S(__builtin_amdgcn_permlane32_swap);
      vd = u32x2{max(t1, k1), max(min(t1, k1), max(t2, k2))};
      if (lane == 0) asm volatile("global_store_dwordx2 %0, %1, %2 sc1\n\tds_write_b64 %3, %1 offset:8" ::"v"(O_VD + (b & 31) * 128), "v"(vd), "s"(ws), "v"(sfa) : "memory");
      TS(29);
      break;
    } else if (hw < 2) {
      const int tok = b & 1, c0 = ((b - NPART) >> 1) * 64 + hw * 32 + (lane & 7) * 4, col = c0 + g4;
      const unsigned td = tick(), kk = tok ? t2 : t1;
      const int ln = tok ? l2 : l1;
      unsigned v;
      POLL2(v == SENT, 1, PCAP, asm volatile("global_load_dword %0, %1, %2 sc1" VW0 : "=&v"(v) : "v"(O_X1C + ((__builtin_amdgcn_readlane(sto, ln) - O_ST2) >> 7) * 2048 + (col >> 1) * 4), "s"(ws) : "memory"));
      float x1 = (col & 1) ? bfhi(v) : bflo(v);
      float h2 = rbf(((x1 - __int_as_float(__builtin_amdgcn_readlane(__float_as_int(mean), ln))) * __int_as_float(__builtin_amdgcn_readlane(__float_as_int(rstd), ln))) * bf2f(svb[2][col]) + bf2f(svb[1][col]));
      float gb = bf2f(svb[3][col]), rsg = rbf(1.0f / (1.0f + __expf(-key_val(kk))));
      unsigned oo_ = (key_row(kk) * D + col) * 2, po = O_PART + ((lane >> 3) * 2048 + tok * 1024 + c0) * 4, p1 = po + 4 * 65536;
      asm volatile("" : "+v"(x1), "+v"(h2), "+v"(gb), "+v"(rsg), "+v"(oo_), "+v"(po), "+v"(p1));
      u32x4 y[7];
      nap_tick(td + F_T - 10);
      wait_tick(td + F_T);
      TS(37);
      P2GET(8192 + (min(lane, 55) * 16 + ((b - NPART) >> 1)) * 8);
      POLL2(max(max(max(max4(y[0]), max4(y[1])), max(max4(y[2]), max4(y[3]))), max(max(max4(y[4]), max4(y[5])), max4(y[6]))) == SENT || vd[1] == 0u, 0, PCAP,
            TS(38);
            asm volatile("global_load_dwordx4 %0, %7, %9 sc1\n\tglobal_load_dwordx4 %1, %7, %10 sc1\n\tglobal_load_dwordx4 %2, %7, %11 sc1\n\t"
                         "global_load_dwordx4 %3, %7, %12 sc1\n\tglobal_load_dwordx4 %4, %8, %9 sc1\n\tglobal_load_dwordx4 %5, %8, %10 sc1\n\t"
                         "global_load_dwordx4 %6, %8, %11 sc1"
                         : "=&v"(y[0]), "=&v"(y[1]), "=&v"(y[2]), "=&v"(y[3]), "=&v"(y[4]), "=&v"(y[5]), "=&v"(y[6])
                         : "v"(po), "v"(p1), "s"(wb), "s"(wb + 65536), "s"(wb + 131072), "s"(wb + 196608)
                         : "memory");
            asm volatile("s_waitcnt vmcnt(2)\n\tds_read_b64 %7, %8 offset:8\n\ts_waitcnt vmcnt(0) lgkmcnt(0)"
                         : "+v"(y[0]), "+v"(y[1]), "+v"(y[2]), "+v"(y[3]), "+v"(y[4]), "+v"(y[5]), "+v"(y[6]), "=&v"(vd) : "v"(sfa) : "memory"));
      TS(28);
#define YF(i) __builtin_bit_cast(f32x4, y[i])
      const f32x4 t = ((YF(0) + YF(1)) + (YF(2) + YF(3))) + ((YF(4) + YF(5)) + YF(6));
      const float sum = dpp_add<0x128>(swap16_add(swap32_add(t[0], t[2]), swap32_add(t[1], t[3])));
      asm volatile("global_store_short %0, %1, %2" ::"v"(oo_), "v"(pack2(x1 + gb * (wb == ws && kk < vd[1] ? h2 : rbf(h2 + rbf(rbf(sum) * rsg))), 0.f)), "s"(out) : "memory");
      TS(15);
    } else {
      break;
    }
    if (__builtin_expect(wb != ws || __all(vd[1] == t2), 1)) break;
    const unsigned tk = (lane & 8) ? vd[1] : vd[0], ns = tk == t1 ? l1 >> 3 : 8 + ((lane >> 3) & 1);
    rk2 = tk & 0xffffu, kmk = lane < 16 ? ~0u : 0u, one = 3u;
    xo_ = O_X1C + ns * 2048 + kc * 2, sto = O_ST2 + ns * 128 + (lane & 7) * 16;
    wb = ws + O_P2;
    }
    TSE();
  }
}

extern "C" void kernel_launch(void* const* d_in, const int* in_sizes, int n_in, void* d_out, int out_size, void* d_ws, size_t ws_size,
                              hipStream_t stream) {
  const u16* x = (const u16*)d_in[0];
  const u16* wsr = (const u16*)d_in[1];
  const u16* wsi = (const u16*)d_in[2];
  const u16* whr = (const u16*)d_in[3];
  const u16* whi = (const u16*)d_in[4];
  const u16* w_r = (const u16*)d_in[6];
  const u16* w_gate = (const u16*)d_in[7];
  const u16* w_up = (const u16*)d_in[8];
  const u16* w_down = (const u16*)d_in[9];
  const u16* c = (const u16*)d_in[10];
  const u16* w_mod = (const u16*)d_in[11];
  u16* out = (u16*)d_out;
  k_main<<<256, 512, 0, stream>>>(x, (char*)d_ws, wsr, wsi, c, w_mod, whr, whi, w_r, w_gate, w_up, w_down, out);
}
```

```cpp
#include <hip/hip_runtime.h>
#include <hip/hip_bf16.h>
#include <stdint.h>
#include <utility>

#define NEWLAY 1
typedef unsigned short u16;
typedef unsigned long long u64;
using bf16x8 = __attribute__((ext_vector_type(8))) short;
using bf16v8 = __attribute__((ext_vector_type(8))) __bf16;
using f32x4 = __attribute__((ext_vector_type(4))) float;
using f32x2 = __attribute__((ext_vector_type(2))) float;
using u32x4 = __attribute__((ext_vector_type(4))) unsigned;
using u32x2 = __attribute__((ext_vector_type(2))) unsigned;

constexpr int S = 2048, D = 1024, F = 3584;

__device__ __forceinline__ float bf2f(unsigned v) { return __uint_as_float(v << 16); }
__device__ __forceinline__ float bflo(unsigned v) { return __uint_as_float(v << 16); }
__device__ __forceinline__ float bfhi(unsigned v) { return __uint_as_float(v & 0xffff0000u); }
__device__ __forceinline__ unsigned f2bf(float f) {
  __hip_bfloat16 b = __float2bfloat16(f);
  return (unsigned)*reinterpret_cast<u16*>(&b);
}
__device__ __forceinline__ float rbf(float f) { return bf2f(f2bf(f)); }
using bf16v2 = __attribute__((ext_vector_type(2))) __bf16;
__device__ __forceinline__ unsigned pack2(float lo, float hi) {
  return __builtin_bit_cast(unsigned, __builtin_convertvector(f32x2{lo, hi}, bf16v2));
}

template <int CTRL>
__device__ __forceinline__ float dpp_f(float v) {
  return __int_as_float(__builtin_amdgcn_mov_dpp(__float_as_int(v), CTRL, 0xf, 0xf, true));
}
template <int CTRL>
__device__ __forceinline__ float dpp_add(float v) { return v + dpp_f<CTRL>(v); }
__device__ __forceinline__ float row_sum(float v) { return dpp_add<0x140>(dpp_add<0x141>(dpp_add<0x4E>(dpp_add<0xB1>(v)))); }
template <int CTRL>
__device__ __forceinline__ unsigned dpp_u(unsigned v) { return (unsigned)__builtin_amdgcn_mov_dpp((int)v, CTRL, 0xf, 0xf, true); }
__device__ __forceinline__ u64 mk64(unsigned hi, unsigned lo) { return ((u64)hi << 32) | lo; }
__device__ __forceinline__ float swap32_add(float a, float b) {
  auto r = __builtin_amdgcn_permlane32_swap(__float_as_uint(a), __float_as_uint(b), false, false);
  return __uint_as_float(r[0]) + __uint_as_float(r[1]);
}
__device__ __forceinline__ float swap16_add(float a, float b) {
  auto r = __builtin_amdgcn_permlane16_swap(__float_as_uint(a), __float_as_uint(b), false, false);
  return __uint_as_float(r[0]) + __uint_as_float(r[1]);
}
__device__ __forceinline__ float ar4(float v) {
  const float t = swap32_add(v, v);
  return swap16_add(t, t);
}
__device__ __forceinline__ float wave_sum_s(float v) {
  v = row_sum(v);
  v += __int_as_float(__builtin_amdgcn_update_dpp(0, __float_as_int(v), 0x142, 0xa, 0xf, false));
  v += __int_as_float(__builtin_amdgcn_update_dpp(0, __float_as_int(v), 0x143, 0xc, 0xf, false));
  return __int_as_float(__builtin_amdgcn_readlane(__float_as_int(v), 63));
}
__device__ __forceinline__ float silu_bf(float c) { return rbf(c * __builtin_amdgcn_rcpf(1.0f + __expf(-c))); }
__device__ __forceinline__ float dsum(unsigned v, float a) {
  return __builtin_amdgcn_fdot2_f32_bf16(__builtin_bit_cast(bf16v2, v), __builtin_bit_cast(bf16v2, 0x3f803f80u), a, false);
}
template <class Fn, int... I>
__device__ __forceinline__ void sfor_(Fn&& f, std::integer_sequence<int, I...>) { (f(std::integral_constant<int, I>{}), ...); }
template <int N, class Fn>
__device__ __forceinline__ void sfor(Fn&& f) { sfor_(f, std::make_integer_sequence<int, N>{}); }
constexpr unsigned SENT = 0xFFFFFFFFu;
constexpr int NPART = 224;

constexpr size_t WS_EPO = 0;
constexpr size_t WS_XFL = 4096;
constexpr size_t WS_MODP = 40960;
constexpr size_t WS_KEYG = 320u << 10;
constexpr size_t WS_UFL = WS_KEYG + 16384;
constexpr size_t WS_MOD = WS_KEYG + (80u << 10);
constexpr size_t WS_RS = WS_KEYG + (92u << 10);
constexpr size_t WS_PART = WS_KEYG + (332u << 10);
constexpr unsigned O_MODP = WS_MODP, O_XFL = WS_XFL, O_KEY = WS_KEYG, O_MOD = WS_MOD, O_RS = WS_RS, O_M2 = WS_RS + 80 * 1024;
constexpr unsigned O_PART = WS_PART, O_GU = O_PART + 56 * 8192, O_STAT = O_GU + 224 * 1024, O_X1C = O_STAT + 262144;
constexpr unsigned O_ST2 = O_X1C + 20480, O_VD = O_ST2 + 6144, O_P2 = 1536u << 10, O_F2 = 3584u << 10;
constexpr unsigned O_GC = O_M2 + 8704, O_VP = O_M2 + 12288, O_NP = O_VP + 65536, O_K2 = O_NP + 65536, O_QT = O_K2 + 4096, O_G2 = 3u << 20, O_G1 = O_G2 + 262144;
static_assert(O_QT + 16384 == O_PART && O_VD + 4096 <= O_KEY + (512 + 832) * 1024 && O_PART + O_P2 >= O_VD + 4096 && O_STAT + O_P2 <= O_G2, "armed area");
constexpr size_t WS_H = 4u << 20;
constexpr size_t WS_T = WS_H + (size_t)S * D * 2;
constexpr size_t WS_P = WS_T + (size_t)2 * D * S * 2;

#define TSG(base, i)
#define TSD(i)
#define PCAP (1 << 20)
#define PDBG(it)
#define TS(i)
#define TSE()
#define BT(k)
#define BTE()
#define BAR(k) do { BT(k); BARRIER(); } while (0)
#define WAIT_V(n) asm volatile("s_waitcnt vmcnt(%0)" ::"n"(n) : "memory")
#define WAIT_L(n) asm volatile("s_waitcnt lgkmcnt(%0)" ::"n"(n) : "memory")
#define BARRIER() asm volatile("s_barrier" ::: "memory")
#define LBAR() asm volatile("s_waitcnt lgkmcnt(0)\n\ts_barrier" ::: "memory")
#define DSR(dst, addr, off) asm volatile("ds_read_b128 %0, %1 offset:%2" : "=v"(dst) : "v"(addr), "n"(off) : "memory")
#define MFMA(c, a, b) asm volatile("v_mfma_f32_16x16x32_bf16 %0, %1, %2, %0" : "+v"(c) : "v"(a), "v"(b))
#define MFMA0(c, a, b) asm volatile("v_mfma_f32_16x16x32_bf16 %0, %1, %2, 0" : "=&v"(c) : "v"(a), "v"(b))
#define GLDS(m0v, voff, gbase) \
  asm volatile("s_mov_b32 m0, %0\n\ts_nop 0\n\tglobal_load_lds_dwordx4 %1, %2" ::"s"(m0v), "v"(voff), "s"(gbase) : "memory")
#define GLDS4(m0v, v, gbase)                                                                                                        \
  asm volatile("s_mov_b32 m0, %0\n\ts_nop 0\n\tglobal_load_lds_dwordx4 %1, %5\n\tglobal_load_lds_dwordx4 %2, %5 offset:1024\n\t"    \
               "global_load_lds_dwordx4 %3, %5 offset:2048\n\tglobal_load_lds_dwordx4 %4, %5 offset:3072" ::"s"(m0v),               \
               "v"((v)[0]), "v"((v)[1]), "v"((v)[2]), "v"((v)[3]), "s"(gbase) : "memory")
#define LDG(dst, voff, base) asm volatile("global_load_dwordx4 %0, %1, %2" : "=v"(dst) : "v"(voff), "s"(base) : "memory")
#define WPOL " nt"
#define LDM(dst, voff, base) asm volatile("global_load_dwordx4 %0, %1, %2" WPOL : "=v"(dst) : "v"(voff), "s"(base) : "memory")
#define LDX(dst, voff, base, off) asm volatile("global_load_dwordx4 %0, %1, %2 offset:%3" : "=v"(dst) : "v"(voff), "s"(base), "n"(off) : "memory")
#define LDXS(dst, voff, base, off) asm volatile("global_load_dwordx4 %0, %1, %2 offset:%3 sc1" : "=v"(dst) : "v"(voff), "s"(base), "n"(off) : "memory")
#define BF8(v) __builtin_bit_cast(bf16v8, v)
#define LSPIN(v, p, cond)                                   \
  _Pragma("unroll 1") for (int it_ = 0; it_ < (1 << 26); ++it_) { \
    v = __atomic_load_n(p, __ATOMIC_RELAXED);               \
    if (cond) break;                                        \
    __builtin_amdgcn_s_sleep(1);                            \
  }
#define POLL(ok, sl, reload)                                                  \
  if (__builtin_expect(!(ok), 0)) {                                           \
    _Pragma("unroll 1") for (int it = 0; it < PCAP; ++it) {                   \
      PDBG(it);                                                               \
      __builtin_amdgcn_s_sleep(sl);                                           \
      reload;                                                                 \
      if (ok) break;                                                          \
    }                                                                         \
  }
#define POLL2(bad, sl, cap, load)                                             \
  {                                                                           \
    int it = (cap);                                                           \
    u64 live;                                                                 \
    _Pragma("unroll 1") do {                                                  \
      if (sl) __builtin_amdgcn_s_sleep(sl);                                   \
      asm volatile("s_sub_u32 %0, %0, 1\n\ts_cselect_b64 %1, 0, -1" : "+s"(it), "=s"(live)::"scc"); \
      load;                                                                   \
    } while (__builtin_amdgcn_ballot_w64(bad) & live);                        \
    PDBG(PCAP - 2 - it);                                                      \
  }
__device__ __forceinline__ unsigned tick() {
  u64 t;
  asm volatile("s_memrealtime %0\n\ts_waitcnt lgkmcnt(0)" : "=s"(t)::"memory");
  return (unsigned)t;
}
__device__ __forceinline__ void wait_tick(unsigned t) {
#pragma unroll 1
  for (int i = 0; i < 256; ++i)
    if ((int)(tick() - t) >= 0) break;
}
__device__ __forceinline__ void nap_tick(unsigned t) {
#pragma unroll 1
  for (int i = 0; i < 1024; ++i) {
    if ((int)(tick() - t) >= 0) break;
    __builtin_amdgcn_s_sleep(2);
  }
}
#define T_C1 245
#define T_A 44
#define T_N 2500
#define T_D1 78
#define VW0 "\n\ts_waitcnt vmcnt(0)"
#define W8(a) "+v"(a[0]), "+v"(a[1]), "+v"(a[2]), "+v"(a[3]), "+v"(a[4]), "+v"(a[5]), "+v"(a[6]), "+v"(a[7])
#define PFL(ptr) asm volatile("s_mov_b32 m0, %0\n\ts_nop 0\n\tglobal_load_lds_dword %1, off" ::"s"(pfl), "v"(ptr) : "memory")

#define CPFL() asm volatile("s_mov_b32 m0, %0\n\ts_mov_b64 exec, 0xfff\n\tglobal_load_lds_dword %1, off\n\ts_mov_b64 exec, -1" ::"s"(pfl), "v"(cpf) : "memory")
#define HHOOK(j)
#define XPV 1557
#define XPW 1557
#define XPM (MODE ? XPW : XPV)
#define XPA0 (XPM / 1000)
#define XPA1 (XPM / 100 % 10)
#define XPB0 (XPM / 10 % 10)
#define XPB1 (XPM % 10)
#define U_X0 6
#define XLD(dst, XP, XH, i)                                                                                   \
  do {                                                                                                        \
    if (MODE) LDXS(dst, (i) ? xo1 : xo0, XP, ((XH)&3) * 2048 - 4096);                                         \
    else LDXS(dst, ((XH)&4) ? xo1 : xo0, XP, ((XH)&3) * 2048 + (i)*1024 - 4096);                              \
  } while (0)
#define HALF(XC, YA, RD, XN, XP, XH, LD, YP)                                                 \
  do {                                                                                       \
    _Pragma("unroll") for (int j = 0; j < 8; ++j) {                                          \
      if ((j & 1) == 0) { if (RD) WAIT_L(5); else WAIT_L(j == 0 ? 5 : 6 - j); }              \
      if (MODE == 2) MFMA0(acc[0][j], Y[j], XC[0]); else if (MODE) MFMA(acc[0][j], Y[j], XC[0]); else MFMA(acc[0][j], XC[0], Y[j]); \
      if (j == 0) DSR(Y[7], YP, 7 * 2048);                                                   \
      else if (RD) DSR(Y[j - 1], YA, (j - 1) * 2048);                                        \
      if (MODE == 2) MFMA0(acc[1][j], Y[j], XC[1]); else if (MODE) MFMA(acc[1][j], Y[j], XC[1]); else MFMA(acc[1][j], XC[1], Y[j]); \
      if ((LD) && j == (((XH)&1) ? XPA0 : XPB0)) XLD(XN[0], XP, XH, 0);                                             \
      if ((LD) && j == (((XH)&1) ? XPA1 : XPB1)) XLD(XN[1], XP, XH, 1);                                             \
      HHOOK(j);                                                                              \
    }                                                                                        \
  } while (0)

__device__ __forceinline__ int key_row(unsigned k) { return (int)((~k >> 1) & 2047u); }
__device__ __forceinline__ unsigned mkkey(float v) {
  const unsigned rb = pack2(0.f, v);
  return rb ^ ((unsigned)((int)rb >> 31) & 0x7fff0000u) ^ 0x80000000u;
}
__device__ __forceinline__ float key_val(unsigned k) {
  const unsigned ord = k >> 16;
  return bf2f(ord ^ ((ord & 0x8000u) ? 0x8000u : 0xffffu));
}
__device__ __forceinline__ unsigned nsent(float v) { return min(__float_as_uint(v), SENT - 1u); }
__device__ __forceinline__ f32x4 lo4(u32x4 q) { return f32x4{bflo(q[0]), bfhi(q[0]), bflo(q[1]), bfhi(q[1])}; }
__device__ __forceinline__ f32x4 hi4(u32x4 q) { return f32x4{bflo(q[2]), bfhi(q[2]), bflo(q[3]), bfhi(q[3])}; }
__device__ __forceinline__ u32x4 pk8(f32x4 a, f32x4 b) { return u32x4{pack2(a[0], a[1]), pack2(a[2], a[3]), pack2(b[0], b[1]), pack2(b[2], b[3])}; }
__device__ __forceinline__ u32x4 nsent4(u32x4 v) { return u32x4{min(v[0], SENT - 1u), min(v[1], SENT - 1u), min(v[2], SENT - 1u), min(v[3], SENT - 1u)}; }
__device__ __forceinline__ unsigned max4(u32x4 v) { return max(max(v[0], v[1]), max(v[2], v[3])); }
__device__ __forceinline__ float amin16(const u32x4* v) {
  float a, b;
  asm("v_min3_f32 %0, |%2|, |%3|, |%4|\n\tv_min3_f32 %1, |%5|, |%6|, |%7|\n\tv_min3_f32 %0, |%8|, |%9|, %0\n\tv_min3_f32 %1, |%10|, |%11|, %1\n\t"
      "v_min3_f32 %0, |%12|, |%13|, %0\n\tv_min3_f32 %1, |%14|, |%15|, %1\n\tv_min3_f32 %0, |%16|, |%17|, %0\n\tv_min_f32 %0, %0, %1"
      : "=&v"(a), "=&v"(b)
      : "v"(v[0][0]), "v"(v[0][1]), "v"(v[0][2]), "v"(v[0][3]), "v"(v[1][0]), "v"(v[1][1]), "v"(v[1][2]), "v"(v[1][3]), "v"(v[2][0]), "v"(v[2][1]),
        "v"(v[2][2]), "v"(v[2][3]), "v"(v[3][0]), "v"(v[3][1]), "v"(v[3][2]), "v"(v[3][3]));
  return a;
}
__device__ __forceinline__ u32x4 selu(unsigned mk, u32x4 a, u32x4 b) { return (a & mk) | (b & ~mk); }
#define KSTR 16
#define TOP2(CTRL)                                                  \
  do {                                                              \
    const unsigned o1 = dpp_u<CTRL>(t1), o2 = dpp_u<CTRL>(t2);      \
    t2 = max(min(t1, o1), max(t2, o2));                             \
    t1 = max(t1, o1);                                               \
  } while (0)
#define TOP2S(SW)                                                   \
  do {                                                              \
    auto a_ = SW(t1, t1, false, false), b_ = SW(t2, t2, false, false); \
    t2 = max(min(a_[0], a_[1]), max(b_[0], b_[1]));                 \
    t1 = max(a_[0], a_[1]);                                         \
  } while (0)
#define T_C3 253
#define T_C4 340
#define E_D 6
#define O_DW 6
#define O_DS 4
#define OPOL " sc1"
#define X_PA 2
#define G1_C 2
#define R_AT 24
#define B_TK 7
#define W_T 4
#define V_K 3
#define V_T 5
#define N_K 9
#define N_D 2
#define T_K 0
#define H_AT 23
#define G1_S 1
#define F_T 142
#define K_S 1
#define N_C (V_T + 1)
#define T_O 256
#define A_S1 9
#define A_S2 9
#define A_P2 12
#define X_K 13
#define H_P 2
#define H_O 2
#define S_R 2
#define T_X 80
#define A_PS 3
#define U_S 4
#define C_LD 2
#define C_DP 8
#define F_D 2
#define F_AT 29
#define PW0 2
#define T_W 70
#define P_D2 1
#define P_D4 1
#define XSTR 16
#define P_D3 20
#ifndef M_NS
#define M_NS 6
#endif
#define A_Q1 27
#define B_RAT 5
#define B_MAT 13
#define U_D 4
#define FSTR 16
#define B_J 4
#define RETIRE()                                                                                                              \
  do {                                                                                                                        \
    if (hw == 3 && lane < (b == 255 ? 2 : 1))                                                                                 \
      asm volatile("global_store_dwordx2 %0, %1, %2 sc1" ::"v"(lane ? (unsigned)WS_EPO : O_XFL + b * XSTR * 4), "v"(lane ? tag : ~tag), "s"(ws) : "memory"); \
  } while (0)
#define U_N1 3
#define B_QAT 12
#define B_QD 2
#define TP_PS 5
#define TP_XC 34
#define TP_P0 2
#define TP_X0 24
#define TP_XL 54
#define W_UO 3
#define CPF_K 24
#define CPF_B 8
#define B_WN 4
#define B_WK 15
struct BSched {
  int cnt[16], rcnt, mcnt, qcnt, vcnt, ncnt;
  constexpr BSched() : cnt{}, rcnt(0), mcnt(0), qcnt(0), vcnt(0), ncnt(0) {
    int pos[16] = {}, n = 0, rpos = 0, mpos = 0, qpos = 0, npos = 0;
    for (int k = 0; k <= B_J - 2 + M_NS; ++k) pos[k] = -8 * (B_J - 2 + M_NS - k);
    for (int k = B_J + 1; k < 16; ++k) {
      cnt[k] = n - pos[k] > 63 ? 63 : n - pos[k];
      if (k - 2 + M_NS < 16) { n += 4; pos[k - 2 + M_NS] = n; }
      if (k == B_RAT + 2) rcnt = n - rpos;
      if (k == B_QAT + B_QD) qcnt = n - qpos;
      if (k == V_T) vcnt = n;
      if (k == N_K + N_D) ncnt = n - npos;
      if (k == N_K) npos = n;
      if (k == B_RAT) rpos = n;
      if (k == X_K) n += 4;
      if (k == B_QAT) { n += 1; qpos = n; }
      if (k == B_MAT) { n += 2; mpos = n; }
      if (k == B_WK) n += B_WN;
    }
    mcnt = n - mpos > 63 ? 63 : n - mpos;
  }
};
__global__ __launch_bounds__(512, 2) void k_main(const u16* __restrict__ x, char* ws, const u16* __restrict__ Y0, const u16* __restrict__ Y1,
                                              const u16* __restrict__ cvec, const u16* __restrict__ wmod, const u16* __restrict__ X0,
                                              const u16* __restrict__ X1, const u16* __restrict__ wr_, const u16* __restrict__ wg,
                                              const u16* __restrict__ wu, const u16* __restrict__ wd, u16* __restrict__ out) {
  constexpr int DX = 4, NS = M_NS, STG = NS * 16384, SPARE = STG + 32768;
  constexpr int LST = SPARE + 17664, SLT = LST + 256, XC = SLT + 512, VT = XC + 4096;
  __shared__ __attribute__((aligned(1024))) char shm[VT + 256];
  u16(*svb)[1024] = reinterpret_cast<u16(*)[1024]>(shm + SPARE);
  float* cwj = reinterpret_cast<float*>(shm + SPARE + 10240);
  unsigned* ssel = reinterpret_cast<unsigned*>(shm + SPARE + 10368);
  float* smod = reinterpret_cast<float*>(shm + SPARE + 15872);
  u16* ssa = reinterpret_cast<u16*>(smod);
  constexpr int TAB = SPARE + 10752, RKA = TAB + 1536, RKB = TAB + 3584;
  u16(*scw)[128] = reinterpret_cast<u16(*)[128]>(shm + TAB + 512);
  const int b = blockIdx.x, xcd = b & 7, j8 = b >> 3;
  const int cc = xcd >> 2, nt = (xcd & 3) * 4 + (j8 & 3), mt = j8 >> 2, et = mt;
  const int tid = threadIdx.x, lane = tid & 63;
  const int wid = __builtin_amdgcn_readfirstlane(tid >> 6);
  const unsigned lds0 = (unsigned)(size_t)(__attribute__((address_space(3))) char*)shm;
  const char* cpf;
  {
    unsigned long long pc;
    unsigned len;
    asm volatile("s_getpc_b64 %0\n.Lcpf_here:\n\ts_mov_b32 %1, .Lcpf_end-.Lcpf_here-4\n\t.subsection 1\n.Lcpf_end:\n\t.subsection 0" : "=s"(pc), "=s"(len));
    cpf = reinterpret_cast<const char*>(pc) + min((unsigned)((lane * 32 + j8) * 128), len);
  }
  const unsigned pfl = lds0 + SPARE + 17408;
  if (wid == 4) CPFL();
  u32x4 xv[2];
  {
    const unsigned xo = (unsigned)((b * 8 + wid) * D + lane * 8) * 2;
    asm volatile("global_load_dwordx4 %0, %2, %3\n\tglobal_load_dwordx4 %1, %2, %3 offset:1024" : "=&v"(xv[0]), "=&v"(xv[1]) : "v"(xo), "s"(x) : "memory");
  }
  asm volatile("" ::"s"(ws), "s"(Y0), "s"(Y1), "s"(cvec), "s"(wmod), "s"(wr_));
  u16* xn = reinterpret_cast<u16*>(ws + WS_H);
  u16* U = reinterpret_cast<u16*>(ws + WS_T);
  unsigned* part = reinterpret_cast<unsigned*>(ws + WS_PART);
  const int g = (b & 7) * 28 + (b >> 3);
  unsigned* ufl = reinterpret_cast<unsigned*>(ws + WS_UFL) + (cc * 16 + nt) * 32 * FSTR;
  unsigned* epo = reinterpret_cast<unsigned*>(ws + WS_EPO);
#define SKEWPT()
  const int g4 = lane >> 4;
  u64 tag0, tag;
#define TAGW(n) asm volatile("s_waitcnt vmcnt(%2)\n\tv_lshl_add_u64 %0, %1, 0, 1" : "=v"(tag) : "v"(tag0), "n"(n) : "memory")
  u32x4 wv[8], cq;
  const char* pfy = reinterpret_cast<const char*>(cc ? Y1 : Y0) + (size_t)((xcd & 3) * 512 + j8 * 16 + (lane & 15)) * 4096 + g4 * 128;
#define PFG(g) (pfy + ((((g) + xcd) & 7) << 9))
  const int u = b * 2 + (wid & 1), cg0 = u & 31, ks0 = u >> 5;
  unsigned ta = lds0 + STG + ((lane >> 2) & 3) * 2368 + (wid * 128 + (lane >> 5) * 32 + (lane & 3) * 8 + (g4 & 1) * 4) * 2;
  unsigned oo = (unsigned)((wid * 4 + (lane >> 5)) * 131072 + (b >> 2) * 2048 + (g4 & 1) * 1024 + ((b & 3) * 16 + (lane & 15)) * 16), oo2 = oo + 262144;
  asm volatile("" : "+v"(ta), "+v"(oo), "+v"(oo2));
  unsigned wa = lds0 + STG + wid * 2368 + lane * 16;
  asm volatile("" : "+v"(wa));
  const unsigned wo = (unsigned)((ks0 * 64 + (lane >> 3) * 8) * 6144 + 2048 + (cg0 >> 4) * 1024 + cg0 * 64 + (lane & 7) * 8) * 2;
  if (wid == 0) TSG(part, 0);
  BT(50);
  if (wid >= 3) {
    asm volatile("global_load_dwordx2 %0, %1, off sc1" : "=v"(tag0) : "v"(epo) : "memory");
    if (wid >= 6) {
      LDG(cq, (ks0 * 64 + g4 * 16 + (lane & 1) * 8) * 2, cvec);
#pragma unroll
      for (int i = 0; i < 8; ++i) LDM(wv[i], wo + i * 12288, wmod);
      const unsigned av = u < 80 ? 0u : ~0u;
      asm volatile("global_store_dwordx4 %0, %1, %2 sc1\n\ts_nop 1" ::"v"(O_KEY + u * 1024 + lane * 16), "v"(u32x4{av, av, av, av}), "s"(ws) : "memory");
      WAIT_V(11);
    } else if (wid == 4) {
      __builtin_amdgcn_s_sleep(C_DP);
      PFL(PFG(0));
      WAIT_V(2);
    } else {
      WAIT_V(1);
    }
  } else {
    WAIT_V(0);
  }
  {
    asm volatile("" : "+v"(xv[0]), "+v"(xv[1])::"memory");
    if (wid == 1) TSG(part, 2);
    BT(51);
    const float s0 = dsum(xv[0][3], dsum(xv[0][2], dsum(xv[0][1], dsum(xv[0][0], 0.f))));
    const float s1 = dsum(xv[1][3], dsum(xv[1][2], dsum(xv[1][1], dsum(xv[1][0], 0.f))));
    const float mf = wave_sum_s(s0 + s1) * (1.0f / D), m = rbf(mf);
    const f32x4 d0 = lo4(xv[0]) - m, d1 = hi4(xv[0]) - m, d2 = lo4(xv[1]) - m, d3 = hi4(xv[1]) - m;
    const f32x4 q = (d0 * d0 + d1 * d1) + (d2 * d2 + d3 * d3);
    const float vf = wave_sum_s((q[0] + q[1]) + (q[2] + q[3])) * (1.0f / D) - (mf - m) * (mf - m);
    const float rden = __builtin_amdgcn_rcpf(rbf(__builtin_amdgcn_sqrtf(rbf(rbf(vf) + 1e-5f))));
    asm volatile("ds_write_b128 %0, %1" ::"v"(wa), "v"(pk8(d0 * rden, d1 * rden)) : "memory");
    asm volatile("ds_write_b128 %0, %1 offset:1024" ::"v"(wa), "v"(pk8(d2 * rden, d3 * rden)) : "memory");
    if (wid == 1) TSG(part, 12);
    BT(52);
    LBAR();
    if (wid == 1) TSG(part, 10);
    BT(53);
    u32x2 t[4];
    asm volatile(
        "ds_read_b64_tr_b16 %0, %4\n\tds_read_b64_tr_b16 %1, %4 offset:%5\n\t"
        "ds_read_b64_tr_b16 %2, %4 offset:128\n\tds_read_b64_tr_b16 %3, %4 offset:%6\n\ts_waitcnt lgkmcnt(0)"
        : "=&v"(t[0]), "=&v"(t[1]), "=&v"(t[2]), "=&v"(t[3])
        : "v"(ta), "n"(4 * 2368), "n"(4 * 2368 + 128)
        : "memory");
    asm volatile("global_store_dwordx4 %0, %2, %4 sc1\n\tglobal_store_dwordx4 %1, %3, %4 sc1\n\ts_nop 1" ::"v"(oo), "v"(oo2),
                 "v"(u32x4{t[0][0], t[0][1], t[1][0], t[1][1]}), "v"(u32x4{t[2][0], t[2][1], t[3][0], t[3][1]}), "s"(xn)
                 : "memory");
    if (wid == 2) TSG(part, 14);
    BT(54);
  }
  u32x2 vd;
#define VDLD(sl) POLL2(max(vd[0], vd[1]) == SENT, sl, PCAP, asm volatile("global_load_dwordx2 %0, %1, %2 sc1" VW0 : "=&v"(vd) : "v"(O_VD + (b & 31) * 128), "s"(ws) : "memory"))
  if (wid < 4) {
    const unsigned ya0 = lds0 + (lane & 15) * 128 + (g4 ^ (lane & 7)) * 16;
    bf16x8 X[DX][2][2], Y[8];
    f32x4 acc[2][8];
    {
      constexpr int MODE = 0;
      const unsigned xo0 = lane * 16 + 4096, xo1 = xo0 + 8192;
      const char* xb = reinterpret_cast<const char*>(xn) + (size_t)(mt * 4 + wid) * 131072;
#define XB(i) (xb + (((xcd + (i)) & 7) << 14))
#pragma unroll
      for (int i = 0; i < 2; ++i)
#pragma unroll
        for (int j = 0; j < 8; ++j) acc[i][j] = f32x4{0.f, 0.f, 0.f, 0.f};
      asm volatile("" : W8(acc[0]), W8(acc[1]));
      __builtin_amdgcn_s_setprio(1);
      WAIT_V(0);
      if (wid == 0) TSG(part, 1);
      const char* xpb = XB(0);
      BAR(55);
      if (wid == 3) {
        TAGW(0);
        const unsigned fp = O_XFL + ((xcd * 32 + lane) & 255) * XSTR * 4;
        if (P_D3) __builtin_amdgcn_s_sleep(P_D3);
#pragma unroll 1
        for (int it = 0; it < PCAP; ++it) { PDBG(it);
          u64 f;
          if (it < 2) BT(58 + 2 * it);
          asm volatile("global_load_dwordx2 %0, %1, %2 sc1\n\ts_waitcnt vmcnt(0)" : "=&v"(f) : "v"(fp), "s"(ws) : "memory");
          if (it < 2) BT(59 + 2 * it);
          if (__all(f == tag)) break;
          __builtin_amdgcn_s_sleep(P_D4);
        }
      }
      BAR(0);
      if (wid == 1) TSG(part, 3);
#pragma unroll
      for (int h = 0; h < 7; ++h) {
        XLD(X[h >> 1][h & 1][0], xpb, h, 0);
        XLD(X[h >> 1][h & 1][1], xpb, h, 1);
      }
#pragma unroll
      for (int j = 0; j < 7; ++j) DSR(Y[j], ya0, j * 2048);
      unsigned so = 0, a0 = ya0;
      f32x4 c0, c1;
#pragma unroll 1
      for (int it = 0; it < 7; ++it) {
        const char* xpa = xpb;
        xpb = XB(it + 1);
#pragma unroll
        for (int s = 0; s < 4; ++s) {
          const unsigned a1 = (ya0 ^ 64) + so;
          so = so == (NS - 1) * 16384 ? 0 : so + 16384;
          WAIT_V(12);
          HALF(X[s][0], a1, true, X[(s + 3) & 3][1], (s == 0 ? xpa : xpb), (2 * s + 7) & 7, true, a0);
          a0 = ya0 + so;
          BAR(1 + it * 4 + s);
          WAIT_V(12);
          HALF(X[s][1], a0, true, X[s][0], xpb, 2 * s, true, a1);
        }
      }
#pragma unroll
      for (int s = 0; s < 4; ++s) {
        const unsigned a1 = (ya0 ^ 64) + so;
        so = so == (NS - 1) * 16384 ? 0 : so + 16384;
        WAIT_V(s == 0 ? 12 : 14 - 4 * s);
        HALF(X[s][0], a1, true, X[3][1], xpb, 7, s == 0, a0);
        a0 = ya0 + so;
        if (s < 3) BAR(29 + s);
        WAIT_V(12 - 4 * s);
        if (s == 2) {
          DSR(c0, lds0 + SPARE + 15872 + 512 + wid * 128 + g4 * 32, 0);
          DSR(c1, lds0 + SPARE + 15872 + 512 + wid * 128 + g4 * 32, 16);
        }
        if (s < 3) HALF(X[s][1], a0, true, X[s][0], xpb, 0, false, a1);
      }
      const char* up = reinterpret_cast<const char*>(U) + (size_t)cc * 4194304 + (size_t)(nt * 4) * 65536 + (mt * 4 + wid) * 2048;
      const unsigned ul = lds0 + STG + wid * 1024 + lane * 16;
#define UEP(j)                                                                                                                             \
  do {                                                                                                                                     \
    u32x4 v_;                                                                                                                              \
    asm volatile("v_pk_mul_f32 %0, %0, %4\n\tv_pk_mul_f32 %1, %1, %5\n\tv_pk_mul_f32 %2, %2, %6\n\tv_pk_mul_f32 %3, %3, %7"            \
                 : "+v"(acc[0][j].lo), "+v"(acc[0][j].hi), "+v"(acc[1][j].lo), "+v"(acc[1][j].hi)                                          \
                 : "v"(c0.lo), "v"(c0.hi), "v"(c1.lo), "v"(c1.hi));                                                                        \
    asm volatile("v_cvt_pk_bf16_f32 %0, %4, %5\n\tv_cvt_pk_bf16_f32 %1, %6, %7\n\tv_cvt_pk_bf16_f32 %2, %8, %9\n\tv_cvt_pk_bf16_f32 %3, %10, %11" \
                 : "=&v"(v_[0]), "=&v"(v_[1]), "=&v"(v_[2]), "=&v"(v_[3])                                                                  \
                 : "v"(acc[0][j][0]), "v"(acc[0][j][1]), "v"(acc[0][j][2]), "v"(acc[0][j][3]), "v"(acc[1][j][0]), "v"(acc[1][j][1]),       \
                   "v"(acc[1][j][2]), "v"(acc[1][j][3]));                                                                                  \
    asm volatile("global_store_dwordx4 %0, %2, %3 offset:%4 sc1\n\tds_write_b128 %1, %2 offset:%5\n\ts_nop 0"                          \
                 ::"v"(lane * 16), "v"(ul), "v"(v_), "s"(up + ((j) >> 1) * 65536), "n"(((j)&1) * 1024), "n"((j)*4096) : "memory");         \
  } while (0)
      {
        const unsigned a1 = (ya0 ^ 64) + (31 % NS) * 16384;
        DSR(Y[7], a1, 7 * 2048);
        WAIT_L(6);
#pragma unroll
        for (int j = 0; j < 8; ++j) {
          if (j == 2) WAIT_L(0);
          MFMA(acc[0][j], X[3][1][0], Y[j]);
          MFMA(acc[1][j], X[3][1][1], Y[j]);
          if (j >= 2) UEP(j - 2);
        }
      }
      if (wid == 0) TSG(part, 4);
      TS(0);
      SKEWPT();
      asm volatile("s_nop 3");
#pragma unroll
      for (int j = 6; j < 8; ++j) UEP(j);
    }
    const int rg = nt * 8 + wid * 2 + cc, row0 = rg * 16 + (lane & 15), col0 = et * 128 + g4 * 8;
    u32x4 xq[4], pv[4], sa4[4];
    f32x4 ga[8];
    f32x2 cj;
    float sw, ce;
    u32x4 dk;
    unsigned spo, soo, th, slt, slf, sla, tha, xca, sca;
    u32x4 e0, e1;
    const char* xr = reinterpret_cast<const char*>(xn) + mt * 524288 + (cc * 16 + nt) * 16384 + wid * 4096;
#define XCHLD()                                                                                                     \
  asm volatile(                                                                                                     \
      "global_load_dwordx4 %0, %4, %5 sc1\n\tglobal_load_dwordx4 %1, %4, %5 offset:1024 sc1\n\t"                   \
      "global_load_dwordx4 %2, %4, %5 offset:2048 sc1\n\tglobal_load_dwordx4 %3, %4, %5 offset:3072 sc1"            \
      : "=&v"(pv[0]), "=&v"(pv[1]), "=&v"(pv[2]), "=&v"(pv[3])                                                      \
      : "v"(lane * 16), "s"(xr)                                                                                     \
      : "memory")
    {
      constexpr int MODE = 1;
      const char* ub = reinterpret_cast<const char*>(U) + (size_t)cc * 4194304 + (size_t)(nt * 4 + wid) * 65536;
      const unsigned xo0 = lane * 16 + cc * 1024 + 4096, xo1 = xo0 ^ 1024;
      BT(32);
      LBAR();
      if (wid == 1) TSG(part, 6);
      {
        const unsigned sa = lds0 + STG + wid * 8192 + lane * 16 + cc * 4096;
#pragma unroll
        for (int q = 0; q < 8; ++q) DSR(X[(q >> 1) & 1][q & 1][q >> 2], (q >> 2) ? sa ^ 4096 : sa, (q & 3) * 1024);
        const unsigned a0 = ya0 + (32 % NS) * 16384;
#pragma unroll
        for (int j = 0; j < 7; ++j) DSR(Y[j], a0, j * 2048);
      }
#define UFLAG()                                                                                                         \
  do {                                                                                                                  \
    WAIT_V(0);                                                                                                          \
    if (lane == 0) __hip_atomic_store(ufl + (mt * 4 + wid) * FSTR, 1u, __ATOMIC_RELAXED, __HIP_MEMORY_SCOPE_AGENT);     \
    if (wid == 0) TSG(part, 5);                                                                                         \
  } while (0)
#define XP(h) (ub + (((mt + ((h) >> 2)) & 7) << 13))
#define XS(h) X[((h) >> 1) & 3][(h)&1]
#define XW(h) do { if ((h) >= 4) WAIT_V(2 * (((h) + 6 > 31 ? 31 : (h) + 6) - (h)) ); } while (0)
      constexpr int H0 = 24, NP = 64, NE = 2 * NP + 8;
      const char* xw;
      unsigned c128, xoff[4], rka, rkb, ea, eb, cwa, cwu;
      auto ESET = [&]() __attribute__((always_inline)) {
        int ln_ = lane;
        asm volatile("" : "+v"(ln_));
        const int lane = ln_, g4 = lane >> 4, row0 = rg * 16 + (lane & 15), col0 = et * 128 + g4 * 8;
        xw = reinterpret_cast<const char*>(xn) + mt * 524288 + ((cc ^ 1) * 16 + nt) * 16384 + wid * 4096;
        asm volatile("" : "+s"(xw), "+s"(xr));
        c128 = 0x43000000u;
        rka = lds0 + RKA + (lane & 15) * 16, rkb = lds0 + RKB + (g4 ? 1024 : wid * 256 + lane * 16);
        ea = lds0 + TAB + g4 * 32, eb = lds0 + TAB + 512 + (lane & 3) * 256 + g4 * 16;
#pragma unroll
        for (int q = 0; q < 4; ++q) {
          dk[q] = (lane & 3) == q ? ~0u : 0u;
          xoff[q] = (lds0 + STG + wid * 8192 + (lane & 15) * 256 + ((g4 ^ lane) & 15) * 16) ^ (q * 64);
        }
        spo = O_STAT + rg * 2048 + g4 * 512 + (lane & 15) * 16, soo = O_STAT + rg * 2048 + et * 256 + (lane & 15) * 16;
        cwa = lds0 + SPARE + 10240 + g4 * 8, cwu = lds0 + SPARE + 10240 + et * 4;
        const unsigned one = g4 == ((lane >> 2) & 3) ? ((lane & 1) ? 0x1f800000u : 0x3f80u) : 0u;
        e0 = u32x4{(lane & 2) ? 0u : one, (lane & 2) ? one : 0u, 0u, 0u}, e1 = u32x4{0u, 0u, e0[0], e0[1]};
        tha = lds0 + SPARE + 10368 + 24;
        sla = lds0 + SLT + (row0 & 127) * 4;
        xca = O_X1C - 2048 + col0 * 2, sca = O_ST2 - 128 + et * 16;
        asm volatile("" : "+v"(dk), "+v"(spo), "+v"(soo), "+v"(cwa), "+v"(cwu), "+v"(tha), "+v"(sla), "+v"(xca), "+v"(sca), "+v"(e0), "+v"(e1), "+v"(c128));
        asm volatile("" : "+v"(xoff[0]), "+v"(xoff[1]), "+v"(xoff[2]), "+v"(xoff[3]), "+v"(rka), "+v"(rkb), "+v"(ea), "+v"(eb));
      };
#pragma unroll
      for (int s = 0; s < H0 / 2; ++s) {
        const unsigned a0 = ya0 + ((32 + s) % NS) * 16384, a1 = a0 ^ 64, an = ya0 + ((33 + s) % NS) * 16384;
        XW(2 * s);
        if (s == 0) {
          constexpr int MODE = 2;
          HALF(XS(0), a1, true, XS(7), XP(7), 7, false, a0);
          UFLAG();
        } else {
          HALF(XS(2 * s), a1, true, XS(2 * s + 7), XP(2 * s + 7), 2 * s + 7, 2 * s >= 4, a0);
        }
        if (s == 1) ESET();
        BAR(33 + s);
        if (s == 1) {
          if (wid == 1) TSG(part, 7);
          TS(4);
#pragma unroll
          for (int h = 4; h < U_X0; ++h) {
            XLD(XS(h)[0], XP(h), h, 0);
            XLD(XS(h)[1], XP(h), h, 1);
          }
        }
        XW(2 * s + 1);
#undef HHOOK
#define HHOOK(j) if (s == 1 && j + 3 >= U_X0) { XLD(XS(j + 3)[0], XP(j + 3), j + 3, 0); XLD(XS(j + 3)[1], XP(j + 3), j + 3, 1); }
        HALF(XS(2 * s + 1), an, true, XS(2 * s + 8), XP(2 * s + 8), 2 * s + 8, 2 * s + 1 >= 5, a1);
#undef HHOOK
#define HHOOK(j)
      }
      u32x4 ov;
      bf16x8 xk;
#define EH(e) (H0 + (((e) < NP ? (e) : (e) - NP - 8) >> 3))
#define EA(e) ((ya0 ^ ((EH(e) & 1) * 64)) + ((32 + (EH(e) >> 1)) % NS) * 16384)
      sfor<NE>([&](auto nn) __attribute__((always_inline)) {
        constexpr int n = decltype(nn)::value, h = EH(n), j = n & 7, m = n - NP - TP_P0, e = n + 7, z = n - (NE - 8);
        (void)sa4, (void)eb, (void)ga, (void)ea, (void)xk, (void)rka, (void)rkb, (void)xq, (void)xoff, (void)ov, (void)xw, (void)x;
        (void)cj, (void)sw, (void)ce, (void)cwa, (void)cwu, (void)th, (void)tha, (void)slt, (void)sla, (void)slf, (void)c128;
        if (n == 0) TS(2);
        if (n == NP) { TS(3); SKEWPT(); }
        if (n < NP && j == 0) {
          if ((h & 1) && h < 31) BAR(33 + (h >> 1));
          XW(h);
        }
        if (n == NP + TP_X0) { TS(14); BAR(48); TS(11); }
        if (n == NP + TP_XL) XCHLD();
        if constexpr ((j & 1) == 0) WAIT_L(z <= 0 ? 5 : 6 - z + 2 * z);
        if constexpr (n < NP) MFMA(acc[1][j], Y[j], XS(h)[1]);
        else if constexpr (n < NP + 8) MFMA(acc[0][j], Y[j], xk);
        else MFMA(acc[0][j], Y[j], XS(h)[0]);
        if constexpr (n == NP - 16) DSR(xk, rkb, 0);
        if constexpr (e >= NP && e < NP + 8) DSR(Y[e & 7], rka, (e & 7) * 256);
        else if constexpr (e < NE) DSR(Y[e & 7], EA(e), (e & 7) * 2048);
        if constexpr (n == 2 || n == 4) XLD(XS(31)[(j >> 1) - 1], XP(31), 31, (j >> 1) - 1);
        if constexpr (m >= TP_XC - 11 && m < TP_XC - 7) DSR(xq[m - TP_XC + 11], xoff[m - TP_XC + 11], 4096);
        if constexpr (m >= 0 && m < 4 * TP_PS) {
          constexpr int q = m / TP_PS, r = m % TP_PS;
#pragma unroll
          for (int u = (r * 13 + TP_PS - 1) / TP_PS; u < ((r + 1) * 13 + TP_PS - 1) / TP_PS; ++u) {
#define AO(i) acc[1][2 * q + ((u >> 1) & 1)][(u & 1) * 2 + (i)]
            if (u < 4) asm volatile("v_and_or_b32 %0, %1, %2, %3" : "=v"(ov[u & 3]) : "v"(AO(1)), "s"(0x80000000u), "v"(c128));
            else if (u < 8) asm volatile("v_fmamk_f32 %0, %1, 0x5f800000, %0" : "+v"(ov[u & 3]) : "v"(AO(1)));
            else if (u < 12) asm volatile("v_cvt_pk_bf16_f32 %0, %1, %0" : "+v"(ov[u & 3]) : "v"(AO(0)));
            else asm volatile("global_store_dwordx4 %0, %1, %2 offset:%3 sc1\n\ts_nop 1" ::"v"(lane * 16), "v"(ov), "s"(xw), "n"(q * 1024) : "memory");
          }
          if (m == 4 * TP_PS - 1) TS(10);
        }
        if constexpr (m == TP_XC) asm volatile("" : "+v"(xq[0]), "+v"(xq[1]), "+v"(xq[2]), "+v"(xq[3]));
        if constexpr (m >= TP_XC && m < TP_XC + 32) {
          constexpr int c = m - TP_XC;
          if constexpr (c & 1) asm volatile("v_and_b32 %0, 0xffff0000, %1" : "=v"(acc[1][c >> 2][c & 3]) : "v"(xq[c >> 3][(c >> 1) & 3]));
          else asm volatile("v_lshlrev_b32 %0, 16, %1" : "=v"(acc[1][c >> 2][c & 3]) : "v"(xq[c >> 3][(c >> 1) & 3]));
        }
        if constexpr (z >= 0 && z < 4) {
          DSR(ga[2 * z], ea, z * 128);
          DSR(ga[2 * z + 1], ea, z * 128 + 16);
        } else if constexpr (z >= 4 && z < 6) {
          DSR(sa4[2 * z - 8], eb, (2 * z - 8) * 64);
          DSR(sa4[2 * z - 7], eb, (2 * z - 7) * 64);
        } else if constexpr (z == 6) {
          asm volatile("ds_read_b64 %0, %2\n\tds_read_b32 %1, %3" : "=&v"(cj), "=&v"(ce) : "v"(cwa), "v"(cwu) : "memory");
        } else if constexpr (z == 7) {
          asm volatile("ds_read_b32 %0, %4 offset:32\n\tds_read_b32 %1, %5 offset:4\n\tds_read_b32 %3, %5 offset:56\n\tds_read_b32 %2, %6"
                       : "=&v"(sw), "=&v"(th), "=&v"(slt), "=&v"(slf) : "v"(cwa), "v"(tha), "v"(sla) : "memory");
        }
      });
    }
    if (wid == 0) TSG(part, 8);
    TS(13);
    BT(49);
    BTE();
    SKEWPT();
    {
      TS(26);
#define XCHW() asm volatile("s_waitcnt vmcnt(0)" : "+v"(pv[0]), "+v"(pv[1]), "+v"(pv[2]), "+v"(pv[3])::"memory")
      XCHW();
#define XBAD (amin16(pv) < 128.0f)
      if (__builtin_expect(__any(XBAD), 1)) POLL2(XBAD, 1, PCAP, XCHLD(); XCHW());
      TS(27);
      asm volatile("s_waitcnt lgkmcnt(0)" : W8(acc[0]), W8(ga), "+v"(sa4[0]), "+v"(sa4[1]), "+v"(sa4[2]), "+v"(sa4[3]), "+v"(cj), "+v"(sw), "+v"(ce), "+v"(th), "+v"(slt), "+v"(slf)::"memory");
#pragma unroll
      for (int q = 0; q < 8; ++q) acc[0][q] = __builtin_amdgcn_mfma_f32_16x16x32_bf16(BF8((q & 1) ? e1 : e0), BF8(pv[q >> 1]), acc[0][q], 0, 0, 0);
      f32x4 s4 = {0.f, 0.f, 0.f, 0.f}, gr = {0.f, 0.f, 0.f, 0.f};
      const unsigned xa1 = xca + slt * 2048, sa1 = sca + slt * 128;
#pragma unroll
      for (int q = 0; q < 4; ++q) {
        xq[q] = pk8(acc[1][2 * q] + ga[2 * q] * acc[0][2 * q], acc[1][2 * q + 1] + ga[2 * q + 1] * acc[0][2 * q + 1]);
        gr = __builtin_amdgcn_mfma_f32_16x16x32_bf16(BF8(xq[q]), BF8(xq[q]), gr, 0, 0, 0);
        s4 = __builtin_amdgcn_mfma_f32_16x16x32_bf16(BF8(sa4[q]), BF8(xq[q]), s4, 0, 0, 0);
        xq[q] = nsent4(xq[q]);
      }
#define XCST(a)                                                                                                              \
  asm volatile(                                                                                                              \
      "global_store_dwordx4 %0, %1, %5 sc1\n\tglobal_store_dwordx4 %0, %2, %5 offset:64 sc1\n\t"                            \
      "global_store_dwordx4 %0, %3, %5 offset:128 sc1\n\tglobal_store_dwordx4 %0, %4, %5 offset:192 sc1\n\ts_nop 1" ::"v"(a), \
      "v"(xq[0]), "v"(xq[1]), "v"(xq[2]), "v"(xq[3]), "s"(ws)                                                                \
      : "memory")
#define SCST(a)                                                                                                              \
  do {                                                                                                                       \
    _Pragma("unroll") for (int r = 0; r < S_R; ++r) asm volatile("global_store_dwordx4 %0, %1, %2 offset:%3 sc1" ::"v"(a), "v"(sg), "s"(ws), "n"(r * 1280) : "memory"); \
    asm volatile("s_nop 1");                                                                                                 \
  } while (0)
      if (slt) XCST(xa1);
      const bool dl = ((lane >> 2) & 3) == g4;
      const u32x4 gd = __builtin_bit_cast(u32x4, gr) & dk;
      const float m = s4[2] * (1.0f / 128);
      float q2 = __uint_as_float((gd[0] | gd[1]) | (gd[2] | gd[3])) - s4[2] * m, lg = (s4[0] + s4[1]) - m * ce;
      if (__builtin_expect(__any(dl && !(s4[2] * m <= 256.0f * q2)), 0)) {
        asm volatile("" : "+v"(xq[0]), "+v"(xq[1]), "+v"(xq[2]), "+v"(xq[3]));
        f32x4 a = {0.f, 0.f, 0.f, 0.f}, l = {0.f, 0.f, 0.f, 0.f};
#pragma unroll
        for (int q = 0; q < 4; ++q) {
          const u32x4 ch = *reinterpret_cast<const u32x4*>(&scw[0][g4 * 8 + q * 32]), cl = *reinterpret_cast<const u32x4*>(&scw[1][g4 * 8 + q * 32]);
          const f32x4 d0 = lo4(xq[q]) - m, d1 = hi4(xq[q]) - m;
          a += d0 * d0 + d1 * d1;
          l += d0 * (lo4(ch) + lo4(cl)) + d1 * (hi4(ch) + hi4(cl));
        }
        q2 = ar4((a[0] + a[1]) + (a[2] + a[3]));
        lg = ar4((l[0] + l[1]) + (l[2] + l[3]));
      }
      const u32x4 sg = {nsent(m), nsent(q2), nsent(lg), 0u};
      if (dl) {
        if (slt) SCST(sa1);
        asm volatile("global_store_dwordx4 %0, %1, %2 sc1\n\ts_nop 1" ::"v"(soo), "v"(sg), "s"(ws) : "memory");
      }
      if (__any(slf == 0u)) {
        POLL2(slf == 0u, 1, 1 << 26, asm volatile("ds_read_b32 %1, %2 offset:56\n\tds_read_b32 %0, %3\n\ts_waitcnt lgkmcnt(0)" : "=&v"(slt), "=&v"(slf) : "v"(tha), "v"(sla) : "memory"));
        if (slt) {
          XCST(xca + slt * 2048);
          if (dl) SCST(sca + slt * 128);
        }
      }
      asm volatile("ds_write_b32 %0, %1 offset:72" ::"v"(tha + wid * 4), "v"(1u) : "memory");
      TS(7);
      SKEWPT();
      const unsigned tp = tick() + T_D1, tq = (unsigned)__builtin_amdgcn_readfirstlane((int)th) + T_C1;
      u32x4 sr[2];
#define STLD(W) asm volatile("global_load_dwordx4 %0, %2, %3 sc1\n\tglobal_load_dwordx4 %1, %2, %3 offset:256 sc1" W : "=&v"(sr[0]), "=&v"(sr[1]) : "v"(spo), "s"(ws) : "memory")
#define SBAD (max(max(max(sr[0][0], sr[0][1]), sr[0][2]), max(max(sr[1][0], sr[1][1]), sr[1][2])) == SENT)
      asm volatile("" : "+v"(xq[0]), "+v"(xq[1]), "+v"(xq[2]), "+v"(xq[3])::"memory");
      f32x4 xf[8], xs[8], gc[8];
      auto PREW = [&](int q0, int q1) __attribute__((always_inline)) {
#pragma unroll
        for (int q = q0; q < q1; ++q) {
          const u32x4 sbq = *reinterpret_cast<const u32x4*>(&svb[1][col0 + q * 32]), gbq = *reinterpret_cast<const u32x4*>(&svb[3][col0 + q * 32]);
          const u32x4 cq = *reinterpret_cast<const u32x4*>(&svb[2][col0 + q * 32]);
          xf[2 * q] = lo4(xq[q]), xf[2 * q + 1] = hi4(xq[q]);
          xs[2 * q] = xf[2 * q] + lo4(gbq) * lo4(sbq), xs[2 * q + 1] = xf[2 * q + 1] + hi4(gbq) * hi4(sbq);
          gc[2 * q] = lo4(gbq) * lo4(cq), gc[2 * q + 1] = hi4(gbq) * hi4(cq);
          asm volatile("" : "+v"(xf[2 * q]), "+v"(xf[2 * q + 1]), "+v"(xs[2 * q]), "+v"(xs[2 * q + 1]), "+v"(gc[2 * q]), "+v"(gc[2 * q + 1]));
        }
      };
      PREW(0, PW0);
      wait_tick((int)(tp - tq) < 0 ? tp : tq);
      STLD("");
      unsigned rk = 0x8ffeu - (unsigned)row0 * 2;
      unsigned twa = lds0 + STG + wid * 8192 + (lane & 15) * 256 + ((g4 ^ lane) & 15) * 16, tra = lds0 + STG + wid * 8192 + g4 * 256 + ((g4 ^ lane) & 15) * 16;
      unsigned oo_ = (unsigned)((rg * 16 + g4) * D + et * 128 + (lane & 15) * 8) * 2;
      asm volatile("" : "+v"(rk), "+v"(twa), "+v"(tra), "+v"(oo_));
      PREW(PW0, 4);
      asm volatile("s_waitcnt vmcnt(0)" : "+v"(sr[0]), "+v"(sr[1])::"memory");
      if (__builtin_expect(__any(SBAD), 1)) POLL2(SBAD, 1, PCAP, STLD(VW0));
      TS(9);
      const f32x2 mk = {__uint_as_float(sr[0][0]), __uint_as_float(sr[1][0])};
      const f32x2 qk = {__uint_as_float(sr[0][1]), __uint_as_float(sr[1][1])};
      const f32x2 lk = {__uint_as_float(sr[0][2]), __uint_as_float(sr[1][2])};
      const float mean = ar4(mk[0] + mk[1]) * (1.0f / 8);
      const f32x2 dm = mk - mean;
      const f32x2 a = qk + 128.0f * (dm * dm), l = lk + dm * cj;
      const float al = swap32_add(a[0] + a[1], l[0] + l[1]), am = swap16_add(al, al);
      auto r = __builtin_amdgcn_permlane32_swap(__float_as_uint(am), __float_as_uint(am), false, false);
      const float rstd = __builtin_amdgcn_rsqf(__uint_as_float(r[0]) * (1.0f / D) + 1e-5f);
      const unsigned key = mkkey(rstd * __uint_as_float(r[1]) + sw) | rk;
      unsigned t1 = slt ? 0u : key, t2 = 0u;
      TOP2(0xB1); TOP2(0x4E); TOP2(0x141); TOP2(0x140);
      if (lane == 0 && et == (rg & 7)) asm volatile("global_store_dwordx2 %0, %1, %2 sc1" ::"v"(O_KEY + rg * KSTR * 8), "v"(u32x2{t1, t2}), "s"(ws) : "memory");
      u32x4 o[4];
#pragma unroll
      for (int q = 0; q < 4; ++q) o[q] = pk8(xs[2 * q] + (gc[2 * q] * (xf[2 * q] - mean)) * rstd, xs[2 * q + 1] + (gc[2 * q + 1] * (xf[2 * q + 1] - mean)) * rstd);
      {
        asm volatile("ds_write_b128 %0, %4\n\tds_write_b128 %1, %5\n\tds_write_b128 %2, %6\n\tds_write_b128 %3, %7" ::"v"(twa), "v"(twa ^ 64), "v"(twa ^ 128), "v"(twa ^ 192),
                     "v"(o[0]), "v"(o[1]), "v"(o[2]), "v"(o[3]) : "memory");
        asm volatile("ds_read_b128 %0, %4\n\tds_read_b128 %1, %5 offset:1024\n\tds_read_b128 %2, %6 offset:2048\n\tds_read_b128 %3, %7 offset:3072\n\ts_waitcnt lgkmcnt(0)"
                     : "=&v"(o[0]), "=&v"(o[1]), "=&v"(o[2]), "=&v"(o[3]) : "v"(tra), "v"(tra ^ 64), "v"(tra ^ 128), "v"(tra ^ 192) : "memory");
      }
      auto OST = [&](bool on) __attribute__((always_inline)) {
        const unsigned mk_ = (unsigned)__builtin_amdgcn_ballot_w64(on) >> g4;
#pragma unroll
        for (int q = 0; q < 4; ++q)
          if (mk_ & (1u << (4 * q))) asm volatile("global_store_dwordx4 %0, %1, %2" OPOL "\n\ts_nop 1" ::"v"(oo_), "v"(o[q]), "s"(out + q * 4096) : "memory");
      };
      bool df = slt;
      {
        const int tn = (int)(tick() - (tq + (T_C3 - T_C1)));
        if (tn > T_X) df = true;
        else if (tn > -T_W) { unsigned sv; LSPIN(sv, ssel + 16 + wid, sv != 0u); }
      }
      OST(!df);
      TS(1);
      u32x2 ks;
      POLL2(ks[1] == 0u, 1, 1 << 26, asm volatile("ds_read_b64 %0, %1\n\ts_waitcnt lgkmcnt(0)" : "=v"(ks) : "v"(lds0 + SPARE + 10368) : "memory"));
      df = df && (ks[0] & 0xffffu) != rk && (ks[1] & 0xffffu) != rk;
      SKEWPT();
      if (__any(df)) {
        if (b < NPART) {
          unsigned sv;
          LSPIN(sv, ssel + O_DW, sv != 0u);
          if (O_DS) __builtin_amdgcn_s_sleep(O_DS);
        }
        OST(df);
      }
      if (__builtin_expect(__any(t1 > ks[1]), 0)) {
        VDLD(8);
        slt = slt ? 0u : key == vd[0] ? 9u : key == vd[1] ? 10u : 0u;
        if (slt) {
          XCST(xca + slt * 2048);
          if (dl) SCST(sca + slt * 128);
        }
      }
    }
    TS(16);
    TSE();
  } else {
    const int hw = wid - 4;
    unsigned ts0 = tick();
    asm volatile("" : "+v"(ts0));
    const char* Hc = reinterpret_cast<const char*>((cc ? X1 : X0) + (size_t)et * 128 * D);
    auto UPOLL = [&](int n) {
      const unsigned fo = (unsigned)WS_UFL + ((cc * 16 + nt) * 32 + ((mt + 1 + (lane >> 2)) & 7) * 4 + (lane & 3)) * FSTR * 4;
      unsigned f;
      POLL2(f == 0u && lane < 4 * n, U_S, PCAP, asm volatile("global_load_dword %0, %1, %2 sc1" VW0 : "=&v"(f) : "v"(fo), "s"(ws) : "memory"));
      return (__builtin_amdgcn_ballot_w64(f == 0u) & 0xFFFFFFFull) == 0;
    };
    bool urdy = true;
    u32x4 gT[8], uT[8], wdv[8];
    unsigned og = (unsigned)(((g & 3) * 256 + hw * 64 + (lane >> 3)) * F + (g >> 2) * 64 + ((lane & 7) ^ ((lane >> 3) & 2) ^ 4) * 8) * 2;
    unsigned wta = lds0 + STG + (g4 * 8 + ((lane >> 2) & 3)) * 128 + (((hw * 2 + ((lane >> 1) & 1)) ^ ((lane >> 2) & 2) ^ ((g4 & 1) * 4)) << 4) + (lane & 1) * 8;
    unsigned od = (unsigned)(((g >> 2) * 64 + (lane >> 3) * 8) * D + (g & 3) * 256 + hw * 64 + (lane & 7) * 8) * 2;
    unsigned ou = og + (unsigned)(((mt + W_UO) % 7 - mt) * 128);
    asm volatile("" : "+v"(og), "+v"(ou), "+v"(od), "+v"(wta));
#define LDWI(dst, voff, base, inc) asm volatile("global_load_dwordx4 %0, %1, %2" WPOL "\n\tv_add_u32 %1, %3, %1" : "=v"(dst), "+v"(voff) : "s"(base), "n"(inc) : "memory")
#define WST(o, w)                                                                                                          \
  if (b < NPART) {                                                                                                         \
    asm volatile("s_mov_b32 m0, %0" ::"s"(lds0 + STG + hw * 8192));                                                        \
    _Pragma("unroll") for (int i = 0; i < 8; ++i) {                                                                        \
      asm volatile("v_xor_b32 %0, 64, %0\n\tglobal_load_lds_dwordx4 %0, %1 offset:%2" WPOL "\n\tv_add_u32 %0, %3, %0"       \
                   : "+v"(o) : "s"(w), "n"((i & 3) * 1024), "n"(F * 16 + (i == 3 ? 3072 : -1024)) : "memory");             \
      if (i == 3) asm volatile("s_add_u32 m0, m0, 4096" ::: "scc");                                                        \
    }                                                                                                                      \
  }
#define WTR2(i) "ds_read_b64_tr_b16 %" #i ", %16 offset:(" #i "/2)*4096+(" #i "&1)*512\n\t"
#define WTK(T)                                                                                                                         \
  if (b < NPART) {                                                                                                                     \
    u32x2 t_[16];                                                                                                                      \
    asm volatile(WTR2(0) WTR2(1) WTR2(2) WTR2(3) WTR2(4) WTR2(5) WTR2(6) WTR2(7) WTR2(8) WTR2(9) WTR2(10) WTR2(11) WTR2(12) WTR2(13)   \
                     WTR2(14) WTR2(15) "s_waitcnt lgkmcnt(0)"                                                                          \
                 : "=&v"(t_[0]), "=&v"(t_[1]), "=&v"(t_[2]), "=&v"(t_[3]), "=&v"(t_[4]), "=&v"(t_[5]), "=&v"(t_[6]), "=&v"(t_[7]),     \
                   "=&v"(t_[8]), "=&v"(t_[9]), "=&v"(t_[10]), "=&v"(t_[11]), "=&v"(t_[12]), "=&v"(t_[13]), "=&v"(t_[14]), "=&v"(t_[15]) \
                 : "v"(wta) : "memory");                                                                                               \
    _Pragma("unroll") for (int f = 0; f < 8; ++f) T[f] = u32x4{t_[2 * f][0], t_[2 * f][1], t_[2 * f + 1][0], t_[2 * f + 1][1]};        \
  }
#define DLD(q0, q1) if (b < NPART) { _Pragma("unroll") for (int q = q0; q < q1; ++q) LDWI(wdv[q], od, wd, D * 2); }
    const int pf0 = A_PS + (((j8 & 7) * A_S1) >> 3), pf1 = A_P2 + (((j8 & 7) * A_S2) >> 3), q1 = A_Q1;
    static_assert(A_PS >= 1 && A_P2 + A_S2 * 7 / 8 + 3 <= R_AT && R_AT < A_Q1 && A_P2 + A_S2 * 7 / 8 + 1 + W_T <= A_Q1 && H_AT + 3 <= A_Q1 && G1_C + G1_S < W_T && A_PS + W_T < A_P2 && A_PS + A_S1 * 7 / 8 + W_T + 1 < A_P2 + A_S2 * 7 / 8 && W_T >= M_NS - 2 && A_Q1 < F_AT && F_AT <= 29 && V_K <= B_J && V_T > B_J && B_WK == 15 && X_K >= N_C && (X_K + M_NS - 1 < 16 || (X_K <= B_MAT && TP_XC - 11 >= TP_X0 - TP_P0)) && TP_XC >= 4 * TP_PS && TP_XC + TP_P0 + 32 <= 72 && N_K > N_C && N_C > V_T && B_RAT > B_J && B_QAT > B_J && B_QAT + B_QD < 15, "barrier counts");
#define HOFF(r) (unsigned)((((r) >> 5) * 32 + (((r) >> 2) & 3) * 8 + (((r) >> 4) & 1) * 4 + ((r)&3)) * 2048 + ((lane & 7) ^ (lane >> 3)) * 16)
#define TR8(T, W)                                                                                                    \
  _Pragma("unroll") for (int c = 0; c < 8; ++c) {                                                                    \
    _Pragma("unroll") for (int p = 0; p < 4; ++p)                                                                    \
        T[c][p] = __builtin_amdgcn_perm(W[2 * p + 1][c >> 1], W[2 * p][c >> 1], (c & 1) ? 0x07060302u : 0x05040100u); \
    asm volatile("" : "+v"(T[c]));                                                                                   \
  }
    u32x4 qm;
#pragma unroll
    for (int i = 0; i < 4; ++i) qm[i] = ((lane >> 2) & 3) == i ? ~0u : 0u;
    auto MVR = [&](f32x4 d) __attribute__((always_inline)) { return ((lane & 8) ? d[3] : d[0]) + dpp_f<0x128>((lane & 8) ? d[1] : d[2]); };
    auto MV64 = [&](const u32x4* T, u32x4 a) __attribute__((always_inline)) {
      f32x4 d[2] = {};
#pragma unroll
      for (int c = 0; c < 8; ++c) d[c & 1] = __builtin_amdgcn_mfma_f32_16x16x32_bf16(BF8(a & qm[c >> 1]), BF8(T[c]), d[c & 1], 0, 0, 0);
      return f32x2{MVR(d[0]), MVR(d[1])};
    };
#define LD1(i, a) "global_load_dwordx4 %" #i ", %" #a ", %10 sc1\n\tv_add_u32 %" #a ", 0x4000, %" #a "\n\t"
#define LD8(r, o0, o1, W)                                                                                                          \
  do {                                                                                                                             \
    unsigned a0_ = (o0), a1_ = (o1);                                                                                               \
    asm volatile(LD1(0, 8) LD1(4, 9) LD1(1, 8) LD1(5, 9) LD1(2, 8) LD1(6, 9) LD1(3, 8) LD1(7, 9) "s_nop 0" W                        \
                 : "=&v"(r[0]), "=&v"(r[1]), "=&v"(r[2]), "=&v"(r[3]), "=&v"(r[4]), "=&v"(r[5]), "=&v"(r[6]), "=&v"(r[7]), "+v"(a0_), "+v"(a1_) \
                 : "s"(ws) : "memory");                                                                                            \
  } while (0)
    u32x4 sel, gp[4];
    unsigned vc;
#define VCLD(W) asm volatile("global_load_dword %0, %1, %2 sc1" W : "=&v"(vc) : "v"(O_VP + ((cc ^ 1) * 8 + mt) * 4096 + (lane & 15) * 256), "s"(ws) : "memory")
#define GP4(o, W)                                                                                                         \
  asm volatile(                                                                                                           \
      "global_load_dwordx4 %0, %4, %5 sc1\n\tglobal_load_dwordx4 %1, %4, %5 offset:1024 sc1\n\t"                         \
      "global_load_dwordx4 %2, %4, %5 offset:2048 sc1\n\tglobal_load_dwordx4 %3, %4, %5 offset:3072 sc1" W                \
      : "=&v"(gp[0]), "=&v"(gp[1]), "=&v"(gp[2]), "=&v"(gp[3])                                                            \
      : "v"(o), "s"(ws)                                                                                                   \
      : "memory")
#define GPBAD (max(max(max4(gp[0]), max4(gp[1])), max(max4(gp[2]), max4(gp[3]))) == SENT)
      auto GPSUM = [&]() __attribute__((always_inline)) {
        f32x4 d = {0.f, 0.f, 0.f, 0.f};
#pragma unroll
        for (int i = 0; i < 4; ++i) d = __builtin_amdgcn_mfma_f32_16x16x32_bf16(BF8(sel), BF8(gp[i]), d, 0, 0, 0);
        return d;
      };
    const bool rdc = b >= NPART && hw == 3, rnp = b >= NPART && hw == 2 && cc == 0;
    const int gbq = (b >> 4) & 1, cj = (b & 15) * 64 + (lane & 31) * 2;
    unsigned voff[4], ro, xo, ra1, wqa, qo, qa, ma, mo2, ma2;
    auto JSET = [&]() {
#pragma unroll
      for (int i = 0; i < 4; ++i) {
        voff[i] = HOFF((hw * 4 + i) * 8 + (lane >> 3)) - i * 1024;
        sel[i] = ((lane & 15) >> 1) == i ? ((lane & 1) ? 0x3f800000u : 0x3f80u) : 0u;
      }
      ro = O_G2 + ((lane >> 5) * 16384 + gbq * 1024 + cj) * 8;
      xo = (unsigned)((nt * 128 + hw * 32 + cc * 16 + g4) * D + et * 128 + ((g4 ^ lane) & 15) * 8) * 2;
      ra1 = lane < 32 ? O_MOD + 2048 + gbq * 4096 + cj * 2 : O_M2 + 8192 + 64 - gbq * 64 + (b & 15) * 4;
      wqa = lds0 + SPARE + 8192 + cj * 2;
      ma = max(hw, 1) * 2048 + (lane < 16 ? et * 256 + lane * 16 : lane < 48 ? (g & 3) * 512 + (lane - 16) * 16 : ((b >> 1) & 15) * 128 + (lane & 7) * 16);
      qo = hw < 2 ? O_QT + et * 2048 + hw * 1024 + lane * 16 : O_RS + ((hw & 1) * S + nt * 128 + (lane >> 4) * 32 + cc * 16 + (lane & 15)) * 4;
      qa = lds0 + (hw < 2 ? RKA + hw * 1024 + lane * 16 : RKB + lane * 16 + (hw & 1) * 8);
      mo2 = O_M2 + (hw == 2 ? 8448u + (lane & 15) * 16 : hw ? 8192u + (lane & 7) * 16 : lane < 32 ? et * 512 + lane * 16 : 4096u + (lane & 16) * 128 + et * 256 + (lane & 15) * 16);
      ma2 = lds0 + (hw ? SPARE + 10240 + (lane & 7) * 8 : TAB + lane * 16);
      asm volatile("" : "+v"(voff[0]), "+v"(voff[1]), "+v"(voff[2]), "+v"(voff[3]), "+v"(ro), "+v"(xo), "+v"(ra1), "+v"(wqa), "+v"(qo), "+v"(qa), "+v"(ma), "+v"(mo2), "+v"(ma2), "+v"(sel));
      *reinterpret_cast<unsigned*>((hw & 1) ? shm + RKB + 1024 + lane * 4 : reinterpret_cast<char*>(scw[2]) + lane * 4) = (hw & 1) ? 0u : 0x3f803f80u;
      if (hw == 3) {
        if (lane < 32) ssel[lane] = 0u;
        *reinterpret_cast<u32x2*>(shm + SLT + lane * 8) = u32x2{0u, 0u};
      }
    };
    if (hw < 2) {
      constexpr int P = 8;
      __builtin_amdgcn_s_setprio(2);
      unsigned voy[P];
#pragma unroll
      for (int i = 0; i < P; ++i) voy[i] = (unsigned)(((hw * 8 + i) * 8 + (lane >> 3)) * 4096 + ((lane & 7) ^ (lane >> 3)) * 16 - (i & 3) * 1024);
      const unsigned lbase = lds0 + hw * 8192;
      const char* Yc = reinterpret_cast<const char*>((cc ? Y1 : Y0) + (size_t)nt * 128 * S);
      auto stage = [&](const char* yp, unsigned l) {
        GLDS4(l, voy, yp);
        GLDS4(l + 4096, voy + 4, yp);
      };
#define YSRC(n) (Yc + ((((n) + xcd * 4) & 31) << 7))
      __builtin_amdgcn_s_sleep(C_LD);
      PFL(hw ? PFG(1) : cpf);
      JSET();
      TAGW(1);
      BAR(55);
      BT(56);
      if (hw == 1 && lane == 0) asm volatile("global_store_dwordx2 %0, %1, %2 sc1" ::"v"(O_XFL + b * XSTR * 4), "v"(tag), "s"(ws) : "memory");
#pragma unroll
      for (int s = 0; s < NS - 1; ++s) stage(YSRC(s), lbase + s * 16384);
      BT(57);
      WAIT_V((NS - 2) * P);
      BAR(0);
      unsigned slot = lbase + (NS - 1) * 16384;
      const u64 evm = (u64)((1u | 1u << W_T) << pf0 | (1u | 1u << W_T) << pf1) | 1ull << CPF_K | 1ull << (33 - NS) | 3ull << 32;
      const char* yb = Yc;
      unsigned yo = (NS - 1 + xcd * 4) << 7, ym = 31 << 7;
#pragma unroll 1
      for (int k = 1; k <= 32 + B_J; ++k) {
        const char* ys = yb + (yo & ym);
        yo += 128;
        asm volatile("" : "+s"(ys));
        WAIT_V((NS - 3) * P);
        BAR(k);
        stage(ys, slot);
        slot = slot == lbase + (NS - 1) * 16384 ? lbase : slot + 16384;
        if (__builtin_expect((evm >> k) & 1, 0)) {
          if (k == pf0) {
            WST(og, wg);
            if (b >= NPART) GLDS(lds0 + SPARE + 8192 + hw * 1024, (unsigned)(hw * 1024 + lane * 16), wr_);
          }
          if (k == pf0 + W_T) { WTK(gT); }
          if (k == pf1) { WST(ou, wu); }
          if (k == pf1 + W_T) { WTK(uT); }
          if (k == CPF_K && hw == 1) CPFL();
          if (k == 33 - NS) {
#pragma unroll
            for (int i = 0; i < P; ++i) voy[i] = HOFF((hw * 8 + i) * 8 + (lane >> 3)) - (i & 3) * 1024;
            yb = Hc, yo = mt << 8, ym = 15 << 7;
          }
          if (k == 32) { DLD(0, 8 - B_WN); }
          if (k == 33 && hw == 0) {
            if (U_D) __builtin_amdgcn_s_sleep(U_D);
            urdy = UPOLL(U_N1);
          }
        }
      }
    } else {
      const int aw = hw - 2;
      unsigned ow = wo - (2048 + (cg0 >> 4) * 1024) * 2;
      const unsigned oq = (unsigned)((nt * 64 + aw * 32 + (lane >> 4)) * D + mt * 128) * 2;
      u32x4 hv[8], wt[8], sc;
      {
        asm volatile("s_waitcnt vmcnt(11)" : "+v"(cq)::"memory");
#pragma unroll
        for (int i = 0; i < 4; ++i) sc[i] = pack2(silu_bf(bflo(cq[i])), silu_bf(bfhi(cq[i])));
        asm volatile("" : "+v"(sc));
      }
#define GPUB(o, off)                                                                                   \
  if ((lane & 8) == 0)                                                                                 \
    asm volatile("global_store_dwordx4 %0, %1, %2 sc1 ; ALWAYS\n\ts_nop 1" ::"v"(off),                          \
                 "v"(u32x4{__float_as_uint(o[0]), (unsigned)tag, __float_as_uint(o[1]), (unsigned)tag}), "s"(ws) : "memory")
      const unsigned gpo = ks0 * 2048 + cg0 * 64 + (lane & 7) * 8 + (lane >> 4) * 2;
      {
        unsigned mpo = O_G1 + gpo * 8;
        asm volatile("s_waitcnt vmcnt(3)" : W8(wv), "+v"(mpo)::"memory");
        TR8(wt, wv);
        f32x2 o = MV64(wt, sc);
        asm volatile("" : "+v"(o));
        TAGW(0);
        BAR(55);
        if (P_D2) __builtin_amdgcn_s_sleep(P_D2);
        PFL(PFG(2 + aw));
        GPUB(o, mpo);
      }
      unsigned vta = lds0 + STG + (g4 * 8 + ((lane >> 2) & 3)) * 256 + (((aw * 8 + ((lane >> 1) & 1)) ^ ((g4 & 1) * 8 + ((lane >> 2) & 3))) << 4) + (lane & 1) * 8;
      unsigned vso = O_VP + ((cc * 8 + mt) * 16 + nt) * 256 + aw * 128 + (lane & 3) * 32 + g4 * 8;
      asm volatile("" : "+v"(vta), "+v"(vso), "+v"(ow));
      unsigned f0 = O_XFL + ((xcd * 32 + 64 + aw * 128 + lane) & 255) * XSTR * 4, f1 = O_XFL + ((xcd * 32 + 128 + aw * 64 + lane) & 255) * XSTR * 4;
      unsigned lao = O_KEY + 512 * 1024 + b * 3328 + aw * 1664 + lane * 16;
      const char* pf4 = PFG(4 + aw);
      const char* pf6 = PFG(6 + aw);
      asm volatile("" : "+v"(f0), "+v"(f1), "+v"(lao), "+v"(pf4), "+v"(pf6));
      BT(57);
      BAR(0);
      f32x4 rs = {0.f, 0.f, 0.f, 0.f};
      u32x4 rsa = (lane & 15) == g4 * 4 ? u32x4{0x3f803f80u, 0x3f803f80u, 0x3f803f80u, 0x3f803f80u} : u32x4{0u, 0u, 0u, 0u}, R_;
      asm volatile("" : "+v"(rs), "+v"(rsa), "=v"(R_));
      const unsigned ra = lds0 + (mt * 16 + aw * 8) * 128 + lane * 16;
      unsigned so = 0;
#define TSX()
#define RSUM()                                                                                                    \
  do {                                                                                                            \
    TSX();                                                                                                        \
    asm volatile("ds_read_b128 %1, %2\n\ts_waitcnt lgkmcnt(0)\n\tv_mfma_f32_16x16x32_bf16 %0, %3, %1, %0" : "+v"(rs), "+v"(R_) : "v"(ra + so), "v"(rsa) : "memory"); \
    so = so == (NS - 1) * 16384 ? 0 : so + 16384;                                                                 \
  } while (0)
      {
        u64 f[2];
#define FLD() asm volatile("global_load_dwordx2 %0, %2, %4 sc1\n\tglobal_load_dwordx2 %1, %3, %4 sc1" : "=&v"(f[0]), "=&v"(f[1]) : "v"(f0), "v"(f1), "s"(ws) : "memory")
        __builtin_amdgcn_s_sleep(F_D);
        FLD();
        asm volatile("global_store_dwordx4 %0, %1, %2 sc1" ::"v"(lao), "v"(u32x4{~0u, ~0u, ~0u, ~0u}), "s"(ws) : "memory");
        if (lane < 40) asm volatile("global_store_dwordx4 %0, %1, %2 offset:1024 sc1 ; ALWAYS" ::"v"(lao), "v"(u32x4{~0u, ~0u, ~0u, ~0u}), "s"(ws) : "memory");
        asm volatile("s_nop 1");
#define GLD() _Pragma("unroll") for (int i = 0; i < 8; ++i) LDWI(wv[i], ow, wmod, 12288)
        RSUM();
        BAR(1);
        asm volatile("s_waitcnt vmcnt(%2)" : "+v"(f[0]), "+v"(f[1]) : "n"(2) : "memory");
        POLL(__all(f[0] == tag && f[1] == tag), 1, FLD(); asm volatile("s_waitcnt vmcnt(0)" : "+v"(f[0]), "+v"(f[1])::"memory"));
      }
      if (X_PA) __builtin_amdgcn_s_setprio(X_PA);
#define ARUN(b0, b1) _Pragma("unroll 1") for (int bi = (b0); bi < (b1); ++bi) { RSUM(); BAR(bi + 1); }
      GLD();
      ARUN(1, pf0);
      PFL(pf4);
#define HVLD() _Pragma("unroll") for (int i = 0; i < 8; ++i) GLDS(lds0 + STG + aw * 8192 + i * 1024, oq + i * 8192 + (((lane & 15) ^ ((i * 4 + (lane >> 4)) & 15)) << 4), cc ? X1 : X0)
      WST(og, wg);
      ARUN(pf0, pf0 + G1_C);
      {
        if (b >= NPART) WAIT_V(0);
        asm volatile("s_waitcnt vmcnt(8)" : W8(wv)::"memory");
        TR8(wt, wv);
        if (G1_S) { RSUM(); BAR(pf0 + G1_C + 1); }
        const f32x2 o = MV64(wt, sc);
        GPUB(o, O_MODP + gpo * 8);
        ow = wo + 2048;
      }
      ARUN(pf0 + G1_C + G1_S, pf0 + W_T - 1);
      WAIT_V(1);
      RSUM();
      BAR(pf0 + W_T);
      WTK(gT);
      ARUN(pf0 + W_T, pf1);
      PFL(pf6);
      WST(ou, wu);
      const int rb_ = b < NPART ? pf1 + W_T : R_AT, hb_ = max(rb_ + 1, H_AT + (j8 & 3));
      const unsigned c0 = (b - NPART) * 64 + aw * 32 + (lane & 15) * 2, c1 = (b - NPART) * 32 + aw * 16 + (lane & 7) * 2;
#define ERLD(W) LD8(wt, O_MODP + (g4 * 8192 + c0) * 8, O_G1 + (g4 * 8192 + (lane & 8) * 128 + c1) * 8, W)
      ARUN(pf1, rb_ - 3);
      if (b >= NPART) ERLD("");
      ARUN(rb_ - 3, rb_ - 1);
      WAIT_V(0);
      RSUM();
      BAR(rb_);
      WTK(uT);
      if (b >= NPART) {
        const unsigned wq = *reinterpret_cast<const unsigned*>(&svb[4][c1]);
#define TG2(r, i) ((r[i][1] ^ (unsigned)tag) | (r[i][3] ^ (unsigned)tag) | (r[i + 1][1] ^ (unsigned)tag) | (r[i + 1][3] ^ (unsigned)tag))
        asm volatile("" : W8(wt));
        POLL(__all((TG2(wt, 0) | TG2(wt, 2) | TG2(wt, 4) | TG2(wt, 6)) == 0u), 2, ERLD(VW0));
        auto FIN = [&](int q, int i, bool inc) __attribute__((always_inline)) {
          return rbf((inc ? 1.0f : 0.0f) + rbf(ar4((__uint_as_float(wt[q][i]) + __uint_as_float(wt[q + 1][i])) + (__uint_as_float(wt[q + 2][i]) + __uint_as_float(wt[q + 3][i])))));
        };
        const float s0 = FIN(0, 0, gbq), s1 = FIN(0, 2, gbq);
        if (lane < 16)
          asm volatile("global_store_dwordx4 %0, %1, %2 sc1\n\ts_nop 1" ::"v"(O_MODP + (16 * 2048 + c0) * 8),
                       "v"(u32x4{__float_as_uint(s0), (unsigned)tag, __float_as_uint(s1), (unsigned)tag}), "s"(ws) : "memory");
        const bool hi = lane & 8;
        const float t0 = FIN(4, 0, hi), t1 = FIN(4, 2, hi), o0 = dpp_f<0x128>(t0), o1 = dpp_f<0x128>(t1);
        const float a0 = hi ? o0 : t0, a1 = hi ? o1 : t1, e0 = hi ? t0 : o0, e1 = hi ? t1 : o1, p0 = e0 * bflo(wq), p1 = e1 * bfhi(wq);
        const unsigned h = pack2(p0, p1);
        const unsigned v = g4 == 0 ? __float_as_uint(hi ? a1 : a0) : g4 == 1 ? (hi ? h : pack2(e0, e1)) : hi ? pack2(a0 * p0, a1 * p1) : pack2(p0 - bflo(h), p1 - bfhi(h));
        const unsigned po = g4 == 0 ? O_M2 + c1 * 4 + (hi ? 4 : 0) : (g4 == 1 ? (hi ? O_M2 + 4096 : O_MOD + 4096) : hi ? O_GC : O_M2 + 6144) + c1 * 2;
        if (lane < 48) asm volatile("global_store_dword %0, %1, %2 sc1" ::"v"(po), "v"(min(v, SENT - 1u)), "s"(ws) : "memory");
      }
      ARUN(rb_, hb_);
      HVLD();
      ARUN(hb_, q1);
      u32x4 fm, g0, g1;
#define FMLD(W)                                                                                                                                     \
  asm volatile("global_load_dwordx4 %0, %3, %5 sc1\n\tglobal_load_dwordx4 %1, %4, %5 sc1\n\tglobal_load_dwordx4 %2, %4, %5 offset:64 sc1" W        \
               : "=&v"(fm), "=&v"(g0), "=&v"(g1) : "v"(O_MODP + (16 * 2048 + aw * 1024 + mt * 128) * 8 + lane * 16), "v"(O_GC + nt * 128 + g4 * 16), "s"(ws) : "memory")
      FMLD("");
      RSUM();
      BAR(q1 + 1);
      unsigned ha[4], sma = lds0 + SPARE + 15872 + (aw ? 512 + lane * 8 : lane * 4);
#pragma unroll
      for (int i = 0; i < 4; ++i) ha[i] = lds0 + STG + aw * 8192 + (lane & 15) * 256 + (((i * 4 + (lane >> 4)) ^ (lane & 15)) << 4);
      asm volatile("" : "+v"(ha[0]), "+v"(ha[1]), "+v"(ha[2]), "+v"(ha[3]), "+v"(sma), "+v"(ow));
      ARUN(q1 + 1, F_AT);
      asm volatile("s_waitcnt vmcnt(0)" : "+v"(fm), "+v"(g0), "+v"(g1)::"memory");
#pragma unroll
      for (int i = 0; i < 8; ++i) DSR(hv[i], ha[i & 3], (i >> 2) * 4096);
      POLL(__all(fm[1] == (unsigned)tag && fm[3] == (unsigned)tag && max(max4(g0), max4(g1)) != SENT), 1, FMLD(VW0));
      {
        const unsigned sg = cc ? 0x80000000u : 0u;
        asm volatile("ds_write_b32 %0, %2\n\tds_write_b32 %1, %3" ::"v"(sma), "v"(sma + (aw ? 4 : 256)), "v"(aw ? fm[0] ^ sg : (fm[0] >> 16) | (fm[2] & 0xffff0000u)), "v"(fm[2] ^ sg) : "memory");
      }
      ARUN(F_AT, 29);
      GLD();
      RSUM();
      BAR(30);
        u32x2 tv[16];
#pragma unroll
        for (int q = 0; q < 16; ++q)
          asm volatile("ds_read_b64_tr_b16 %0, %1 offset:%2" : "=v"(tv[q]) : "v"(vta ^ ((((q >> 2) * 2) ^ ((q & 1) * 4)) << 4)), "n"((q & 1) * 1024 + ((q >> 1) & 1) * 8192) : "memory");
      RSUM();
      BAR(31);
      RSUM();
      {
        asm volatile("s_nop 7\n\ts_nop 7" : "+v"(rs));
        const float v0 = dpp_add<0x141>(dpp_add<0x4E>(dpp_add<0xB1>(rs[0]))), v = cc ? -v0 : v0;
        if ((lane & 7) == 0)
          asm volatile("global_store_dword %0, %1, %2 sc1 ; ALWAYS" ::"v"(O_RS + (cc * S + nt * 128 + mt * 16 + aw * 8 + (lane >> 3)) * 4), "v"(min(pack2(v, v - rbf(v)), SENT - 1u)), "s"(ws) : "memory");
      }
      BAR(32);
      __builtin_amdgcn_s_sleep(2);
      DLD(0, 8 - B_WN);
      __builtin_amdgcn_s_setprio(2);
      {
        asm volatile("" : W8(tv), W8((tv + 8))::"memory");
        f32x4 d[4];
#pragma unroll
        for (int j = 0; j < 4; ++j) {
          d[j] = __builtin_amdgcn_mfma_f32_16x16x32_bf16(BF8((u32x4{tv[4 * j][0], tv[4 * j][1], tv[4 * j + 1][0], tv[4 * j + 1][1]})), BF8(g0), f32x4{0.f, 0.f, 0.f, 0.f}, 0, 0, 0);
          d[j] = __builtin_amdgcn_mfma_f32_16x16x32_bf16(BF8((u32x4{tv[4 * j + 2][0], tv[4 * j + 2][1], tv[4 * j + 3][0], tv[4 * j + 3][1]})), BF8(g1), d[j], 0, 0, 0);
        }
        u32x2 vo;
#pragma unroll
        for (int i = 0; i < 2; ++i) {
          const unsigned e0 = pack2(d[0][2 * i], d[0][2 * i + 1]), e1 = pack2(d[1][2 * i], d[1][2 * i + 1]), e2 = pack2(d[2][2 * i], d[2][2 * i + 1]), e3 = pack2(d[3][2 * i], d[3][2 * i + 1]);
          vo[i] = min((lane & 2) ? ((lane & 1) ? e3 : e2) : ((lane & 1) ? e1 : e0), SENT - 1u);
        }
        if ((lane & 12) == 0) asm volatile("global_store_dwordx2 %0, %1, %2 sc1 ; ALWAYS" ::"v"(vso), "v"(vo), "s"(ws) : "memory");
      }
      asm volatile("" : W8(hv)::"memory");
      {
        f32x4 qa[2] = {};
#pragma unroll
        for (int kk = 0; kk < 4; ++kk) {
          const bf16x8 sb8 = *reinterpret_cast<const bf16x8*>(ssa + kk * 32 + (lane >> 4) * 8);
#pragma unroll
          for (int r = 0; r < 2; ++r)
            qa[r] = __builtin_amdgcn_mfma_f32_16x16x32_bf16(__builtin_bit_cast(bf16v8, hv[r * 4 + kk]), __builtin_bit_cast(bf16v8, sb8), qa[r], 0, 0, 0);
        }
        if ((lane & 15) < 2)
          asm volatile("global_store_dwordx4 %0, %1, %2 sc1 ; ALWAYS\n\ts_nop 1" ::"v"(O_RS + (4096 + (cc * 8 + mt) * D + nt * 64 + aw * 32 + (lane & 1) * 16 + (lane >> 4) * 4) * 4),
                       "v"(nsent4(selu((lane & 1) ? ~0u : 0u, __builtin_bit_cast(u32x4, qa[1]), __builtin_bit_cast(u32x4, qa[0])))), "s"(ws) : "memory");
      }
      BAR(33);
      {
        if (b >= NPART) WAIT_V(3);
        asm volatile("s_waitcnt vmcnt(%8)" : W8(wv) : "n"(8 - B_WN + 3) : "memory");
        TR8(wt, wv);
        const f32x2 o = MV64(wt, sc);
        GPUB(o, O_G2 + gpo * 8);
      }
      JSET();
      BAR(34);
#pragma unroll
      for (int k = 3; k <= B_J; ++k) {
        BAR(32 + k);
        if (k == V_K) {
          if (!aw) GP4(O_VP + (cc * 8 + mt) * 4096 + lane * 16, "");
          else VCLD("");
        }
      }
    }
    u32x4 dT[8];
    u32x4 m[3];
    u32x2 kk = {SENT, SENT};
#define K2LD(W) asm volatile("global_load_dwordx2 %0, %1, %2 sc1" W : "+v"(kk) : "v"(O_K2 + (lane & 15) * 64), "s"(ws) : "memory")
    {
      constexpr BSched sch;

      const unsigned lbase = lds0 + hw * 4096;
      u32x4 rr[8];
      unsigned cbv, t1, t2;
      float t[2];
#define RRLD(W)                                                                                                          \
  do {                                                                                                                   \
    asm volatile("global_load_dword %0, %1, %2 sc1" : "=&v"(cbv) : "v"(O_MOD + 4096 + cj * 2), "s"(ws) : "memory");      \
    LD8(rr, ro, ro + 65536, W);                                                                                          \
  } while (0)
      unsigned wq;
      u32x4 mq = {0u, 0u, 0u, 0u};
#define QLD(W)                                                                                                        \
  do {                                                                                                                \
    if (hw < 2) asm volatile("global_load_dwordx4 %0, %1, %2 sc1" W : "+v"(mq) : "v"(qo), "s"(ws) : "memory");        \
    else asm volatile("global_load_dword %0, %1, %2 sc1" W : "+v"(mq[0]) : "v"(qo), "s"(ws) : "memory");              \
  } while (0)
#define QRLD(W)                                                                                                       \
  asm volatile("global_load_dwordx4 %0, %2, %4 sc1\n\tglobal_load_dwordx4 %1, %3, %4 sc1" W : "=&v"(gp[0]), "=&v"(gp[1]) \
               : "v"(qr), "v"(qr + 32768), "s"(ws) : "memory")
      const bool rq_ = b >= NPART && hw == 1;
      const unsigned qr = O_RS + (4096 + (lane >> 3) * D + (b - NPART) * 32 + (lane & 7) * 4) * 4;
#define MODLD() asm volatile("global_load_dwordx4 %0, %2, %4 sc1\n\tglobal_load_dwordx4 %1, %3, %4 sc1" : "=&v"(m[0]), "=&v"(m[2]) : "v"(O_MOD + ma), "v"(mo2), "s"(ws) : "memory")
#define MODW(n) asm volatile("s_waitcnt vmcnt(%2)" : "+v"(m[0]), "+v"(m[2]) : "n"(n) : "memory")
#pragma unroll
      for (int k = B_J; k < 16; ++k) {
        if (k > B_J) {
          if (k == 2 * U_N1 - 1 && !urdy) UPOLL(7);
          WAIT_V(sch.cnt[k]);
          if (k == 15) WAIT_L(0);
          BAR(32 + k);
        }
        if (k > B_J && k - 2 + NS < 16) {
          const char* yp = Hc + ((mt * 2 + k - 2 + NS) & 15) * 128;
          const unsigned l = lbase + ((30 + k + NS) % NS) * 16384;
          GLDS4(l, voff, yp);
        }
        if (k == B_TK && hw == 1 && b >= 192) {
          const unsigned t = tick(), h = t + T_O + (((int)(t - ts0) - T_N) * T_A >> 8);
          if (lane == 0) asm volatile("global_store_dword %0, %1, %2 sc1" ::"v"(O_M2 + 8448 + (b - 192) * 4), "v"(min(h, SENT - 1u)), "s"(ws) : "memory");
        }
        if (k == B_RAT + 2) {
          if (rdc) {
            WAIT_V(sch.rcnt);
            asm volatile("" : W8(rr), "+v"(cbv));
            POLL(__all((TG2(rr, 0) | TG2(rr, 2) | TG2(rr, 4) | TG2(rr, 6)) == 0u && cbv != SENT), 2, RRLD(VW0); asm volatile("" : "+v"(cbv)));
#pragma unroll
            for (int j = 0; j < 2; ++j) {
#define RF(q) __uint_as_float(rr[q][2 * j])
              t[j] = ((RF(0) + RF(1)) + (RF(2) + RF(3))) + ((RF(4) + RF(5)) + (RF(6) + RF(7)));
            }
            asm volatile("" : "+v"(t[0]), "+v"(t[1]));
          }
        }
        if (k == B_RAT + 2 && rdc) {
          const unsigned o = pack2(swap32_add(t[0], t[0]), swap32_add(t[1], t[1])), f = gbq ? cbv : o;
          const float pr = row_sum(bflo(f) * bflo(wq) + bfhi(f) * bfhi(wq)), ps = swap16_add(pr, pr);
          if (lane <= 32) asm volatile("global_store_dword %0, %1, %2 sc1" ::"v"(ra1), "v"(min(lane < 32 ? o : __float_as_uint(ps), SENT - 1u)), "s"(ws) : "memory");
          TS(12);
        }
        if (k == V_T && hw == 3) {
          WAIT_V(sch.vcnt);
          asm volatile("" : "+v"(vc));
          POLL(__all(vc != SENT), 2, VCLD(VW0));
        }
        if (k == V_T && hw == 2) {
          WAIT_V(sch.vcnt);
          asm volatile("" : "+v"(gp[0]), "+v"(gp[1]), "+v"(gp[2]), "+v"(gp[3]));
          POLL(!__any(GPBAD), 2, GP4(O_VP + (cc * 8 + mt) * 4096 + lane * 16, VW0));
          const f32x4 d = GPSUM();
          if (lane < 32) *reinterpret_cast<u32x2*>(shm + VT + (lane & 15) * 16 + g4 * 8) = u32x2{pack2(d[0], d[1]), pack2(d[2], d[3])};
          WAIT_L(0);
        }
        if (k == N_C) {
          f32x4 n[2] = {};
#pragma unroll
          for (int q = 0; q < 8; ++q)
            n[q >> 2] = __builtin_amdgcn_mfma_f32_16x16x32_bf16(BF8(*reinterpret_cast<const u32x4*>(shm + VT + (q & 3) * 64 + g4 * 16)),
                                                                BF8(*reinterpret_cast<const u32x4*>(shm + STG + hw * 8192 + q * 1024 + lane * 16)), n[q >> 2], 0, 0, 0);
          if (lane < 16)
            asm volatile("global_store_dword %0, %1, %2 sc1" ::"v"(O_NP + nt * 4096 + (cc * 8 + mt) * 256 + hw * 64 + lane * 4), "v"(min(pack2(n[0][0], n[1][0]), SENT - 1u)), "s"(ws) : "memory");
          TS(33);
        }
        if (k == N_K + N_D && rnp) {
            WAIT_V(sch.ncnt);
            asm volatile("" : "+v"(gp[0]), "+v"(gp[1]), "+v"(gp[2]), "+v"(gp[3]));
            POLL(!__any(GPBAD), 2, GP4(O_NP + nt * 4096 + lane * 16, VW0));
            const unsigned r0 = nt * 128 + ((lane >> 2) & 3) * 32 + (lane & 3) * 4 + (g4 & 1) * 2;
        const f32x4 d = GPSUM();
        unsigned ky[4];
#pragma unroll
        for (int i = 0; i < 4; ++i) {
          const unsigned f = __float_as_uint(d[i]);
          ky[i] = (((f ^ ((unsigned)((int)f >> 31) | 0x80000000u)) >> 1) & ~2047u) | (r0 + (i & 1) * 16 + (i >> 1));
        }
        t1 = max(max(ky[0], ky[1]), max(ky[2], ky[3])), t2 = max(min(max(ky[0], ky[1]), max(ky[2], ky[3])), max(min(ky[0], ky[1]), min(ky[2], ky[3])));
        asm volatile("" : "+v"(t1), "+v"(t2));
        }
        if (k == N_K + N_D && rnp) {
        TOP2(0xB1); TOP2(0x4E); TOP2(0x141); TOP2(0x140);
        TOP2S(__builtin_amdgcn_permlane16_swap);
            if (lane == 0) asm volatile("global_store_dwordx2 %0, %1, %2 sc1" ::"v"(O_K2 + nt * 64), "v"(u32x2{t1, t2}), "s"(ws) : "memory");
            TS(34);
        }
        if (k == N_K && rnp) GP4(O_NP + nt * 4096 + lane * 16, "");
        if (k == B_RAT + 2 && rq_) {
          WAIT_V(sch.rcnt);
          asm volatile("" : "+v"(gp[0]), "+v"(gp[1]));
          POLL(__all(max(max4(gp[0]), max4(gp[1])) != SENT), 2, QRLD(VW0));
          u32x4 a;
#pragma unroll
          for (int c = 0; c < 2; ++c) {
            const f32x4 t = __builtin_bit_cast(f32x4, gp[c]);
            const float r = dpp_add<0x128>(swap16_add(swap32_add(t[0], t[2]), swap32_add(t[1], t[3])));
            a[2 * c] = pack2(r, r);
            a[2 * c + 1] = pack2(r - bflo(a[2 * c]), r - bflo(a[2 * c]));
          }
          if ((lane & 8) == 0)
            asm volatile("global_store_dwordx4 %0, %1, %2 sc1\n\ts_nop 1" ::"v"(O_QT + ((b - NPART) * 32 + (lane & 1) * 16 + ((lane >> 1) & 3) * 4 + g4) * 16), "v"(nsent4(a)), "s"(ws) : "memory");
        }
        if (k == B_QAT + B_QD) {
          asm volatile("s_waitcnt vmcnt(%1)" : "+v"(mq) : "n"(sch.qcnt) : "memory");
          POLL(__all(max4(mq) != SENT), 2, QLD(VW0));
          if (hw < 2) asm volatile("ds_write_b128 %0, %1" ::"v"(qa), "v"(mq) : "memory");
          else asm volatile("ds_write2_b32 %0, %1, %1 offset1:1" ::"v"(qa), "v"(mq[0]) : "memory");
        }
        if (k == CPF_B && hw == 2) CPFL();
        if (k == B_RAT) {
          if (rdc) {
            RRLD("");
            asm volatile("ds_read_b32 %0, %1\n\ts_waitcnt lgkmcnt(0)" : "=v"(wq) : "v"(wqa) : "memory");
          }
          if (rq_) QRLD("");
        }
        if (k == X_K) {
          const unsigned v4[4] = {xo, (xo ^ 64) + 8192 - 1024, (xo ^ 128) + 16384 - 2048, (xo ^ 192) + 24576 - 3072};
          unsigned l2 = lbase;
          asm volatile("" : "+s"(l2));
          GLDS4(l2 * 2 - lds0 + STG + 4096, v4, x);
        }
        if (k == V_T + 1 || k == N_K + 1) SKEWPT();
        if (k == B_QAT) QLD("");
        if (k == B_MAT) MODLD();
        if (k == B_WK) { DLD(8 - B_WN, 8); }
      }
      if (b >= NPART) WAIT_V(0);
      MODW(sch.mcnt);
      TS(30);
      SKEWPT();
      POLL(__all(max(max4(m[0]), max4(m[2])) != SENT), 4, MODLD(); MODW(0));
      *reinterpret_cast<u32x4*>(shm + SPARE + ma) = m[0];
      if (hw == 0) {
        asm volatile("ds_write_b128 %0, %1" ::"v"(ma2), "v"(m[2]) : "memory");
      } else if (hw == 2) {
        int d = max(max((int)(m[2][0] - ts0), (int)(m[2][1] - ts0)), max((int)(m[2][2] - ts0), (int)(m[2][3] - ts0)));
        d = max(d, (int)dpp_u<0xB1>(d));
        d = max(d, (int)dpp_u<0x4E>(d));
        d = max(d, (int)dpp_u<0x141>(d));
        d = max(d, (int)dpp_u<0x140>(d));
        const unsigned hh = ts0 + d;
        ssel[7] = hh;
        if ((int)(tick() - (unsigned)__builtin_amdgcn_readfirstlane((int)hh) - T_K) >= 0) K2LD("");
      } else {
        const f32x4 v = __builtin_bit_cast(f32x4, m[2]);
        const f32x2 c = {v[0] + v[1], v[2] + v[3]};
        const float s4 = dpp_add<0x4E>(dpp_add<0xB1>(c[0] + c[1]));
        if (lane < 8) asm volatile("ds_write_b64 %0, %1" ::"v"(ma2), "v"(lane < 4 ? c : f32x2{s4, s4}) : "memory");
      }
    }
    if (hw != 2) __builtin_amdgcn_s_setprio(0);
    TS(17);
    BT(48);
    LBAR();
    BTE();
    TS(18);
    if (hw == 2) {
      WAIT_V(0);
      asm volatile("" : "+v"(kk));
      if (__any(max(kk[0], kk[1]) == SENT)) {
        nap_tick((unsigned)__builtin_amdgcn_readfirstlane((int)ssel[7]) + T_K);
        POLL2(max(kk[0], kk[1]) == SENT, K_S, PCAP, K2LD(VW0));
      }
      TS(35);
      const unsigned me = lane < 32 ? kk[0] : kk[1], ot = (lane & 16) ? kk[1] : kk[0];
      unsigned rk = ot > me;
      sfor<15>([&](auto r) __attribute__((always_inline)) { rk += dpp_u<0x121 + decltype(r)::value>(ot) > me; });
      auto rs_ = __builtin_amdgcn_permlane16_swap(rk, rk, false, false);
      rk = rs_[0] + rs_[1];
      if (rk < 8u) {
        *reinterpret_cast<unsigned*>(shm + LST + rk * 4) = 0x8ffeu - (me & 2047u) * 2;
        if ((lane & 15) == nt) *reinterpret_cast<unsigned*>(shm + SLT + (me & 127u) * 4) = rk + 1;
      }
      asm volatile("" ::: "memory");
      ssel[20] = 1u;
    }
    TS(19);
    unsigned sfa = lds0 + SPARE + 10368, sv, one = 1u;
    RETIRE();
    const int kc = (g & 3) * 256 + hw * 64 + (lane & 7) * 8;
    const u32x4 cbq = *reinterpret_cast<const u32x4*>(&svb[2][kc]), sbq = *reinterpret_cast<const u32x4*>(&svb[1][kc]);
    f32x4 cb2[2] = {lo4(cbq), hi4(cbq)}, sb2[2] = {lo4(sbq), hi4(sbq)};
    float sw = cwj[8], cj8 = cwj[lane & 7];
    unsigned rk2 = lds0 + LST + (lane >> 3) * 4;
    unsigned xo_ = O_X1C + (lane >> 3) * 2048 + kc * 2, hwa = lds0 + XC + hw * 1024 + (lane & 7) * 128 + (((lane >> 3) ^ lane) & 7) * 16;
    unsigned hra = lds0 + XC + g4 * 128 + ((lane ^ g4) & 7) * 16, hrb = hra ^ 64, ara = lds0 + SPARE + 15872 + (lane & 2) * 64 + g4 * 32 + (lane & 1) * 16;
    unsigned pso = O_PART + ((g >> 2) * 2048 + (lane & 8) * 128 + (g & 3) * 256 + hw * 64 + (lane & 7) * 8 + g4 * 2) * 4;
    unsigned gso = O_GU + g * 1024 + (lane >> 5) * 256 + hw * 64 + (lane & 15) * 4, awa = lds0 + SPARE + 15872 + lane * 4, sfw = sfa + hw * 4;
    unsigned gl0 = O_GU + (g >> 2) * 4096 + lane * 8, gl1 = O_GU + (xcd * 7 + (mt + 7 - W_UO) % 7) * 4096 + 512 + lane * 8;
    asm volatile("" : "+v"(sfa), "+v"(one), "+v"(hra), "+v"(hrb), "+v"(ara), "+v"(pso), "+v"(gso), "+v"(awa), "+v"(gl0), "+v"(gl1), "+v"(sfw), "+v"(qm));
    asm volatile("" : "+v"(cb2[0]), "+v"(cb2[1]), "+v"(sb2[0]), "+v"(sb2[1]), "+v"(hwa), "+v"(sw), "+v"(cj8), "+v"(rk2), "+v"(xo_));
    unsigned sto = O_ST2 + (hw & (S_R - 1)) * 1280 + lane * 16, kmk = ~0u;
    asm volatile("" : "+v"(sto), "+v"(kmk));
    const unsigned th = (unsigned)__builtin_amdgcn_readfirstlane((int)ssel[7]);
    const char* wb = ws;
#define P2PUT(i) if (__builtin_expect(wb != ws, 0)) { WAIT_V(0); if (lane == 0) asm volatile("global_store_dwordx2 %0, %1, %2 sc1" ::"v"(O_F2 + (i)*8192 + (g * 4 + hw) * 8), "v"(tag), "s"(ws) : "memory"); }
#define P2GET(o) if (__builtin_expect(wb != ws, 0)) { u64 f; POLL2(f != tag, 4, PCAP, asm volatile("global_load_dwordx2 %0, %1, %2 sc1" VW0 : "=&v"(f) : "v"(O_F2 + (o)), "s"(ws) : "memory")); }
#pragma unroll 1
    for (;;) {
    TS(20);
    if (wb == ws) {
      unsigned r_;
      POLL2(sv == 0u, 2, 1 << 26, asm volatile("ds_read_b32 %0, %2 offset:80\n\tds_read_b32 %1, %3\n\ts_waitcnt lgkmcnt(0)" : "=&v"(sv), "=&v"(r_) : "v"(sfa), "v"(rk2) : "memory"));
      rk2 = r_;
    }
    TS(21);
    nap_tick(th + T_C3 - 10);
    {
      u32x4 dn;
      POLL2((dn[0] & dn[1] & dn[2] & dn[3]) == 0u, 1, 1 << 26, asm volatile("ds_read_b128 %0, %1 offset:96\n\ts_waitcnt lgkmcnt(0)" : "=v"(dn) : "v"(sfa) : "memory"));
    }
    __builtin_amdgcn_s_setprio(H_P);
    wait_tick(th + T_C3);
    TS(36);
    u32x4 st, xv_;
    POLL2(max(max4(xv_), max4(st)) == SENT, 0, PCAP,
          asm volatile("global_load_dwordx4 %0, %2, %4 sc1\n\tglobal_load_dwordx4 %1, %3, %4 sc1" VW0 : "=&v"(st), "=&v"(xv_) : "v"(sto), "v"(xo_), "s"(ws) : "memory"));
    TS(22);
    SKEWPT();
#define SUM8(v) dpp_add<0x141>(dpp_add<0x4E>(dpp_add<0xB1>(v)))
    const f32x4 sf = __builtin_bit_cast(f32x4, st);
    const float mean = SUM8(sf[0]) * 0.125f, dm = sf[0] - mean;
    const float rstd = __builtin_amdgcn_rsqf(SUM8(sf[1] + 128.0f * (dm * dm)) * (1.0f / D) + 1e-5f);
    asm volatile("ds_write_b128 %0, %1\n\tds_write_b32 %2, %3 offset:64" ::"v"(hwa),
                 "v"(pk8(((lo4(xv_) - mean) * rstd) * cb2[0] + sb2[0], ((hi4(xv_) - mean) * rstd) * cb2[1] + sb2[1])), "v"(sfw), "v"(one) : "memory");
    TS(3);
    u32x4 ha[8], fg;
#define HSLD(W)                                                                                                                             \
  asm volatile(                                                                                                                             \
      "ds_read_b128 %8, %11 offset:64\n\tds_read_b128 %0, %9\n\tds_read_b128 %1, %10 offset:512\n\tds_read_b128 %2, %9 offset:1024\n\t"       \
      "ds_read_b128 %3, %10 offset:1536\n\tds_read_b128 %4, %9 offset:2048\n\tds_read_b128 %5, %10 offset:2560\n\t"                          \
      "ds_read_b128 %6, %9 offset:3072\n\tds_read_b128 %7, %10 offset:3584" W                                                               \
      : "=&v"(ha[0]), "=&v"(ha[1]), "=&v"(ha[2]), "=&v"(ha[3]), "=&v"(ha[4]), "=&v"(ha[5]), "=&v"(ha[6]), "=&v"(ha[7]), "=&v"(fg)           \
      : "v"(hra), "v"(hrb), "v"(sfa)                                                                                                        \
      : "memory")
    unsigned key = (mkkey(rstd * SUM8(sf[2] + dm * cj8) + sw) | rk2) & kmk;
    asm volatile("" : "+v"(key));
    if (H_O == 0) HSLD("");
    unsigned t1 = key, t2 = 0u;
    TOP2(0x140);
    if (H_O == 1) { asm volatile("" : "+v"(t1), "+v"(t2)); HSLD(""); asm volatile("" : "+v"(t1), "+v"(t2)); }
    TOP2S(__builtin_amdgcn_permlane16_swap);
    if (H_O == 2) { asm volatile("" : "+v"(t1), "+v"(t2)); HSLD(""); asm volatile("" : "+v"(t1), "+v"(t2)); }
    TOP2S(__builtin_amdgcn_permlane32_swap);
    const int l1 = __builtin_ctzll(__builtin_amdgcn_ballot_w64(key == t1)), l2 = __builtin_ctzll(__builtin_amdgcn_ballot_w64(key == t2));
    unsigned mq[6];
    asm volatile("s_bfe_i32 %0, %6, 0x10003\n\ts_bfe_i32 %1, %7, 0x10003\n\ts_bfe_i32 %2, %6, 0x10004\n\ts_bfe_i32 %3, %7, 0x10004\n\t"
                 "s_bfe_i32 %4, %6, 0x10005\n\ts_bfe_i32 %5, %7, 0x10005\n\ts_xor_b32 %4, %4, 0xffff\n\ts_xor_b32 %5, %5, 0xffff"
                 : "=&s"(mq[0]), "=&s"(mq[1]), "=&s"(mq[2]), "=&s"(mq[3]), "=&s"(mq[4]), "=&s"(mq[5]) : "s"(l1), "s"(l2) : "scc");
    const u64 m0 = mk64(mq[1], mq[0]), m1 = mk64(mq[3], mq[2]), m2 = mk64(mq[5], mq[4]);
    asm volatile("s_waitcnt lgkmcnt(0)" : W8(ha), "+v"(fg)::"memory");
    TS(2);
    if (b < NPART) {
#define HBAD ((fg[0] & fg[1] & fg[2] & fg[3]) != one)
      if (__any(HBAD)) POLL2(HBAD, 0, 1 << 26, HSLD("\n\ts_waitcnt lgkmcnt(0)"));
      TS(23);
      f32x4 dg = {0.f, 0.f, 0.f, 0.f}, du = {0.f, 0.f, 0.f, 0.f};
#pragma unroll
      for (int f = 0; f < 8; ++f) {
        dg = __builtin_amdgcn_mfma_f32_16x16x32_bf16(BF8(ha[f]), BF8(gT[f]), dg, 0, 0, 0);
        du = __builtin_amdgcn_mfma_f32_16x16x32_bf16(BF8(ha[f]), BF8(uT[f]), du, 0, 0, 0);
      }
      const bool s0 = __builtin_amdgcn_inverse_ballot_w64(m0), s1 = __builtin_amdgcn_inverse_ballot_w64(m1);
      if (__builtin_amdgcn_inverse_ballot_w64(m2))
        asm volatile("global_store_dword %0, %1, %3 sc1 ; ALWAYS\n\tglobal_store_dword %0, %2, %3 offset:512 sc1" ::"v"(gso),
                     "v"(nsent(s1 ? (s0 ? dg[3] : dg[2]) : (s0 ? dg[1] : dg[0]))), "v"(nsent(s1 ? (s0 ? du[3] : du[2]) : (s0 ? du[1] : du[0]))), "s"(wb) : "memory");
    }
    if (hw == 0 && wb == ws) asm volatile("ds_write_b64 %0, %1" ::"v"(sfa), "v"(u32x2{t1, t2}) : "memory");
    if (b < NPART) {
      TS(24);
      asm volatile("" : W8(wdv)::"memory");
      TR8(dT, wdv);
      P2PUT(0);
      if (hw == 0) {
        u32x2 rr[8];
        if (E_D) __builtin_amdgcn_s_sleep(E_D);
        TS(39);
        P2GET(((((lane & 16) ? gl1 : gl0) - O_GU) >> 12) * 128 + (lane & 15) * 8);
#define MX2(i) max(max(rr[i][0], rr[i][1]), max(rr[i + 1][0], rr[i + 1][1]))
        POLL2(max(max(MX2(0), MX2(2)), max(MX2(4), MX2(6))) == SENT, 1, PCAP,
              TS(40);
              asm volatile("global_load_dwordx2 %0, %8, %10 sc1\n\tglobal_load_dwordx2 %1, %8, %10 offset:1024 sc1\n\t"
                           "global_load_dwordx2 %2, %8, %10 offset:2048 sc1\n\tglobal_load_dwordx2 %3, %8, %10 offset:3072 sc1\n\t"
                           "global_load_dwordx2 %4, %9, %10 sc1\n\tglobal_load_dwordx2 %5, %9, %10 offset:1024 sc1\n\t"
                           "global_load_dwordx2 %6, %9, %10 offset:2048 sc1\n\tglobal_load_dwordx2 %7, %9, %10 offset:3072 sc1" VW0
                           : "=&v"(rr[0]), "=&v"(rr[1]), "=&v"(rr[2]), "=&v"(rr[3]), "=&v"(rr[4]), "=&v"(rr[5]), "=&v"(rr[6]), "=&v"(rr[7])
                           : "v"(gl0), "v"(gl1), "s"(wb)
                           : "memory"));
        TS(8);
#define R2(i) __builtin_bit_cast(f32x2, rr[i])
        const f32x2 gs = (R2(0) + R2(1)) + (R2(2) + R2(3)), us = (R2(4) + R2(5)) + (R2(6) + R2(7));
        const unsigned gp = pack2(gs[0], gs[1]), up = pack2(us[0], us[1]);
        const unsigned sp = pack2(bflo(gp) * __builtin_amdgcn_rcpf(1.0f + __expf(-bflo(gp))), bfhi(gp) * __builtin_amdgcn_rcpf(1.0f + __expf(-bfhi(gp))));
        asm volatile("ds_write_b32 %0, %1\n\tds_write_b32 %2, %3 offset:24" ::"v"(awa), "v"(pack2(bflo(sp) * bflo(up), bfhi(sp) * bfhi(up))), "v"(sfa), "v"(one) : "memory");
      }
      TS(25);
      SKEWPT();
      POLL2(sv != one, 0, 1 << 26, asm volatile("ds_read_b32 %1, %3 offset:24\n\tds_read_b128 %0, %2\n\ts_waitcnt lgkmcnt(0)" : "=&v"(fg), "=&v"(sv) : "v"(ara), "v"(sfa) : "memory"));
      asm volatile("global_load_dwordx2 %0, %1, %2 sc1" : "=&v"(vd) : "v"(O_VD + (b & 31) * 128), "s"(ws) : "memory");
      const f32x2 dd = MV64(dT, fg);
      asm volatile("global_store_dwordx2 %0, %1, %2 sc1" ::"v"(pso), "v"(u32x2{nsent(dd[0]), nsent(dd[1])}), "s"(wb) : "memory");
      TS(6);
      P2PUT(1);
      asm volatile("s_waitcnt vmcnt(1)" : "+v"(vd)::"memory");
      if (__any(max(vd[0], vd[1]) == SENT)) VDLD(2);
      TS(5);
    } else if (hw == 3) {
      u32x2 g0, g1;
      nap_tick(th + T_C4);
      POLL2(min(min(g0[0], g0[1]), min(g1[0], g1[1])) == 0u, 2, PCAP,
            asm volatile("global_load_dwordx2 %0, %2, %4 sc1\n\tglobal_load_dwordx2 %1, %3, %4 sc1" VW0
                         : "=&v"(g0), "=&v"(g1) : "v"(O_KEY + lane * 8 * KSTR), "v"(O_KEY + (lane + 64) * 8 * KSTR), "s"(ws) : "memory"));
      const unsigned k1 = t1, k2 = t2;
      t1 = max(g0[0], g1[0]), t2 = max(min(g0[0], g1[0]), max(g0[1], g1[1]));
      TOP2(0xB1); TOP2(0x4E); TOP2(0x141); TOP2(0x140);
      TOP2S(__builtin_amdgcn_permlane16_swap);
      TOP2S(__builtin_amdgcn_permlane32_swap);
      vd = u32x2{max(t1, k1), max(min(t1, k1), max(t2, k2))};
      if (lane == 0) asm volatile("global_store_dwordx2 %0, %1, %2 sc1\n\tds_write_b64 %3, %1 offset:8" ::"v"(O_VD + (b & 31) * 128), "v"(vd), "s"(ws), "v"(sfa) : "memory");
      TS(29);
      break;
    } else if (hw < 2) {
      const int tok = b & 1, c0 = ((b - NPART) >> 1) * 64 + hw * 32 + (lane & 7) * 4, col = c0 + g4;
      const unsigned td = tick(), kk = tok ? t2 : t1;
      const int ln = tok ? l2 : l1;
      unsigned v;
      POLL2(v == SENT, 1, PCAP, asm volatile("global_load_dword %0, %1, %2 sc1" VW0 : "=&v"(v) : "v"(O_X1C + ((__builtin_amdgcn_readlane(xo_, ln) - O_X1C) >> 11) * 2048 + (col >> 1) * 4), "s"(ws) : "memory"));
      float x1 = (col & 1) ? bfhi(v) : bflo(v);
      float h2 = rbf(((x1 - __int_as_float(__builtin_amdgcn_readlane(__float_as_int(mean), ln))) * __int_as_float(__builtin_amdgcn_readlane(__float_as_int(rstd), ln))) * bf2f(svb[2][col]) + bf2f(svb[1][col]));
      float gb = bf2f(svb[3][col]), rsg = rbf(1.0f / (1.0f + __expf(-key_val(kk))));
      unsigned oo_ = (key_row(kk) * D + col) * 2, po = O_PART + ((lane >> 3) * 2048 + tok * 1024 + c0) * 4, p1 = po + 4 * 65536;
      asm volatile("" : "+v"(x1), "+v"(h2), "+v"(gb), "+v"(rsg), "+v"(oo_), "+v"(po), "+v"(p1));
      u32x4 y[7];
      nap_tick(td + F_T - 10);
      wait_tick(td + F_T);
      TS(37);
      P2GET(8192 + (min(lane, 55) * 16 + ((b - NPART) >> 1)) * 8);
      POLL2(max(max(max(max4(y[0]), max4(y[1])), max(max4(y[2]), max4(y[3]))), max(max(max4(y[4]), max4(y[5])), max4(y[6]))) == SENT || vd[1] == 0u, 0, PCAP,
            TS(38);
            asm volatile("global_load_dwordx4 %0, %7, %9 sc1\n\tglobal_load_dwordx4 %1, %7, %10 sc1\n\tglobal_load_dwordx4 %2, %7, %11 sc1\n\t"
                         "global_load_dwordx4 %3, %7, %12 sc1\n\tglobal_load_dwordx4 %4, %8, %9 sc1\n\tglobal_load_dwordx4 %5, %8, %10 sc1\n\t"
                         "global_load_dwordx4 %6, %8, %11 sc1"
                         : "=&v"(y[0]), "=&v"(y[1]), "=&v"(y[2]), "=&v"(y[3]), "=&v"(y[4]), "=&v"(y[5]), "=&v"(y[6])
                         : "v"(po), "v"(p1), "s"(wb), "s"(wb + 65536), "s"(wb + 131072), "s"(wb + 196608)
                         : "memory");
            asm volatile("s_waitcnt vmcnt(2)\n\tds_read_b64 %7, %8 offset:8\n\ts_waitcnt vmcnt(0) lgkmcnt(0)"
                         : "+v"(y[0]), "+v"(y[1]), "+v"(y[2]), "+v"(y[3]), "+v"(y[4]), "+v"(y[5]), "+v"(y[6]), "=&v"(vd) : "v"(sfa) : "memory"));
      TS(28);
#define YF(i) __builtin_bit_cast(f32x4, y[i])
      const f32x4 t = ((YF(0) + YF(1)) + (YF(2) + YF(3))) + ((YF(4) + YF(5)) + YF(6));
      const float sum = dpp_add<0x128>(swap16_add(swap32_add(t[0], t[2]), swap32_add(t[1], t[3])));
      asm volatile("global_store_short %0, %1, %2" ::"v"(oo_), "v"(pack2(x1 + gb * (wb == ws && kk < vd[1] ? h2 : rbf(h2 + rbf(rbf(sum) * rsg))), 0.f)), "s"(out) : "memory");
      TS(15);
    } else {
      break;
    }
    if (__builtin_expect(wb != ws || __all(vd[1] == t2), 1)) break;
    const unsigned tk = (lane & 8) ? vd[1] : vd[0], ns = tk == t1 ? l1 >> 3 : 8 + ((lane >> 3) & 1);
    rk2 = tk & 0xffffu, kmk = lane < 16 ? ~0u : 0u, one = 3u;
    xo_ = O_X1C + ns * 2048 + kc * 2, sto = O_ST2 + ns * 128 + (lane & 7) * 16;
    wb = ws + O_P2;
    }
    TSE();
  }
}

extern "C" void kernel_launch(void* const* d_in, const int* in_sizes, int n_in, void* d_out, int out_size, void* d_ws, size_t ws_size,
                              hipStream_t stream) {
  const u16* x = (const u16*)d_in[0];
  const u16* wsr = (const u16*)d_in[1];
  const u16* wsi = (const u16*)d_in[2];
  const u16* whr = (const u16*)d_in[3];
  const u16* whi = (const u16*)d_in[4];
  const u16* w_r = (const u16*)d_in[6];
  const u16* w_gate = (const u16*)d_in[7];
  const u16* w_up = (const u16*)d_in[8];
  const u16* w_down = (const u16*)d_in[9];
  const u16* c = (const u16*)d_in[10];
  const u16* w_mod = (const u16*)d_in[11];
  u16* out = (u16*)d_out;
  k_main<<<256, 512, 0, stream>>>(x, (char*)d_ws, wsr, wsi, c, w_mod, whr, whi, w_r, w_gate, w_up, w_down, out);
}
```

```cpp
#include <hip/hip_runtime.h>
#include <hip/hip_bf16.h>
#include <stdint.h>
#include <utility>

#define NEWLAY 1
typedef unsigned short u16;
typedef unsigned long long u64;
using bf16x8 = __attribute__((ext_vector_type(8))) short;
using bf16v8 = __attribute__((ext_vector_type(8))) __bf16;
using f32x4 = __attribute__((ext_vector_type(4))) float;
using f32x2 = __attribute__((ext_vector_type(2))) float;
using u32x4 = __attribute__((ext_vector_type(4))) unsigned;
using u32x2 = __attribute__((ext_vector_type(2))) unsigned;

constexpr int S = 2048, D = 1024, F = 3584;

__device__ __forceinline__ float bf2f(unsigned v) { return __uint_as_float(v << 16); }
__device__ __forceinline__ float bflo(unsigned v) { return __uint_as_float(v << 16); }
__device__ __forceinline__ float bfhi(unsigned v) { return __uint_as_float(v & 0xffff0000u); }
__device__ __forceinline__ unsigned f2bf(float f) {
  __hip_bfloat16 b = __float2bfloat16(f);
  return (unsigned)*reinterpret_cast<u16*>(&b);
}
__device__ __forceinline__ float rbf(float f) { return bf2f(f2bf(f)); }
using bf16v2 = __attribute__((ext_vector_type(2))) __bf16;
__device__ __forceinline__ unsigned pack2(float lo, float hi) {
  return __builtin_bit_cast(unsigned, __builtin_convertvector(f32x2{lo, hi}, bf16v2));
}

template <int CTRL>
__device__ __forceinline__ float dpp_f(float v) {
  return __int_as_float(__builtin_amdgcn_mov_dpp(__float_as_int(v), CTRL, 0xf, 0xf, true));
}
template <int CTRL>
__device__ __forceinline__ float dpp_add(float v) { return v + dpp_f<CTRL>(v); }
__device__ __forceinline__ float row_sum(float v) { return dpp_add<0x140>(dpp_add<0x141>(dpp_add<0x4E>(dpp_add<0xB1>(v)))); }
template <int CTRL>
__device__ __forceinline__ unsigned dpp_u(unsigned v) { return (unsigned)__builtin_amdgcn_mov_dpp((int)v, CTRL, 0xf, 0xf, true); }
__device__ __forceinline__ u64 mk64(unsigned hi, unsigned lo) { return ((u64)hi << 32) | lo; }
__device__ __forceinline__ float swap32_add(float a, float b) {
  auto r = __builtin_amdgcn_permlane32_swap(__float_as_uint(a), __float_as_uint(b), false, false);
  return __uint_as_float(r[0]) + __uint_as_float(r[1]);
}
__device__ __forceinline__ float swap16_add(float a, float b) {
  auto r = __builtin_amdgcn_permlane16_swap(__float_as_uint(a), __float_as_uint(b), false, false);
  return __uint_as_float(r[0]) + __uint_as_float(r[1]);
}
__device__ __forceinline__ float ar4(float v) {
  const float t = swap32_add(v, v);
  return swap16_add(t, t);
}
__device__ __forceinline__ float wave_sum_s(float v) {
  v = row_sum(v);
  v += __int_as_float(__builtin_amdgcn_update_dpp(0, __float_as_int(v), 0x142, 0xa, 0xf, false));
  v += __int_as_float(__builtin_amdgcn_update_dpp(0, __float_as_int(v), 0x143, 0xc, 0xf, false));
  return __int_as_float(__builtin_amdgcn_readlane(__float_as_int(v), 63));
}
__device__ __forceinline__ float silu_bf(float c) { return rbf(c * __builtin_amdgcn_rcpf(1.0f + __expf(-c))); }
__device__ __forceinline__ float dsum(unsigned v, float a) {
  return __builtin_amdgcn_fdot2_f32_bf16(__builtin_bit_cast(bf16v2, v), __builtin_bit_cast(bf16v2, 0x3f803f80u), a, false);
}
template <class Fn, int... I>
__device__ __forceinline__ void sfor_(Fn&& f, std::integer_sequence<int, I...>) { (f(std::integral_constant<int, I>{}), ...); }
template <int N, class Fn>
__device__ __forceinline__ void sfor(Fn&& f) { sfor_(f, std::make_integer_sequence<int, N>{}); }
constexpr unsigned SENT = 0xFFFFFFFFu;
constexpr int NPART = 224;

constexpr size_t WS_EPO = 0;
constexpr size_t WS_XFL = 4096;
constexpr size_t WS_MODP = 40960;
constexpr size_t WS_KEYG = 320u << 10;
constexpr size_t WS_UFL = WS_KEYG + 16384;
constexpr size_t WS_MOD = WS_KEYG + (80u << 10);
constexpr size_t WS_RS = WS_KEYG + (92u << 10);
constexpr size_t WS_PART = WS_KEYG + (332u << 10);
constexpr unsigned O_MODP = WS_MODP, O_XFL = WS_XFL, O_KEY = WS_KEYG, O_MOD = WS_MOD, O_RS = WS_RS, O_M2 = WS_RS + 80 * 1024;
constexpr unsigned O_PART = WS_PART, O_GU = O_PART + 56 * 8192, O_STAT = O_GU + 224 * 1024, O_X1C = O_STAT + 262144;
constexpr unsigned O_ST2 = O_X1C + 20480, O_VD = O_ST2 + 6144, O_P2 = 1536u << 10, O_F2 = 3584u << 10;
constexpr unsigned O_GC = O_M2 + 8704, O_VP = O_M2 + 12288, O_NP = O_VP + 65536, O_K2 = O_NP + 65536, O_QT = O_K2 + 4096, O_G2 = 3u << 20, O_G1 = O_G2 + 262144;
static_assert(O_QT + 16384 == O_PART && O_VD + 4096 <= O_KEY + (512 + 832) * 1024 && O_PART + O_P2 >= O_VD + 4096 && O_STAT + O_P2 <= O_G2, "armed area");
constexpr size_t WS_H = 4u << 20;
constexpr size_t WS_T = WS_H + (size_t)S * D * 2;
constexpr size_t WS_P = WS_T + (size_t)2 * D * S * 2;

#define TSG(base, i)
#define TSD(i)
#define PCAP (1 << 20)
#define PDBG(it)
#define TS(i)
#define TSE()
#define BT(k)
#define BTE()
#define BAR(k) do { BT(k); BARRIER(); } while (0)
#define WAIT_V(n) asm volatile("s_waitcnt vmcnt(%0)" ::"n"(n) : "memory")
#define WAIT_L(n) asm volatile("s_waitcnt lgkmcnt(%0)" ::"n"(n) : "memory")
#define BARRIER() asm volatile("s_barrier" ::: "memory")
#define LBAR() asm volatile("s_waitcnt lgkmcnt(0)\n\ts_barrier" ::: "memory")
#define DSR(dst, addr, off) asm volatile("ds_read_b128 %0, %1 offset:%2" : "=v"(dst) : "v"(addr), "n"(off) : "memory")
#define MFMA(c, a, b) asm volatile("v_mfma_f32_16x16x32_bf16 %0, %1, %2, %0" : "+v"(c) : "v"(a), "v"(b))
#define MFMA0(c, a, b) asm volatile("v_mfma_f32_16x16x32_bf16 %0, %1, %2, 0" : "=&v"(c) : "v"(a), "v"(b))
#define GLDS(m0v, voff, gbase) \
  asm volatile("s_mov_b32 m0, %0\n\ts_nop 0\n\tglobal_load_lds_dwordx4 %1, %2" ::"s"(m0v), "v"(voff), "s"(gbase) : "memory")
#define GLDS4(m0v, v, gbase)                                                                                                        \
  asm volatile("s_mov_b32 m0, %0\n\ts_nop 0\n\tglobal_load_lds_dwordx4 %1, %5\n\tglobal_load_lds_dwordx4 %2, %5 offset:1024\n\t"    \
               "global_load_lds_dwordx4 %3, %5 offset:2048\n\tglobal_load_lds_dwordx4 %4, %5 offset:3072" ::"s"(m0v),               \
               "v"((v)[0]), "v"((v)[1]), "v"((v)[2]), "v"((v)[3]), "s"(gbase) : "memory")
#define LDG(dst, voff, base) asm volatile("global_load_dwordx4 %0, %1, %2" : "=v"(dst) : "v"(voff), "s"(base) : "memory")
#define HPOL ""
#define XPOL ""
#define WPOL " nt"
#define LDM(dst, voff, base) asm volatile("global_load_dwordx4 %0, %1, %2" WPOL : "=v"(dst) : "v"(voff), "s"(base) : "memory")
#define LDX(dst, voff, base, off) asm volatile("global_load_dwordx4 %0, %1, %2 offset:%3" : "=v"(dst) : "v"(voff), "s"(base), "n"(off) : "memory")
#define LDXS(dst, voff, base, off) asm volatile("global_load_dwordx4 %0, %1, %2 offset:%3 sc1" : "=v"(dst) : "v"(voff), "s"(base), "n"(off) : "memory")
#define BF8(v) __builtin_bit_cast(bf16v8, v)
#define LSPIN(v, p, cond)                                   \
  _Pragma("unroll 1") for (int it_ = 0; it_ < (1 << 26); ++it_) { \
    v = __atomic_load_n(p, __ATOMIC_RELAXED);               \
    if (cond) break;                                        \
    __builtin_amdgcn_s_sleep(1);                            \
  }
#define POLL(ok, sl, reload)                                                  \
  if (__builtin_expect(!(ok), 0)) {                                           \
    _Pragma("unroll 1") for (int it = 0; it < PCAP; ++it) {                   \
      PDBG(it);                                                               \
      __builtin_amdgcn_s_sleep(sl);                                           \
      reload;                                                                 \
      if (ok) break;                                                          \
    }                                                                         \
  }
#define POLL2(bad, sl, cap, load)                                             \
  {                                                                           \
    int it = (cap);                                                           \
    u64 live;                                                                 \
    _Pragma("unroll 1") do {                                                  \
      if (sl) __builtin_amdgcn_s_sleep(sl);                                   \
      asm volatile("s_sub_u32 %0, %0, 1\n\ts_cselect_b64 %1, 0, -1" : "+s"(it), "=s"(live)::"scc"); \
      load;                                                                   \
    } while (__builtin_amdgcn_ballot_w64(bad) & live);                        \
    PDBG(PCAP - 2 - it);                                                      \
  }
__device__ __forceinline__ unsigned tick() {
  u64 t;
  asm volatile("s_memrealtime %0\n\ts_waitcnt lgkmcnt(0)" : "=s"(t)::"memory");
  return (unsigned)t;
}
__device__ __forceinline__ void wait_tick(unsigned t) {
#pragma unroll 1
  for (int i = 0; i < 256; ++i)
    if ((int)(tick() - t) >= 0) break;
}
__device__ __forceinline__ void nap_tick(unsigned t) {
#pragma unroll 1
  for (int i = 0; i < 1024; ++i) {
    if ((int)(tick() - t) >= 0) break;
    __builtin_amdgcn_s_sleep(2);
  }
}
#define T_C1 245
#define T_A 44
#define T_N 2500
#define T_D1 78
#define VW0 "\n\ts_waitcnt vmcnt(0)"
#define W8(a) "+v"(a[0]), "+v"(a[1]), "+v"(a[2]), "+v"(a[3]), "+v"(a[4]), "+v"(a[5]), "+v"(a[6]), "+v"(a[7])
#define PFL(ptr) asm volatile("s_mov_b32 m0, %0\n\ts_nop 0\n\tglobal_load_lds_dword %1, off" ::"s"(pfl), "v"(ptr) : "memory")

#define CPFL() asm volatile("s_mov_b32 m0, %0\n\ts_mov_b64 exec, 0xfff\n\tglobal_load_lds_dword %1, off\n\ts_mov_b64 exec, -1" ::"s"(pfl), "v"(cpf) : "memory")
#define HHOOK(j)
#define XPV 1557
#define XPW 1557
#define XPM (MODE ? XPW : XPV)
#define XPA0 (XPM / 1000)
#define XPA1 (XPM / 100 % 10)
#define XPB0 (XPM / 10 % 10)
#define XPB1 (XPM % 10)
#define U_X0 6
#define XLD(dst, XP, XH, i)                                                                                   \
  do {                                                                                                        \
    if (MODE) LDXS(dst, (i) ? xo1 : xo0, XP, ((XH)&3) * 2048 - 4096);                                         \
    else LDXS(dst, ((XH)&4) ? xo1 : xo0, XP, ((XH)&3) * 2048 + (i)*1024 - 4096);                              \
  } while (0)
#define HALF(XC, YA, RD, XN, XP, XH, LD, YP)                                                 \
  do {                                                                                       \
    _Pragma("unroll") for (int j = 0; j < 8; ++j) {                                          \
      if ((j & 1) == 0) { if (RD) WAIT_L(5); else WAIT_L(j == 0 ? 5 : 6 - j); }              \
      if (MODE == 2) MFMA0(acc[0][j], Y[j], XC[0]); else if (MODE) MFMA(acc[0][j], Y[j], XC[0]); else MFMA(acc[0][j], XC[0], Y[j]); \
      if (j == 0) DSR(Y[7], YP, 7 * 2048);                                                   \
      else if (RD) DSR(Y[j - 1], YA, (j - 1) * 2048);                                        \
      if (MODE == 2) MFMA0(acc[1][j], Y[j], XC[1]); else if (MODE) MFMA(acc[1][j], Y[j], XC[1]); else MFMA(acc[1][j], XC[1], Y[j]); \
      if ((LD) && j == (((XH)&1) ? XPA0 : XPB0)) XLD(XN[0], XP, XH, 0);                                             \
      if ((LD) && j == (((XH)&1) ? XPA1 : XPB1)) XLD(XN[1], XP, XH, 1);                                             \
      HHOOK(j);                                                                              \
    }                                                                                        \
  } while (0)

__device__ __forceinline__ int key_row(unsigned k) { return (int)((~k >> 1) & 2047u); }
__device__ __forceinline__ unsigned mkkey(float v) {
  const unsigned rb = pack2(0.f, v);
  return rb ^ ((unsigned)((int)rb >> 31) & 0x7fff0000u) ^ 0x80000000u;
}
__device__ __forceinline__ float key_val(unsigned k) {
  const unsigned ord = k >> 16;
  return bf2f(ord ^ ((ord & 0x8000u) ? 0x8000u : 0xffffu));
}
__device__ __forceinline__ unsigned nsent(float v) { return min(__float_as_uint(v), SENT - 1u); }
__device__ __forceinline__ f32x4 lo4(u32x4 q) { return f32x4{bflo(q[0]), bfhi(q[0]), bflo(q[1]), bfhi(q[1])}; }
__device__ __forceinline__ f32x4 hi4(u32x4 q) { return f32x4{bflo(q[2]), bfhi(q[2]), bflo(q[3]), bfhi(q[3])}; }
__device__ __forceinline__ u32x4 pk8(f32x4 a, f32x4 b) { return u32x4{pack2(a[0], a[1]), pack2(a[2], a[3]), pack2(b[0], b[1]), pack2(b[2], b[3])}; }
__device__ __forceinline__ u32x4 nsent4(u32x4 v) { return u32x4{min(v[0], SENT - 1u), min(v[1], SENT - 1u), min(v[2], SENT - 1u), min(v[3], SENT - 1u)}; }
__device__ __forceinline__ unsigned max4(u32x4 v) { return max(max(v[0], v[1]), max(v[2], v[3])); }
__device__ __forceinline__ float amin16(const u32x4* v) {
  float a, b;
  asm("v_min3_f32 %0, |%2|, |%3|, |%4|\n\tv_min3_f32 %1, |%5|, |%6|, |%7|\n\tv_min3_f32 %0, |%8|, |%9|, %0\n\tv_min3_f32 %1, |%10|, |%11|, %1\n\t"
      "v_min3_f32 %0, |%12|, |%13|, %0\n\tv_min3_f32 %1, |%14|, |%15|, %1\n\tv_min3_f32 %0, |%16|, |%17|, %0\n\tv_min_f32 %0, %0, %1"
      : "=&v"(a), "=&v"(b)
      : "v"(v[0][0]), "v"(v[0][1]), "v"(v[0][2]), "v"(v[0][3]), "v"(v[1][0]), "v"(v[1][1]), "v"(v[1][2]), "v"(v[1][3]), "v"(v[2][0]), "v"(v[2][1]),
        "v"(v[2][2]), "v"(v[2][3]), "v"(v[3][0]), "v"(v[3][1]), "v"(v[3][2]), "v"(v[3][3]));
  return a;
}
__device__ __forceinline__ u32x4 selu(unsigned mk, u32x4 a, u32x4 b) { return (a & mk) | (b & ~mk); }
#define KSTR 16
#define TOP2(CTRL)                                                  \
  do {                                                              \
    const unsigned o1 = dpp_u<CTRL>(t1), o2 = dpp_u<CTRL>(t2);      \
    t2 = max(min(t1, o1), max(t2, o2));                             \
    t1 = max(t1, o1);                                               \
  } while (0)
#define TOP2S(SW)                                                   \
  do {                                                              \
    auto a_ = SW(t1, t1, false, false), b_ = SW(t2, t2, false, false); \
    t2 = max(min(a_[0], a_[1]), max(b_[0], b_[1]));                 \
    t1 = max(a_[0], a_[1]);                                         \
  } while (0)
#define T_C3 253
#define T_C4 340
#define E_D 6
#define O_DW 6
#define O_DS 4
#define OPOL " sc1"
#define X_PA 2
#define G1_C 2
#define R_AT 24
#define B_TK 7
#define W_T 4
#define V_K 3
#define V_T 5
#define N_K 9
#define N_D 2
#define T_K 0
#define H_AT 23
#define G1_S 1
#define F_T 142
#define K_S 1
#define N_C (V_T + 1)
#define T_O 250
#define A_S1 9
#define A_S2 9
#define A_P2 12
#define X_K 13
#define H_P 2
#define H_O 2
#define S_R 2
#define T_X 80
#define A_PS 3
#define U_S 4
#define C_LD 2
#define C_DP 8
#define F_D 2
#define F_AT 29
#define PW0 2
#define T_W 70
#define P_D2 1
#define P_D4 1
#define XSTR 16
#define P_D3 20
#ifndef M_NS
#define M_NS 6
#endif
#define A_Q1 27
#define B_RAT 5
#define B_MAT 13
#define U_D 4
#define FSTR 16
#define B_J 4
#define RETIRE()                                                                                                              \
  do {                                                                                                                        \
    if (hw == 3 && lane < (b == 255 ? 2 : 1))                                                                                 \
      asm volatile("global_store_dwordx2 %0, %1, %2 sc1" ::"v"(lane ? (unsigned)WS_EPO : O_XFL + b * XSTR * 4), "v"(lane ? tag : ~tag), "s"(ws) : "memory"); \
  } while (0)
#define U_N1 3
#define B_QAT 12
#define B_QD 2
#define TP_PS 5
#define TP_XC 34
#define TP_P0 2
#define TP_X0 24
#define TP_XL 54
#define W_UO 3
#define CPF_K 24
#define CPF_B 8
#define B_WN 4
#define B_WK 15
struct BSched {
  int cnt[16], rcnt, mcnt, qcnt, vcnt, ncnt;
  constexpr BSched() : cnt{}, rcnt(0), mcnt(0), qcnt(0), vcnt(0), ncnt(0) {
    int pos[16] = {}, n = 0, rpos = 0, mpos = 0, qpos = 0, npos = 0;
    for (int k = 0; k <= B_J - 2 + M_NS; ++k) pos[k] = -8 * (B_J - 2 + M_NS - k);
    for (int k = B_J + 1; k < 16; ++k) {
      cnt[k] = n - pos[k] > 63 ? 63 : n - pos[k];
      if (k - 2 + M_NS < 16) { n += 4; pos[k - 2 + M_NS] = n; }
      if (k == B_RAT + 2) rcnt = n - rpos;
      if (k == B_QAT + B_QD) qcnt = n - qpos;
      if (k == V_T) vcnt = n;
      if (k == N_K + N_D) ncnt = n - npos;
      if (k == N_K) npos = n;
      if (k == B_RAT) rpos = n;
      if (k == X_K) n += 4;
      if (k == B_QAT) { n += 1; qpos = n; }
      if (k == B_MAT) { n += 2; mpos = n; }
      if (k == B_WK) n += B_WN;
    }
    mcnt = n - mpos > 63 ? 63 : n - mpos;
  }
};
__global__ __launch_bounds__(512, 2) void k_main(const u16* __restrict__ x, char* ws, const u16* __restrict__ Y0, const u16* __restrict__ Y1,
                                              const u16* __restrict__ cvec, const u16* __restrict__ wmod, const u16* __restrict__ X0,
                                              const u16* __restrict__ X1, const u16* __restrict__ wr_, const u16* __restrict__ wg,
                                              const u16* __restrict__ wu, const u16* __restrict__ wd, u16* __restrict__ out) {
  constexpr int DX = 4, NS = M_NS, STG = NS * 16384, SPARE = STG + 32768;
  constexpr int LST = SPARE + 17664, SLT = LST + 256, XC = SLT + 512, VT = XC + 4096;
  __shared__ __attribute__((aligned(1024))) char shm[VT + 256];
  u16(*svb)[1024] = reinterpret_cast<u16(*)[1024]>(shm + SPARE);
  float* cwj = reinterpret_cast<float*>(shm + SPARE + 10240);
  unsigned* ssel = reinterpret_cast<unsigned*>(shm + SPARE + 10368);
  float* smod = reinterpret_cast<float*>(shm + SPARE + 15872);
  u16* ssa = reinterpret_cast<u16*>(smod);
  constexpr int TAB = SPARE + 10752, RKA = TAB + 1536, RKB = TAB + 3584;
  u16(*scw)[128] = reinterpret_cast<u16(*)[128]>(shm + TAB + 512);
  const int b = blockIdx.x, xcd = b & 7, j8 = b >> 3;
  const int cc = xcd >> 2, nt = (xcd & 3) * 4 + (j8 & 3), mt = j8 >> 2, et = mt;
  const int tid = threadIdx.x, lane = tid & 63;
  const int wid = __builtin_amdgcn_readfirstlane(tid >> 6);
  const unsigned lds0 = (unsigned)(size_t)(__attribute__((address_space(3))) char*)shm;
  const char* cpf;
  {
    unsigned long long pc;
    unsigned len;
    asm volatile("s_getpc_b64 %0\n.Lcpf_here:\n\ts_mov_b32 %1, .Lcpf_end-.Lcpf_here-4\n\t.subsection 1\n.Lcpf_end:\n\t.subsection 0" : "=s"(pc), "=s"(len));
    cpf = reinterpret_cast<const char*>(pc) + min((unsigned)((lane * 32 + j8) * 128), len);
  }
  const unsigned pfl = lds0 + SPARE + 17408;
  if (wid == 4) CPFL();
  u32x4 xv[2];
  {
    const unsigned xo = (unsigned)((b * 8 + wid) * D + lane * 8) * 2;
    asm volatile("global_load_dwordx4 %0, %2, %3" XPOL "\n\tglobal_load_dwordx4 %1, %2, %3 offset:1024" XPOL : "=&v"(xv[0]), "=&v"(xv[1]) : "v"(xo), "s"(x) : "memory");
  }
  asm volatile("" ::"s"(ws), "s"(Y0), "s"(Y1), "s"(cvec), "s"(wmod), "s"(wr_));
  u16* xn = reinterpret_cast<u16*>(ws + WS_H);
  u16* U = reinterpret_cast<u16*>(ws + WS_T);
  unsigned* part = reinterpret_cast<unsigned*>(ws + WS_PART);
  const int g = (b & 7) * 28 + (b >> 3);
  unsigned* ufl = reinterpret_cast<unsigned*>(ws + WS_UFL) + (cc * 16 + nt) * 32 * FSTR;
  unsigned* epo = reinterpret_cast<unsigned*>(ws + WS_EPO);
#define SKEWPT()
  const int g4 = lane >> 4;
  u64 tag0, tag;
#define TAGW(n) asm volatile("s_waitcnt vmcnt(%2)\n\tv_lshl_add_u64 %0, %1, 0, 1" : "=v"(tag) : "v"(tag0), "n"(n) : "memory")
  u32x4 wv[8], cq;
  const char* pfy = reinterpret_cast<const char*>(cc ? Y1 : Y0) + (size_t)((xcd & 3) * 512 + j8 * 16 + (lane & 15)) * 4096 + g4 * 128;
#define PFG(g) (pfy + ((((g) + xcd) & 7) << 9))
  const int u = b * 2 + (wid & 1), cg0 = u & 31, ks0 = u >> 5;
  unsigned ta = lds0 + STG + ((lane >> 2) & 3) * 2368 + (wid * 128 + (lane >> 5) * 32 + (lane & 3) * 8 + (g4 & 1) * 4) * 2;
  unsigned oo = (unsigned)((wid * 4 + (lane >> 5)) * 131072 + (b >> 2) * 2048 + (g4 & 1) * 1024 + ((b & 3) * 16 + (lane & 15)) * 16), oo2 = oo + 262144;
  asm volatile("" : "+v"(ta), "+v"(oo), "+v"(oo2));
  unsigned wa = lds0 + STG + wid * 2368 + lane * 16;
  asm volatile("" : "+v"(wa));
  const unsigned wo = (unsigned)((ks0 * 64 + (lane >> 3) * 8) * 6144 + 2048 + (cg0 >> 4) * 1024 + cg0 * 64 + (lane & 7) * 8) * 2;
  if (wid == 0) TSG(part, 0);
  BT(50);
  if (wid >= 3) {
    asm volatile("global_load_dwordx2 %0, %1, off sc1" : "=v"(tag0) : "v"(epo) : "memory");
    if (wid >= 6) {
      LDG(cq, (ks0 * 64 + g4 * 16 + (lane & 1) * 8) * 2, cvec);
#pragma unroll
      for (int i = 0; i < 8; ++i) LDM(wv[i], wo + i * 12288, wmod);
      const unsigned av = u < 80 ? 0u : ~0u;
      asm volatile("global_store_dwordx4 %0, %1, %2 sc1\n\ts_nop 1" ::"v"(O_KEY + u * 1024 + lane * 16), "v"(u32x4{av, av, av, av}), "s"(ws) : "memory");
      WAIT_V(11);
    } else if (wid == 4) {
      __builtin_amdgcn_s_sleep(C_DP);
      PFL(PFG(0));
      WAIT_V(2);
    } else {
      WAIT_V(1);
    }
  } else {
    WAIT_V(0);
  }
  {
    asm volatile("" : "+v"(xv[0]), "+v"(xv[1])::"memory");
    if (wid == 1) TSG(part, 2);
    BT(51);
    const float s0 = dsum(xv[0][3], dsum(xv[0][2], dsum(xv[0][1], dsum(xv[0][0], 0.f))));
    const float s1 = dsum(xv[1][3], dsum(xv[1][2], dsum(xv[1][1], dsum(xv[1][0], 0.f))));
    const float mf = wave_sum_s(s0 + s1) * (1.0f / D), m = rbf(mf);
    const f32x4 d0 = lo4(xv[0]) - m, d1 = hi4(xv[0]) - m, d2 = lo4(xv[1]) - m, d3 = hi4(xv[1]) - m;
    const f32x4 q = (d0 * d0 + d1 * d1) + (d2 * d2 + d3 * d3);
    const float vf = wave_sum_s((q[0] + q[1]) + (q[2] + q[3])) * (1.0f / D) - (mf - m) * (mf - m);
    const float rden = __builtin_amdgcn_rcpf(rbf(__builtin_amdgcn_sqrtf(rbf(rbf(vf) + 1e-5f))));
    asm volatile("ds_write_b128 %0, %1" ::"v"(wa), "v"(pk8(d0 * rden, d1 * rden)) : "memory");
    asm volatile("ds_write_b128 %0, %1 offset:1024" ::"v"(wa), "v"(pk8(d2 * rden, d3 * rden)) : "memory");
    if (wid == 1) TSG(part, 12);
    BT(52);
    LBAR();
    if (wid == 1) TSG(part, 10);
    BT(53);
    u32x2 t[4];
    asm volatile(
        "ds_read_b64_tr_b16 %0, %4\n\tds_read_b64_tr_b16 %1, %4 offset:%5\n\t"
        "ds_read_b64_tr_b16 %2, %4 offset:128\n\tds_read_b64_tr_b16 %3, %4 offset:%6\n\ts_waitcnt lgkmcnt(0)"
        : "=&v"(t[0]), "=&v"(t[1]), "=&v"(t[2]), "=&v"(t[3])
        : "v"(ta), "n"(4 * 2368), "n"(4 * 2368 + 128)
        : "memory");
    asm volatile("global_store_dwordx4 %0, %2, %4 sc1\n\tglobal_store_dwordx4 %1, %3, %4 sc1\n\ts_nop 1" ::"v"(oo), "v"(oo2),
                 "v"(u32x4{t[0][0], t[0][1], t[1][0], t[1][1]}), "v"(u32x4{t[2][0], t[2][1], t[3][0], t[3][1]}), "s"(xn)
                 : "memory");
    if (wid == 2) TSG(part, 14);
    BT(54);
  }
  u32x2 vd;
#define VDLD(sl) POLL2(max(vd[0], vd[1]) == SENT, sl, PCAP, asm volatile("global_load_dwordx2 %0, %1, %2 sc1" VW0 : "=&v"(vd) : "v"(O_VD + (b & 31) * 128), "s"(ws) : "memory"))
  if (wid < 4) {
    const unsigned ya0 = lds0 + (lane & 15) * 128 + (g4 ^ (lane & 7)) * 16;
    bf16x8 X[DX][2][2], Y[8];
    f32x4 acc[2][8];
    {
      constexpr int MODE = 0;
      const unsigned xo0 = lane * 16 + 4096, xo1 = xo0 + 8192;
      const char* xb = reinterpret_cast<const char*>(xn) + (size_t)(mt * 4 + wid) * 131072;
#define XB(i) (xb + (((xcd + (i)) & 7) << 14))
#pragma unroll
      for (int i = 0; i < 2; ++i)
#pragma unroll
        for (int j = 0; j < 8; ++j) acc[i][j] = f32x4{0.f, 0.f, 0.f, 0.f};
      asm volatile("" : W8(acc[0]), W8(acc[1]));
      __builtin_amdgcn_s_setprio(1);
      WAIT_V(0);
      if (wid == 0) TSG(part, 1);
      const char* xpb = XB(0);
      BAR(55);
      if (wid == 3) {
        TAGW(0);
        const unsigned fp = O_XFL + ((xcd * 32 + lane) & 255) * XSTR * 4;
        if (P_D3) __builtin_amdgcn_s_sleep(P_D3);
#pragma unroll 1
        for (int it = 0; it < PCAP; ++it) { PDBG(it);
          u64 f;
          if (it < 2) BT(58 + 2 * it);
          asm volatile("global_load_dwordx2 %0, %1, %2 sc1\n\ts_waitcnt vmcnt(0)" : "=&v"(f) : "v"(fp), "s"(ws) : "memory");
          if (it < 2) BT(59 + 2 * it);
          if (__all(f == tag)) break;
          __builtin_amdgcn_s_sleep(P_D4);
        }
      }
      BAR(0);
      if (wid == 1) TSG(part, 3);
#pragma unroll
      for (int h = 0; h < 7; ++h) {
        XLD(X[h >> 1][h & 1][0], xpb, h, 0);
        XLD(X[h >> 1][h & 1][1], xpb, h, 1);
      }
#pragma unroll
      for (int j = 0; j < 7; ++j) DSR(Y[j], ya0, j * 2048);
      unsigned so = 0, a0 = ya0;
      f32x4 c0, c1;
#pragma unroll 1
      for (int it = 0; it < 7; ++it) {
        const char* xpa = xpb;
        xpb = XB(it + 1);
#pragma unroll
        for (int s = 0; s < 4; ++s) {
          const unsigned a1 = (ya0 ^ 64) + so;
          so = so == (NS - 1) * 16384 ? 0 : so + 16384;
          WAIT_V(12);
          HALF(X[s][0], a1, true, X[(s + 3) & 3][1], (s == 0 ? xpa : xpb), (2 * s + 7) & 7, true, a0);
          a0 = ya0 + so;
          BAR(1 + it * 4 + s);
          WAIT_V(12);
          HALF(X[s][1], a0, true, X[s][0], xpb, 2 * s, true, a1);
        }
      }
#pragma unroll
      for (int s = 0; s < 4; ++s) {
        const unsigned a1 = (ya0 ^ 64) + so;
        so = so == (NS - 1) * 16384 ? 0 : so + 16384;
        WAIT_V(s == 0 ? 12 : 14 - 4 * s);
        HALF(X[s][0], a1, true, X[3][1], xpb, 7, s == 0, a0);
        a0 = ya0 + so;
        if (s < 3) BAR(29 + s);
        WAIT_V(12 - 4 * s);
        if (s == 2) {
          DSR(c0, lds0 + SPARE + 15872 + 512 + wid * 128 + g4 * 32, 0);
          DSR(c1, lds0 + SPARE + 15872 + 512 + wid * 128 + g4 * 32, 16);
        }
        if (s < 3) HALF(X[s][1], a0, true, X[s][0], xpb, 0, false, a1);
      }
      const char* up = reinterpret_cast<const char*>(U) + (size_t)cc * 4194304 + (size_t)(nt * 4) * 65536 + (mt * 4 + wid) * 2048;
      const unsigned ul = lds0 + STG + wid * 1024 + lane * 16;
#define UEP(j)                                                                                                                             \
  do {                                                                                                                                     \
    u32x4 v_;                                                                                                                              \
    asm volatile("v_pk_mul_f32 %0, %0, %4\n\tv_pk_mul_f32 %1, %1, %5\n\tv_pk_mul_f32 %2, %2, %6\n\tv_pk_mul_f32 %3, %3, %7"            \
                 : "+v"(acc[0][j].lo), "+v"(acc[0][j].hi), "+v"(acc[1][j].lo), "+v"(acc[1][j].hi)                                          \
                 : "v"(c0.lo), "v"(c0.hi), "v"(c1.lo), "v"(c1.hi));                                                                        \
    asm volatile("v_cvt_pk_bf16_f32 %0, %4, %5\n\tv_cvt_pk_bf16_f32 %1, %6, %7\n\tv_cvt_pk_bf16_f32 %2, %8, %9\n\tv_cvt_pk_bf16_f32 %3, %10, %11" \
                 : "=&v"(v_[0]), "=&v"(v_[1]), "=&v"(v_[2]), "=&v"(v_[3])                                                                  \
                 : "v"(acc[0][j][0]), "v"(acc[0][j][1]), "v"(acc[0][j][2]), "v"(acc[0][j][3]), "v"(acc[1][j][0]), "v"(acc[1][j][1]),       \
                   "v"(acc[1][j][2]), "v"(acc[1][j][3]));                                                                                  \
    asm volatile("global_store_dwordx4 %0, %2, %3 offset:%4 sc1\n\tds_write_b128 %1, %2 offset:%5\n\ts_nop 0"                          \
                 ::"v"(lane * 16), "v"(ul), "v"(v_), "s"(up + ((j) >> 1) * 65536), "n"(((j)&1) * 1024), "n"((j)*4096) : "memory");         \
  } while (0)
      {
        const unsigned a1 = (ya0 ^ 64) + (31 % NS) * 16384;
        DSR(Y[7], a1, 7 * 2048);
        WAIT_L(6);
#pragma unroll
        for (int j = 0; j < 8; ++j) {
          if (j == 2) WAIT_L(0);
          MFMA(acc[0][j], X[3][1][0], Y[j]);
          MFMA(acc[1][j], X[3][1][1], Y[j]);
          if (j >= 2) UEP(j - 2);
        }
      }
      if (wid == 0) TSG(part, 4);
      TS(0);
      SKEWPT();
      asm volatile("s_nop 3");
#pragma unroll
      for (int j = 6; j < 8; ++j) UEP(j);
    }
    const int rg = nt * 8 + wid * 2 + cc, row0 = rg * 16 + (lane & 15), col0 = et * 128 + g4 * 8;
    u32x4 xq[4], pv[4], sa4[4];
    f32x4 ga[8];
    f32x2 cj;
    float sw, ce;
    u32x4 dk;
    unsigned spo, soo, th, slt, slf, sla, tha, xca, sca;
    u32x4 e0, e1;
    const char* xr = reinterpret_cast<const char*>(xn) + mt * 524288 + (cc * 16 + nt) * 16384 + wid * 4096;
#define XCHLD()                                                                                                     \
  asm volatile(                                                                                                     \
      "global_load_dwordx4 %0, %4, %5 sc1\n\tglobal_load_dwordx4 %1, %4, %5 offset:1024 sc1\n\t"                   \
      "global_load_dwordx4 %2, %4, %5 offset:2048 sc1\n\tglobal_load_dwordx4 %3, %4, %5 offset:3072 sc1"            \
      : "=&v"(pv[0]), "=&v"(pv[1]), "=&v"(pv[2]), "=&v"(pv[3])                                                      \
      : "v"(lane * 16), "s"(xr)                                                                                     \
      : "memory")
    {
      constexpr int MODE = 1;
      const char* ub = reinterpret_cast<const char*>(U) + (size_t)cc * 4194304 + (size_t)(nt * 4 + wid) * 65536;
      const unsigned xo0 = lane * 16 + cc * 1024 + 4096, xo1 = xo0 ^ 1024;
      BT(32);
      LBAR();
      if (wid == 1) TSG(part, 6);
      {
        const unsigned sa = lds0 + STG + wid * 8192 + lane * 16 + cc * 4096;
#pragma unroll
        for (int q = 0; q < 8; ++q) DSR(X[(q >> 1) & 1][q & 1][q >> 2], (q >> 2) ? sa ^ 4096 : sa, (q & 3) * 1024);
        const unsigned a0 = ya0 + (32 % NS) * 16384;
#pragma unroll
        for (int j = 0; j < 7; ++j) DSR(Y[j], a0, j * 2048);
      }
#define UFLAG()                                                                                                         \
  do {                                                                                                                  \
    WAIT_V(0);                                                                                                          \
    if (lane == 0) __hip_atomic_store(ufl + (mt * 4 + wid) * FSTR, 1u, __ATOMIC_RELAXED, __HIP_MEMORY_SCOPE_AGENT);     \
    if (wid == 0) TSG(part, 5);                                                                                         \
  } while (0)
#define XP(h) (ub + (((mt + ((h) >> 2)) & 7) << 13))
#define XS(h) X[((h) >> 1) & 3][(h)&1]
#define XW(h) do { if ((h) >= 4) WAIT_V(2 * (((h) + 6 > 31 ? 31 : (h) + 6) - (h)) ); } while (0)
      constexpr int H0 = 24, NP = 64, NE = 2 * NP + 8;
      const char* xw;
      unsigned c128, xoff[4], rka, rkb, ea, eb, cwa, cwu;
      auto ESET = [&]() __attribute__((always_inline)) {
        int ln_ = lane;
        asm volatile("" : "+v"(ln_));
        const int lane = ln_, g4 = lane >> 4, row0 = rg * 16 + (lane & 15), col0 = et * 128 + g4 * 8;
        xw = reinterpret_cast<const char*>(xn) + mt * 524288 + ((cc ^ 1) * 16 + nt) * 16384 + wid * 4096;
        asm volatile("" : "+s"(xw), "+s"(xr));
        c128 = 0x43000000u;
        rka = lds0 + RKA + (lane & 15) * 16, rkb = lds0 + RKB + (g4 ? 1024 : wid * 256 + lane * 16);
        ea = lds0 + TAB + g4 * 32, eb = lds0 + TAB + 512 + (lane & 3) * 256 + g4 * 16;
#pragma unroll
        for (int q = 0; q < 4; ++q) {
          dk[q] = (lane & 3) == q ? ~0u : 0u;
          xoff[q] = (lds0 + STG + wid * 8192 + (lane & 15) * 256 + ((g4 ^ lane) & 15) * 16) ^ (q * 64);
        }
        spo = O_STAT + rg * 2048 + g4 * 512 + (lane & 15) * 16, soo = O_STAT + rg * 2048 + et * 256 + (lane & 15) * 16;
        cwa = lds0 + SPARE + 10240 + g4 * 8, cwu = lds0 + SPARE + 10240 + et * 4;
        const unsigned one = g4 == ((lane >> 2) & 3) ? ((lane & 1) ? 0x1f800000u : 0x3f80u) : 0u;
        e0 = u32x4{(lane & 2) ? 0u : one, (lane & 2) ? one : 0u, 0u, 0u}, e1 = u32x4{0u, 0u, e0[0], e0[1]};
        tha = lds0 + SPARE + 10368 + 24;
        sla = lds0 + SLT + (row0 & 127) * 4;
        xca = O_X1C - 2048 + col0 * 2, sca = O_ST2 - 128 + et * 16;
        asm volatile("" : "+v"(dk), "+v"(spo), "+v"(soo), "+v"(cwa), "+v"(cwu), "+v"(tha), "+v"(sla), "+v"(xca), "+v"(sca), "+v"(e0), "+v"(e1), "+v"(c128));
        asm volatile("" : "+v"(xoff[0]), "+v"(xoff[1]), "+v"(xoff[2]), "+v"(xoff[3]), "+v"(rka), "+v"(rkb), "+v"(ea), "+v"(eb));
      };
#pragma unroll
      for (int s = 0; s < H0 / 2; ++s) {
        const unsigned a0 = ya0 + ((32 + s) % NS) * 16384, a1 = a0 ^ 64, an = ya0 + ((33 + s) % NS) * 16384;
        XW(2 * s);
        if (s == 0) {
          constexpr int MODE = 2;
          HALF(XS(0), a1, true, XS(7), XP(7), 7, false, a0);
          UFLAG();
        } else {
          HALF(XS(2 * s), a1, true, XS(2 * s + 7), XP(2 * s + 7), 2 * s + 7, 2 * s >= 4, a0);
        }
        if (s == 1) ESET();
        BAR(33 + s);
        if (s == 1) {
          if (wid == 1) TSG(part, 7);
          TS(4);
#pragma unroll
          for (int h = 4; h < U_X0; ++h) {
            XLD(XS(h)[0], XP(h), h, 0);
            XLD(XS(h)[1], XP(h), h, 1);
          }
        }
        XW(2 * s + 1);
#undef HHOOK
#define HHOOK(j) if (s == 1 && j + 3 >= U_X0) { XLD(XS(j + 3)[0], XP(j + 3), j + 3, 0); XLD(XS(j + 3)[1], XP(j + 3), j + 3, 1); }
        HALF(XS(2 * s + 1), an, true, XS(2 * s + 8), XP(2 * s + 8), 2 * s + 8, 2 * s + 1 >= 5, a1);
#undef HHOOK
#define HHOOK(j)
      }
      u32x4 ov;
      bf16x8 xk;
#define EH(e) (H0 + (((e) < NP ? (e) : (e) - NP - 8) >> 3))
#define EA(e) ((ya0 ^ ((EH(e) & 1) * 64)) + ((32 + (EH(e) >> 1)) % NS) * 16384)
      sfor<NE>([&](auto nn) __attribute__((always_inline)) {
        constexpr int n = decltype(nn)::value, h = EH(n), j = n & 7, m = n - NP - TP_P0, e = n + 7, z = n - (NE - 8);
        (void)sa4, (void)eb, (void)ga, (void)ea, (void)xk, (void)rka, (void)rkb, (void)xq, (void)xoff, (void)ov, (void)xw, (void)x;
        (void)cj, (void)sw, (void)ce, (void)cwa, (void)cwu, (void)th, (void)tha, (void)slt, (void)sla, (void)slf, (void)c128;
        if (n == 0) TS(2);
        if (n == NP) { TS(3); SKEWPT(); }
        if (n < NP && j == 0) {
          if ((h & 1) && h < 31) BAR(33 + (h >> 1));
          XW(h);
        }
        if (n == NP + TP_X0) { TS(14); BAR(48); TS(11); }
        if (n == NP + TP_XL) XCHLD();
        if constexpr ((j & 1) == 0) WAIT_L(z <= 0 ? 5 : 6 - z + 2 * z);
        if constexpr (n < NP) MFMA(acc[1][j], Y[j], XS(h)[1]);
        else if constexpr (n < NP + 8) MFMA(acc[0][j], Y[j], xk);
        else MFMA(acc[0][j], Y[j], XS(h)[0]);
        if constexpr (n == NP - 16) DSR(xk, rkb, 0);
        if constexpr (e >= NP && e < NP + 8) DSR(Y[e & 7], rka, (e & 7) * 256);
        else if constexpr (e < NE) DSR(Y[e & 7], EA(e), (e & 7) * 2048);
        if constexpr (n == 2 || n == 4) XLD(XS(31)[(j >> 1) - 1], XP(31), 31, (j >> 1) - 1);
        if constexpr (m >= TP_XC - 11 && m < TP_XC - 7) DSR(xq[m - TP_XC + 11], xoff[m - TP_XC + 11], 4096);
        if constexpr (m >= 0 && m < 4 * TP_PS) {
          constexpr int q = m / TP_PS, r = m % TP_PS;
#pragma unroll
          for (int u = (r * 13 + TP_PS - 1) / TP_PS; u < ((r + 1) * 13 + TP_PS - 1) / TP_PS; ++u) {
#define AO(i) acc[1][2 * q + ((u >> 1) & 1)][(u & 1) * 2 + (i)]
            if (u < 4) asm volatile("v_and_or_b32 %0, %1, %2, %3" : "=v"(ov[u & 3]) : "v"(AO(1)), "s"(0x80000000u), "v"(c128));
            else if (u < 8) asm volatile("v_fmamk_f32 %0, %1, 0x5f800000, %0" : "+v"(ov[u & 3]) : "v"(AO(1)));
            else if (u < 12) asm volatile("v_cvt_pk_bf16_f32 %0, %1, %0" : "+v"(ov[u & 3]) : "v"(AO(0)));
            else asm volatile("global_store_dwordx4 %0, %1, %2 offset:%3 sc1\n\ts_nop 1" ::"v"(lane * 16), "v"(ov), "s"(xw), "n"(q * 1024) : "memory");
          }
          if (m == 4 * TP_PS - 1) TS(10);
        }
        if constexpr (m == TP_XC) asm volatile("" : "+v"(xq[0]), "+v"(xq[1]), "+v"(xq[2]), "+v"(xq[3]));
        if constexpr (m >= TP_XC && m < TP_XC + 32) {
          constexpr int c = m - TP_XC;
          if constexpr (c & 1) asm volatile("v_and_b32 %0, 0xffff0000, %1" : "=v"(acc[1][c >> 2][c & 3]) : "v"(xq[c >> 3][(c >> 1) & 3]));
          else asm volatile("v_lshlrev_b32 %0, 16, %1" : "=v"(acc[1][c >> 2][c & 3]) : "v"(xq[c >> 3][(c >> 1) & 3]));
        }
        if constexpr (z >= 0 && z < 4) {
          DSR(ga[2 * z], ea, z * 128);
          DSR(ga[2 * z + 1], ea, z * 128 + 16);
        } else if constexpr (z >= 4 && z < 6) {
          DSR(sa4[2 * z - 8], eb, (2 * z - 8) * 64);
          DSR(sa4[2 * z - 7], eb, (2 * z - 7) * 64);
        } else if constexpr (z == 6) {
          asm volatile("ds_read_b64 %0, %2\n\tds_read_b32 %1, %3" : "=&v"(cj), "=&v"(ce) : "v"(cwa), "v"(cwu) : "memory");
        } else if constexpr (z == 7) {
          asm volatile("ds_read_b32 %0, %4 offset:32\n\tds_read_b32 %1, %5 offset:4\n\tds_read_b32 %3, %5 offset:56\n\tds_read_b32 %2, %6"
                       : "=&v"(sw), "=&v"(th), "=&v"(slt), "=&v"(slf) : "v"(cwa), "v"(tha), "v"(sla) : "memory");
        }
      });
    }
    if (wid == 0) TSG(part, 8);
    TS(13);
    BT(49);
    BTE();
    SKEWPT();
    {
      TS(26);
#define XCHW() asm volatile("s_waitcnt vmcnt(0)" : "+v"(pv[0]), "+v"(pv[1]), "+v"(pv[2]), "+v"(pv[3])::"memory")
      XCHW();
#define XBAD (amin16(pv) < 128.0f)
      if (__builtin_expect(__any(XBAD), 1)) POLL2(XBAD, 1, PCAP, XCHLD(); XCHW());
      TS(27);
      asm volatile("s_waitcnt lgkmcnt(0)" : W8(acc[0]), W8(ga), "+v"(sa4[0]), "+v"(sa4[1]), "+v"(sa4[2]), "+v"(sa4[3]), "+v"(cj), "+v"(sw), "+v"(ce), "+v"(th), "+v"(slt), "+v"(slf)::"memory");
#pragma unroll
      for (int q = 0; q < 8; ++q) acc[0][q] = __builtin_amdgcn_mfma_f32_16x16x32_bf16(BF8((q & 1) ? e1 : e0), BF8(pv[q >> 1]), acc[0][q], 0, 0, 0);
      f32x4 s4 = {0.f, 0.f, 0.f, 0.f}, gr = {0.f, 0.f, 0.f, 0.f};
      const unsigned xa1 = xca + slt * 2048, sa1 = sca + slt * 128;
#pragma unroll
      for (int q = 0; q < 4; ++q) {
        xq[q] = pk8(acc[1][2 * q] + ga[2 * q] * acc[0][2 * q], acc[1][2 * q + 1] + ga[2 * q + 1] * acc[0][2 * q + 1]);
        gr = __builtin_amdgcn_mfma_f32_16x16x32_bf16(BF8(xq[q]), BF8(xq[q]), gr, 0, 0, 0);
        s4 = __builtin_amdgcn_mfma_f32_16x16x32_bf16(BF8(sa4[q]), BF8(xq[q]), s4, 0, 0, 0);
        xq[q] = nsent4(xq[q]);
      }
#define XCST(a)                                                                                                              \
  asm volatile(                                                                                                              \
      "global_store_dwordx4 %0, %1, %5 sc1\n\tglobal_store_dwordx4 %0, %2, %5 offset:64 sc1\n\t"                            \
      "global_store_dwordx4 %0, %3, %5 offset:128 sc1\n\tglobal_store_dwordx4 %0, %4, %5 offset:192 sc1\n\ts_nop 1" ::"v"(a), \
      "v"(xq[0]), "v"(xq[1]), "v"(xq[2]), "v"(xq[3]), "s"(ws)                                                                \
      : "memory")
#define SCST(a)                                                                                                              \
  do {                                                                                                                       \
    _Pragma("unroll") for (int r = 0; r < S_R; ++r) asm volatile("global_store_dwordx4 %0, %1, %2 offset:%3 sc1" ::"v"(a), "v"(sg), "s"(ws), "n"(r * 1280) : "memory"); \
    asm volatile("s_nop 1");                                                                                                 \
  } while (0)
      if (slt) XCST(xa1);
      const bool dl = ((lane >> 2) & 3) == g4;
      const u32x4 gd = __builtin_bit_cast(u32x4, gr) & dk;
      const float m = s4[2] * (1.0f / 128);
      float q2 = __uint_as_float((gd[0] | gd[1]) | (gd[2] | gd[3])) - s4[2] * m, lg = (s4[0] + s4[1]) - m * ce;
      if (__builtin_expect(__any(dl && !(s4[2] * m <= 256.0f * q2)), 0)) {
        asm volatile("" : "+v"(xq[0]), "+v"(xq[1]), "+v"(xq[2]), "+v"(xq[3]));
        f32x4 a = {0.f, 0.f, 0.f, 0.f}, l = {0.f, 0.f, 0.f, 0.f};
#pragma unroll
        for (int q = 0; q < 4; ++q) {
          const u32x4 ch = *reinterpret_cast<const u32x4*>(&scw[0][g4 * 8 + q * 32]), cl = *reinterpret_cast<const u32x4*>(&scw[1][g4 * 8 + q * 32]);
          const f32x4 d0 = lo4(xq[q]) - m, d1 = hi4(xq[q]) - m;
          a += d0 * d0 + d1 * d1;
          l += d0 * (lo4(ch) + lo4(cl)) + d1 * (hi4(ch) + hi4(cl));
        }
        q2 = ar4((a[0] + a[1]) + (a[2] + a[3]));
        lg = ar4((l[0] + l[1]) + (l[2] + l[3]));
      }
      const u32x4 sg = {nsent(m), nsent(q2), nsent(lg), 0u};
      if (dl) {
        if (slt) SCST(sa1);
        asm volatile("global_store_dwordx4 %0, %1, %2 sc1\n\ts_nop 1" ::"v"(soo), "v"(sg), "s"(ws) : "memory");
      }
      if (__any(slf == 0u)) {
        POLL2(slf == 0u, 1, 1 << 26, asm volatile("ds_read_b32 %1, %2 offset:56\n\tds_read_b32 %0, %3\n\ts_waitcnt lgkmcnt(0)" : "=&v"(slt), "=&v"(slf) : "v"(tha), "v"(sla) : "memory"));
        if (slt) {
          XCST(xca + slt * 2048);
          if (dl) SCST(sca + slt * 128);
        }
      }
      asm volatile("ds_write_b32 %0, %1 offset:72" ::"v"(tha + wid * 4), "v"(1u) : "memory");
      TS(7);
      SKEWPT();
      const unsigned tp = tick() + T_D1, tq = (unsigned)__builtin_amdgcn_readfirstlane((int)th) + T_C1;
      u32x4 sr[2];
#define STLD(W) asm volatile("global_load_dwordx4 %0, %2, %3 sc1\n\tglobal_load_dwordx4 %1, %2, %3 offset:256 sc1" W : "=&v"(sr[0]), "=&v"(sr[1]) : "v"(spo), "s"(ws) : "memory")
#define SBAD (max(max(max(sr[0][0], sr[0][1]), sr[0][2]), max(max(sr[1][0], sr[1][1]), sr[1][2])) == SENT)
      asm volatile("" : "+v"(xq[0]), "+v"(xq[1]), "+v"(xq[2]), "+v"(xq[3])::"memory");
      f32x4 xf[8], xs[8], gc[8];
      auto PREW = [&](int q0, int q1) __attribute__((always_inline)) {
#pragma unroll
        for (int q = q0; q < q1; ++q) {
          const u32x4 sbq = *reinterpret_cast<const u32x4*>(&svb[1][col0 + q * 32]), gbq = *reinterpret_cast<const u32x4*>(&svb[3][col0 + q * 32]);
          const u32x4 cq = *reinterpret_cast<const u32x4*>(&svb[2][col0 + q * 32]);
          xf[2 * q] = lo4(xq[q]), xf[2 * q + 1] = hi4(xq[q]);
          xs[2 * q] = xf[2 * q] + lo4(gbq) * lo4(sbq), xs[2 * q + 1] = xf[2 * q + 1] + hi4(gbq) * hi4(sbq);
          gc[2 * q] = lo4(gbq) * lo4(cq), gc[2 * q + 1] = hi4(gbq) * hi4(cq);
          asm volatile("" : "+v"(xf[2 * q]), "+v"(xf[2 * q + 1]), "+v"(xs[2 * q]), "+v"(xs[2 * q + 1]), "+v"(gc[2 * q]), "+v"(gc[2 * q + 1]));
        }
      };
      PREW(0, PW0);
      wait_tick((int)(tp - tq) < 0 ? tp : tq);
      STLD("");
      unsigned rk = 0x8ffeu - (unsigned)row0 * 2;
      unsigned twa = lds0 + STG + wid * 8192 + (lane & 15) * 256 + ((g4 ^ lane) & 15) * 16, tra = lds0 + STG + wid * 8192 + g4 * 256 + ((g4 ^ lane) & 15) * 16;
      unsigned oo_ = (unsigned)((rg * 16 + g4) * D + et * 128 + (lane & 15) * 8) * 2;
      asm volatile("" : "+v"(rk), "+v"(twa), "+v"(tra), "+v"(oo_));
      PREW(PW0, 4);
      asm volatile("s_waitcnt vmcnt(0)" : "+v"(sr[0]), "+v"(sr[1])::"memory");
      if (__builtin_expect(__any(SBAD), 1)) POLL2(SBAD, 1, PCAP, STLD(VW0));
      TS(9);
      const f32x2 mk = {__uint_as_float(sr[0][0]), __uint_as_float(sr[1][0])};
      const f32x2 qk = {__uint_as_float(sr[0][1]), __uint_as_float(sr[1][1])};
      const f32x2 lk = {__uint_as_float(sr[0][2]), __uint_as_float(sr[1][2])};
      const float mean = ar4(mk[0] + mk[1]) * (1.0f / 8);
      const f32x2 dm = mk - mean;
      const f32x2 a = qk + 128.0f * (dm * dm), l = lk + dm * cj;
      const float al = swap32_add(a[0] + a[1], l[0] + l[1]), am = swap16_add(al, al);
      auto r = __builtin_amdgcn_permlane32_swap(__float_as_uint(am), __float_as_uint(am), false, false);
      const float rstd = __builtin_amdgcn_rsqf(__uint_as_float(r[0]) * (1.0f / D) + 1e-5f);
      const unsigned key = mkkey(rstd * __uint_as_float(r[1]) + sw) | rk;
      unsigned t1 = slt ? 0u : key, t2 = 0u;
      TOP2(0xB1); TOP2(0x4E); TOP2(0x141); TOP2(0x140);
      if (lane == 0 && et == (rg & 7)) asm volatile("global_store_dwordx2 %0, %1, %2 sc1" ::"v"(O_KEY + rg * KSTR * 8), "v"(u32x2{t1, t2}), "s"(ws) : "memory");
      u32x4 o[4];
#pragma unroll
      for (int q = 0; q < 4; ++q) o[q] = pk8(xs[2 * q] + (gc[2 * q] * (xf[2 * q] - mean)) * rstd, xs[2 * q + 1] + (gc[2 * q + 1] * (xf[2 * q + 1] - mean)) * rstd);
      {
        asm volatile("ds_write_b128 %0, %4\n\tds_write_b128 %1, %5\n\tds_write_b128 %2, %6\n\tds_write_b128 %3, %7" ::"v"(twa), "v"(twa ^ 64), "v"(twa ^ 128), "v"(twa ^ 192),
                     "v"(o[0]), "v"(o[1]), "v"(o[2]), "v"(o[3]) : "memory");
        asm volatile("ds_read_b128 %0, %4\n\tds_read_b128 %1, %5 offset:1024\n\tds_read_b128 %2, %6 offset:2048\n\tds_read_b128 %3, %7 offset:3072\n\ts_waitcnt lgkmcnt(0)"
                     : "=&v"(o[0]), "=&v"(o[1]), "=&v"(o[2]), "=&v"(o[3]) : "v"(tra), "v"(tra ^ 64), "v"(tra ^ 128), "v"(tra ^ 192) : "memory");
      }
      auto OST = [&](bool on) __attribute__((always_inline)) {
        const unsigned mk_ = (unsigned)__builtin_amdgcn_ballot_w64(on) >> g4;
#pragma unroll
        for (int q = 0; q < 4; ++q)
          if (mk_ & (1u << (4 * q))) asm volatile("global_store_dwordx4 %0, %1, %2" OPOL "\n\ts_nop 1" ::"v"(oo_), "v"(o[q]), "s"(out + q * 4096) : "memory");
      };
      bool df = slt;
      {
        const int tn = (int)(tick() - (tq + (T_C3 - T_C1)));
        if (tn > T_X) df = true;
        else if (tn > -T_W) { unsigned sv; LSPIN(sv, ssel + 16 + wid, sv != 0u); }
      }
      OST(!df);
      TS(1);
      u32x2 ks;
      POLL2(ks[1] == 0u, 1, 1 << 26, asm volatile("ds_read_b64 %0, %1\n\ts_waitcnt lgkmcnt(0)" : "=v"(ks) : "v"(lds0 + SPARE + 10368) : "memory"));
      df = df && (ks[0] & 0xffffu) != rk && (ks[1] & 0xffffu) != rk;
      SKEWPT();
      if (__any(df)) {
        if (b < NPART) {
          unsigned sv;
          LSPIN(sv, ssel + O_DW, sv != 0u);
          if (O_DS) __builtin_amdgcn_s_sleep(O_DS);
        }
        OST(df);
      }
      if (__builtin_expect(__any(t1 > ks[1]), 0)) {
        VDLD(8);
        slt = slt ? 0u : key == vd[0] ? 9u : key == vd[1] ? 10u : 0u;
        if (slt) {
          XCST(xca + slt * 2048);
          if (dl) SCST(sca + slt * 128);
        }
      }
    }
    TS(16);
    TSE();
  } else {
    const int hw = wid - 4;
    unsigned ts0 = tick();
    asm volatile("" : "+v"(ts0));
    const char* Hc = reinterpret_cast<const char*>((cc ? X1 : X0) + (size_t)et * 128 * D);
    auto UPOLL = [&](int n) {
      const unsigned fo = (unsigned)WS_UFL + ((cc * 16 + nt) * 32 + ((mt + 1 + (lane >> 2)) & 7) * 4 + (lane & 3)) * FSTR * 4;
      unsigned f;
      POLL2(f == 0u && lane < 4 * n, U_S, PCAP, asm volatile("global_load_dword %0, %1, %2 sc1" VW0 : "=&v"(f) : "v"(fo), "s"(ws) : "memory"));
      return (__builtin_amdgcn_ballot_w64(f == 0u) & 0xFFFFFFFull) == 0;
    };
    bool urdy = true;
    u32x4 gT[8], uT[8], wdv[8];
    unsigned og = (unsigned)(((g & 3) * 256 + hw * 64 + (lane >> 3)) * F + (g >> 2) * 64 + ((lane & 7) ^ ((lane >> 3) & 2) ^ 4) * 8) * 2;
    unsigned wta = lds0 + STG + (g4 * 8 + ((lane >> 2) & 3)) * 128 + (((hw * 2 + ((lane >> 1) & 1)) ^ ((lane >> 2) & 2) ^ ((g4 & 1) * 4)) << 4) + (lane & 1) * 8;
    unsigned od = (unsigned)(((g >> 2) * 64 + (lane >> 3) * 8) * D + (g & 3) * 256 + hw * 64 + (lane & 7) * 8) * 2;
    unsigned ou = og + (unsigned)(((mt + W_UO) % 7 - mt) * 128);
    asm volatile("" : "+v"(og), "+v"(ou), "+v"(od), "+v"(wta));
#define LDWI(dst, voff, base, inc) asm volatile("global_load_dwordx4 %0, %1, %2" WPOL "\n\tv_add_u32 %1, %3, %1" : "=v"(dst), "+v"(voff) : "s"(base), "n"(inc) : "memory")
#define WST(o, w)                                                                                                          \
  if (b < NPART) {                                                                                                         \
    asm volatile("s_mov_b32 m0, %0" ::"s"(lds0 + STG + hw * 8192));                                                        \
    _Pragma("unroll") for (int i = 0; i < 8; ++i) {                                                                        \
      asm volatile("v_xor_b32 %0, 64, %0\n\tglobal_load_lds_dwordx4 %0, %1 offset:%2" WPOL "\n\tv_add_u32 %0, %3, %0"       \
                   : "+v"(o) : "s"(w), "n"((i & 3) * 1024), "n"(F * 16 + (i == 3 ? 3072 : -1024)) : "memory");             \
      if (i == 3) asm volatile("s_add_u32 m0, m0, 4096" ::: "scc");                                                        \
    }                                                                                                                      \
  }
#define WTR2(i) "ds_read_b64_tr_b16 %" #i ", %16 offset:(" #i "/2)*4096+(" #i "&1)*512\n\t"
#define WTK(T)                                                                                                                         \
  if (b < NPART) {                                                                                                                     \
    u32x2 t_[16];                                                                                                                      \
    asm volatile(WTR2(0) WTR2(1) WTR2(2) WTR2(3) WTR2(4) WTR2(5) WTR2(6) WTR2(7) WTR2(8) WTR2(9) WTR2(10) WTR2(11) WTR2(12) WTR2(13)   \
                     WTR2(14) WTR2(15) "s_waitcnt lgkmcnt(0)"                                                                          \
                 : "=&v"(t_[0]), "=&v"(t_[1]), "=&v"(t_[2]), "=&v"(t_[3]), "=&v"(t_[4]), "=&v"(t_[5]), "=&v"(t_[6]), "=&v"(t_[7]),     \
                   "=&v"(t_[8]), "=&v"(t_[9]), "=&v"(t_[10]), "=&v"(t_[11]), "=&v"(t_[12]), "=&v"(t_[13]), "=&v"(t_[14]), "=&v"(t_[15]) \
                 : "v"(wta) : "memory");                                                                                               \
    _Pragma("unroll") for (int f = 0; f < 8; ++f) T[f] = u32x4{t_[2 * f][0], t_[2 * f][1], t_[2 * f + 1][0], t_[2 * f + 1][1]};        \
  }
#define DLD(q0, q1) if (b < NPART) { _Pragma("unroll") for (int q = q0; q < q1; ++q) LDWI(wdv[q], od, wd, D * 2); }
    const int pf0 = A_PS + (((j8 & 7) * A_S1) >> 3), pf1 = A_P2 + (((j8 & 7) * A_S2) >> 3), q1 = A_Q1;
    static_assert(A_PS >= 1 && A_P2 + A_S2 * 7 / 8 + 3 <= R_AT && R_AT < A_Q1 && A_P2 + A_S2 * 7 / 8 + 1 + W_T <= A_Q1 && H_AT + 3 <= A_Q1 && G1_C + G1_S < W_T && A_PS + W_T < A_P2 && A_PS + A_S1 * 7 / 8 + W_T + 1 < A_P2 + A_S2 * 7 / 8 && W_T >= M_NS - 2 && A_Q1 < F_AT && F_AT <= 29 && V_K <= B_J && V_T > B_J && B_WK == 15 && X_K >= N_C && (X_K + M_NS - 1 < 16 || (X_K <= B_MAT && TP_XC - 11 >= TP_X0 - TP_P0)) && TP_XC >= 4 * TP_PS && TP_XC + TP_P0 + 32 <= 72 && N_K > N_C && N_C > V_T && B_RAT > B_J && B_QAT > B_J && B_QAT + B_QD < 15, "barrier counts");
#define HOFF(r) (unsigned)((((r) >> 5) * 32 + (((r) >> 2) & 3) * 8 + (((r) >> 4) & 1) * 4 + ((r)&3)) * 2048 + ((lane & 7) ^ (lane >> 3)) * 16)
#define TR8(T, W)                                                                                                    \
  _Pragma("unroll") for (int c = 0; c < 8; ++c) {                                                                    \
    _Pragma("unroll") for (int p = 0; p < 4; ++p)                                                                    \
        T[c][p] = __builtin_amdgcn_perm(W[2 * p + 1][c >> 1], W[2 * p][c >> 1], (c & 1) ? 0x07060302u : 0x05040100u); \
    asm volatile("" : "+v"(T[c]));                                                                                   \
  }
    unsigned qm[4];
#pragma unroll
    for (int i = 0; i < 4; ++i) qm[i] = ((lane >> 2) & 3) == i ? ~0u : 0u;
    asm volatile("" : "+v"(qm[0]), "+v"(qm[1]), "+v"(qm[2]), "+v"(qm[3]));
    auto MVR = [&](f32x4 d) __attribute__((always_inline)) { return ((lane & 8) ? d[3] : d[0]) + dpp_f<0x128>((lane & 8) ? d[1] : d[2]); };
    auto MV64 = [&](const u32x4* T, u32x4 a) __attribute__((always_inline)) {
      f32x4 d[2] = {};
#pragma unroll
      for (int c = 0; c < 8; ++c) {
        const unsigned k_ = qm[c >> 1];
        d[c & 1] = __builtin_amdgcn_mfma_f32_16x16x32_bf16(BF8((u32x4{a[0] & k_, a[1] & k_, a[2] & k_, a[3] & k_})), BF8(T[c]), d[c & 1], 0, 0, 0);
      }
      return f32x2{MVR(d[0]), MVR(d[1])};
    };
#define LD1(i, a) "global_load_dwordx4 %" #i ", %" #a ", %10 sc1\n\tv_add_u32 %" #a ", 0x4000, %" #a "\n\t"
#define LD8(r, o0, o1, W)                                                                                                          \
  do {                                                                                                                             \
    unsigned a0_ = (o0), a1_ = (o1);                                                                                               \
    asm volatile(LD1(0, 8) LD1(4, 9) LD1(1, 8) LD1(5, 9) LD1(2, 8) LD1(6, 9) LD1(3, 8) LD1(7, 9) "s_nop 0" W                        \
                 : "=&v"(r[0]), "=&v"(r[1]), "=&v"(r[2]), "=&v"(r[3]), "=&v"(r[4]), "=&v"(r[5]), "=&v"(r[6]), "=&v"(r[7]), "+v"(a0_), "+v"(a1_) \
                 : "s"(ws) : "memory");                                                                                            \
  } while (0)
    u32x4 sel, gp[4];
    unsigned vc;
#define VCLD(W) asm volatile("global_load_dword %0, %1, %2 sc1" W : "=&v"(vc) : "v"(O_VP + ((cc ^ 1) * 8 + mt) * 4096 + (lane & 15) * 256), "s"(ws) : "memory")
#define GP4(o, W)                                                                                                         \
  asm volatile(                                                                                                           \
      "global_load_dwordx4 %0, %4, %5 sc1\n\tglobal_load_dwordx4 %1, %4, %5 offset:1024 sc1\n\t"                         \
      "global_load_dwordx4 %2, %4, %5 offset:2048 sc1\n\tglobal_load_dwordx4 %3, %4, %5 offset:3072 sc1" W                \
      : "=&v"(gp[0]), "=&v"(gp[1]), "=&v"(gp[2]), "=&v"(gp[3])                                                            \
      : "v"(o), "s"(ws)                                                                                                   \
      : "memory")
#define GPBAD (max(max(max4(gp[0]), max4(gp[1])), max(max4(gp[2]), max4(gp[3]))) == SENT)
      auto GPSUM = [&]() __attribute__((always_inline)) {
        f32x4 d = {0.f, 0.f, 0.f, 0.f};
#pragma unroll
        for (int i = 0; i < 4; ++i) d = __builtin_amdgcn_mfma_f32_16x16x32_bf16(BF8(sel), BF8(gp[i]), d, 0, 0, 0);
        return d;
      };
    const bool rdc = b >= NPART && hw == 3, rnp = b >= NPART && hw == 2 && cc == 0;
    const int gbq = (b >> 4) & 1, cj = (b & 15) * 64 + (lane & 31) * 2;
    unsigned voff[4], ro, xo, ra1, wqa, qo, qa, ma, mo2, ma2;
    unsigned sfa, rk2, xo_, hwa, hra, hrb, ara, pso, gso, awa, sfw, gl0, gl1, sto, kca;
    auto JSET = [&]() {
#pragma unroll
      for (int i = 0; i < 4; ++i) {
        voff[i] = HOFF((hw * 4 + i) * 8 + (lane >> 3)) - i * 1024;
        sel[i] = ((lane & 15) >> 1) == i ? ((lane & 1) ? 0x3f800000u : 0x3f80u) : 0u;
      }
      ro = O_G2 + ((lane >> 5) * 16384 + gbq * 1024 + cj) * 8;
      xo = (unsigned)((nt * 128 + hw * 32 + cc * 16 + g4) * D + et * 128 + ((g4 ^ lane) & 15) * 8) * 2;
      ra1 = lane < 32 ? O_MOD + 2048 + gbq * 4096 + cj * 2 : O_M2 + 8192 + 64 - gbq * 64 + (b & 15) * 4;
      wqa = lds0 + SPARE + 8192 + cj * 2;
      ma = max(hw, 1) * 2048 + (lane < 16 ? et * 256 + lane * 16 : lane < 48 ? (g & 3) * 512 + (lane - 16) * 16 : ((b >> 1) & 15) * 128 + (lane & 7) * 16);
      qo = hw < 2 ? O_QT + et * 2048 + hw * 1024 + lane * 16 : O_RS + ((hw & 1) * S + nt * 128 + (lane >> 4) * 32 + cc * 16 + (lane & 15)) * 4;
      qa = lds0 + (hw < 2 ? RKA + hw * 1024 + lane * 16 : RKB + lane * 16 + (hw & 1) * 8);
      mo2 = O_M2 + (hw == 2 ? 8448u + (lane & 15) * 16 : hw ? 8192u + (lane & 7) * 16 : lane < 32 ? et * 512 + lane * 16 : 4096u + (lane & 16) * 128 + et * 256 + (lane & 15) * 16);
      ma2 = lds0 + (hw ? SPARE + 10240 + (lane & 7) * 8 : TAB + lane * 16);
      asm volatile("" : "+v"(voff[0]), "+v"(voff[1]), "+v"(voff[2]), "+v"(voff[3]), "+v"(ro), "+v"(xo), "+v"(ra1), "+v"(wqa), "+v"(qo), "+v"(qa), "+v"(ma), "+v"(mo2), "+v"(ma2), "+v"(sel));
      *reinterpret_cast<unsigned*>((hw & 1) ? shm + RKB + 1024 + lane * 4 : reinterpret_cast<char*>(scw[2]) + lane * 4) = (hw & 1) ? 0u : 0x3f803f80u;
      if (hw == 3) {
        if (lane < 32) ssel[lane] = 0u;
        *reinterpret_cast<u32x2*>(shm + SLT + lane * 8) = u32x2{0u, 0u};
      }
    };
#define OWNL() int ln_ = lane; asm volatile("" : "+v"(ln_)); const int lane = ln_, g4 = lane >> 4; (void)g4
    auto TSET = [&]() {
      OWNL();
      kca = ((g & 3) * 256 + hw * 64 + (lane & 7) * 8) * 2;
      sfa = lds0 + SPARE + 10368, rk2 = lds0 + LST + (lane >> 3) * 4, sfw = sfa + hw * 4;
      xo_ = O_X1C + (lane >> 3) * 2048 + kca, sto = O_ST2 + (hw & (S_R - 1)) * 1280 + lane * 16;
      hwa = lds0 + XC + hw * 1024 + (lane & 7) * 128 + (((lane >> 3) ^ lane) & 7) * 16, hra = lds0 + XC + g4 * 128 + ((lane ^ g4) & 7) * 16, hrb = hra ^ 64;
      awa = lds0 + SPARE + 15872 + lane * 4, ara = lds0 + SPARE + 15872 + (lane & 2) * 64 + g4 * 32 + (lane & 1) * 16;
      pso = O_PART + ((g >> 2) * 2048 + (lane & 8) * 128 + (g & 3) * 256 + hw * 64 + (lane & 7) * 8 + g4 * 2) * 4;
      gso = O_GU + g * 1024 + (lane >> 5) * 256 + hw * 64 + (lane & 15) * 4;
      gl0 = O_GU + (g >> 2) * 4096 + lane * 8, gl1 = O_GU + (xcd * 7 + (mt + 7 - W_UO) % 7) * 4096 + 512 + lane * 8;
      asm volatile("" : "+v"(sfa), "+v"(rk2), "+v"(xo_), "+v"(hwa), "+v"(hra), "+v"(hrb), "+v"(ara), "+v"(pso), "+v"(gso), "+v"(awa), "+v"(sfw), "+v"(gl0), "+v"(gl1), "+v"(sto), "+v"(kca));
    };
    if (hw < 2) {
      constexpr int P = 8;
      __builtin_amdgcn_s_setprio(2);
      unsigned voy[P];
#pragma unroll
      for (int i = 0; i < P; ++i) voy[i] = (unsigned)(((hw * 8 + i) * 8 + (lane >> 3)) * 4096 + ((lane & 7) ^ (lane >> 3)) * 16 - (i & 3) * 1024);
      const unsigned lbase = lds0 + hw * 8192;
      const char* Yc = reinterpret_cast<const char*>((cc ? Y1 : Y0) + (size_t)nt * 128 * S);
      auto stage = [&](const char* yp, unsigned l) {
        GLDS4(l, voy, yp);
        GLDS4(l + 4096, voy + 4, yp);
      };
#define YSRC(n) (Yc + ((((n) + xcd * 4) & 31) << 7))
      __builtin_amdgcn_s_sleep(C_LD);
      PFL(hw ? PFG(1) : cpf);
      JSET();
      TAGW(1);
      BAR(55);
      BT(56);
      if (hw == 1 && lane == 0) asm volatile("global_store_dwordx2 %0, %1, %2 sc1" ::"v"(O_XFL + b * XSTR * 4), "v"(tag), "s"(ws) : "memory");
#pragma unroll
      for (int s = 0; s < NS - 1; ++s) stage(YSRC(s), lbase + s * 16384);
      BT(57);
      WAIT_V((NS - 2) * P);
      BAR(0);
      unsigned slot = lbase + (NS - 1) * 16384;
      const u64 evm = (u64)((1u | 1u << W_T) << pf0 | (1u | 1u << W_T) << pf1) | 1ull << CPF_K | 1ull << (33 - NS) | 3ull << 32;
      const char* yb = Yc;
      unsigned yo = (NS - 1 + xcd * 4) << 7, ym = 31 << 7;
#pragma unroll 1
      for (int k = 1; k <= 32 + B_J; ++k) {
        const char* ys = yb + (yo & ym);
        yo += 128;
        asm volatile("" : "+s"(ys));
        WAIT_V((NS - 3) * P);
        BAR(k);
        stage(ys, slot);
        slot = slot == lbase + (NS - 1) * 16384 ? lbase : slot + 16384;
        if (__builtin_expect((evm >> k) & 1, 0)) {
          if (k == pf0) {
            WST(og, wg);
            if (b >= NPART) GLDS(lds0 + SPARE + 8192 + hw * 1024, (unsigned)(hw * 1024 + lane * 16), wr_);
          }
          if (k == pf0 + W_T) { WTK(gT); }
          if (k == pf1) { WST(ou, wu); }
          if (k == pf1 + W_T) { WTK(uT); }
          if (k == CPF_K && hw == 1) CPFL();
          if (k == 33 - NS) {
#pragma unroll
            for (int i = 0; i < P; ++i) voy[i] = HOFF((hw * 8 + i) * 8 + (lane >> 3)) - (i & 3) * 1024;
            yb = Hc, yo = mt << 8, ym = 15 << 7;
          }
          if (k == 32) {
            DLD(0, 8 - B_WN);
            TSET();
          }
          if (k == 33 && hw == 0) {
            if (U_D) __builtin_amdgcn_s_sleep(U_D);
            urdy = UPOLL(U_N1);
          }
        }
      }
    } else {
      const int aw = hw - 2;
      unsigned ow = wo - (2048 + (cg0 >> 4) * 1024) * 2;
      const unsigned oq = (unsigned)((nt * 64 + aw * 32 + (lane >> 4)) * D + mt * 128) * 2;
      u32x4 hv[8], wt[8], sc;
      {
        asm volatile("s_waitcnt vmcnt(11)" : "+v"(cq)::"memory");
#pragma unroll
        for (int i = 0; i < 4; ++i) sc[i] = pack2(silu_bf(bflo(cq[i])), silu_bf(bfhi(cq[i])));
        asm volatile("" : "+v"(sc));
      }
#define GPUB(o, off)                                                                                   \
  if ((lane & 8) == 0)                                                                                 \
    asm volatile("global_store_dwordx4 %0, %1, %2 sc1 ; ALWAYS\n\ts_nop 1" ::"v"(off),                          \
                 "v"(u32x4{__float_as_uint(o[0]), (unsigned)tag, __float_as_uint(o[1]), (unsigned)tag}), "s"(ws) : "memory")
      const unsigned gpo = ks0 * 2048 + cg0 * 64 + (lane & 7) * 8 + (lane >> 4) * 2;
      {
        unsigned mpo = O_G1 + gpo * 8;
        asm volatile("s_waitcnt vmcnt(3)" : W8(wv), "+v"(mpo)::"memory");
        TR8(wt, wv);
        f32x2 o = MV64(wt, sc);
        asm volatile("" : "+v"(o));
        TAGW(0);
        BAR(55);
        if (P_D2) __builtin_amdgcn_s_sleep(P_D2);
        PFL(PFG(2 + aw));
        GPUB(o, mpo);
      }
      unsigned vta = lds0 + STG + (g4 * 8 + ((lane >> 2) & 3)) * 256 + (((aw * 8 + ((lane >> 1) & 1)) ^ ((g4 & 1) * 8 + ((lane >> 2) & 3))) << 4) + (lane & 1) * 8;
      unsigned vso = O_VP + ((cc * 8 + mt) * 16 + nt) * 256 + aw * 128 + (lane & 3) * 32 + g4 * 8;
      asm volatile("" : "+v"(vta), "+v"(vso), "+v"(ow));
      unsigned f0 = O_XFL + ((xcd * 32 + 64 + aw * 128 + lane) & 255) * XSTR * 4, f1 = O_XFL + ((xcd * 32 + 128 + aw * 64 + lane) & 255) * XSTR * 4;
      unsigned lao = O_KEY + 512 * 1024 + b * 3328 + aw * 1664 + lane * 16;
      const char* pf4 = PFG(4 + aw);
      const char* pf6 = PFG(6 + aw);
      asm volatile("" : "+v"(f0), "+v"(f1), "+v"(lao), "+v"(pf4), "+v"(pf6));
      BT(57);
      BAR(0);
      f32x4 rs = {0.f, 0.f, 0.f, 0.f};
      u32x4 rsa = (lane & 15) == g4 * 4 ? u32x4{0x3f803f80u, 0x3f803f80u, 0x3f803f80u, 0x3f803f80u} : u32x4{0u, 0u, 0u, 0u}, R_;
      asm volatile("" : "+v"(rs), "+v"(rsa), "=v"(R_));
      const unsigned ra = lds0 + (mt * 16 + aw * 8) * 128 + lane * 16;
      unsigned so = 0;
#define TSX()
#define RSUM()                                                                                                    \
  do {                                                                                                            \
    TSX();                                                                                                        \
    asm volatile("ds_read_b128 %1, %2\n\ts_waitcnt lgkmcnt(0)\n\tv_mfma_f32_16x16x32_bf16 %0, %3, %1, %0" : "+v"(rs), "+v"(R_) : "v"(ra + so), "v"(rsa) : "memory"); \
    so = so == (NS - 1) * 16384 ? 0 : so + 16384;                                                                 \
  } while (0)
      {
        u64 f[2];
#define FLD() asm volatile("global_load_dwordx2 %0, %2, %4 sc1\n\tglobal_load_dwordx2 %1, %3, %4 sc1" : "=&v"(f[0]), "=&v"(f[1]) : "v"(f0), "v"(f1), "s"(ws) : "memory")
        __builtin_amdgcn_s_sleep(F_D);
        FLD();
        asm volatile("global_store_dwordx4 %0, %1, %2 sc1" ::"v"(lao), "v"(u32x4{~0u, ~0u, ~0u, ~0u}), "s"(ws) : "memory");
        if (lane < 40) asm volatile("global_store_dwordx4 %0, %1, %2 offset:1024 sc1 ; ALWAYS" ::"v"(lao), "v"(u32x4{~0u, ~0u, ~0u, ~0u}), "s"(ws) : "memory");
        asm volatile("s_nop 1");
#define GLD() _Pragma("unroll") for (int i = 0; i < 8; ++i) LDWI(wv[i], ow, wmod, 12288)
        RSUM();
        BAR(1);
        asm volatile("s_waitcnt vmcnt(%2)" : "+v"(f[0]), "+v"(f[1]) : "n"(2) : "memory");
        POLL(__all(f[0] == tag && f[1] == tag), 1, FLD(); asm volatile("s_waitcnt vmcnt(0)" : "+v"(f[0]), "+v"(f[1])::"memory"));
      }
      if (X_PA) __builtin_amdgcn_s_setprio(X_PA);
#define ARUN(b0, b1) _Pragma("unroll 1") for (int bi = (b0); bi < (b1); ++bi) { RSUM(); BAR(bi + 1); }
      GLD();
      ARUN(1, pf0);
      PFL(pf4);
#define HVLD()                                                                                                                    \
  do {                                                                                                                            \
    unsigned o_ = oq + (((lane & 15) ^ g4) << 4);                                                                                 \
    asm volatile("s_mov_b32 m0, %0" ::"s"(lds0 + STG + aw * 8192));                                                               \
    _Pragma("unroll") for (int i = 0; i < 8; ++i) {                                                                               \
      asm volatile("v_xor_b32 %0, %4, %0\n\tglobal_load_lds_dwordx4 %0, %1 offset:%2" HPOL "\n\tv_add_u32 %0, %3, %0"              \
                   : "+v"(o_) : "s"(cc ? X1 : X0), "n"((i & 3) * 1024), "n"(8192 + (i == 3 ? 3072 : -1024)), "n"(i ? ((i ^ (i - 1)) & 3) * 64 : 0) : "memory"); \
      if (i == 3) asm volatile("s_add_u32 m0, m0, 4096" ::: "scc");                                                               \
    }                                                                                                                             \
  } while (0)
      WST(og, wg);
      ARUN(pf0, pf0 + G1_C);
      {
        if (b >= NPART) WAIT_V(0);
        asm volatile("s_waitcnt vmcnt(8)" : W8(wv)::"memory");
        TR8(wt, wv);
        if (G1_S) { RSUM(); BAR(pf0 + G1_C + 1); }
        const f32x2 o = MV64(wt, sc);
        GPUB(o, O_MODP + gpo * 8);
        ow = wo + 2048;
      }
      ARUN(pf0 + G1_C + G1_S, pf0 + W_T - 1);
      WAIT_V(1);
      RSUM();
      BAR(pf0 + W_T);
      WTK(gT);
      ARUN(pf0 + W_T, pf1);
      PFL(pf6);
      WST(ou, wu);
      const int rb_ = b < NPART ? pf1 + W_T : R_AT, hb_ = max(rb_ + 1, H_AT + (j8 & 3));
      const unsigned c0 = (b - NPART) * 64 + aw * 32 + (lane & 15) * 2, c1 = (b - NPART) * 32 + aw * 16 + (lane & 7) * 2;
#define ERLD(W) LD8(wt, O_MODP + (g4 * 8192 + c0) * 8, O_G1 + (g4 * 8192 + (lane & 8) * 128 + c1) * 8, W)
      ARUN(pf1, rb_ - 3);
      if (b >= NPART) ERLD("");
      ARUN(rb_ - 3, rb_ - 1);
      WAIT_V(0);
      RSUM();
      BAR(rb_);
      WTK(uT);
      if (b >= NPART) {
        const unsigned wq = *reinterpret_cast<const unsigned*>(&svb[4][c1]);
#define TG2(r, i) ((r[i][1] ^ (unsigned)tag) | (r[i][3] ^ (unsigned)tag) | (r[i + 1][1] ^ (unsigned)tag) | (r[i + 1][3] ^ (unsigned)tag))
        asm volatile("" : W8(wt));
        POLL(__all((TG2(wt, 0) | TG2(wt, 2) | TG2(wt, 4) | TG2(wt, 6)) == 0u), 2, ERLD(VW0));
        auto FIN = [&](int q, int i, bool inc) __attribute__((always_inline)) {
          return rbf((inc ? 1.0f : 0.0f) + rbf(ar4((__uint_as_float(wt[q][i]) + __uint_as_float(wt[q + 1][i])) + (__uint_as_float(wt[q + 2][i]) + __uint_as_float(wt[q + 3][i])))));
        };
        const float s0 = FIN(0, 0, gbq), s1 = FIN(0, 2, gbq);
        if (lane < 16)
          asm volatile("global_store_dwordx4 %0, %1, %2 sc1\n\ts_nop 1" ::"v"(O_MODP + (16 * 2048 + c0) * 8),
                       "v"(u32x4{__float_as_uint(s0), (unsigned)tag, __float_as_uint(s1), (unsigned)tag}), "s"(ws) : "memory");
        const bool hi = lane & 8;
        const float t0 = FIN(4, 0, hi), t1 = FIN(4, 2, hi), o0 = dpp_f<0x128>(t0), o1 = dpp_f<0x128>(t1);
        const float a0 = hi ? o0 : t0, a1 = hi ? o1 : t1, e0 = hi ? t0 : o0, e1 = hi ? t1 : o1, p0 = e0 * bflo(wq), p1 = e1 * bfhi(wq);
        const unsigned h = pack2(p0, p1);
        const unsigned v = g4 == 0 ? __float_as_uint(hi ? a1 : a0) : g4 == 1 ? (hi ? h : pack2(e0, e1)) : hi ? pack2(a0 * p0, a1 * p1) : pack2(p0 - bflo(h), p1 - bfhi(h));
        const unsigned po = g4 == 0 ? O_M2 + c1 * 4 + (hi ? 4 : 0) : (g4 == 1 ? (hi ? O_M2 + 4096 : O_MOD + 4096) : hi ? O_GC : O_M2 + 6144) + c1 * 2;
        if (lane < 48) asm volatile("global_store_dword %0, %1, %2 sc1" ::"v"(po), "v"(min(v, SENT - 1u)), "s"(ws) : "memory");
      }
      ARUN(rb_, hb_);
      HVLD();
      ARUN(hb_, q1);
      u32x4 fm, g0, g1;
#define FMLD(W)                                                                                                                                     \
  asm volatile("global_load_dwordx4 %0, %3, %5 sc1\n\tglobal_load_dwordx4 %1, %4, %5 sc1\n\tglobal_load_dwordx4 %2, %4, %5 offset:64 sc1" W        \
               : "=&v"(fm), "=&v"(g0), "=&v"(g1) : "v"(O_MODP + (16 * 2048 + aw * 1024 + mt * 128) * 8 + lane * 16), "v"(O_GC + nt * 128 + g4 * 16), "s"(ws) : "memory")
      FMLD("");
      RSUM();
      BAR(q1 + 1);
      unsigned ha[4], sma = lds0 + SPARE + 15872 + (aw ? 512 + lane * 8 : lane * 4);
#pragma unroll
      for (int i = 0; i < 4; ++i) ha[i] = lds0 + STG + aw * 8192 + (lane & 15) * 256 + (((i * 4 + (lane >> 4)) ^ (lane & 15)) << 4);
      asm volatile("" : "+v"(ha[0]), "+v"(ha[1]), "+v"(ha[2]), "+v"(ha[3]), "+v"(sma), "+v"(ow));
      ARUN(q1 + 1, F_AT);
      asm volatile("s_waitcnt vmcnt(0)" : "+v"(fm), "+v"(g0), "+v"(g1)::"memory");
#pragma unroll
      for (int i = 0; i < 8; ++i) DSR(hv[i], ha[i & 3], (i >> 2) * 4096);
      POLL(__all(fm[1] == (unsigned)tag && fm[3] == (unsigned)tag && max(max4(g0), max4(g1)) != SENT), 1, FMLD(VW0));
      {
        const unsigned sg = cc ? 0x80000000u : 0u;
        asm volatile("ds_write_b32 %0, %2\n\tds_write_b32 %1, %3" ::"v"(sma), "v"(sma + (aw ? 4 : 256)), "v"(aw ? fm[0] ^ sg : (fm[0] >> 16) | (fm[2] & 0xffff0000u)), "v"(fm[2] ^ sg) : "memory");
      }
      ARUN(F_AT, 29);
      GLD();
      RSUM();
      BAR(30);
        u32x2 tv[16];
#pragma unroll
        for (int q = 0; q < 16; ++q)
          asm volatile("ds_read_b64_tr_b16 %0, %1 offset:%2" : "=v"(tv[q]) : "v"(vta ^ ((((q >> 2) * 2) ^ ((q & 1) * 4)) << 4)), "n"((q & 1) * 1024 + ((q >> 1) & 1) * 8192) : "memory");
      RSUM();
      BAR(31);
      RSUM();
      {
        asm volatile("s_nop 7\n\ts_nop 7" : "+v"(rs));
        const float v0 = dpp_add<0x141>(dpp_add<0x4E>(dpp_add<0xB1>(rs[0]))), v = cc ? -v0 : v0;
        if ((lane & 7) == 0)
          asm volatile("global_store_dword %0, %1, %2 sc1 ; ALWAYS" ::"v"(O_RS + (cc * S + nt * 128 + mt * 16 + aw * 8 + (lane >> 3)) * 4), "v"(min(pack2(v, v - rbf(v)), SENT - 1u)), "s"(ws) : "memory");
      }
      BAR(32);
      __builtin_amdgcn_s_sleep(2);
      DLD(0, 8 - B_WN);
      __builtin_amdgcn_s_setprio(2);
      {
        asm volatile("" : W8(tv), W8((tv + 8))::"memory");
        f32x4 d[4];
#pragma unroll
        for (int j = 0; j < 4; ++j) {
          d[j] = __builtin_amdgcn_mfma_f32_16x16x32_bf16(BF8((u32x4{tv[4 * j][0], tv[4 * j][1], tv[4 * j + 1][0], tv[4 * j + 1][1]})), BF8(g0), f32x4{0.f, 0.f, 0.f, 0.f}, 0, 0, 0);
          d[j] = __builtin_amdgcn_mfma_f32_16x16x32_bf16(BF8((u32x4{tv[4 * j + 2][0], tv[4 * j + 2][1], tv[4 * j + 3][0], tv[4 * j + 3][1]})), BF8(g1), d[j], 0, 0, 0);
        }
        u32x2 vo;
#pragma unroll
        for (int i = 0; i < 2; ++i) {
          const unsigned e0 = pack2(d[0][2 * i], d[0][2 * i + 1]), e1 = pack2(d[1][2 * i], d[1][2 * i + 1]), e2 = pack2(d[2][2 * i], d[2][2 * i + 1]), e3 = pack2(d[3][2 * i], d[3][2 * i + 1]);
          vo[i] = min((lane & 2) ? ((lane & 1) ? e3 : e2) : ((lane & 1) ? e1 : e0), SENT - 1u);
        }
        if ((lane & 12) == 0) asm volatile("global_store_dwordx2 %0, %1, %2 sc1 ; ALWAYS" ::"v"(vso), "v"(vo), "s"(ws) : "memory");
      }
      asm volatile("" : W8(hv)::"memory");
      {
        f32x4 qa[2] = {};
#pragma unroll
        for (int kk = 0; kk < 4; ++kk) {
          const bf16x8 sb8 = *reinterpret_cast<const bf16x8*>(ssa + kk * 32 + (lane >> 4) * 8);
#pragma unroll
          for (int r = 0; r < 2; ++r)
            qa[r] = __builtin_amdgcn_mfma_f32_16x16x32_bf16(__builtin_bit_cast(bf16v8, hv[r * 4 + kk]), __builtin_bit_cast(bf16v8, sb8), qa[r], 0, 0, 0);
        }
        if ((lane & 15) < 2)
          asm volatile("global_store_dwordx4 %0, %1, %2 sc1 ; ALWAYS\n\ts_nop 1" ::"v"(O_RS + (4096 + (cc * 8 + mt) * D + nt * 64 + aw * 32 + (lane & 1) * 16 + (lane >> 4) * 4) * 4),
                       "v"(nsent4(selu((lane & 1) ? ~0u : 0u, __builtin_bit_cast(u32x4, qa[1]), __builtin_bit_cast(u32x4, qa[0])))), "s"(ws) : "memory");
      }
      BAR(33);
      {
        if (b >= NPART) WAIT_V(3);
        asm volatile("s_waitcnt vmcnt(%8)" : W8(wv) : "n"(8 - B_WN + 3) : "memory");
        TR8(wt, wv);
        const f32x2 o = MV64(wt, sc);
        GPUB(o, O_G2 + gpo * 8);
      }
      JSET();
      TSET();
      BAR(34);
#pragma unroll
      for (int k = 3; k <= B_J; ++k) {
        BAR(32 + k);
        if (k == V_K) {
          if (!aw) GP4(O_VP + (cc * 8 + mt) * 4096 + lane * 16, "");
          else VCLD("");
        }
      }
    }
    u32x4 dT[8];
    u32x4 m[3];
    u32x2 kk = {SENT, SENT};
#define K2LD(W) asm volatile("global_load_dwordx2 %0, %1, %2 sc1" W : "+v"(kk) : "v"(O_K2 + (lane & 15) * 64), "s"(ws) : "memory")
    {
      constexpr BSched sch;

      const unsigned lbase = lds0 + hw * 4096;
      u32x4 rr[8];
      unsigned cbv, t1, t2;
      float t[2];
#define RRLD(W)                                                                                                          \
  do {                                                                                                                   \
    asm volatile("global_load_dword %0, %1, %2 sc1" : "=&v"(cbv) : "v"(O_MOD + 4096 + cj * 2), "s"(ws) : "memory");      \
    LD8(rr, ro, ro + 65536, W);                                                                                          \
  } while (0)
      unsigned wq;
      u32x4 mq = {0u, 0u, 0u, 0u};
#define QLD(W)                                                                                                        \
  do {                                                                                                                \
    if (hw < 2) asm volatile("global_load_dwordx4 %0, %1, %2 sc1" W : "+v"(mq) : "v"(qo), "s"(ws) : "memory");        \
    else asm volatile("global_load_dword %0, %1, %2 sc1" W : "+v"(mq[0]) : "v"(qo), "s"(ws) : "memory");              \
  } while (0)
#define QRLD(W)                                                                                                       \
  asm volatile("global_load_dwordx4 %0, %2, %4 sc1\n\tglobal_load_dwordx4 %1, %3, %4 sc1" W : "=&v"(gp[0]), "=&v"(gp[1]) \
               : "v"(qr), "v"(qr + 32768), "s"(ws) : "memory")
      const bool rq_ = b >= NPART && hw == 1;
      const unsigned qr = O_RS + (4096 + (lane >> 3) * D + (b - NPART) * 32 + (lane & 7) * 4) * 4;
#define MODLD() asm volatile("global_load_dwordx4 %0, %2, %4 sc1\n\tglobal_load_dwordx4 %1, %3, %4 sc1" : "=&v"(m[0]), "=&v"(m[2]) : "v"(O_MOD + ma), "v"(mo2), "s"(ws) : "memory")
#define MODW(n) asm volatile("s_waitcnt vmcnt(%2)" : "+v"(m[0]), "+v"(m[2]) : "n"(n) : "memory")
#pragma unroll
      for (int k = B_J; k < 16; ++k) {
        if (k > B_J) {
          if (k == 2 * U_N1 - 1 && !urdy) UPOLL(7);
          WAIT_V(sch.cnt[k]);
          if (k == 15) WAIT_L(0);
          BAR(32 + k);
        }
        if (k > B_J && k - 2 + NS < 16) {
          const char* yp = Hc + ((mt * 2 + k - 2 + NS) & 15) * 128;
          const unsigned l = lbase + ((30 + k + NS) % NS) * 16384;
          GLDS4(l, voff, yp);
        }
        if (k == B_TK && hw == 1 && b >= 192) {
          const unsigned t = tick(), h = t + T_O + (((int)(t - ts0) - T_N) * T_A >> 8);
          if (lane == 0) asm volatile("global_store_dword %0, %1, %2 sc1" ::"v"(O_M2 + 8448 + (b - 192) * 4), "v"(min(h, SENT - 1u)), "s"(ws) : "memory");
        }
        if (k == B_RAT + 2) {
          if (rdc) {
            WAIT_V(sch.rcnt);
            asm volatile("" : W8(rr), "+v"(cbv));
            POLL(__all((TG2(rr, 0) | TG2(rr, 2) | TG2(rr, 4) | TG2(rr, 6)) == 0u && cbv != SENT), 2, RRLD(VW0); asm volatile("" : "+v"(cbv)));
#pragma unroll
            for (int j = 0; j < 2; ++j) {
#define RF(q) __uint_as_float(rr[q][2 * j])
              t[j] = ((RF(0) + RF(1)) + (RF(2) + RF(3))) + ((RF(4) + RF(5)) + (RF(6) + RF(7)));
            }
            asm volatile("" : "+v"(t[0]), "+v"(t[1]));
          }
        }
        if (k == B_RAT + 2 && rdc) {
          const unsigned o = pack2(swap32_add(t[0], t[0]), swap32_add(t[1], t[1])), f = gbq ? cbv : o;
          const float pr = row_sum(bflo(f) * bflo(wq) + bfhi(f) * bfhi(wq)), ps = swap16_add(pr, pr);
          if (lane <= 32) asm volatile("global_store_dword %0, %1, %2 sc1" ::"v"(ra1), "v"(min(lane < 32 ? o : __float_as_uint(ps), SENT - 1u)), "s"(ws) : "memory");
          TS(12);
        }
        if (k == V_T && hw == 3) {
          WAIT_V(sch.vcnt);
          asm volatile("" : "+v"(vc));
          POLL(__all(vc != SENT), 2, VCLD(VW0));
        }
        if (k == V_T && hw == 2) {
          WAIT_V(sch.vcnt);
          asm volatile("" : "+v"(gp[0]), "+v"(gp[1]), "+v"(gp[2]), "+v"(gp[3]));
          POLL(!__any(GPBAD), 2, GP4(O_VP + (cc * 8 + mt) * 4096 + lane * 16, VW0));
          const f32x4 d = GPSUM();
          if (lane < 32) *reinterpret_cast<u32x2*>(shm + VT + (lane & 15) * 16 + g4 * 8) = u32x2{pack2(d[0], d[1]), pack2(d[2], d[3])};
          WAIT_L(0);
        }
        if (k == N_C) {
          f32x4 n[2] = {};
#pragma unroll
          for (int q = 0; q < 8; ++q)
            n[q >> 2] = __builtin_amdgcn_mfma_f32_16x16x32_bf16(BF8(*reinterpret_cast<const u32x4*>(shm + VT + (q & 3) * 64 + g4 * 16)),
                                                                BF8(*reinterpret_cast<const u32x4*>(shm + STG + hw * 8192 + q * 1024 + lane * 16)), n[q >> 2], 0, 0, 0);
          if (lane < 16)
            asm volatile("global_store_dword %0, %1, %2 sc1" ::"v"(O_NP + nt * 4096 + (cc * 8 + mt) * 256 + hw * 64 + lane * 4), "v"(min(pack2(n[0][0], n[1][0]), SENT - 1u)), "s"(ws) : "memory");
          TS(33);
        }
        if (k == N_K + N_D && rnp) {
            WAIT_V(sch.ncnt);
            asm volatile("" : "+v"(gp[0]), "+v"(gp[1]), "+v"(gp[2]), "+v"(gp[3]));
            POLL(!__any(GPBAD), 2, GP4(O_NP + nt * 4096 + lane * 16, VW0));
            const unsigned r0 = nt * 128 + ((lane >> 2) & 3) * 32 + (lane & 3) * 4 + (g4 & 1) * 2;
        const f32x4 d = GPSUM();
        unsigned ky[4];
#pragma unroll
        for (int i = 0; i < 4; ++i) {
          const unsigned f = __float_as_uint(d[i]);
          ky[i] = (((f ^ ((unsigned)((int)f >> 31) | 0x80000000u)) >> 1) & ~2047u) | (r0 + (i & 1) * 16 + (i >> 1));
        }
        t1 = max(max(ky[0], ky[1]), max(ky[2], ky[3])), t2 = max(min(max(ky[0], ky[1]), max(ky[2], ky[3])), max(min(ky[0], ky[1]), min(ky[2], ky[3])));
        asm volatile("" : "+v"(t1), "+v"(t2));
        }
        if (k == N_K + N_D && rnp) {
        TOP2(0xB1); TOP2(0x4E); TOP2(0x141); TOP2(0x140);
        TOP2S(__builtin_amdgcn_permlane16_swap);
            if (lane == 0) asm volatile("global_store_dwordx2 %0, %1, %2 sc1" ::"v"(O_K2 + nt * 64), "v"(u32x2{t1, t2}), "s"(ws) : "memory");
            TS(34);
        }
        if (k == N_K && rnp) GP4(O_NP + nt * 4096 + lane * 16, "");
        if (k == B_RAT + 2 && rq_) {
          WAIT_V(sch.rcnt);
          asm volatile("" : "+v"(gp[0]), "+v"(gp[1]));
          POLL(__all(max(max4(gp[0]), max4(gp[1])) != SENT), 2, QRLD(VW0));
          u32x4 a;
#pragma unroll
          for (int c = 0; c < 2; ++c) {
            const f32x4 t = __builtin_bit_cast(f32x4, gp[c]);
            const float r = dpp_add<0x128>(swap16_add(swap32_add(t[0], t[2]), swap32_add(t[1], t[3])));
            a[2 * c] = pack2(r, r);
            a[2 * c + 1] = pack2(r - bflo(a[2 * c]), r - bflo(a[2 * c]));
          }
          if ((lane & 8) == 0)
            asm volatile("global_store_dwordx4 %0, %1, %2 sc1\n\ts_nop 1" ::"v"(O_QT + ((b - NPART) * 32 + (lane & 1) * 16 + ((lane >> 1) & 3) * 4 + g4) * 16), "v"(nsent4(a)), "s"(ws) : "memory");
        }
        if (k == B_QAT + B_QD) {
          asm volatile("s_waitcnt vmcnt(%1)" : "+v"(mq) : "n"(sch.qcnt) : "memory");
          POLL(__all(max4(mq) != SENT), 2, QLD(VW0));
          if (hw < 2) asm volatile("ds_write_b128 %0, %1" ::"v"(qa), "v"(mq) : "memory");
          else asm volatile("ds_write2_b32 %0, %1, %1 offset1:1" ::"v"(qa), "v"(mq[0]) : "memory");
        }
        if (k == CPF_B && hw == 2) CPFL();
        if (k == B_RAT) {
          if (rdc) {
            RRLD("");
            asm volatile("ds_read_b32 %0, %1\n\ts_waitcnt lgkmcnt(0)" : "=v"(wq) : "v"(wqa) : "memory");
          }
          if (rq_) QRLD("");
        }
        if (k == X_K) {
          const unsigned v4[4] = {xo, (xo ^ 64) + 8192 - 1024, (xo ^ 128) + 16384 - 2048, (xo ^ 192) + 24576 - 3072};
          unsigned l2 = lbase;
          asm volatile("" : "+s"(l2));
          GLDS4(l2 * 2 - lds0 + STG + 4096, v4, x);
        }
        if (k == V_T + 1 || k == N_K + 1) SKEWPT();
        if (k == B_QAT) QLD("");
        if (k == B_MAT) MODLD();
        if (k == B_WK) { DLD(8 - B_WN, 8); }
      }
      if (b >= NPART) WAIT_V(0);
      MODW(sch.mcnt);
      TS(30);
      SKEWPT();
      POLL(__all(max(max4(m[0]), max4(m[2])) != SENT), 4, MODLD(); MODW(0));
      *reinterpret_cast<u32x4*>(shm + SPARE + ma) = m[0];
      if (hw == 0) {
        asm volatile("ds_write_b128 %0, %1" ::"v"(ma2), "v"(m[2]) : "memory");
      } else if (hw == 2) {
        int d = max(max((int)(m[2][0] - ts0), (int)(m[2][1] - ts0)), max((int)(m[2][2] - ts0), (int)(m[2][3] - ts0)));
        d = max(d, (int)dpp_u<0xB1>(d));
        d = max(d, (int)dpp_u<0x4E>(d));
        d = max(d, (int)dpp_u<0x141>(d));
        d = max(d, (int)dpp_u<0x140>(d));
        const unsigned hh = ts0 + d;
        ssel[7] = hh;
        if ((int)(tick() - (unsigned)__builtin_amdgcn_readfirstlane((int)hh) - T_K) >= 0) K2LD("");
      } else {
        const f32x4 v = __builtin_bit_cast(f32x4, m[2]);
        const f32x2 c = {v[0] + v[1], v[2] + v[3]};
        const float s4 = dpp_add<0x4E>(dpp_add<0xB1>(c[0] + c[1]));
        if (lane < 8) asm volatile("ds_write_b64 %0, %1" ::"v"(ma2), "v"(lane < 4 ? c : f32x2{s4, s4}) : "memory");
      }
    }
    if (hw != 2) __builtin_amdgcn_s_setprio(0);
    TS(17);
    BT(48);
    LBAR();
    BTE();
    TS(18);
    if (hw == 2) {
      WAIT_V(0);
      asm volatile("" : "+v"(kk));
      if (__any(max(kk[0], kk[1]) == SENT)) {
        nap_tick((unsigned)__builtin_amdgcn_readfirstlane((int)ssel[7]) + T_K);
        POLL2(max(kk[0], kk[1]) == SENT, K_S, PCAP, K2LD(VW0));
      }
      TS(35);
      const unsigned me = lane < 32 ? kk[0] : kk[1], ot = (lane & 16) ? kk[1] : kk[0];
      unsigned rk = ot > me;
      sfor<15>([&](auto r) __attribute__((always_inline)) { rk += dpp_u<0x121 + decltype(r)::value>(ot) > me; });
      auto rs_ = __builtin_amdgcn_permlane16_swap(rk, rk, false, false);
      rk = rs_[0] + rs_[1];
      if (rk < 8u) {
        *reinterpret_cast<unsigned*>(shm + LST + rk * 4) = 0x8ffeu - (me & 2047u) * 2;
        if ((lane & 15) == nt) *reinterpret_cast<unsigned*>(shm + SLT + (me & 127u) * 4) = rk + 1;
      }
      asm volatile("" ::: "memory");
      ssel[20] = 1u;
    }
    TS(19);
    unsigned sv, one = 1u, kmk = ~0u;
    RETIRE();
    const u32x4 cbq = *reinterpret_cast<const u32x4*>(shm + SPARE + 4096 + kca), sbq = *reinterpret_cast<const u32x4*>(shm + SPARE + 2048 + kca);
    f32x4 cb2[2] = {lo4(cbq), hi4(cbq)}, sb2[2] = {lo4(sbq), hi4(sbq)};
    float sw = cwj[8], cj8 = cwj[lane & 7];
    asm volatile("" : "+v"(cb2[0]), "+v"(cb2[1]), "+v"(sb2[0]), "+v"(sb2[1]), "+v"(sw), "+v"(cj8), "+v"(one), "+v"(kmk));
    const unsigned th = (unsigned)__builtin_amdgcn_readfirstlane((int)ssel[7]);
    const char* wb = ws;
#define P2PUT(i) if (__builtin_expect(wb != ws, 0)) { OWNL(); WAIT_V(0); if (lane == 0) asm volatile("global_store_dwordx2 %0, %1, %2 sc1" ::"v"(O_F2 + (i)*8192 + (g * 4 + hw) * 8 + lane), "v"(tag), "s"(ws) : "memory"); }
#define P2GET(o) if (__builtin_expect(wb != ws, 0)) { OWNL(); u64 f; POLL2(f != tag, 4, PCAP, asm volatile("global_load_dwordx2 %0, %1, %2 sc1" VW0 : "=&v"(f) : "v"(O_F2 + (o)), "s"(ws) : "memory")); }
#pragma unroll 1
    for (;;) {
    TS(20);
    if (wb == ws) {
      unsigned r_;
      POLL2(sv == 0u, 2, 1 << 26, asm volatile("ds_read_b32 %0, %2 offset:80\n\tds_read_b32 %1, %3\n\ts_waitcnt lgkmcnt(0)" : "=&v"(sv), "=&v"(r_) : "v"(sfa), "v"(rk2) : "memory"));
      rk2 = r_;
    }
    TS(21);
    nap_tick(th + T_C3 - 10);
    {
      u32x4 dn;
      POLL2((dn[0] & dn[1] & dn[2] & dn[3]) == 0u, 1, 1 << 26, asm volatile("ds_read_b128 %0, %1 offset:96\n\ts_waitcnt lgkmcnt(0)" : "=v"(dn) : "v"(sfa) : "memory"));
    }
    __builtin_amdgcn_s_setprio(H_P);
    wait_tick(th + T_C3);
    TS(36);
    u32x4 st, xv_;
    POLL2(max(max4(xv_), max4(st)) == SENT, 0, PCAP,
          asm volatile("global_load_dwordx4 %0, %2, %4 sc1\n\tglobal_load_dwordx4 %1, %3, %4 sc1" VW0 : "=&v"(st), "=&v"(xv_) : "v"(sto), "v"(xo_), "s"(ws) : "memory"));
    TS(22);
    SKEWPT();
#define SUM8(v) dpp_add<0x141>(dpp_add<0x4E>(dpp_add<0xB1>(v)))
    const f32x4 sf = __builtin_bit_cast(f32x4, st);
    const float mean = SUM8(sf[0]) * 0.125f, dm = sf[0] - mean;
    const float rstd = __builtin_amdgcn_rsqf(SUM8(sf[1] + 128.0f * (dm * dm)) * (1.0f / D) + 1e-5f);
    asm volatile("ds_write_b128 %0, %1\n\tds_write_b32 %2, %3 offset:64" ::"v"(hwa),
                 "v"(pk8(((lo4(xv_) - mean) * rstd) * cb2[0] + sb2[0], ((hi4(xv_) - mean) * rstd) * cb2[1] + sb2[1])), "v"(sfw), "v"(one) : "memory");
    TS(3);
    u32x4 ha[8], fg;
#define HSLD(W)                                                                                                                             \
  asm volatile(                                                                                                                             \
      "ds_read_b128 %8, %11 offset:64\n\tds_read_b128 %0, %9\n\tds_read_b128 %1, %10 offset:512\n\tds_read_b128 %2, %9 offset:1024\n\t"       \
      "ds_read_b128 %3, %10 offset:1536\n\tds_read_b128 %4, %9 offset:2048\n\tds_read_b128 %5, %10 offset:2560\n\t"                          \
      "ds_read_b128 %6, %9 offset:3072\n\tds_read_b128 %7, %10 offset:3584" W                                                               \
      : "=&v"(ha[0]), "=&v"(ha[1]), "=&v"(ha[2]), "=&v"(ha[3]), "=&v"(ha[4]), "=&v"(ha[5]), "=&v"(ha[6]), "=&v"(ha[7]), "=&v"(fg)           \
      : "v"(hra), "v"(hrb), "v"(sfa)                                                                                                        \
      : "memory")
    unsigned key = (mkkey(rstd * SUM8(sf[2] + dm * cj8) + sw) | rk2) & kmk;
    asm volatile("" : "+v"(key));
    if (H_O == 0) HSLD("");
    unsigned t1 = key, t2 = 0u;
    TOP2(0x140);
    if (H_O == 1) { asm volatile("" : "+v"(t1), "+v"(t2)); HSLD(""); asm volatile("" : "+v"(t1), "+v"(t2)); }
    TOP2S(__builtin_amdgcn_permlane16_swap);
    if (H_O == 2) { asm volatile("" : "+v"(t1), "+v"(t2)); HSLD(""); asm volatile("" : "+v"(t1), "+v"(t2)); }
    TOP2S(__builtin_amdgcn_permlane32_swap);
    const int l1 = __builtin_ctzll(__builtin_amdgcn_ballot_w64(key == t1)), l2 = __builtin_ctzll(__builtin_amdgcn_ballot_w64(key == t2));
    unsigned mq[6];
    asm volatile("s_bfe_i32 %0, %6, 0x10003\n\ts_bfe_i32 %1, %7, 0x10003\n\ts_bfe_i32 %2, %6, 0x10004\n\ts_bfe_i32 %3, %7, 0x10004\n\t"
                 "s_bfe_i32 %4, %6, 0x10005\n\ts_bfe_i32 %5, %7, 0x10005\n\ts_xor_b32 %4, %4, 0xffff\n\ts_xor_b32 %5, %5, 0xffff"
                 : "=&s"(mq[0]), "=&s"(mq[1]), "=&s"(mq[2]), "=&s"(mq[3]), "=&s"(mq[4]), "=&s"(mq[5]) : "s"(l1), "s"(l2) : "scc");
    const u64 m0 = mk64(mq[1], mq[0]), m1 = mk64(mq[3], mq[2]), m2 = mk64(mq[5], mq[4]);
    asm volatile("s_waitcnt lgkmcnt(0)" : W8(ha), "+v"(fg)::"memory");
    TS(2);
    if (b < NPART) {
#define HBAD ((fg[0] & fg[1] & fg[2] & fg[3]) != one)
      if (__any(HBAD)) POLL2(HBAD, 0, 1 << 26, HSLD("\n\ts_waitcnt lgkmcnt(0)"));
      TS(23);
      f32x4 dg = {0.f, 0.f, 0.f, 0.f}, du = {0.f, 0.f, 0.f, 0.f};
#pragma unroll
      for (int f = 0; f < 8; ++f) {
        dg = __builtin_amdgcn_mfma_f32_16x16x32_bf16(BF8(ha[f]), BF8(gT[f]), dg, 0, 0, 0);
        du = __builtin_amdgcn_mfma_f32_16x16x32_bf16(BF8(ha[f]), BF8(uT[f]), du, 0, 0, 0);
      }
      const bool s0 = __builtin_amdgcn_inverse_ballot_w64(m0), s1 = __builtin_amdgcn_inverse_ballot_w64(m1);
      if (__builtin_amdgcn_inverse_ballot_w64(m2))
        asm volatile("global_store_dword %0, %1, %3 sc1 ; ALWAYS\n\tglobal_store_dword %0, %2, %3 offset:512 sc1" ::"v"(gso),
                     "v"(nsent(s1 ? (s0 ? dg[3] : dg[2]) : (s0 ? dg[1] : dg[0]))), "v"(nsent(s1 ? (s0 ? du[3] : du[2]) : (s0 ? du[1] : du[0]))), "s"(wb) : "memory");
    }
    if (hw == 0 && wb == ws) asm volatile("ds_write_b64 %0, %1" ::"v"(sfa), "v"(u32x2{t1, t2}) : "memory");
    if (b < NPART) {
      TS(24);
      asm volatile("" : W8(wdv), "+v"(qm[0]), "+v"(qm[1]), "+v"(qm[2]), "+v"(qm[3])::"memory");
      TR8(dT, wdv);
      P2PUT(0);
      if (hw == 0) {
        u32x2 rr[8];
        if (E_D) __builtin_amdgcn_s_sleep(E_D);
        TS(39);
        P2GET(((((lane & 16) ? gl1 : gl0) - O_GU) >> 12) * 128 + (lane & 15) * 8);
#define MX2(i) max(max(rr[i][0], rr[i][1]), max(rr[i + 1][0], rr[i + 1][1]))
        POLL2(max(max(MX2(0), MX2(2)), max(MX2(4), MX2(6))) == SENT, 1, PCAP,
              TS(40);
              asm volatile("global_load_dwordx2 %0, %8, %10 sc1\n\tglobal_load_dwordx2 %1, %8, %10 offset:1024 sc1\n\t"
                           "global_load_dwordx2 %2, %8, %10 offset:2048 sc1\n\tglobal_load_dwordx2 %3, %8, %10 offset:3072 sc1\n\t"
                           "global_load_dwordx2 %4, %9, %10 sc1\n\tglobal_load_dwordx2 %5, %9, %10 offset:1024 sc1\n\t"
                           "global_load_dwordx2 %6, %9, %10 offset:2048 sc1\n\tglobal_load_dwordx2 %7, %9, %10 offset:3072 sc1" VW0
                           : "=&v"(rr[0]), "=&v"(rr[1]), "=&v"(rr[2]), "=&v"(rr[3]), "=&v"(rr[4]), "=&v"(rr[5]), "=&v"(rr[6]), "=&v"(rr[7])
                           : "v"(gl0), "v"(gl1), "s"(wb)
                           : "memory"));
        TS(8);
#define R2(i) __builtin_bit_cast(f32x2, rr[i])
        const f32x2 gs = (R2(0) + R2(1)) + (R2(2) + R2(3)), us = (R2(4) + R2(5)) + (R2(6) + R2(7));
        const unsigned gp = pack2(gs[0], gs[1]), up = pack2(us[0], us[1]);
        const unsigned sp = pack2(bflo(gp) * __builtin_amdgcn_rcpf(1.0f + __expf(-bflo(gp))), bfhi(gp) * __builtin_amdgcn_rcpf(1.0f + __expf(-bfhi(gp))));
        asm volatile("ds_write_b32 %0, %1\n\tds_write_b32 %2, %3 offset:24" ::"v"(awa), "v"(pack2(bflo(sp) * bflo(up), bfhi(sp) * bfhi(up))), "v"(sfa), "v"(one) : "memory");
      }
      TS(25);
      SKEWPT();
      POLL2(sv != one, 0, 1 << 26, asm volatile("ds_read_b32 %1, %3 offset:24\n\tds_read_b128 %0, %2\n\ts_waitcnt lgkmcnt(0)" : "=&v"(fg), "=&v"(sv) : "v"(ara), "v"(sfa) : "memory"));
      asm volatile("global_load_dwordx2 %0, %1, %2 sc1" : "=&v"(vd) : "v"(O_VD + (b & 31) * 128), "s"(ws) : "memory");
      const f32x2 dd = MV64(dT, fg);
      asm volatile("global_store_dwordx2 %0, %1, %2 sc1" ::"v"(pso), "v"(u32x2{nsent(dd[0]), nsent(dd[1])}), "s"(wb) : "memory");
      TS(6);
      P2PUT(1);
      asm volatile("s_waitcnt vmcnt(1)" : "+v"(vd)::"memory");
      if (__any(max(vd[0], vd[1]) == SENT)) VDLD(2);
      TS(5);
    } else if (hw == 3) {
      OWNL();
      u32x2 g0, g1;
      nap_tick(th + T_C4);
      POLL2(min(min(g0[0], g0[1]), min(g1[0], g1[1])) == 0u, 2, PCAP,
            asm volatile("global_load_dwordx2 %0, %2, %4 sc1\n\tglobal_load_dwordx2 %1, %3, %4 sc1" VW0
                         : "=&v"(g0), "=&v"(g1) : "v"(O_KEY + lane * 8 * KSTR), "v"(O_KEY + (lane + 64) * 8 * KSTR), "s"(ws) : "memory"));
      const unsigned k1 = t1, k2 = t2;
      t1 = max(g0[0], g1[0]), t2 = max(min(g0[0], g1[0]), max(g0[1], g1[1]));
      TOP2(0xB1); TOP2(0x4E); TOP2(0x141); TOP2(0x140);
      TOP2S(__builtin_amdgcn_permlane16_swap);
      TOP2S(__builtin_amdgcn_permlane32_swap);
      vd = u32x2{max(t1, k1), max(min(t1, k1), max(t2, k2))};
      if (lane == 0) asm volatile("global_store_dwordx2 %0, %1, %2 sc1\n\tds_write_b64 %3, %1 offset:8" ::"v"(O_VD + (b & 31) * 128), "v"(vd), "s"(ws), "v"(sfa) : "memory");
      TS(29);
      break;
    } else if (hw < 2) {
      OWNL();
      const int tok = b & 1, c0 = ((b - NPART) >> 1) * 64 + hw * 32 + (lane & 7) * 4, col = c0 + g4;
      const unsigned td = tick(), kk = tok ? t2 : t1;
      const int ln = tok ? l2 : l1;
      unsigned v;
      POLL2(v == SENT, 1, PCAP, asm volatile("global_load_dword %0, %1, %2 sc1" VW0 : "=&v"(v) : "v"(O_X1C + ((__builtin_amdgcn_readlane(xo_, ln) - O_X1C) >> 11) * 2048 + (col >> 1) * 4), "s"(ws) : "memory"));
      float x1 = (col & 1) ? bfhi(v) : bflo(v);
      float h2 = rbf(((x1 - __int_as_float(__builtin_amdgcn_readlane(__float_as_int(mean), ln))) * __int_as_float(__builtin_amdgcn_readlane(__float_as_int(rstd), ln))) * bf2f(svb[2][col]) + bf2f(svb[1][col]));
      float gb = bf2f(svb[3][col]), rsg = rbf(1.0f / (1.0f + __expf(-key_val(kk))));
      unsigned oo_ = (key_row(kk) * D + col) * 2, po = O_PART + ((lane >> 3) * 2048 + tok * 1024 + c0) * 4, p1 = po + 4 * 65536;
      asm volatile("" : "+v"(x1), "+v"(h2), "+v"(gb), "+v"(rsg), "+v"(oo_), "+v"(po), "+v"(p1));
      u32x4 y[7];
      nap_tick(td + F_T - 10);
      wait_tick(td + F_T);
      TS(37);
      P2GET(8192 + (min(lane, 55) * 16 + ((b - NPART) >> 1)) * 8);
      POLL2(max(max(max(max4(y[0]), max4(y[1])), max(max4(y[2]), max4(y[3]))), max(max(max4(y[4]), max4(y[5])), max4(y[6]))) == SENT || vd[1] == 0u, 0, PCAP,
            TS(38);
            asm volatile("global_load_dwordx4 %0, %7, %9 sc1\n\tglobal_load_dwordx4 %1, %7, %10 sc1\n\tglobal_load_dwordx4 %2, %7, %11 sc1\n\t"
                         "global_load_dwordx4 %3, %7, %12 sc1\n\tglobal_load_dwordx4 %4, %8, %9 sc1\n\tglobal_load_dwordx4 %5, %8, %10 sc1\n\t"
                         "global_load_dwordx4 %6, %8, %11 sc1"
                         : "=&v"(y[0]), "=&v"(y[1]), "=&v"(y[2]), "=&v"(y[3]), "=&v"(y[4]), "=&v"(y[5]), "=&v"(y[6])
                         : "v"(po), "v"(p1), "s"(wb), "s"(wb + 65536), "s"(wb + 131072), "s"(wb + 196608)
                         : "memory");
            asm volatile("s_waitcnt vmcnt(2)\n\tds_read_b64 %7, %8 offset:8\n\ts_waitcnt vmcnt(0) lgkmcnt(0)"
                         : "+v"(y[0]), "+v"(y[1]), "+v"(y[2]), "+v"(y[3]), "+v"(y[4]), "+v"(y[5]), "+v"(y[6]), "=&v"(vd) : "v"(sfa) : "memory"));
      TS(28);
#define YF(i) __builtin_bit_cast(f32x4, y[i])
      const f32x4 t = ((YF(0) + YF(1)) + (YF(2) + YF(3))) + ((YF(4) + YF(5)) + YF(6));
      const float sum = dpp_add<0x128>(swap16_add(swap32_add(t[0], t[2]), swap32_add(t[1], t[3])));
      asm volatile("global_store_short %0, %1, %2" ::"v"(oo_), "v"(pack2(x1 + gb * (wb == ws && kk < vd[1] ? h2 : rbf(h2 + rbf(rbf(sum) * rsg))), 0.f)), "s"(out) : "memory");
      TS(15);
    } else {
      break;
    }
    if (__builtin_expect(wb != ws || __all(vd[1] == t2), 1)) break;
    OWNL();
    const unsigned tk = (lane & 8) ? vd[1] : vd[0], ns = tk == t1 ? l1 >> 3 : 8 + ((lane >> 3) & 1);
    rk2 = tk & 0xffffu, kmk = lane < 16 ? ~0u : 0u, one = 3u;
    xo_ = O_X1C + ns * 2048 + kca, sto = O_ST2 + ns * 128 + (lane & 7) * 16;
    wb = ws + O_P2;
    }
    TSE();
  }
}

extern "C" void kernel_launch(void* const* d_in, const int* in_sizes, int n_in, void* d_out, int out_size, void* d_ws, size_t ws_size,
                              hipStream_t stream) {
  const u16* x = (const u16*)d_in[0];
  const u16* wsr = (const u16*)d_in[1];
  const u16* wsi = (const u16*)d_in[2];
  const u16* whr = (const u16*)d_in[3];
  const u16* whi = (const u16*)d_in[4];
  const u16* w_r = (const u16*)d_in[6];
  const u16* w_gate = (const u16*)d_in[7];
  const u16* w_up = (const u16*)d_in[8];
  const u16* w_down = (const u16*)d_in[9];
  const u16* c = (const u16*)d_in[10];
  const u16* w_mod = (const u16*)d_in[11];
  u16* out = (u16*)d_out;
  k_main<<<256, 512, 0, stream>>>(x, (char*)d_ws, wsr, wsi, c, w_mod, whr, whi, w_r, w_gate, w_up, w_down, out);
}
```

```cpp
#include <hip/hip_runtime.h>
#include <hip/hip_bf16.h>
#include <stdint.h>
#include <utility>

#define NEWLAY 1
typedef unsigned short u16;
typedef unsigned long long u64;
using bf16x8 = __attribute__((ext_vector_type(8))) short;
using bf16v8 = __attribute__((ext_vector_type(8))) __bf16;
using f32x4 = __attribute__((ext_vector_type(4))) float;
using f32x2 = __attribute__((ext_vector_type(2))) float;
using u32x4 = __attribute__((ext_vector_type(4))) unsigned;
using u32x2 = __attribute__((ext_vector_type(2))) unsigned;

constexpr int S = 2048, D = 1024, F = 3584;

__device__ __forceinline__ float bf2f(unsigned v) { return __uint_as_float(v << 16); }
__device__ __forceinline__ float bflo(unsigned v) { return __uint_as_float(v << 16); }
__device__ __forceinline__ float bfhi(unsigned v) { return __uint_as_float(v & 0xffff0000u); }
__device__ __forceinline__ unsigned f2bf(float f) {
  __hip_bfloat16 b = __float2bfloat16(f);
  return (unsigned)*reinterpret_cast<u16*>(&b);
}
__device__ __forceinline__ float rbf(float f) { return bf2f(f2bf(f)); }
using bf16v2 = __attribute__((ext_vector_type(2))) __bf16;
__device__ __forceinline__ unsigned pack2(float lo, float hi) {
  return __builtin_bit_cast(unsigned, __builtin_convertvector(f32x2{lo, hi}, bf16v2));
}

template <int CTRL>
__device__ __forceinline__ float dpp_f(float v) {
  return __int_as_float(__builtin_amdgcn_mov_dpp(__float_as_int(v), CTRL, 0xf, 0xf, true));
}
template <int CTRL>
__device__ __forceinline__ float dpp_add(float v) { return v + dpp_f<CTRL>(v); }
__device__ __forceinline__ float row_sum(float v) { return dpp_add<0x140>(dpp_add<0x141>(dpp_add<0x4E>(dpp_add<0xB1>(v)))); }
template <int CTRL>
__device__ __forceinline__ unsigned dpp_u(unsigned v) { return (unsigned)__builtin_amdgcn_mov_dpp((int)v, CTRL, 0xf, 0xf, true); }
__device__ __forceinline__ u64 mk64(unsigned hi, unsigned lo) { return ((u64)hi << 32) | lo; }
__device__ __forceinline__ float swap32_add(float a, float b) {
  auto r = __builtin_amdgcn_permlane32_swap(__float_as_uint(a), __float_as_uint(b), false, false);
  return __uint_as_float(r[0]) + __uint_as_float(r[1]);
}
__device__ __forceinline__ float swap16_add(float a, float b) {
  auto r = __builtin_amdgcn_permlane16_swap(__float_as_uint(a), __float_as_uint(b), false, false);
  return __uint_as_float(r[0]) + __uint_as_float(r[1]);
}
__device__ __forceinline__ float ar4(float v) {
  const float t = swap32_add(v, v);
  return swap16_add(t, t);
}
__device__ __forceinline__ float wave_sum_s(float v) {
  v = row_sum(v);
  v += __int_as_float(__builtin_amdgcn_update_dpp(0, __float_as_int(v), 0x142, 0xa, 0xf, false));
  v += __int_as_float(__builtin_amdgcn_update_dpp(0, __float_as_int(v), 0x143, 0xc, 0xf, false));
  return __int_as_float(__builtin_amdgcn_readlane(__float_as_int(v), 63));
}
__device__ __forceinline__ float silu_bf(float c) { return rbf(c * __builtin_amdgcn_rcpf(1.0f + __expf(-c))); }
__device__ __forceinline__ float dsum(unsigned v, float a) {
  return __builtin_amdgcn_fdot2_f32_bf16(__builtin_bit_cast(bf16v2, v), __builtin_bit_cast(bf16v2, 0x3f803f80u), a, false);
}
template <class Fn, int... I>
__device__ __forceinline__ void sfor_(Fn&& f, std::integer_sequence<int, I...>) { (f(std::integral_constant<int, I>{}), ...); }
template <int N, class Fn>
__device__ __forceinline__ void sfor(Fn&& f) { sfor_(f, std::make_integer_sequence<int, N>{}); }
constexpr unsigned SENT = 0xFFFFFFFFu;
constexpr int NPART = 224;

constexpr size_t WS_EPO = 0;
constexpr size_t WS_XFL = 4096;
constexpr size_t WS_MODP = 40960;
constexpr size_t WS_KEYG = 320u << 10;
constexpr size_t WS_UFL = WS_KEYG + 16384;
constexpr size_t WS_MOD = WS_KEYG + (80u << 10);
constexpr size_t WS_RS = WS_KEYG + (92u << 10);
constexpr size_t WS_PART = WS_KEYG + (332u << 10);
constexpr unsigned O_MODP = WS_MODP, O_XFL = WS_XFL, O_KEY = WS_KEYG, O_MOD = WS_MOD, O_RS = WS_RS, O_M2 = WS_RS + 80 * 1024;
constexpr unsigned O_PART = WS_PART, O_GU = O_PART + 56 * 8192, O_STAT = O_GU + 224 * 1024, O_X1C = O_STAT + 262144;
constexpr unsigned O_ST2 = O_X1C + 20480, O_VD = O_ST2 + 6144, O_P2 = 1536u << 10, O_F2 = 3584u << 10;
constexpr unsigned O_GC = O_M2 + 8704, O_VP = O_M2 + 12288, O_NP = O_VP + 65536, O_K2 = O_NP + 65536, O_QT = O_K2 + 4096, O_G2 = 3u << 20, O_G1 = O_G2 + 262144;
static_assert(O_QT + 16384 == O_PART && O_VD + 4096 <= O_KEY + (512 + 832) * 1024 && O_PART + O_P2 >= O_VD + 4096 && O_STAT + O_P2 <= O_G2, "armed area");
constexpr size_t WS_H = 4u << 20;
constexpr size_t WS_T = WS_H + (size_t)S * D * 2;
constexpr size_t WS_P = WS_T + (size_t)2 * D * S * 2;

#define TSG(base, i)
#define TSD(i)
#define PCAP (1 << 20)
#define PDBG(it)
#define TS(i)
#define TSE()
#define BT(k)
#define BTE()
#define BAR(k) do { BT(k); BARRIER(); } while (0)
#define WAIT_V(n) asm volatile("s_waitcnt vmcnt(%0)" ::"n"(n) : "memory")
#define WAIT_L(n) asm volatile("s_waitcnt lgkmcnt(%0)" ::"n"(n) : "memory")
#define BARRIER() asm volatile("s_barrier" ::: "memory")
#define LBAR() asm volatile("s_waitcnt lgkmcnt(0)\n\ts_barrier" ::: "memory")
#define DSR(dst, addr, off) asm volatile("ds_read_b128 %0, %1 offset:%2" : "=v"(dst) : "v"(addr), "n"(off) : "memory")
#define MFMA(c, a, b) asm volatile("v_mfma_f32_16x16x32_bf16 %0, %1, %2, %0" : "+v"(c) : "v"(a), "v"(b))
#define MFMA0(c, a, b) asm volatile("v_mfma_f32_16x16x32_bf16 %0, %1, %2, 0" : "=&v"(c) : "v"(a), "v"(b))
#define GLDS(m0v, voff, gbase) \
  asm volatile("s_mov_b32 m0, %0\n\ts_nop 0\n\tglobal_load_lds_dwordx4 %1, %2" ::"s"(m0v), "v"(voff), "s"(gbase) : "memory")
#define GLDS4(m0v, v, gbase)                                                                                                        \
  asm volatile("s_mov_b32 m0, %0\n\ts_nop 0\n\tglobal_load_lds_dwordx4 %1, %5\n\tglobal_load_lds_dwordx4 %2, %5 offset:1024\n\t"    \
               "global_load_lds_dwordx4 %3, %5 offset:2048\n\tglobal_load_lds_dwordx4 %4, %5 offset:3072" ::"s"(m0v),               \
               "v"((v)[0]), "v"((v)[1]), "v"((v)[2]), "v"((v)[3]), "s"(gbase) : "memory")
#define LDG(dst, voff, base) asm volatile("global_load_dwordx4 %0, %1, %2" : "=v"(dst) : "v"(voff), "s"(base) : "memory")
#define HPOL ""
#define XPOL ""
#define WPOL " nt"
#define LDM(dst, voff, base) asm volatile("global_load_dwordx4 %0, %1, %2" WPOL : "=v"(dst) : "v"(voff), "s"(base) : "memory")
#define LDX(dst, voff, base, off) asm volatile("global_load_dwordx4 %0, %1, %2 offset:%3" : "=v"(dst) : "v"(voff), "s"(base), "n"(off) : "memory")
#define LDXS(dst, voff, base, off) asm volatile("global_load_dwordx4 %0, %1, %2 offset:%3 sc1" : "=v"(dst) : "v"(voff), "s"(base), "n"(off) : "memory")
#define BF8(v) __builtin_bit_cast(bf16v8, v)
#define LSPIN(v, p, cond)                                   \
  _Pragma("unroll 1") for (int it_ = 0; it_ < (1 << 26); ++it_) { \
    v = __atomic_load_n(p, __ATOMIC_RELAXED);               \
    if (cond) break;                                        \
    __builtin_amdgcn_s_sleep(1);                            \
  }
#define POLL(ok, sl, reload)                                                  \
  if (__builtin_expect(!(ok), 0)) {                                           \
    _Pragma("unroll 1") for (int it = 0; it < PCAP; ++it) {                   \
      PDBG(it);                                                               \
      __builtin_amdgcn_s_sleep(sl);                                           \
      reload;                                                                 \
      if (ok) break;                                                          \
    }                                                                         \
  }
#define POLL2(bad, sl, cap, load)                                             \
  {                                                                           \
    int it = (cap);                                                           \
    u64 live;                                                                 \
    _Pragma("unroll 1") do {                                                  \
      if (sl) __builtin_amdgcn_s_sleep(sl);                                   \
      asm volatile("s_sub_u32 %0, %0, 1\n\ts_cselect_b64 %1, 0, -1" : "+s"(it), "=s"(live)::"scc"); \
      load;                                                                   \
    } while (__builtin_amdgcn_ballot_w64(bad) & live);                        \
    PDBG(PCAP - 2 - it);                                                      \
  }
__device__ __forceinline__ unsigned tick() {
  u64 t;
  asm volatile("s_memrealtime %0\n\ts_waitcnt lgkmcnt(0)" : "=s"(t)::"memory");
  return (unsigned)t;
}
__device__ __forceinline__ void wait_tick(unsigned t) {
#pragma unroll 1
  for (int i = 0; i < 256; ++i)
    if ((int)(tick() - t) >= 0) break;
}
__device__ __forceinline__ void nap_tick(unsigned t) {
#pragma unroll 1
  for (int i = 0; i < 1024; ++i) {
    if ((int)(tick() - t) >= 0) break;
    __builtin_amdgcn_s_sleep(2);
  }
}
#define T_C1 245
#define T_A 44
#define T_N 2500
#define T_D1 78
#define VW0 "\n\ts_waitcnt vmcnt(0)"
#define W8(a) "+v"(a[0]), "+v"(a[1]), "+v"(a[2]), "+v"(a[3]), "+v"(a[4]), "+v"(a[5]), "+v"(a[6]), "+v"(a[7])
#define PFL(ptr) asm volatile("s_mov_b32 m0, %0\n\ts_nop 0\n\tglobal_load_lds_dword %1, off" ::"s"(pfl), "v"(ptr) : "memory")

#define CPFL() asm volatile("s_mov_b32 m0, %0\n\ts_mov_b64 exec, 0xfff\n\tglobal_load_lds_dword %1, off\n\ts_mov_b64 exec, -1" ::"s"(pfl), "v"(cpf) : "memory")
#define HHOOK(j)
#define XPV 1557
#define XPW 1557
#define XPM (MODE ? XPW : XPV)
#define XPA0 (XPM / 1000)
#define XPA1 (XPM / 100 % 10)
#define XPB0 (XPM / 10 % 10)
#define XPB1 (XPM % 10)
#define U_X0 6
#define XLD(dst, XP, XH, i)                                                                                   \
  do {                                                                                                        \
    if (MODE) LDXS(dst, (i) ? xo1 : xo0, XP, ((XH)&3) * 2048 - 4096);                                         \
    else LDXS(dst, ((XH)&4) ? xo1 : xo0, XP, ((XH)&3) * 2048 + (i)*1024 - 4096);                              \
  } while (0)
#define HALF(XC, YA, RD, XN, XP, XH, LD, YP)                                                 \
  do {                                                                                       \
    _Pragma("unroll") for (int j = 0; j < 8; ++j) {                                          \
      if ((j & 1) == 0) { if (RD) WAIT_L(5); else WAIT_L(j == 0 ? 5 : 6 - j); }              \
      if (MODE == 2) MFMA0(acc[0][j], Y[j], XC[0]); else if (MODE) MFMA(acc[0][j], Y[j], XC[0]); else MFMA(acc[0][j], XC[0], Y[j]); \
      if (j == 0) DSR(Y[7], YP, 7 * 2048);                                                   \
      else if (RD) DSR(Y[j - 1], YA, (j - 1) * 2048);                                        \
      if (MODE == 2) MFMA0(acc[1][j], Y[j], XC[1]); else if (MODE) MFMA(acc[1][j], Y[j], XC[1]); else MFMA(acc[1][j], XC[1], Y[j]); \
      if ((LD) && j == (((XH)&1) ? XPA0 : XPB0)) XLD(XN[0], XP, XH, 0);                                             \
      if ((LD) && j == (((XH)&1) ? XPA1 : XPB1)) XLD(XN[1], XP, XH, 1);                                             \
      HHOOK(j);                                                                              \
    }                                                                                        \
  } while (0)

__device__ __forceinline__ int key_row(unsigned k) { return (int)((~k >> 1) & 2047u); }
__device__ __forceinline__ unsigned mkkey(float v) {
  const unsigned rb = pack2(0.f, v);
  return rb ^ ((unsigned)((int)rb >> 31) & 0x7fff0000u) ^ 0x80000000u;
}
__device__ __forceinline__ float key_val(unsigned k) {
  const unsigned ord = k >> 16;
  return bf2f(ord ^ ((ord & 0x8000u) ? 0x8000u : 0xffffu));
}
__device__ __forceinline__ unsigned nsent(float v) { return min(__float_as_uint(v), SENT - 1u); }
__device__ __forceinline__ f32x4 lo4(u32x4 q) { return f32x4{bflo(q[0]), bfhi(q[0]), bflo(q[1]), bfhi(q[1])}; }
__device__ __forceinline__ f32x4 hi4(u32x4 q) { return f32x4{bflo(q[2]), bfhi(q[2]), bflo(q[3]), bfhi(q[3])}; }
__device__ __forceinline__ u32x4 pk8(f32x4 a, f32x4 b) { return u32x4{pack2(a[0], a[1]), pack2(a[2], a[3]), pack2(b[0], b[1]), pack2(b[2], b[3])}; }
__device__ __forceinline__ u32x4 nsent4(u32x4 v) { return u32x4{min(v[0], SENT - 1u), min(v[1], SENT - 1u), min(v[2], SENT - 1u), min(v[3], SENT - 1u)}; }
__device__ __forceinline__ unsigned max4(u32x4 v) { return max(max(v[0], v[1]), max(v[2], v[3])); }
__device__ __forceinline__ float amin16(const u32x4* v) {
  float a, b;
  asm("v_min3_f32 %0, |%2|, |%3|, |%4|\n\tv_min3_f32 %1, |%5|, |%6|, |%7|\n\tv_min3_f32 %0, |%8|, |%9|, %0\n\tv_min3_f32 %1, |%10|, |%11|, %1\n\t"
      "v_min3_f32 %0, |%12|, |%13|, %0\n\tv_min3_f32 %1, |%14|, |%15|, %1\n\tv_min3_f32 %0, |%16|, |%17|, %0\n\tv_min_f32 %0, %0, %1"
      : "=&v"(a), "=&v"(b)
      : "v"(v[0][0]), "v"(v[0][1]), "v"(v[0][2]), "v"(v[0][3]), "v"(v[1][0]), "v"(v[1][1]), "v"(v[1][2]), "v"(v[1][3]), "v"(v[2][0]), "v"(v[2][1]),
        "v"(v[2][2]), "v"(v[2][3]), "v"(v[3][0]), "v"(v[3][1]), "v"(v[3][2]), "v"(v[3][3]));
  return a;
}
__device__ __forceinline__ u32x4 selu(unsigned mk, u32x4 a, u32x4 b) { return (a & mk) | (b & ~mk); }
#define KSTR 16
#define TOP2(CTRL)                                                  \
  do {                                                              \
    const unsigned o1 = dpp_u<CTRL>(t1), o2 = dpp_u<CTRL>(t2);      \
    t2 = max(min(t1, o1), max(t2, o2));                             \
    t1 = max(t1, o1);                                               \
  } while (0)
#define TOP2S(SW)                                                   \
  do {                                                              \
    auto a_ = SW(t1, t1, false, false), b_ = SW(t2, t2, false, false); \
    t2 = max(min(a_[0], a_[1]), max(b_[0], b_[1]));                 \
    t1 = max(a_[0], a_[1]);                                         \
  } while (0)
#define T_C3 253
#define T_C4 340
#define E_D 6
#define O_DW 6
#define O_DS 4
#define OPOL " sc1"
#define X_PA 2
#define G1_C 2
#define R_AT 24
#define B_TK 7
#define W_T 4
#define V_K 3
#define V_T 5
#define N_K 9
#define N_D 2
#define T_K 0
#define H_AT 23
#define G1_S 1
#define F_T 142
#define K_S 1
#define N_C (V_T + 1)
#define T_O 250
#define A_S1 9
#define A_S2 9
#define A_P2 12
#define X_K 13
#define H_P 2
#define H_O 2
#define S_R 2
#define R_S 0
#define N_S 0
#define T_X 80
#define A_PS 3
#define U_S 4
#define C_LD 2
#define C_DP 8
#define F_D 2
#define F_AT 29
#define PW0 2
#define T_W 70
#define P_D2 1
#define P_D4 1
#define XSTR 16
#define P_D3 20
#ifndef M_NS
#define M_NS 6
#endif
#define A_Q1 27
#define B_RAT 5
#define B_MAT 13
#define U_D 4
#define FSTR 16
#define B_J 4
#define RETIRE()                                                                                                              \
  do {                                                                                                                        \
    if (hw == 3 && lane < (b == 255 ? 2 : 1))                                                                                 \
      asm volatile("global_store_dwordx2 %0, %1, %2 sc1" ::"v"(lane ? (unsigned)WS_EPO : O_XFL + b * XSTR * 4), "v"(lane ? tag : ~tag), "s"(ws) : "memory"); \
  } while (0)
#define U_N1 3
#define B_QAT 12
#define B_QD 2
#define TP_PS 5
#define TP_XC 34
#define TP_P0 2
#define TP_X0 24
#define TP_XL 54
#define W_UO 3
#define CPF_K 24
#define CPF_B 8
#define B_WN 4
#define B_WK 15
struct BSched {
  int cnt[16], rcnt, mcnt, qcnt, vcnt, ncnt;
  constexpr BSched() : cnt{}, rcnt(0), mcnt(0), qcnt(0), vcnt(0), ncnt(0) {
    int pos[16] = {}, n = 0, rpos = 0, mpos = 0, qpos = 0, npos = 0;
    for (int k = 0; k <= B_J - 2 + M_NS; ++k) pos[k] = -8 * (B_J - 2 + M_NS - k);
    for (int k = B_J + 1; k < 16; ++k) {
      cnt[k] = n - pos[k] > 63 ? 63 : n - pos[k];
      if (k - 2 + M_NS < 16) { n += 4; pos[k - 2 + M_NS] = n; }
      if (k == B_RAT + 2) rcnt = n - rpos;
      if (k == B_QAT + B_QD) qcnt = n - qpos;
      if (k == V_T) vcnt = n;
      if (k == N_K + N_D) ncnt = n - npos;
      if (k == N_K) npos = n;
      if (k == B_RAT) rpos = n;
      if (k == X_K) n += 4;
      if (k == B_QAT) { n += 1; qpos = n; }
      if (k == B_MAT) { n += 2; mpos = n; }
      if (k == B_WK) n += B_WN;
    }
    mcnt = n - mpos > 63 ? 63 : n - mpos;
  }
};
__global__ __launch_bounds__(512, 2) void k_main(const u16* __restrict__ x, char* ws, const u16* __restrict__ Y0, const u16* __restrict__ Y1,
                                              const u16* __restrict__ cvec, const u16* __restrict__ wmod, const u16* __restrict__ X0,
                                              const u16* __restrict__ X1, const u16* __restrict__ wr_, const u16* __restrict__ wg,
                                              const u16* __restrict__ wu, const u16* __restrict__ wd, u16* __restrict__ out) {
  constexpr int DX = 4, NS = M_NS, STG = NS * 16384, SPARE = STG + 32768;
  constexpr int LST = SPARE + 17664, SLT = LST + 256, XC = SLT + 512, VT = XC + 4096;
  __shared__ __attribute__((aligned(1024))) char shm[VT + 256];
  u16(*svb)[1024] = reinterpret_cast<u16(*)[1024]>(shm + SPARE);
  float* cwj = reinterpret_cast<float*>(shm + SPARE + 10240);
  unsigned* ssel = reinterpret_cast<unsigned*>(shm + SPARE + 10368);
  float* smod = reinterpret_cast<float*>(shm + SPARE + 15872);
  u16* ssa = reinterpret_cast<u16*>(smod);
  constexpr int TAB = SPARE + 10752, RKA = TAB + 1536, RKB = TAB + 3584;
  u16(*scw)[128] = reinterpret_cast<u16(*)[128]>(shm + TAB + 512);
  const int b = blockIdx.x, xcd = b & 7, j8 = b >> 3;
  const int cc = xcd >> 2, nt = (xcd & 3) * 4 + (j8 & 3), mt = j8 >> 2, et = mt;
  const int tid = threadIdx.x, lane = tid & 63;
  const int wid = __builtin_amdgcn_readfirstlane(tid >> 6);
  const unsigned lds0 = (unsigned)(size_t)(__attribute__((address_space(3))) char*)shm;
  const char* cpf;
  {
    unsigned long long pc;
    unsigned len;
    asm volatile("s_getpc_b64 %0\n.Lcpf_here:\n\ts_mov_b32 %1, .Lcpf_end-.Lcpf_here-4\n\t.subsection 1\n.Lcpf_end:\n\t.subsection 0" : "=s"(pc), "=s"(len));
    cpf = reinterpret_cast<const char*>(pc) + min((unsigned)((lane * 32 + j8) * 128), len);
  }
  const unsigned pfl = lds0 + SPARE + 17408;
  if (wid == 4) CPFL();
  u32x4 xv[2];
  {
    const unsigned xo = (unsigned)((b * 8 + wid) * D + lane * 8) * 2;
    asm volatile("global_load_dwordx4 %0, %2, %3" XPOL "\n\tglobal_load_dwordx4 %1, %2, %3 offset:1024" XPOL : "=&v"(xv[0]), "=&v"(xv[1]) : "v"(xo), "s"(x) : "memory");
  }
  asm volatile("" ::"s"(ws), "s"(Y0), "s"(Y1), "s"(cvec), "s"(wmod), "s"(wr_));
  u16* xn = reinterpret_cast<u16*>(ws + WS_H);
  u16* U = reinterpret_cast<u16*>(ws + WS_T);
  unsigned* part = reinterpret_cast<unsigned*>(ws + WS_PART);
  const int g = (b & 7) * 28 + (b >> 3);
  unsigned* ufl = reinterpret_cast<unsigned*>(ws + WS_UFL) + (cc * 16 + nt) * 32 * FSTR;
  unsigned* epo = reinterpret_cast<unsigned*>(ws + WS_EPO);
#define SKEWPT()
  const int g4 = lane >> 4;
  u64 tag0, tag;
#define TAGW(n) asm volatile("s_waitcnt vmcnt(%2)\n\tv_lshl_add_u64 %0, %1, 0, 1" : "=v"(tag) : "v"(tag0), "n"(n) : "memory")
  u32x4 wv[8], cq;
  const char* pfy = reinterpret_cast<const char*>(cc ? Y1 : Y0) + (size_t)((xcd & 3) * 512 + j8 * 16 + (lane & 15)) * 4096 + g4 * 128;
#define PFG(g) (pfy + ((((g) + xcd) & 7) << 9))
  const int u = b * 2 + (wid & 1), cg0 = u & 31, ks0 = u >> 5;
  unsigned ta = lds0 + STG + ((lane >> 2) & 3) * 2368 + (wid * 128 + (lane >> 5) * 32 + (lane & 3) * 8 + (g4 & 1) * 4) * 2;
  unsigned oo = (unsigned)((wid * 4 + (lane >> 5)) * 131072 + (b >> 2) * 2048 + (g4 & 1) * 1024 + ((b & 3) * 16 + (lane & 15)) * 16), oo2 = oo + 262144;
  asm volatile("" : "+v"(ta), "+v"(oo), "+v"(oo2));
  unsigned wa = lds0 + STG + wid * 2368 + lane * 16;
  asm volatile("" : "+v"(wa));
  const unsigned wo = (unsigned)((ks0 * 64 + (lane >> 3) * 8) * 6144 + 2048 + (cg0 >> 4) * 1024 + cg0 * 64 + (lane & 7) * 8) * 2;
  if (wid == 0) TSG(part, 0);
  BT(50);
  if (wid >= 3) {
    asm volatile("global_load_dwordx2 %0, %1, off sc1" : "=v"(tag0) : "v"(epo) : "memory");
    if (wid >= 6) {
      LDG(cq, (ks0 * 64 + g4 * 16 + (lane & 1) * 8) * 2, cvec);
#pragma unroll
      for (int i = 0; i < 8; ++i) LDM(wv[i], wo + i * 12288, wmod);
      const unsigned av = u < 80 ? 0u : ~0u;
      asm volatile("global_store_dwordx4 %0, %1, %2 sc1\n\ts_nop 1" ::"v"(O_KEY + u * 1024 + lane * 16), "v"(u32x4{av, av, av, av}), "s"(ws) : "memory");
      WAIT_V(11);
    } else if (wid == 4) {
      __builtin_amdgcn_s_sleep(C_DP);
      PFL(PFG(0));
      WAIT_V(2);
    } else {
      WAIT_V(1);
    }
  } else {
    WAIT_V(0);
  }
  {
    asm volatile("" : "+v"(xv[0]), "+v"(xv[1])::"memory");
    if (wid == 1) TSG(part, 2);
    BT(51);
    const float s0 = dsum(xv[0][3], dsum(xv[0][2], dsum(xv[0][1], dsum(xv[0][0], 0.f))));
    const float s1 = dsum(xv[1][3], dsum(xv[1][2], dsum(xv[1][1], dsum(xv[1][0], 0.f))));
    const float mf = wave_sum_s(s0 + s1) * (1.0f / D), m = rbf(mf);
    const f32x4 d0 = lo4(xv[0]) - m, d1 = hi4(xv[0]) - m, d2 = lo4(xv[1]) - m, d3 = hi4(xv[1]) - m;
    const f32x4 q = (d0 * d0 + d1 * d1) + (d2 * d2 + d3 * d3);
    const float vf = wave_sum_s((q[0] + q[1]) + (q[2] + q[3])) * (1.0f / D) - (mf - m) * (mf - m);
    const float rden = __builtin_amdgcn_rcpf(rbf(__builtin_amdgcn_sqrtf(rbf(rbf(vf) + 1e-5f))));
    asm volatile("ds_write_b128 %0, %1" ::"v"(wa), "v"(pk8(d0 * rden, d1 * rden)) : "memory");
    asm volatile("ds_write_b128 %0, %1 offset:1024" ::"v"(wa), "v"(pk8(d2 * rden, d3 * rden)) : "memory");
    if (wid == 1) TSG(part, 12);
    BT(52);
    LBAR();
    if (wid == 1) TSG(part, 10);
    BT(53);
    u32x2 t[4];
    asm volatile(
        "ds_read_b64_tr_b16 %0, %4\n\tds_read_b64_tr_b16 %1, %4 offset:%5\n\t"
        "ds_read_b64_tr_b16 %2, %4 offset:128\n\tds_read_b64_tr_b16 %3, %4 offset:%6\n\ts_waitcnt lgkmcnt(0)"
        : "=&v"(t[0]), "=&v"(t[1]), "=&v"(t[2]), "=&v"(t[3])
        : "v"(ta), "n"(4 * 2368), "n"(4 * 2368 + 128)
        : "memory");
    asm volatile("global_store_dwordx4 %0, %2, %4 sc1\n\tglobal_store_dwordx4 %1, %3, %4 sc1\n\ts_nop 1" ::"v"(oo), "v"(oo2),
                 "v"(u32x4{t[0][0], t[0][1], t[1][0], t[1][1]}), "v"(u32x4{t[2][0], t[2][1], t[3][0], t[3][1]}), "s"(xn)
                 : "memory");
    if (wid == 2) TSG(part, 14);
    BT(54);
  }
  u32x2 vd;
#define VDLD(sl) POLL2(max(vd[0], vd[1]) == SENT, sl, PCAP, asm volatile("global_load_dwordx2 %0, %1, %2 sc1" VW0 : "=&v"(vd) : "v"(O_VD + (b & 31) * 128), "s"(ws) : "memory"))
  if (wid < 4) {
    const unsigned ya0 = lds0 + (lane & 15) * 128 + (g4 ^ (lane & 7)) * 16;
    bf16x8 X[DX][2][2], Y[8];
    f32x4 acc[2][8];
    {
      constexpr int MODE = 0;
      const unsigned xo0 = lane * 16 + 4096, xo1 = xo0 + 8192;
      const char* xb = reinterpret_cast<const char*>(xn) + (size_t)(mt * 4 + wid) * 131072;
#define XB(i) (xb + (((xcd + (i)) & 7) << 14))
#pragma unroll
      for (int i = 0; i < 2; ++i)
#pragma unroll
        for (int j = 0; j < 8; ++j) acc[i][j] = f32x4{0.f, 0.f, 0.f, 0.f};
      asm volatile("" : W8(acc[0]), W8(acc[1]));
      __builtin_amdgcn_s_setprio(1);
      WAIT_V(0);
      if (wid == 0) TSG(part, 1);
      const char* xpb = XB(0);
      BAR(55);
      if (wid == 3) {
        TAGW(0);
        const unsigned fp = O_XFL + ((xcd * 32 + lane) & 255) * XSTR * 4;
        if (P_D3) __builtin_amdgcn_s_sleep(P_D3);
#pragma unroll 1
        for (int it = 0; it < PCAP; ++it) { PDBG(it);
          u64 f;
          if (it < 2) BT(58 + 2 * it);
          asm volatile("global_load_dwordx2 %0, %1, %2 sc1\n\ts_waitcnt vmcnt(0)" : "=&v"(f) : "v"(fp), "s"(ws) : "memory");
          if (it < 2) BT(59 + 2 * it);
          if (__all(f == tag)) break;
          __builtin_amdgcn_s_sleep(P_D4);
        }
      }
      BAR(0);
      if (wid == 1) TSG(part, 3);
#pragma unroll
      for (int h = 0; h < 7; ++h) {
        XLD(X[h >> 1][h & 1][0], xpb, h, 0);
        XLD(X[h >> 1][h & 1][1], xpb, h, 1);
      }
#pragma unroll
      for (int j = 0; j < 7; ++j) DSR(Y[j], ya0, j * 2048);
      unsigned so = 0, a0 = ya0;
      f32x4 c0, c1;
#pragma unroll 1
      for (int it = 0; it < 7; ++it) {
        const char* xpa = xpb;
        xpb = XB(it + 1);
#pragma unroll
        for (int s = 0; s < 4; ++s) {
          const unsigned a1 = (ya0 ^ 64) + so;
          so = so == (NS - 1) * 16384 ? 0 : so + 16384;
          WAIT_V(12);
          HALF(X[s][0], a1, true, X[(s + 3) & 3][1], (s == 0 ? xpa : xpb), (2 * s + 7) & 7, true, a0);
          a0 = ya0 + so;
          BAR(1 + it * 4 + s);
          WAIT_V(12);
          HALF(X[s][1], a0, true, X[s][0], xpb, 2 * s, true, a1);
        }
      }
#pragma unroll
      for (int s = 0; s < 4; ++s) {
        const unsigned a1 = (ya0 ^ 64) + so;
        so = so == (NS - 1) * 16384 ? 0 : so + 16384;
        WAIT_V(s == 0 ? 12 : 14 - 4 * s);
        HALF(X[s][0], a1, true, X[3][1], xpb, 7, s == 0, a0);
        a0 = ya0 + so;
        if (s < 3) BAR(29 + s);
        WAIT_V(12 - 4 * s);
        if (s == 2) {
          DSR(c0, lds0 + SPARE + 15872 + 512 + wid * 128 + g4 * 32, 0);
          DSR(c1, lds0 + SPARE + 15872 + 512 + wid * 128 + g4 * 32, 16);
        }
        if (s < 3) HALF(X[s][1], a0, true, X[s][0], xpb, 0, false, a1);
      }
      const char* up = reinterpret_cast<const char*>(U) + (size_t)cc * 4194304 + (size_t)(nt * 4) * 65536 + (mt * 4 + wid) * 2048;
      const unsigned ul = lds0 + STG + wid * 1024 + lane * 16;
#define UEP(j)                                                                                                                             \
  do {                                                                                                                                     \
    u32x4 v_;                                                                                                                              \
    asm volatile("v_pk_mul_f32 %0, %0, %4\n\tv_pk_mul_f32 %1, %1, %5\n\tv_pk_mul_f32 %2, %2, %6\n\tv_pk_mul_f32 %3, %3, %7"            \
                 : "+v"(acc[0][j].lo), "+v"(acc[0][j].hi), "+v"(acc[1][j].lo), "+v"(acc[1][j].hi)                                          \
                 : "v"(c0.lo), "v"(c0.hi), "v"(c1.lo), "v"(c1.hi));                                                                        \
    asm volatile("v_cvt_pk_bf16_f32 %0, %4, %5\n\tv_cvt_pk_bf16_f32 %1, %6, %7\n\tv_cvt_pk_bf16_f32 %2, %8, %9\n\tv_cvt_pk_bf16_f32 %3, %10, %11" \
                 : "=&v"(v_[0]), "=&v"(v_[1]), "=&v"(v_[2]), "=&v"(v_[3])                                                                  \
                 : "v"(acc[0][j][0]), "v"(acc[0][j][1]), "v"(acc[0][j][2]), "v"(acc[0][j][3]), "v"(acc[1][j][0]), "v"(acc[1][j][1]),       \
                   "v"(acc[1][j][2]), "v"(acc[1][j][3]));                                                                                  \
    asm volatile("global_store_dwordx4 %0, %2, %3 offset:%4 sc1\n\tds_write_b128 %1, %2 offset:%5\n\ts_nop 0"                          \
                 ::"v"(lane * 16), "v"(ul), "v"(v_), "s"(up + ((j) >> 1) * 65536), "n"(((j)&1) * 1024), "n"((j)*4096) : "memory");         \
  } while (0)
      {
        const unsigned a1 = (ya0 ^ 64) + (31 % NS) * 16384;
        DSR(Y[7], a1, 7 * 2048);
        WAIT_L(6);
#pragma unroll
        for (int j = 0; j < 8; ++j) {
          if (j == 2) WAIT_L(0);
          MFMA(acc[0][j], X[3][1][0], Y[j]);
          MFMA(acc[1][j], X[3][1][1], Y[j]);
          if (j >= 2) UEP(j - 2);
        }
      }
      if (wid == 0) TSG(part, 4);
      TS(0);
      SKEWPT();
      asm volatile("s_nop 3");
#pragma unroll
      for (int j = 6; j < 8; ++j) UEP(j);
    }
    const int rg = nt * 8 + wid * 2 + cc, row0 = rg * 16 + (lane & 15), col0 = et * 128 + g4 * 8;
    u32x4 xq[4], pv[4], sa4[4];
    f32x4 ga[8];
    f32x2 cj;
    float sw, ce;
    u32x4 dk;
    unsigned spo, soo, th, slt, slf, sla, tha, xca, sca;
    u32x4 e0, e1;
    const char* xr = reinterpret_cast<const char*>(xn) + mt * 524288 + (cc * 16 + nt) * 16384 + wid * 4096;
#define XCHLD()                                                                                                     \
  asm volatile(                                                                                                     \
      "global_load_dwordx4 %0, %4, %5 sc1\n\tglobal_load_dwordx4 %1, %4, %5 offset:1024 sc1\n\t"                   \
      "global_load_dwordx4 %2, %4, %5 offset:2048 sc1\n\tglobal_load_dwordx4 %3, %4, %5 offset:3072 sc1"            \
      : "=&v"(pv[0]), "=&v"(pv[1]), "=&v"(pv[2]), "=&v"(pv[3])                                                      \
      : "v"(lane * 16), "s"(xr)                                                                                     \
      : "memory")
    {
      constexpr int MODE = 1;
      const char* ub = reinterpret_cast<const char*>(U) + (size_t)cc * 4194304 + (size_t)(nt * 4 + wid) * 65536;
      const unsigned xo0 = lane * 16 + cc * 1024 + 4096, xo1 = xo0 ^ 1024;
      BT(32);
      LBAR();
      if (wid == 1) TSG(part, 6);
      {
        const unsigned sa = lds0 + STG + wid * 8192 + lane * 16 + cc * 4096;
#pragma unroll
        for (int q = 0; q < 8; ++q) DSR(X[(q >> 1) & 1][q & 1][q >> 2], (q >> 2) ? sa ^ 4096 : sa, (q & 3) * 1024);
        const unsigned a0 = ya0 + (32 % NS) * 16384;
#pragma unroll
        for (int j = 0; j < 7; ++j) DSR(Y[j], a0, j * 2048);
      }
#define UFLAG()                                                                                                         \
  do {                                                                                                                  \
    WAIT_V(0);                                                                                                          \
    if (lane == 0) __hip_atomic_store(ufl + (mt * 4 + wid) * FSTR, 1u, __ATOMIC_RELAXED, __HIP_MEMORY_SCOPE_AGENT);     \
    if (wid == 0) TSG(part, 5);                                                                                         \
  } while (0)
#define XP(h) (ub + (((mt + ((h) >> 2)) & 7) << 13))
#define XS(h) X[((h) >> 1) & 3][(h)&1]
#define XW(h) do { if ((h) >= 4) WAIT_V(2 * (((h) + 6 > 31 ? 31 : (h) + 6) - (h)) ); } while (0)
      constexpr int H0 = 24, NP = 64, NE = 2 * NP + 8;
      const char* xw;
      unsigned c128, xoff[4], rka, rkb, ea, eb, cwa, cwu;
      auto ESET = [&]() __attribute__((always_inline)) {
        int ln_ = lane;
        asm volatile("" : "+v"(ln_));
        const int lane = ln_, g4 = lane >> 4, row0 = rg * 16 + (lane & 15), col0 = et * 128 + g4 * 8;
        xw = reinterpret_cast<const char*>(xn) + mt * 524288 + ((cc ^ 1) * 16 + nt) * 16384 + wid * 4096;
        asm volatile("" : "+s"(xw), "+s"(xr));
        c128 = 0x43000000u;
        rka = lds0 + RKA + (lane & 15) * 16, rkb = lds0 + RKB + (g4 ? 1024 : wid * 256 + lane * 16);
        ea = lds0 + TAB + g4 * 32, eb = lds0 + TAB + 512 + (lane & 3) * 256 + g4 * 16;
#pragma unroll
        for (int q = 0; q < 4; ++q) {
          dk[q] = (lane & 3) == q ? ~0u : 0u;
          xoff[q] = (lds0 + STG + wid * 8192 + (lane & 15) * 256 + ((g4 ^ lane) & 15) * 16) ^ (q * 64);
        }
        spo = O_STAT + rg * 2048 + g4 * 512 + (lane & 15) * 16, soo = O_STAT + rg * 2048 + et * 256 + (lane & 15) * 16;
        cwa = lds0 + SPARE + 10240 + g4 * 8, cwu = lds0 + SPARE + 10240 + et * 4;
        const unsigned one = g4 == ((lane >> 2) & 3) ? ((lane & 1) ? 0x1f800000u : 0x3f80u) : 0u;
        e0 = u32x4{(lane & 2) ? 0u : one, (lane & 2) ? one : 0u, 0u, 0u}, e1 = u32x4{0u, 0u, e0[0], e0[1]};
        tha = lds0 + SPARE + 10368 + 24;
        sla = lds0 + SLT + (row0 & 127) * 4;
        xca = O_X1C - 2048 + col0 * 2, sca = O_ST2 - 128 + et * 16;
        asm volatile("" : "+v"(dk), "+v"(spo), "+v"(soo), "+v"(cwa), "+v"(cwu), "+v"(tha), "+v"(sla), "+v"(xca), "+v"(sca), "+v"(e0), "+v"(e1), "+v"(c128));
        asm volatile("" : "+v"(xoff[0]), "+v"(xoff[1]), "+v"(xoff[2]), "+v"(xoff[3]), "+v"(rka), "+v"(rkb), "+v"(ea), "+v"(eb));
      };
#pragma unroll
      for (int s = 0; s < H0 / 2; ++s) {
        const unsigned a0 = ya0 + ((32 + s) % NS) * 16384, a1 = a0 ^ 64, an = ya0 + ((33 + s) % NS) * 16384;
        XW(2 * s);
        if (s == 0) {
          constexpr int MODE = 2;
          HALF(XS(0), a1, true, XS(7), XP(7), 7, false, a0);
          UFLAG();
        } else {
          HALF(XS(2 * s), a1, true, XS(2 * s + 7), XP(2 * s + 7), 2 * s + 7, 2 * s >= 4, a0);
        }
        if (s == 1) ESET();
        BAR(33 + s);
        if (s == 1) {
          if (wid == 1) TSG(part, 7);
          TS(4);
#pragma unroll
          for (int h = 4; h < U_X0; ++h) {
            XLD(XS(h)[0], XP(h), h, 0);
            XLD(XS(h)[1], XP(h), h, 1);
          }
        }
        XW(2 * s + 1);
#undef HHOOK
#define HHOOK(j) if (s == 1 && j + 3 >= U_X0) { XLD(XS(j + 3)[0], XP(j + 3), j + 3, 0); XLD(XS(j + 3)[1], XP(j + 3), j + 3, 1); }
        HALF(XS(2 * s + 1), an, true, XS(2 * s + 8), XP(2 * s + 8), 2 * s + 8, 2 * s + 1 >= 5, a1);
#undef HHOOK
#define HHOOK(j)
      }
      u32x4 ov;
      bf16x8 xk;
#define EH(e) (H0 + (((e) < NP ? (e) : (e) - NP - 8) >> 3))
#define EA(e) ((ya0 ^ ((EH(e) & 1) * 64)) + ((32 + (EH(e) >> 1)) % NS) * 16384)
      sfor<NE>([&](auto nn) __attribute__((always_inline)) {
        constexpr int n = decltype(nn)::value, h = EH(n), j = n & 7, m = n - NP - TP_P0, e = n + 7, z = n - (NE - 8);
        (void)sa4, (void)eb, (void)ga, (void)ea, (void)xk, (void)rka, (void)rkb, (void)xq, (void)xoff, (void)ov, (void)xw, (void)x;
        (void)cj, (void)sw, (void)ce, (void)cwa, (void)cwu, (void)th, (void)tha, (void)slt, (void)sla, (void)slf, (void)c128;
        if (n == 0) TS(2);
        if (n == NP) { TS(3); SKEWPT(); }
        if (n < NP && j == 0) {
          if ((h & 1) && h < 31) BAR(33 + (h >> 1));
          XW(h);
        }
        if (n == NP + TP_X0) { TS(14); BAR(48); TS(11); }
        if (n == NP + TP_XL) XCHLD();
        if constexpr ((j & 1) == 0) WAIT_L(z <= 0 ? 5 : 6 - z + 2 * z);
        if constexpr (n < NP) MFMA(acc[1][j], Y[j], XS(h)[1]);
        else if constexpr (n < NP + 8) MFMA(acc[0][j], Y[j], xk);
        else MFMA(acc[0][j], Y[j], XS(h)[0]);
        if constexpr (n == NP - 16) DSR(xk, rkb, 0);
        if constexpr (e >= NP && e < NP + 8) DSR(Y[e & 7], rka, (e & 7) * 256);
        else if constexpr (e < NE) DSR(Y[e & 7], EA(e), (e & 7) * 2048);
        if constexpr (n == 2 || n == 4) XLD(XS(31)[(j >> 1) - 1], XP(31), 31, (j >> 1) - 1);
        if constexpr (m >= TP_XC - 11 && m < TP_XC - 7) DSR(xq[m - TP_XC + 11], xoff[m - TP_XC + 11], 4096);
        if constexpr (m >= 0 && m < 4 * TP_PS) {
          constexpr int q = m / TP_PS, r = m % TP_PS;
#pragma unroll
          for (int u = (r * 13 + TP_PS - 1) / TP_PS; u < ((r + 1) * 13 + TP_PS - 1) / TP_PS; ++u) {
#define AO(i) acc[1][2 * q + ((u >> 1) & 1)][(u & 1) * 2 + (i)]
            if (u < 4) asm volatile("v_and_or_b32 %0, %1, %2, %3" : "=v"(ov[u & 3]) : "v"(AO(1)), "s"(0x80000000u), "v"(c128));
            else if (u < 8) asm volatile("v_fmamk_f32 %0, %1, 0x5f800000, %0" : "+v"(ov[u & 3]) : "v"(AO(1)));
            else if (u < 12) asm volatile("v_cvt_pk_bf16_f32 %0, %1, %0" : "+v"(ov[u & 3]) : "v"(AO(0)));
            else asm volatile("global_store_dwordx4 %0, %1, %2 offset:%3 sc1\n\ts_nop 1" ::"v"(lane * 16), "v"(ov), "s"(xw), "n"(q * 1024) : "memory");
          }
          if (m == 4 * TP_PS - 1) TS(10);
        }
        if constexpr (m == TP_XC) asm volatile("" : "+v"(xq[0]), "+v"(xq[1]), "+v"(xq[2]), "+v"(xq[3]));
        if constexpr (m >= TP_XC && m < TP_XC + 32) {
          constexpr int c = m - TP_XC;
          if constexpr (c & 1) asm volatile("v_and_b32 %0, 0xffff0000, %1" : "=v"(acc[1][c >> 2][c & 3]) : "v"(xq[c >> 3][(c >> 1) & 3]));
          else asm volatile("v_lshlrev_b32 %0, 16, %1" : "=v"(acc[1][c >> 2][c & 3]) : "v"(xq[c >> 3][(c >> 1) & 3]));
        }
        if constexpr (z >= 0 && z < 4) {
          DSR(ga[2 * z], ea, z * 128);
          DSR(ga[2 * z + 1], ea, z * 128 + 16);
        } else if constexpr (z >= 4 && z < 6) {
          DSR(sa4[2 * z - 8], eb, (2 * z - 8) * 64);
          DSR(sa4[2 * z - 7], eb, (2 * z - 7) * 64);
        } else if constexpr (z == 6) {
          asm volatile("ds_read_b64 %0, %2\n\tds_read_b32 %1, %3" : "=&v"(cj), "=&v"(ce) : "v"(cwa), "v"(cwu) : "memory");
        } else if constexpr (z == 7) {
          asm volatile("ds_read_b32 %0, %4 offset:32\n\tds_read_b32 %1, %5 offset:4\n\tds_read_b32 %3, %5 offset:56\n\tds_read_b32 %2, %6"
                       : "=&v"(sw), "=&v"(th), "=&v"(slt), "=&v"(slf) : "v"(cwa), "v"(tha), "v"(sla) : "memory");
        }
      });
    }
    if (wid == 0) TSG(part, 8);
    TS(13);
    BT(49);
    BTE();
    SKEWPT();
    {
      TS(26);
#define XCHW() asm volatile("s_waitcnt vmcnt(0)" : "+v"(pv[0]), "+v"(pv[1]), "+v"(pv[2]), "+v"(pv[3])::"memory")
      XCHW();
#define XBAD (amin16(pv) < 128.0f)
      if (__builtin_expect(__any(XBAD), 1)) POLL2(XBAD, 1, PCAP, XCHLD(); XCHW());
      TS(27);
      asm volatile("s_waitcnt lgkmcnt(0)" : W8(acc[0]), W8(ga), "+v"(sa4[0]), "+v"(sa4[1]), "+v"(sa4[2]), "+v"(sa4[3]), "+v"(cj), "+v"(sw), "+v"(ce), "+v"(th), "+v"(slt), "+v"(slf)::"memory");
#pragma unroll
      for (int q = 0; q < 8; ++q) acc[0][q] = __builtin_amdgcn_mfma_f32_16x16x32_bf16(BF8((q & 1) ? e1 : e0), BF8(pv[q >> 1]), acc[0][q], 0, 0, 0);
      f32x4 s4 = {0.f, 0.f, 0.f, 0.f}, gr = {0.f, 0.f, 0.f, 0.f};
      const unsigned xa1 = xca + slt * 2048, sa1 = sca + slt * 128;
#pragma unroll
      for (int q = 0; q < 4; ++q) {
        xq[q] = pk8(acc[1][2 * q] + ga[2 * q] * acc[0][2 * q], acc[1][2 * q + 1] + ga[2 * q + 1] * acc[0][2 * q + 1]);
        gr = __builtin_amdgcn_mfma_f32_16x16x32_bf16(BF8(xq[q]), BF8(xq[q]), gr, 0, 0, 0);
        s4 = __builtin_amdgcn_mfma_f32_16x16x32_bf16(BF8(sa4[q]), BF8(xq[q]), s4, 0, 0, 0);
        xq[q] = nsent4(xq[q]);
      }
#define XCST(a)                                                                                                              \
  asm volatile(                                                                                                              \
      "global_store_dwordx4 %0, %1, %5 sc1\n\tglobal_store_dwordx4 %0, %2, %5 offset:64 sc1\n\t"                            \
      "global_store_dwordx4 %0, %3, %5 offset:128 sc1\n\tglobal_store_dwordx4 %0, %4, %5 offset:192 sc1\n\ts_nop 1" ::"v"(a), \
      "v"(xq[0]), "v"(xq[1]), "v"(xq[2]), "v"(xq[3]), "s"(ws)                                                                \
      : "memory")
#define SCST(a)                                                                                                              \
  do {                                                                                                                       \
    _Pragma("unroll") for (int r = 0; r < S_R; ++r) asm volatile("global_store_dwordx4 %0, %1, %2 offset:%3 sc1" ::"v"(a), "v"(sg), "s"(ws), "n"(r * 1280) : "memory"); \
    asm volatile("s_nop 1");                                                                                                 \
  } while (0)
      if (slt) XCST(xa1);
      const bool dl = ((lane >> 2) & 3) == g4;
      const u32x4 gd = __builtin_bit_cast(u32x4, gr) & dk;
      const float m = s4[2] * (1.0f / 128);
      float q2 = __uint_as_float((gd[0] | gd[1]) | (gd[2] | gd[3])) - s4[2] * m, lg = (s4[0] + s4[1]) - m * ce;
      if (__builtin_expect(__any(dl && !(s4[2] * m <= 256.0f * q2)), 0)) {
        asm volatile("" : "+v"(xq[0]), "+v"(xq[1]), "+v"(xq[2]), "+v"(xq[3]));
        f32x4 a = {0.f, 0.f, 0.f, 0.f}, l = {0.f, 0.f, 0.f, 0.f};
#pragma unroll
        for (int q = 0; q < 4; ++q) {
          const u32x4 ch = *reinterpret_cast<const u32x4*>(&scw[0][g4 * 8 + q * 32]), cl = *reinterpret_cast<const u32x4*>(&scw[1][g4 * 8 + q * 32]);
          const f32x4 d0 = lo4(xq[q]) - m, d1 = hi4(xq[q]) - m;
          a += d0 * d0 + d1 * d1;
          l += d0 * (lo4(ch) + lo4(cl)) + d1 * (hi4(ch) + hi4(cl));
        }
        q2 = ar4((a[0] + a[1]) + (a[2] + a[3]));
        lg = ar4((l[0] + l[1]) + (l[2] + l[3]));
      }
      const u32x4 sg = {nsent(m), nsent(q2), nsent(lg), 0u};
      if (dl) {
        if (slt) SCST(sa1);
        asm volatile("global_store_dwordx4 %0, %1, %2 sc1\n\ts_nop 1" ::"v"(soo), "v"(sg), "s"(ws) : "memory");
      }
      if (__any(slf == 0u)) {
        POLL2(slf == 0u, 1, 1 << 26, asm volatile("ds_read_b32 %1, %2 offset:56\n\tds_read_b32 %0, %3\n\ts_waitcnt lgkmcnt(0)" : "=&v"(slt), "=&v"(slf) : "v"(tha), "v"(sla) : "memory"));
        if (slt) {
          XCST(xca + slt * 2048);
          if (dl) SCST(sca + slt * 128);
        }
      }
      asm volatile("ds_write_b32 %0, %1 offset:72" ::"v"(tha + wid * 4), "v"(1u) : "memory");
      TS(7);
      SKEWPT();
      const unsigned tp = tick() + T_D1, tq = (unsigned)__builtin_amdgcn_readfirstlane((int)th) + T_C1;
      u32x4 sr[2];
#define STLD(W) asm volatile("global_load_dwordx4 %0, %2, %3 sc1\n\tglobal_load_dwordx4 %1, %2, %3 offset:256 sc1" W : "=&v"(sr[0]), "=&v"(sr[1]) : "v"(spo), "s"(ws) : "memory")
#define SBAD (max(max(max(sr[0][0], sr[0][1]), sr[0][2]), max(max(sr[1][0], sr[1][1]), sr[1][2])) == SENT)
      asm volatile("" : "+v"(xq[0]), "+v"(xq[1]), "+v"(xq[2]), "+v"(xq[3])::"memory");
      f32x4 xf[8], xs[8], gc[8];
      auto PREW = [&](int q0, int q1) __attribute__((always_inline)) {
#pragma unroll
        for (int q = q0; q < q1; ++q) {
          const u32x4 sbq = *reinterpret_cast<const u32x4*>(&svb[1][col0 + q * 32]), gbq = *reinterpret_cast<const u32x4*>(&svb[3][col0 + q * 32]);
          const u32x4 cq = *reinterpret_cast<const u32x4*>(&svb[2][col0 + q * 32]);
          xf[2 * q] = lo4(xq[q]), xf[2 * q + 1] = hi4(xq[q]);
          xs[2 * q] = xf[2 * q] + lo4(gbq) * lo4(sbq), xs[2 * q + 1] = xf[2 * q + 1] + hi4(gbq) * hi4(sbq);
          gc[2 * q] = lo4(gbq) * lo4(cq), gc[2 * q + 1] = hi4(gbq) * hi4(cq);
          asm volatile("" : "+v"(xf[2 * q]), "+v"(xf[2 * q + 1]), "+v"(xs[2 * q]), "+v"(xs[2 * q + 1]), "+v"(gc[2 * q]), "+v"(gc[2 * q + 1]));
        }
      };
      PREW(0, PW0);
      wait_tick((int)(tp - tq) < 0 ? tp : tq);
      STLD("");
      unsigned rk = 0x8ffeu - (unsigned)row0 * 2;
      unsigned twa = lds0 + STG + wid * 8192 + (lane & 15) * 256 + ((g4 ^ lane) & 15) * 16, tra = lds0 + STG + wid * 8192 + g4 * 256 + ((g4 ^ lane) & 15) * 16;
      unsigned oo_ = (unsigned)((rg * 16 + g4) * D + et * 128 + (lane & 15) * 8) * 2;
      asm volatile("" : "+v"(rk), "+v"(twa), "+v"(tra), "+v"(oo_));
      PREW(PW0, 4);
      asm volatile("s_waitcnt vmcnt(0)" : "+v"(sr[0]), "+v"(sr[1])::"memory");
      if (__builtin_expect(__any(SBAD), 1)) POLL2(SBAD, 1, PCAP, STLD(VW0));
      TS(9);
      const f32x2 mk = {__uint_as_float(sr[0][0]), __uint_as_float(sr[1][0])};
      const f32x2 qk = {__uint_as_float(sr[0][1]), __uint_as_float(sr[1][1])};
      const f32x2 lk = {__uint_as_float(sr[0][2]), __uint_as_float(sr[1][2])};
      const float mean = ar4(mk[0] + mk[1]) * (1.0f / 8);
      const f32x2 dm = mk - mean;
      const f32x2 a = qk + 128.0f * (dm * dm), l = lk + dm * cj;
      const float al = swap32_add(a[0] + a[1], l[0] + l[1]), am = swap16_add(al, al);
      auto r = __builtin_amdgcn_permlane32_swap(__float_as_uint(am), __float_as_uint(am), false, false);
      const float rstd = __builtin_amdgcn_rsqf(__uint_as_float(r[0]) * (1.0f / D) + 1e-5f);
      const unsigned key = mkkey(rstd * __uint_as_float(r[1]) + sw) | rk;
      unsigned t1 = slt ? 0u : key, t2 = 0u;
      TOP2(0xB1); TOP2(0x4E); TOP2(0x141); TOP2(0x140);
      if (lane == 0 && et == (rg & 7)) asm volatile("global_store_dwordx2 %0, %1, %2 sc1" ::"v"(O_KEY + rg * KSTR * 8), "v"(u32x2{t1, t2}), "s"(ws) : "memory");
      u32x4 o[4];
#pragma unroll
      for (int q = 0; q < 4; ++q) o[q] = pk8(xs[2 * q] + (gc[2 * q] * (xf[2 * q] - mean)) * rstd, xs[2 * q + 1] + (gc[2 * q + 1] * (xf[2 * q + 1] - mean)) * rstd);
      {
        asm volatile("ds_write_b128 %0, %4\n\tds_write_b128 %1, %5\n\tds_write_b128 %2, %6\n\tds_write_b128 %3, %7" ::"v"(twa), "v"(twa ^ 64), "v"(twa ^ 128), "v"(twa ^ 192),
                     "v"(o[0]), "v"(o[1]), "v"(o[2]), "v"(o[3]) : "memory");
        asm volatile("ds_read_b128 %0, %4\n\tds_read_b128 %1, %5 offset:1024\n\tds_read_b128 %2, %6 offset:2048\n\tds_read_b128 %3, %7 offset:3072\n\ts_waitcnt lgkmcnt(0)"
                     : "=&v"(o[0]), "=&v"(o[1]), "=&v"(o[2]), "=&v"(o[3]) : "v"(tra), "v"(tra ^ 64), "v"(tra ^ 128), "v"(tra ^ 192) : "memory");
      }
      auto OST = [&](bool on) __attribute__((always_inline)) {
        const unsigned mk_ = (unsigned)__builtin_amdgcn_ballot_w64(on) >> g4;
#pragma unroll
        for (int q = 0; q < 4; ++q)
          if (mk_ & (1u << (4 * q))) asm volatile("global_store_dwordx4 %0, %1, %2" OPOL "\n\ts_nop 1" ::"v"(oo_), "v"(o[q]), "s"(out + q * 4096) : "memory");
      };
      bool df = slt;
      {
        const int tn = (int)(tick() - (tq + (T_C3 - T_C1)));
        if (tn > T_X) df = true;
        else if (tn > -T_W) { unsigned sv; LSPIN(sv, ssel + 16 + wid, sv != 0u); }
      }
      OST(!df);
      TS(1);
      u32x2 ks;
      POLL2(ks[1] == 0u, 1, 1 << 26, asm volatile("ds_read_b64 %0, %1\n\ts_waitcnt lgkmcnt(0)" : "=v"(ks) : "v"(lds0 + SPARE + 10368) : "memory"));
      df = df && (ks[0] & 0xffffu) != rk && (ks[1] & 0xffffu) != rk;
      SKEWPT();
      if (__any(df)) {
        if (b < NPART) {
          unsigned sv;
          LSPIN(sv, ssel + O_DW, sv != 0u);
          if (O_DS) __builtin_amdgcn_s_sleep(O_DS);
        }
        OST(df);
      }
      if (__builtin_expect(__any(t1 > ks[1]), 0)) {
        VDLD(8);
        slt = slt ? 0u : key == vd[0] ? 9u : key == vd[1] ? 10u : 0u;
        if (slt) {
          XCST(xca + slt * 2048);
          if (dl) SCST(sca + slt * 128);
        }
      }
    }
    TS(16);
    TSE();
  } else {
    const int hw = wid - 4;
    unsigned ts0 = tick();
    asm volatile("" : "+v"(ts0));
    const char* Hc = reinterpret_cast<const char*>((cc ? X1 : X0) + (size_t)et * 128 * D);
    auto UPOLL = [&](int n) {
      const unsigned fo = (unsigned)WS_UFL + ((cc * 16 + nt) * 32 + ((mt + 1 + (lane >> 2)) & 7) * 4 + (lane & 3)) * FSTR * 4;
      unsigned f;
      POLL2(f == 0u && lane < 4 * n, U_S, PCAP, asm volatile("global_load_dword %0, %1, %2 sc1" VW0 : "=&v"(f) : "v"(fo), "s"(ws) : "memory"));
      return (__builtin_amdgcn_ballot_w64(f == 0u) & 0xFFFFFFFull) == 0;
    };
    bool urdy = true;
    u32x4 gT[8], uT[8], wdv[8];
    unsigned og = (unsigned)(((g & 3) * 256 + hw * 64 + (lane >> 3)) * F + (g >> 2) * 64 + ((lane & 7) ^ ((lane >> 3) & 2) ^ 4) * 8) * 2;
    unsigned wta = lds0 + STG + (g4 * 8 + ((lane >> 2) & 3)) * 128 + (((hw * 2 + ((lane >> 1) & 1)) ^ ((lane >> 2) & 2) ^ ((g4 & 1) * 4)) << 4) + (lane & 1) * 8;
    unsigned od = (unsigned)(((g >> 2) * 64 + (lane >> 3) * 8) * D + (g & 3) * 256 + hw * 64 + (lane & 7) * 8) * 2;
    unsigned ou = og + (unsigned)(((mt + W_UO) % 7 - mt) * 128);
    asm volatile("" : "+v"(og), "+v"(ou), "+v"(od), "+v"(wta));
#define LDWI(dst, voff, base, inc) asm volatile("global_load_dwordx4 %0, %1, %2" WPOL "\n\tv_add_u32 %1, %3, %1" : "=v"(dst), "+v"(voff) : "s"(base), "n"(inc) : "memory")
#define WST(o, w)                                                                                                          \
  if (b < NPART) {                                                                                                         \
    asm volatile("s_mov_b32 m0, %0" ::"s"(lds0 + STG + hw * 8192));                                                        \
    _Pragma("unroll") for (int i = 0; i < 8; ++i) {                                                                        \
      asm volatile("v_xor_b32 %0, 64, %0\n\tglobal_load_lds_dwordx4 %0, %1 offset:%2" WPOL "\n\tv_add_u32 %0, %3, %0"       \
                   : "+v"(o) : "s"(w), "n"((i & 3) * 1024), "n"(F * 16 + (i == 3 ? 3072 : -1024)) : "memory");             \
      if (i == 3) asm volatile("s_add_u32 m0, m0, 4096" ::: "scc");                                                        \
    }                                                                                                                      \
  }
#define WTR2(i) "ds_read_b64_tr_b16 %" #i ", %16 offset:(" #i "/2)*4096+(" #i "&1)*512\n\t"
#define WTK(T)                                                                                                                         \
  if (b < NPART) {                                                                                                                     \
    u32x2 t_[16];                                                                                                                      \
    asm volatile(WTR2(0) WTR2(1) WTR2(2) WTR2(3) WTR2(4) WTR2(5) WTR2(6) WTR2(7) WTR2(8) WTR2(9) WTR2(10) WTR2(11) WTR2(12) WTR2(13)   \
                     WTR2(14) WTR2(15) "s_waitcnt lgkmcnt(0)"                                                                          \
                 : "=&v"(t_[0]), "=&v"(t_[1]), "=&v"(t_[2]), "=&v"(t_[3]), "=&v"(t_[4]), "=&v"(t_[5]), "=&v"(t_[6]), "=&v"(t_[7]),     \
                   "=&v"(t_[8]), "=&v"(t_[9]), "=&v"(t_[10]), "=&v"(t_[11]), "=&v"(t_[12]), "=&v"(t_[13]), "=&v"(t_[14]), "=&v"(t_[15]) \
                 : "v"(wta) : "memory");                                                                                               \
    _Pragma("unroll") for (int f = 0; f < 8; ++f) T[f] = u32x4{t_[2 * f][0], t_[2 * f][1], t_[2 * f + 1][0], t_[2 * f + 1][1]};        \
  }
#define DLD(q0, q1) if (b < NPART) { _Pragma("unroll") for (int q = q0; q < q1; ++q) LDWI(wdv[q], od, wd, D * 2); }
    const int pf0 = A_PS + (((j8 & 7) * A_S1) >> 3), pf1 = A_P2 + (((j8 & 7) * A_S2) >> 3), q1 = A_Q1;
    static_assert(A_PS >= 1 && A_P2 + A_S2 * 7 / 8 + 3 <= R_AT && R_AT < A_Q1 && A_P2 + A_S2 * 7 / 8 + 1 + W_T <= A_Q1 && H_AT + 3 <= A_Q1 && G1_C + G1_S < W_T && A_PS + W_T < A_P2 && A_PS + A_S1 * 7 / 8 + W_T + 1 < A_P2 + A_S2 * 7 / 8 && W_T >= M_NS - 2 && A_Q1 < F_AT && F_AT <= 29 && V_K <= B_J && V_T > B_J && B_WK == 15 && X_K >= N_C && (X_K + M_NS - 1 < 16 || (X_K <= B_MAT && TP_XC - 11 >= TP_X0 - TP_P0)) && TP_XC >= 4 * TP_PS && TP_XC + TP_P0 + 32 <= 72 && N_K > N_C && N_C > V_T && B_RAT > B_J && B_QAT > B_J && B_QAT + B_QD < 15, "barrier counts");
#define HOFF(r) (unsigned)((((r) >> 5) * 32 + (((r) >> 2) & 3) * 8 + (((r) >> 4) & 1) * 4 + ((r)&3)) * 2048 + ((lane & 7) ^ (lane >> 3)) * 16)
#define TR8(T, W)                                                                                                    \
  _Pragma("unroll") for (int c = 0; c < 8; ++c) {                                                                    \
    _Pragma("unroll") for (int p = 0; p < 4; ++p)                                                                    \
        T[c][p] = __builtin_amdgcn_perm(W[2 * p + 1][c >> 1], W[2 * p][c >> 1], (c & 1) ? 0x07060302u : 0x05040100u); \
    asm volatile("" : "+v"(T[c]));                                                                                   \
  }
    unsigned qm[4];
#pragma unroll
    for (int i = 0; i < 4; ++i) qm[i] = ((lane >> 2) & 3) == i ? ~0u : 0u;
    asm volatile("" : "+v"(qm[0]), "+v"(qm[1]), "+v"(qm[2]), "+v"(qm[3]));
    auto MVR = [&](f32x4 d) __attribute__((always_inline)) { return ((lane & 8) ? d[3] : d[0]) + dpp_f<0x128>((lane & 8) ? d[1] : d[2]); };
    auto MV64 = [&](const u32x4* T, u32x4 a) __attribute__((always_inline)) {
      f32x4 d[2] = {};
#pragma unroll
      for (int c = 0; c < 8; ++c) {
        const unsigned k_ = qm[c >> 1];
        d[c & 1] = __builtin_amdgcn_mfma_f32_16x16x32_bf16(BF8((u32x4{a[0] & k_, a[1] & k_, a[2] & k_, a[3] & k_})), BF8(T[c]), d[c & 1], 0, 0, 0);
      }
      return f32x2{MVR(d[0]), MVR(d[1])};
    };
#define LD1(i, a) "global_load_dwordx4 %" #i ", %" #a ", %10 sc1\n\tv_add_u32 %" #a ", 0x4000, %" #a "\n\t"
#define LD8(r, o0, o1, W)                                                                                                          \
  do {                                                                                                                             \
    unsigned a0_ = (o0), a1_ = (o1);                                                                                               \
    asm volatile(LD1(0, 8) LD1(4, 9) LD1(1, 8) LD1(5, 9) LD1(2, 8) LD1(6, 9) LD1(3, 8) LD1(7, 9) "s_nop 0" W                        \
                 : "=&v"(r[0]), "=&v"(r[1]), "=&v"(r[2]), "=&v"(r[3]), "=&v"(r[4]), "=&v"(r[5]), "=&v"(r[6]), "=&v"(r[7]), "+v"(a0_), "+v"(a1_) \
                 : "s"(ws) : "memory");                                                                                            \
  } while (0)
    u32x4 sel, gp[4];
    unsigned vc;
#define VCLD(W) asm volatile("global_load_dword %0, %1, %2 sc1" W : "=&v"(vc) : "v"(O_VP + ((cc ^ 1) * 8 + mt) * 4096 + (lane & 15) * 256), "s"(ws) : "memory")
#define GP4(o, W)                                                                                                         \
  asm volatile(                                                                                                           \
      "global_load_dwordx4 %0, %4, %5 sc1\n\tglobal_load_dwordx4 %1, %4, %5 offset:1024 sc1\n\t"                         \
      "global_load_dwordx4 %2, %4, %5 offset:2048 sc1\n\tglobal_load_dwordx4 %3, %4, %5 offset:3072 sc1" W                \
      : "=&v"(gp[0]), "=&v"(gp[1]), "=&v"(gp[2]), "=&v"(gp[3])                                                            \
      : "v"(o), "s"(ws)                                                                                                   \
      : "memory")
#define GPBAD (max(max(max4(gp[0]), max4(gp[1])), max(max4(gp[2]), max4(gp[3]))) == SENT)
      auto GPSUM = [&]() __attribute__((always_inline)) {
        f32x4 d = {0.f, 0.f, 0.f, 0.f};
#pragma unroll
        for (int i = 0; i < 4; ++i) d = __builtin_amdgcn_mfma_f32_16x16x32_bf16(BF8(sel), BF8(gp[i]), d, 0, 0, 0);
        return d;
      };
    const bool rdc = b >= NPART && hw == 3, rnp = b >= NPART && hw == 2 && cc == 0;
    const int gbq = (b >> 4) & 1, cj = (b & 15) * 64 + (lane & 31) * 2;
    unsigned voff[4], ro, xo, ra1, wqa, qo, qa, ma, mo2, ma2;
    unsigned sfa, rk2, xo_, hwa, hra, hrb, ara, pso, gso, awa, sfw, gl0, gl1, sto, kca;
    auto JSET = [&]() {
#pragma unroll
      for (int i = 0; i < 4; ++i) {
        voff[i] = HOFF((hw * 4 + i) * 8 + (lane >> 3)) - i * 1024;
        sel[i] = ((lane & 15) >> 1) == i ? ((lane & 1) ? 0x3f800000u : 0x3f80u) : 0u;
      }
      ro = O_G2 + ((lane >> 5) * 16384 + gbq * 1024 + cj) * 8;
      xo = (unsigned)((nt * 128 + hw * 32 + cc * 16 + g4) * D + et * 128 + ((g4 ^ lane) & 15) * 8) * 2;
      ra1 = lane < 32 ? O_MOD + 2048 + gbq * 4096 + cj * 2 : O_M2 + 8192 + 64 - gbq * 64 + (b & 15) * 4;
      wqa = lds0 + SPARE + 8192 + cj * 2;
      ma = max(hw, 1) * 2048 + (lane < 16 ? et * 256 + lane * 16 : lane < 48 ? (g & 3) * 512 + (lane - 16) * 16 : ((b >> 1) & 15) * 128 + (lane & 7) * 16);
      qo = hw < 2 ? O_QT + et * 2048 + hw * 1024 + lane * 16 : O_RS + ((hw & 1) * S + nt * 128 + (lane >> 4) * 32 + cc * 16 + (lane & 15)) * 4;
      qa = lds0 + (hw < 2 ? RKA + hw * 1024 + lane * 16 : RKB + lane * 16 + (hw & 1) * 8);
      mo2 = O_M2 + (hw == 2 ? 8448u + (lane & 15) * 16 : hw ? 8192u + (lane & 7) * 16 : lane < 32 ? et * 512 + lane * 16 : 4096u + (lane & 16) * 128 + et * 256 + (lane & 15) * 16);
      ma2 = lds0 + (hw ? SPARE + 10240 + (lane & 7) * 8 : TAB + lane * 16);
      asm volatile("" : "+v"(voff[0]), "+v"(voff[1]), "+v"(voff[2]), "+v"(voff[3]), "+v"(ro), "+v"(xo), "+v"(ra1), "+v"(wqa), "+v"(qo), "+v"(qa), "+v"(ma), "+v"(mo2), "+v"(ma2), "+v"(sel));
      *reinterpret_cast<unsigned*>((hw & 1) ? shm + RKB + 1024 + lane * 4 : reinterpret_cast<char*>(scw[2]) + lane * 4) = (hw & 1) ? 0u : 0x3f803f80u;
      if (hw == 3) {
        if (lane < 32) ssel[lane] = 0u;
        *reinterpret_cast<u32x2*>(shm + SLT + lane * 8) = u32x2{0u, 0u};
      }
    };
#define OWNL() int ln_ = lane; asm volatile("" : "+v"(ln_)); const int lane = ln_, g4 = lane >> 4; (void)g4
    auto TSET = [&]() {
      OWNL();
      kca = ((g & 3) * 256 + hw * 64 + (lane & 7) * 8) * 2;
      sfa = lds0 + SPARE + 10368, rk2 = lds0 + LST + (lane >> 3) * 4, sfw = sfa + hw * 4;
      xo_ = O_X1C + (lane >> 3) * 2048 + kca, sto = O_ST2 + (hw & (S_R - 1)) * 1280 + lane * 16;
      hwa = lds0 + XC + hw * 1024 + (lane & 7) * 128 + (((lane >> 3) ^ lane) & 7) * 16, hra = lds0 + XC + g4 * 128 + ((lane ^ g4) & 7) * 16, hrb = hra ^ 64;
      awa = lds0 + SPARE + 15872 + lane * 4, ara = lds0 + SPARE + 15872 + (lane & 2) * 64 + g4 * 32 + (lane & 1) * 16;
      pso = O_PART + ((g >> 2) * 2048 + (lane & 8) * 128 + (g & 3) * 256 + hw * 64 + (lane & 7) * 8 + g4 * 2) * 4;
      gso = O_GU + g * 1024 + (lane >> 5) * 256 + hw * 64 + (lane & 15) * 4;
      gl0 = O_GU + (g >> 2) * 4096 + lane * 8, gl1 = O_GU + (xcd * 7 + (mt + 7 - W_UO) % 7) * 4096 + 512 + lane * 8;
      asm volatile("" : "+v"(sfa), "+v"(rk2), "+v"(xo_), "+v"(hwa), "+v"(hra), "+v"(hrb), "+v"(ara), "+v"(pso), "+v"(gso), "+v"(awa), "+v"(sfw), "+v"(gl0), "+v"(gl1), "+v"(sto), "+v"(kca));
    };
    if (hw < 2) {
      constexpr int P = 8;
      __builtin_amdgcn_s_setprio(2);
      unsigned voy[P];
#pragma unroll
      for (int i = 0; i < P; ++i) voy[i] = (unsigned)(((hw * 8 + i) * 8 + (lane >> 3)) * 4096 + ((lane & 7) ^ (lane >> 3)) * 16 - (i & 3) * 1024);
      const unsigned lbase = lds0 + hw * 8192;
      const char* Yc = reinterpret_cast<const char*>((cc ? Y1 : Y0) + (size_t)nt * 128 * S);
      auto stage = [&](const char* yp, unsigned l) {
        GLDS4(l, voy, yp);
        GLDS4(l + 4096, voy + 4, yp);
      };
#define YSRC(n) (Yc + ((((n) + xcd * 4) & 31) << 7))
      __builtin_amdgcn_s_sleep(C_LD);
      PFL(hw ? PFG(1) : cpf);
      JSET();
      TAGW(1);
      BAR(55);
      BT(56);
      if (hw == 1 && lane == 0) asm volatile("global_store_dwordx2 %0, %1, %2 sc1" ::"v"(O_XFL + b * XSTR * 4), "v"(tag), "s"(ws) : "memory");
#pragma unroll
      for (int s = 0; s < NS - 1; ++s) stage(YSRC(s), lbase + s * 16384);
      BT(57);
      WAIT_V((NS - 2) * P);
      BAR(0);
      unsigned slot = lbase + (NS - 1) * 16384;
      const u64 evm = (u64)((1u | 1u << W_T) << pf0 | (1u | 1u << W_T) << pf1) | 1ull << CPF_K | 1ull << (33 - NS) | 3ull << 32;
      const char* yb = Yc;
      unsigned yo = (NS - 1 + xcd * 4) << 7, ym = 31 << 7;
#pragma unroll 1
      for (int k = 1; k <= 32 + B_J; ++k) {
        const char* ys = yb + (yo & ym);
        yo += 128;
        asm volatile("" : "+s"(ys));
        WAIT_V((NS - 3) * P);
        BAR(k);
        stage(ys, slot);
        slot = slot == lbase + (NS - 1) * 16384 ? lbase : slot + 16384;
        if (__builtin_expect((evm >> k) & 1, 0)) {
          if (k == pf0) {
            WST(og, wg);
            if (b >= NPART) GLDS(lds0 + SPARE + 8192 + hw * 1024, (unsigned)(hw * 1024 + lane * 16), wr_);
          }
          if (k == pf0 + W_T) { WTK(gT); }
          if (k == pf1) { WST(ou, wu); }
          if (k == pf1 + W_T) { WTK(uT); }
          if (k == CPF_K && hw == 1) CPFL();
          if (k == 33 - NS) {
#pragma unroll
            for (int i = 0; i < P; ++i) voy[i] = HOFF((hw * 8 + i) * 8 + (lane >> 3)) - (i & 3) * 1024;
            yb = Hc, yo = mt << 8, ym = 15 << 7;
          }
          if (k == 32) {
            DLD(0, 8 - B_WN);
            TSET();
          }
          if (k == 33 && hw == 0) {
            if (U_D) __builtin_amdgcn_s_sleep(U_D);
            urdy = UPOLL(U_N1);
          }
        }
      }
    } else {
      const int aw = hw - 2;
      unsigned ow = wo - (2048 + (cg0 >> 4) * 1024) * 2;
      const unsigned oq = (unsigned)((nt * 64 + aw * 32 + (lane >> 4)) * D + mt * 128) * 2;
      u32x4 hv[8], wt[8], sc;
      {
        asm volatile("s_waitcnt vmcnt(11)" : "+v"(cq)::"memory");
#pragma unroll
        for (int i = 0; i < 4; ++i) sc[i] = pack2(silu_bf(bflo(cq[i])), silu_bf(bfhi(cq[i])));
        asm volatile("" : "+v"(sc));
      }
#define GPUB(o, off)                                                                                   \
  if ((lane & 8) == 0)                                                                                 \
    asm volatile("global_store_dwordx4 %0, %1, %2 sc1 ; ALWAYS\n\ts_nop 1" ::"v"(off),                          \
                 "v"(u32x4{__float_as_uint(o[0]), (unsigned)tag, __float_as_uint(o[1]), (unsigned)tag}), "s"(ws) : "memory")
      const unsigned gpo = ks0 * 2048 + cg0 * 64 + (lane & 7) * 8 + (lane >> 4) * 2;
      {
        unsigned mpo = O_G1 + gpo * 8;
        asm volatile("s_waitcnt vmcnt(3)" : W8(wv), "+v"(mpo)::"memory");
        TR8(wt, wv);
        f32x2 o = MV64(wt, sc);
        asm volatile("" : "+v"(o));
        TAGW(0);
        BAR(55);
        if (P_D2) __builtin_amdgcn_s_sleep(P_D2);
        PFL(PFG(2 + aw));
        GPUB(o, mpo);
      }
      unsigned vta = lds0 + STG + (g4 * 8 + ((lane >> 2) & 3)) * 256 + (((aw * 8 + ((lane >> 1) & 1)) ^ ((g4 & 1) * 8 + ((lane >> 2) & 3))) << 4) + (lane & 1) * 8;
      unsigned vso = O_VP + ((cc * 8 + mt) * 16 + nt) * 256 + aw * 128 + (lane & 3) * 32 + g4 * 8;
      asm volatile("" : "+v"(vta), "+v"(vso), "+v"(ow));
      unsigned f0 = O_XFL + ((xcd * 32 + 64 + aw * 128 + lane) & 255) * XSTR * 4, f1 = O_XFL + ((xcd * 32 + 128 + aw * 64 + lane) & 255) * XSTR * 4;
      unsigned lao = O_KEY + 512 * 1024 + b * 3328 + aw * 1664 + lane * 16;
      const char* pf4 = PFG(4 + aw);
      const char* pf6 = PFG(6 + aw);
      asm volatile("" : "+v"(f0), "+v"(f1), "+v"(lao), "+v"(pf4), "+v"(pf6));
      BT(57);
      BAR(0);
      f32x4 rs = {0.f, 0.f, 0.f, 0.f};
      u32x4 rsa = (lane & 15) == g4 * 4 ? u32x4{0x3f803f80u, 0x3f803f80u, 0x3f803f80u, 0x3f803f80u} : u32x4{0u, 0u, 0u, 0u}, R_;
      asm volatile("" : "+v"(rs), "+v"(rsa), "=v"(R_));
      const unsigned ra = lds0 + (mt * 16 + aw * 8) * 128 + lane * 16;
      unsigned so = 0;
#define TSX()
#define RSUM()                                                                                                    \
  do {                                                                                                            \
    TSX();                                                                                                        \
    asm volatile("ds_read_b128 %1, %2\n\ts_waitcnt lgkmcnt(0)\n\tv_mfma_f32_16x16x32_bf16 %0, %3, %1, %0" : "+v"(rs), "+v"(R_) : "v"(ra + so), "v"(rsa) : "memory"); \
    so = so == (NS - 1) * 16384 ? 0 : so + 16384;                                                                 \
  } while (0)
      {
        u64 f[2];
#define FLD() asm volatile("global_load_dwordx2 %0, %2, %4 sc1\n\tglobal_load_dwordx2 %1, %3, %4 sc1" : "=&v"(f[0]), "=&v"(f[1]) : "v"(f0), "v"(f1), "s"(ws) : "memory")
        __builtin_amdgcn_s_sleep(F_D);
        FLD();
        asm volatile("global_store_dwordx4 %0, %1, %2 sc1" ::"v"(lao), "v"(u32x4{~0u, ~0u, ~0u, ~0u}), "s"(ws) : "memory");
        if (lane < 40) asm volatile("global_store_dwordx4 %0, %1, %2 offset:1024 sc1 ; ALWAYS" ::"v"(lao), "v"(u32x4{~0u, ~0u, ~0u, ~0u}), "s"(ws) : "memory");
        asm volatile("s_nop 1");
#define GLD() _Pragma("unroll") for (int i = 0; i < 8; ++i) LDWI(wv[i], ow, wmod, 12288)
        RSUM();
        BAR(1);
        asm volatile("s_waitcnt vmcnt(%2)" : "+v"(f[0]), "+v"(f[1]) : "n"(2) : "memory");
        POLL(__all(f[0] == tag && f[1] == tag), 1, FLD(); asm volatile("s_waitcnt vmcnt(0)" : "+v"(f[0]), "+v"(f[1])::"memory"));
      }
      if (X_PA) __builtin_amdgcn_s_setprio(X_PA);
#define ARUN(b0, b1) _Pragma("unroll 1") for (int bi = (b0); bi < (b1); ++bi) { RSUM(); BAR(bi + 1); }
      GLD();
      ARUN(1, pf0);
      PFL(pf4);
#define HVLD()                                                                                                                    \
  do {                                                                                                                            \
    unsigned o_ = oq + (((lane & 15) ^ g4) << 4);                                                                                 \
    asm volatile("s_mov_b32 m0, %0" ::"s"(lds0 + STG + aw * 8192));                                                               \
    _Pragma("unroll") for (int i = 0; i < 8; ++i) {                                                                               \
      asm volatile("v_xor_b32 %0, %4, %0\n\tglobal_load_lds_dwordx4 %0, %1 offset:%2" HPOL "\n\tv_add_u32 %0, %3, %0"              \
                   : "+v"(o_) : "s"(cc ? X1 : X0), "n"((i & 3) * 1024), "n"(8192 + (i == 3 ? 3072 : -1024)), "n"(i ? ((i ^ (i - 1)) & 3) * 64 : 0) : "memory"); \
      if (i == 3) asm volatile("s_add_u32 m0, m0, 4096" ::: "scc");                                                               \
    }                                                                                                                             \
  } while (0)
      WST(og, wg);
      ARUN(pf0, pf0 + G1_C);
      {
        if (b >= NPART) WAIT_V(0);
        asm volatile("s_waitcnt vmcnt(8)" : W8(wv)::"memory");
        TR8(wt, wv);
        if (G1_S) { RSUM(); BAR(pf0 + G1_C + 1); }
        const f32x2 o = MV64(wt, sc);
        GPUB(o, O_MODP + gpo * 8);
        ow = wo + 2048;
      }
      ARUN(pf0 + G1_C + G1_S, pf0 + W_T - 1);
      WAIT_V(1);
      RSUM();
      BAR(pf0 + W_T);
      WTK(gT);
      ARUN(pf0 + W_T, pf1);
      PFL(pf6);
      WST(ou, wu);
      const int rb_ = b < NPART ? pf1 + W_T : R_AT, hb_ = max(rb_ + 1, H_AT + (j8 & 3));
      const unsigned c0 = (b - NPART) * 64 + aw * 32 + (lane & 15) * 2, c1 = (b - NPART) * 32 + aw * 16 + (lane & 7) * 2;
#define ERLD(W) LD8(wt, O_MODP + (g4 * 8192 + c0) * 8, O_G1 + (g4 * 8192 + (lane & 8) * 128 + c1) * 8, W)
      ARUN(pf1, rb_ - 3);
      if (b >= NPART) ERLD("");
      ARUN(rb_ - 3, rb_ - 1);
      WAIT_V(0);
      RSUM();
      BAR(rb_);
      WTK(uT);
      if (b >= NPART) {
        const unsigned wq = *reinterpret_cast<const unsigned*>(&svb[4][c1]);
#define TG2(r, i) ((r[i][1] ^ (unsigned)tag) | (r[i][3] ^ (unsigned)tag) | (r[i + 1][1] ^ (unsigned)tag) | (r[i + 1][3] ^ (unsigned)tag))
        asm volatile("" : W8(wt));
        POLL(__all((TG2(wt, 0) | TG2(wt, 2) | TG2(wt, 4) | TG2(wt, 6)) == 0u), 2, ERLD(VW0));
        auto FIN = [&](int q, int i, bool inc) __attribute__((always_inline)) {
          return rbf((inc ? 1.0f : 0.0f) + rbf(ar4((__uint_as_float(wt[q][i]) + __uint_as_float(wt[q + 1][i])) + (__uint_as_float(wt[q + 2][i]) + __uint_as_float(wt[q + 3][i])))));
        };
        const float s0 = FIN(0, 0, gbq), s1 = FIN(0, 2, gbq);
        if (lane < 16)
          asm volatile("global_store_dwordx4 %0, %1, %2 sc1\n\ts_nop 1" ::"v"(O_MODP + (16 * 2048 + c0) * 8),
                       "v"(u32x4{__float_as_uint(s0), (unsigned)tag, __float_as_uint(s1), (unsigned)tag}), "s"(ws) : "memory");
        const bool hi = lane & 8;
        const float t0 = FIN(4, 0, hi), t1 = FIN(4, 2, hi), o0 = dpp_f<0x128>(t0), o1 = dpp_f<0x128>(t1);
        const float a0 = hi ? o0 : t0, a1 = hi ? o1 : t1, e0 = hi ? t0 : o0, e1 = hi ? t1 : o1, p0 = e0 * bflo(wq), p1 = e1 * bfhi(wq);
        const unsigned h = pack2(p0, p1);
        const unsigned v = g4 == 0 ? __float_as_uint(hi ? a1 : a0) : g4 == 1 ? (hi ? h : pack2(e0, e1)) : hi ? pack2(a0 * p0, a1 * p1) : pack2(p0 - bflo(h), p1 - bfhi(h));
        const unsigned po = g4 == 0 ? O_M2 + c1 * 4 + (hi ? 4 : 0) : (g4 == 1 ? (hi ? O_M2 + 4096 : O_MOD + 4096) : hi ? O_GC : O_M2 + 6144) + c1 * 2;
        if (lane < 48) asm volatile("global_store_dword %0, %1, %2 sc1" ::"v"(po), "v"(min(v, SENT - 1u)), "s"(ws) : "memory");
      }
      ARUN(rb_, hb_);
      HVLD();
      ARUN(hb_, q1);
      u32x4 fm, g0, g1;
#define FMLD(W)                                                                                                                                     \
  asm volatile("global_load_dwordx4 %0, %3, %5 sc1\n\tglobal_load_dwordx4 %1, %4, %5 sc1\n\tglobal_load_dwordx4 %2, %4, %5 offset:64 sc1" W        \
               : "=&v"(fm), "=&v"(g0), "=&v"(g1) : "v"(O_MODP + (16 * 2048 + aw * 1024 + mt * 128) * 8 + lane * 16), "v"(O_GC + nt * 128 + g4 * 16), "s"(ws) : "memory")
      FMLD("");
      RSUM();
      BAR(q1 + 1);
      unsigned ha[4], sma = lds0 + SPARE + 15872 + (aw ? 512 + lane * 8 : lane * 4);
#pragma unroll
      for (int i = 0; i < 4; ++i) ha[i] = lds0 + STG + aw * 8192 + (lane & 15) * 256 + (((i * 4 + (lane >> 4)) ^ (lane & 15)) << 4);
      asm volatile("" : "+v"(ha[0]), "+v"(ha[1]), "+v"(ha[2]), "+v"(ha[3]), "+v"(sma), "+v"(ow));
      ARUN(q1 + 1, F_AT);
      asm volatile("s_waitcnt vmcnt(0)" : "+v"(fm), "+v"(g0), "+v"(g1)::"memory");
#pragma unroll
      for (int i = 0; i < 8; ++i) DSR(hv[i], ha[i & 3], (i >> 2) * 4096);
      POLL(__all(fm[1] == (unsigned)tag && fm[3] == (unsigned)tag && max(max4(g0), max4(g1)) != SENT), 1, FMLD(VW0));
      {
        const unsigned sg = cc ? 0x80000000u : 0u;
        asm volatile("ds_write_b32 %0, %2\n\tds_write_b32 %1, %3" ::"v"(sma), "v"(sma + (aw ? 4 : 256)), "v"(aw ? fm[0] ^ sg : (fm[0] >> 16) | (fm[2] & 0xffff0000u)), "v"(fm[2] ^ sg) : "memory");
      }
      ARUN(F_AT, 29);
      GLD();
      RSUM();
      BAR(30);
        u32x2 tv[16];
#pragma unroll
        for (int q = 0; q < 16; ++q)
          asm volatile("ds_read_b64_tr_b16 %0, %1 offset:%2" : "=v"(tv[q]) : "v"(vta ^ ((((q >> 2) * 2) ^ ((q & 1) * 4)) << 4)), "n"((q & 1) * 1024 + ((q >> 1) & 1) * 8192) : "memory");
      RSUM();
      BAR(31);
      RSUM();
      {
        asm volatile("s_nop 7\n\ts_nop 7" : "+v"(rs));
        const float v0 = dpp_add<0x141>(dpp_add<0x4E>(dpp_add<0xB1>(rs[0]))), v = cc ? -v0 : v0;
        if ((lane & 7) == 0)
          asm volatile("global_store_dword %0, %1, %2 sc1 ; ALWAYS" ::"v"(O_RS + (cc * S + nt * 128 + mt * 16 + aw * 8 + (lane >> 3)) * 4), "v"(min(pack2(v, v - rbf(v)), SENT - 1u)), "s"(ws) : "memory");
      }
      BAR(32);
      __builtin_amdgcn_s_sleep(2);
      DLD(0, 8 - B_WN);
      __builtin_amdgcn_s_setprio(2);
      {
        asm volatile("" : W8(tv), W8((tv + 8))::"memory");
        f32x4 d[4];
#pragma unroll
        for (int j = 0; j < 4; ++j) {
          d[j] = __builtin_amdgcn_mfma_f32_16x16x32_bf16(BF8((u32x4{tv[4 * j][0], tv[4 * j][1], tv[4 * j + 1][0], tv[4 * j + 1][1]})), BF8(g0), f32x4{0.f, 0.f, 0.f, 0.f}, 0, 0, 0);
          d[j] = __builtin_amdgcn_mfma_f32_16x16x32_bf16(BF8((u32x4{tv[4 * j + 2][0], tv[4 * j + 2][1], tv[4 * j + 3][0], tv[4 * j + 3][1]})), BF8(g1), d[j], 0, 0, 0);
        }
        u32x2 vo;
#pragma unroll
        for (int i = 0; i < 2; ++i) {
          const unsigned e0 = pack2(d[0][2 * i], d[0][2 * i + 1]), e1 = pack2(d[1][2 * i], d[1][2 * i + 1]), e2 = pack2(d[2][2 * i], d[2][2 * i + 1]), e3 = pack2(d[3][2 * i], d[3][2 * i + 1]);
          vo[i] = min((lane & 2) ? ((lane & 1) ? e3 : e2) : ((lane & 1) ? e1 : e0), SENT - 1u);
        }
        if ((lane & 12) == 0) asm volatile("global_store_dwordx2 %0, %1, %2 sc1 ; ALWAYS" ::"v"(vso), "v"(vo), "s"(ws) : "memory");
      }
      asm volatile("" : W8(hv)::"memory");
      {
        f32x4 qa[2] = {};
#pragma unroll
        for (int kk = 0; kk < 4; ++kk) {
          const bf16x8 sb8 = *reinterpret_cast<const bf16x8*>(ssa + kk * 32 + (lane >> 4) * 8);
#pragma unroll
          for (int r = 0; r < 2; ++r)
            qa[r] = __builtin_amdgcn_mfma_f32_16x16x32_bf16(__builtin_bit_cast(bf16v8, hv[r * 4 + kk]), __builtin_bit_cast(bf16v8, sb8), qa[r], 0, 0, 0);
        }
        if ((lane & 15) < 2)
          asm volatile("global_store_dwordx4 %0, %1, %2 sc1 ; ALWAYS\n\ts_nop 1" ::"v"(O_RS + (4096 + (cc * 8 + mt) * D + nt * 64 + aw * 32 + (lane & 1) * 16 + (lane >> 4) * 4) * 4),
                       "v"(nsent4(selu((lane & 1) ? ~0u : 0u, __builtin_bit_cast(u32x4, qa[1]), __builtin_bit_cast(u32x4, qa[0])))), "s"(ws) : "memory");
      }
      BAR(33);
      {
        if (b >= NPART) WAIT_V(3);
        asm volatile("s_waitcnt vmcnt(%8)" : W8(wv) : "n"(8 - B_WN + 3) : "memory");
        TR8(wt, wv);
        const f32x2 o = MV64(wt, sc);
        GPUB(o, O_G2 + gpo * 8);
      }
      JSET();
      TSET();
      BAR(34);
#pragma unroll
      for (int k = 3; k <= B_J; ++k) {
        BAR(32 + k);
        if (k == V_K) {
          if (!aw) GP4(O_VP + (cc * 8 + mt) * 4096 + lane * 16, "");
          else VCLD("");
        }
      }
    }
    u32x4 dT[8];
    u32x4 m[3];
    u32x2 kk = {SENT, SENT};
#define K2LD(W) asm volatile("global_load_dwordx2 %0, %1, %2 sc1" W : "+v"(kk) : "v"(O_K2 + (lane & 15) * 64), "s"(ws) : "memory")
    {
      constexpr BSched sch;

      const unsigned lbase = lds0 + hw * 4096;
      u32x4 rr[8];
      unsigned cbv, t1, t2;
#define RRLD(W)                                                                                                          \
  do {                                                                                                                   \
    asm volatile("global_load_dword %0, %1, %2 sc1" : "=&v"(cbv) : "v"(O_MOD + 4096 + cj * 2), "s"(ws) : "memory");      \
    LD8(rr, ro, ro + 65536, W);                                                                                          \
  } while (0)
      unsigned wq;
      u32x4 mq = {0u, 0u, 0u, 0u};
#define QLD(W)                                                                                                        \
  do {                                                                                                                \
    if (hw < 2) asm volatile("global_load_dwordx4 %0, %1, %2 sc1" W : "+v"(mq) : "v"(qo), "s"(ws) : "memory");        \
    else asm volatile("global_load_dword %0, %1, %2 sc1" W : "+v"(mq[0]) : "v"(qo), "s"(ws) : "memory");              \
  } while (0)
#define QRLD(W)                                                                                                       \
  asm volatile("global_load_dwordx4 %0, %2, %4 sc1\n\tglobal_load_dwordx4 %1, %3, %4 sc1" W : "=&v"(gp[0]), "=&v"(gp[1]) \
               : "v"(qr), "v"(qr + 32768), "s"(ws) : "memory")
      const bool rq_ = b >= NPART && hw == 1;
      const unsigned qr = O_RS + (4096 + (lane >> 3) * D + (b - NPART) * 32 + (lane & 7) * 4) * 4;
#define MODLD() asm volatile("global_load_dwordx4 %0, %2, %4 sc1\n\tglobal_load_dwordx4 %1, %3, %4 sc1" : "=&v"(m[0]), "=&v"(m[2]) : "v"(O_MOD + ma), "v"(mo2), "s"(ws) : "memory")
#define MODW(n) asm volatile("s_waitcnt vmcnt(%2)" : "+v"(m[0]), "+v"(m[2]) : "n"(n) : "memory")
#pragma unroll
      for (int k = B_J; k < 16; ++k) {
        if (k > B_J) {
          if (k == 2 * U_N1 - 1 && !urdy) UPOLL(7);
          WAIT_V(sch.cnt[k]);
          if (k == 15) WAIT_L(0);
          BAR(32 + k);
        }
        if (k > B_J && k - 2 + NS < 16) {
          const char* yp = Hc + ((mt * 2 + k - 2 + NS) & 15) * 128;
          const unsigned l = lbase + ((30 + k + NS) % NS) * 16384;
          GLDS4(l, voff, yp);
        }
        if (k == B_TK && hw == 1 && b >= 192) {
          const unsigned t = tick(), h = t + T_O + (((int)(t - ts0) - T_N) * T_A >> 8);
          if (lane == 0) asm volatile("global_store_dword %0, %1, %2 sc1" ::"v"(O_M2 + 8448 + (b - 192) * 4), "v"(min(h, SENT - 1u)), "s"(ws) : "memory");
        }
        if (k == B_RAT + 2 && rdc) {
          WAIT_V(sch.rcnt);
          asm volatile("" : W8(rr), "+v"(cbv));
          POLL(__all((TG2(rr, 0) | TG2(rr, 2) | TG2(rr, 4) | TG2(rr, 6)) == 0u && cbv != SENT), 2, RRLD(VW0); asm volatile("" : "+v"(cbv)));
        }
        if (k == B_RAT + 2 + R_S && rdc) {
          asm volatile("" : W8(rr));
          float t[2];
#pragma unroll
          for (int j = 0; j < 2; ++j) {
#define RF(q) __uint_as_float(rr[q][2 * j])
            t[j] = ((RF(0) + RF(1)) + (RF(2) + RF(3))) + ((RF(4) + RF(5)) + (RF(6) + RF(7)));
          }
          const unsigned o = pack2(swap32_add(t[0], t[0]), swap32_add(t[1], t[1])), f = gbq ? cbv : o;
          const float pr = row_sum(bflo(f) * bflo(wq) + bfhi(f) * bfhi(wq)), ps = swap16_add(pr, pr);
          if (lane <= 32) asm volatile("global_store_dword %0, %1, %2 sc1" ::"v"(ra1), "v"(min(lane < 32 ? o : __float_as_uint(ps), SENT - 1u)), "s"(ws) : "memory");
          TS(12);
        }
        if (k == V_T && hw == 3) {
          WAIT_V(sch.vcnt);
          asm volatile("" : "+v"(vc));
          POLL(__all(vc != SENT), 2, VCLD(VW0));
        }
        if (k == V_T && hw == 2) {
          WAIT_V(sch.vcnt);
          asm volatile("" : "+v"(gp[0]), "+v"(gp[1]), "+v"(gp[2]), "+v"(gp[3]));
          POLL(!__any(GPBAD), 2, GP4(O_VP + (cc * 8 + mt) * 4096 + lane * 16, VW0));
          const f32x4 d = GPSUM();
          if (lane < 32) *reinterpret_cast<u32x2*>(shm + VT + (lane & 15) * 16 + g4 * 8) = u32x2{pack2(d[0], d[1]), pack2(d[2], d[3])};
          WAIT_L(0);
        }
        if (k == N_C) {
          f32x4 n[2] = {};
#pragma unroll
          for (int q = 0; q < 8; ++q)
            n[q >> 2] = __builtin_amdgcn_mfma_f32_16x16x32_bf16(BF8(*reinterpret_cast<const u32x4*>(shm + VT + (q & 3) * 64 + g4 * 16)),
                                                                BF8(*reinterpret_cast<const u32x4*>(shm + STG + hw * 8192 + q * 1024 + lane * 16)), n[q >> 2], 0, 0, 0);
          if (lane < 16)
            asm volatile("global_store_dword %0, %1, %2 sc1" ::"v"(O_NP + nt * 4096 + (cc * 8 + mt) * 256 + hw * 64 + lane * 4), "v"(min(pack2(n[0][0], n[1][0]), SENT - 1u)), "s"(ws) : "memory");
          TS(33);
        }
        if (k == N_K + N_D && rnp) {
          WAIT_V(sch.ncnt);
          asm volatile("" : "+v"(gp[0]), "+v"(gp[1]), "+v"(gp[2]), "+v"(gp[3]));
          POLL(!__any(GPBAD), 2, GP4(O_NP + nt * 4096 + lane * 16, VW0));
          const unsigned r0 = nt * 128 + ((lane >> 2) & 3) * 32 + (lane & 3) * 4 + (g4 & 1) * 2;
          const f32x4 d = GPSUM();
          unsigned ky[4];
#pragma unroll
          for (int i = 0; i < 4; ++i) {
            const unsigned f = __float_as_uint(d[i]);
            ky[i] = (((f ^ ((unsigned)((int)f >> 31) | 0x80000000u)) >> 1) & ~2047u) | (r0 + (i & 1) * 16 + (i >> 1));
          }
          t1 = max(max(ky[0], ky[1]), max(ky[2], ky[3])), t2 = max(min(max(ky[0], ky[1]), max(ky[2], ky[3])), max(min(ky[0], ky[1]), min(ky[2], ky[3])));
          asm volatile("" : "+v"(t1), "+v"(t2));
        }
        if (k == N_K + N_D + N_S && rnp) {
          TOP2(0xB1); TOP2(0x4E); TOP2(0x141); TOP2(0x140);
          TOP2S(__builtin_amdgcn_permlane16_swap);
          if (lane == 0) asm volatile("global_store_dwordx2 %0, %1, %2 sc1" ::"v"(O_K2 + nt * 64), "v"(u32x2{t1, t2}), "s"(ws) : "memory");
          TS(34);
        }
        if (k == N_K && rnp) GP4(O_NP + nt * 4096 + lane * 16, "");
        if (k == B_RAT + 2 && rq_) {
          WAIT_V(sch.rcnt);
          asm volatile("" : "+v"(gp[0]), "+v"(gp[1]));
          POLL(__all(max(max4(gp[0]), max4(gp[1])) != SENT), 2, QRLD(VW0));
          u32x4 a;
#pragma unroll
          for (int c = 0; c < 2; ++c) {
            const f32x4 t = __builtin_bit_cast(f32x4, gp[c]);
            const float r = dpp_add<0x128>(swap16_add(swap32_add(t[0], t[2]), swap32_add(t[1], t[3])));
            a[2 * c] = pack2(r, r);
            a[2 * c + 1] = pack2(r - bflo(a[2 * c]), r - bflo(a[2 * c]));
          }
          if ((lane & 8) == 0)
            asm volatile("global_store_dwordx4 %0, %1, %2 sc1\n\ts_nop 1" ::"v"(O_QT + ((b - NPART) * 32 + (lane & 1) * 16 + ((lane >> 1) & 3) * 4 + g4) * 16), "v"(nsent4(a)), "s"(ws) : "memory");
        }
        if (k == B_QAT + B_QD) {
          asm volatile("s_waitcnt vmcnt(%1)" : "+v"(mq) : "n"(sch.qcnt) : "memory");
          POLL(__all(max4(mq) != SENT), 2, QLD(VW0));
          if (hw < 2) asm volatile("ds_write_b128 %0, %1" ::"v"(qa), "v"(mq) : "memory");
          else asm volatile("ds_write2_b32 %0, %1, %1 offset1:1" ::"v"(qa), "v"(mq[0]) : "memory");
        }
        if (k == CPF_B && hw == 2) CPFL();
        if (k == B_RAT) {
          if (rdc) {
            RRLD("");
            asm volatile("ds_read_b32 %0, %1\n\ts_waitcnt lgkmcnt(0)" : "=v"(wq) : "v"(wqa) : "memory");
          }
          if (rq_) QRLD("");
        }
        if (k == X_K) {
          const unsigned v4[4] = {xo, (xo ^ 64) + 8192 - 1024, (xo ^ 128) + 16384 - 2048, (xo ^ 192) + 24576 - 3072};
          unsigned l2 = lbase;
          asm volatile("" : "+s"(l2));
          GLDS4(l2 * 2 - lds0 + STG + 4096, v4, x);
        }
        if (k == V_T + 1 || k == N_K + 1) SKEWPT();
        if (k == B_QAT) QLD("");
        if (k == B_MAT) MODLD();
        if (k == B_WK) { DLD(8 - B_WN, 8); }
      }
      if (b >= NPART) WAIT_V(0);
      MODW(sch.mcnt);
      TS(30);
      SKEWPT();
      POLL(__all(max(max4(m[0]), max4(m[2])) != SENT), 4, MODLD(); MODW(0));
      *reinterpret_cast<u32x4*>(shm + SPARE + ma) = m[0];
      if (hw == 0) {
        asm volatile("ds_write_b128 %0, %1" ::"v"(ma2), "v"(m[2]) : "memory");
      } else if (hw == 2) {
        int d = max(max((int)(m[2][0] - ts0), (int)(m[2][1] - ts0)), max((int)(m[2][2] - ts0), (int)(m[2][3] - ts0)));
        d = max(d, (int)dpp_u<0xB1>(d));
        d = max(d, (int)dpp_u<0x4E>(d));
        d = max(d, (int)dpp_u<0x141>(d));
        d = max(d, (int)dpp_u<0x140>(d));
        const unsigned hh = ts0 + d;
        ssel[7] = hh;
        if ((int)(tick() - (unsigned)__builtin_amdgcn_readfirstlane((int)hh) - T_K) >= 0) K2LD("");
      } else {
        const f32x4 v = __builtin_bit_cast(f32x4, m[2]);
        const f32x2 c = {v[0] + v[1], v[2] + v[3]};
        const float s4 = dpp_add<0x4E>(dpp_add<0xB1>(c[0] + c[1]));
        if (lane < 8) asm volatile("ds_write_b64 %0, %1" ::"v"(ma2), "v"(lane < 4 ? c : f32x2{s4, s4}) : "memory");
      }
    }
    if (hw != 2) __builtin_amdgcn_s_setprio(0);
    TS(17);
    BT(48);
    LBAR();
    BTE();
    TS(18);
    if (hw == 2) {
      WAIT_V(0);
      asm volatile("" : "+v"(kk));
      if (__any(max(kk[0], kk[1]) == SENT)) {
        nap_tick((unsigned)__builtin_amdgcn_readfirstlane((int)ssel[7]) + T_K);
        POLL2(max(kk[0], kk[1]) == SENT, K_S, PCAP, K2LD(VW0));
      }
      TS(35);
      const unsigned me = lane < 32 ? kk[0] : kk[1], ot = (lane & 16) ? kk[1] : kk[0];
      unsigned rk = ot > me;
      sfor<15>([&](auto r) __attribute__((always_inline)) { rk += dpp_u<0x121 + decltype(r)::value>(ot) > me; });
      auto rs_ = __builtin_amdgcn_permlane16_swap(rk, rk, false, false);
      rk = rs_[0] + rs_[1];
      if (rk < 8u) {
        *reinterpret_cast<unsigned*>(shm + LST + rk * 4) = 0x8ffeu - (me & 2047u) * 2;
        if ((lane & 15) == nt) *reinterpret_cast<unsigned*>(shm + SLT + (me & 127u) * 4) = rk + 1;
      }
      asm volatile("" ::: "memory");
      ssel[20] = 1u;
    }
    TS(19);
    unsigned sv, one = 1u, kmk = ~0u;
    RETIRE();
    const u32x4 cbq = *reinterpret_cast<const u32x4*>(shm + SPARE + 4096 + kca), sbq = *reinterpret_cast<const u32x4*>(shm + SPARE + 2048 + kca);
    f32x4 cb2[2] = {lo4(cbq), hi4(cbq)}, sb2[2] = {lo4(sbq), hi4(sbq)};
    float sw = cwj[8], cj8 = cwj[lane & 7];
    asm volatile("" : "+v"(cb2[0]), "+v"(cb2[1]), "+v"(sb2[0]), "+v"(sb2[1]), "+v"(sw), "+v"(cj8), "+v"(one), "+v"(kmk));
    const unsigned th = (unsigned)__builtin_amdgcn_readfirstlane((int)ssel[7]);
    const char* wb = ws;
#define P2PUT(i) if (__builtin_expect(wb != ws, 0)) { OWNL(); WAIT_V(0); if (lane == 0) asm volatile("global_store_dwordx2 %0, %1, %2 sc1" ::"v"(O_F2 + (i)*8192 + (g * 4 + hw) * 8 + lane), "v"(tag), "s"(ws) : "memory"); }
#define P2GET(o) if (__builtin_expect(wb != ws, 0)) { OWNL(); u64 f; POLL2(f != tag, 4, PCAP, asm volatile("global_load_dwordx2 %0, %1, %2 sc1" VW0 : "=&v"(f) : "v"(O_F2 + (o)), "s"(ws) : "memory")); }
#pragma unroll 1
    for (;;) {
    TS(20);
    if (wb == ws) {
      unsigned r_;
      POLL2(sv == 0u, 2, 1 << 26, asm volatile("ds_read_b32 %0, %2 offset:80\n\tds_read_b32 %1, %3\n\ts_waitcnt lgkmcnt(0)" : "=&v"(sv), "=&v"(r_) : "v"(sfa), "v"(rk2) : "memory"));
      rk2 = r_;
    }
    TS(21);
    nap_tick(th + T_C3 - 10);
    {
      u32x4 dn;
      POLL2((dn[0] & dn[1] & dn[2] & dn[3]) == 0u, 1, 1 << 26, asm volatile("ds_read_b128 %0, %1 offset:96\n\ts_waitcnt lgkmcnt(0)" : "=v"(dn) : "v"(sfa) : "memory"));
    }
    __builtin_amdgcn_s_setprio(H_P);
    wait_tick(th + T_C3);
    TS(36);
    u32x4 st, xv_;
    POLL2(max(max4(xv_), max4(st)) == SENT, 0, PCAP,
          asm volatile("global_load_dwordx4 %0, %2, %4 sc1\n\tglobal_load_dwordx4 %1, %3, %4 sc1" VW0 : "=&v"(st), "=&v"(xv_) : "v"(sto), "v"(xo_), "s"(ws) : "memory"));
    TS(22);
    SKEWPT();
#define SUM8(v) dpp_add<0x141>(dpp_add<0x4E>(dpp_add<0xB1>(v)))
    const f32x4 sf = __builtin_bit_cast(f32x4, st);
    const float mean = SUM8(sf[0]) * 0.125f, dm = sf[0] - mean;
    const float rstd = __builtin_amdgcn_rsqf(SUM8(sf[1] + 128.0f * (dm * dm)) * (1.0f / D) + 1e-5f);
    asm volatile("ds_write_b128 %0, %1\n\tds_write_b32 %2, %3 offset:64" ::"v"(hwa),
                 "v"(pk8(((lo4(xv_) - mean) * rstd) * cb2[0] + sb2[0], ((hi4(xv_) - mean) * rstd) * cb2[1] + sb2[1])), "v"(sfw), "v"(one) : "memory");
    TS(3);
    u32x4 ha[8], fg;
#define HSLD(W)                                                                                                                             \
  asm volatile(                                                                                                                             \
      "ds_read_b128 %8, %11 offset:64\n\tds_read_b128 %0, %9\n\tds_read_b128 %1, %10 offset:512\n\tds_read_b128 %2, %9 offset:1024\n\t"       \
      "ds_read_b128 %3, %10 offset:1536\n\tds_read_b128 %4, %9 offset:2048\n\tds_read_b128 %5, %10 offset:2560\n\t"                          \
      "ds_read_b128 %6, %9 offset:3072\n\tds_read_b128 %7, %10 offset:3584" W                                                               \
      : "=&v"(ha[0]), "=&v"(ha[1]), "=&v"(ha[2]), "=&v"(ha[3]), "=&v"(ha[4]), "=&v"(ha[5]), "=&v"(ha[6]), "=&v"(ha[7]), "=&v"(fg)           \
      : "v"(hra), "v"(hrb), "v"(sfa)                                                                                                        \
      : "memory")
    unsigned key = (mkkey(rstd * SUM8(sf[2] + dm * cj8) + sw) | rk2) & kmk;
    asm volatile("" : "+v"(key));
    if (H_O == 0) HSLD("");
    unsigned t1 = key, t2 = 0u;
    TOP2(0x140);
    if (H_O == 1) { asm volatile("" : "+v"(t1), "+v"(t2)); HSLD(""); asm volatile("" : "+v"(t1), "+v"(t2)); }
    TOP2S(__builtin_amdgcn_permlane16_swap);
    if (H_O == 2) { asm volatile("" : "+v"(t1), "+v"(t2)); HSLD(""); asm volatile("" : "+v"(t1), "+v"(t2)); }
    TOP2S(__builtin_amdgcn_permlane32_swap);
    const int l1 = __builtin_ctzll(__builtin_amdgcn_ballot_w64(key == t1)), l2 = __builtin_ctzll(__builtin_amdgcn_ballot_w64(key == t2));
    unsigned mq[6];
    asm volatile("s_bfe_i32 %0, %6, 0x10003\n\ts_bfe_i32 %1, %7, 0x10003\n\ts_bfe_i32 %2, %6, 0x10004\n\ts_bfe_i32 %3, %7, 0x10004\n\t"
                 "s_bfe_i32 %4, %6, 0x10005\n\ts_bfe_i32 %5, %7, 0x10005\n\ts_xor_b32 %4, %4, 0xffff\n\ts_xor_b32 %5, %5, 0xffff"
                 : "=&s"(mq[0]), "=&s"(mq[1]), "=&s"(mq[2]), "=&s"(mq[3]), "=&s"(mq[4]), "=&s"(mq[5]) : "s"(l1), "s"(l2) : "scc");
    const u64 m0 = mk64(mq[1], mq[0]), m1 = mk64(mq[3], mq[2]), m2 = mk64(mq[5], mq[4]);
    asm volatile("s_waitcnt lgkmcnt(0)" : W8(ha), "+v"(fg)::"memory");
    TS(2);
    if (b < NPART) {
#define HBAD ((fg[0] & fg[1] & fg[2] & fg[3]) != one)
      if (__any(HBAD)) POLL2(HBAD, 0, 1 << 26, HSLD("\n\ts_waitcnt lgkmcnt(0)"));
      TS(23);
      f32x4 dg = {0.f, 0.f, 0.f, 0.f}, du = {0.f, 0.f, 0.f, 0.f};
#pragma unroll
      for (int f = 0; f < 8; ++f) {
        dg = __builtin_amdgcn_mfma_f32_16x16x32_bf16(BF8(ha[f]), BF8(gT[f]), dg, 0, 0, 0);
        du = __builtin_amdgcn_mfma_f32_16x16x32_bf16(BF8(ha[f]), BF8(uT[f]), du, 0, 0, 0);
      }
      const bool s0 = __builtin_amdgcn_inverse_ballot_w64(m0), s1 = __builtin_amdgcn_inverse_ballot_w64(m1);
      if (__builtin_amdgcn_inverse_ballot_w64(m2))
        asm volatile("global_store_dword %0, %1, %3 sc1 ; ALWAYS\n\tglobal_store_dword %0, %2, %3 offset:512 sc1" ::"v"(gso),
                     "v"(nsent(s1 ? (s0 ? dg[3] : dg[2]) : (s0 ? dg[1] : dg[0]))), "v"(nsent(s1 ? (s0 ? du[3] : du[2]) : (s0 ? du[1] : du[0]))), "s"(wb) : "memory");
    }
    if (hw == 0 && wb == ws) asm volatile("ds_write_b64 %0, %1" ::"v"(sfa), "v"(u32x2{t1, t2}) : "memory");
    if (b < NPART) {
      TS(24);
      asm volatile("" : W8(wdv), "+v"(qm[0]), "+v"(qm[1]), "+v"(qm[2]), "+v"(qm[3])::"memory");
      TR8(dT, wdv);
      P2PUT(0);
      if (hw == 0) {
        u32x2 rr[8];
        if (E_D) __builtin_amdgcn_s_sleep(E_D);
        TS(39);
        P2GET(((((lane & 16) ? gl1 : gl0) - O_GU) >> 12) * 128 + (lane & 15) * 8);
#define MX2(i) max(max(rr[i][0], rr[i][1]), max(rr[i + 1][0], rr[i + 1][1]))
        POLL2(max(max(MX2(0), MX2(2)), max(MX2(4), MX2(6))) == SENT, 1, PCAP,
              TS(40);
              asm volatile("global_load_dwordx2 %0, %8, %10 sc1\n\tglobal_load_dwordx2 %1, %8, %10 offset:1024 sc1\n\t"
                           "global_load_dwordx2 %2, %8, %10 offset:2048 sc1\n\tglobal_load_dwordx2 %3, %8, %10 offset:3072 sc1\n\t"
                           "global_load_dwordx2 %4, %9, %10 sc1\n\tglobal_load_dwordx2 %5, %9, %10 offset:1024 sc1\n\t"
                           "global_load_dwordx2 %6, %9, %10 offset:2048 sc1\n\tglobal_load_dwordx2 %7, %9, %10 offset:3072 sc1" VW0
                           : "=&v"(rr[0]), "=&v"(rr[1]), "=&v"(rr[2]), "=&v"(rr[3]), "=&v"(rr[4]), "=&v"(rr[5]), "=&v"(rr[6]), "=&v"(rr[7])
                           : "v"(gl0), "v"(gl1), "s"(wb)
                           : "memory"));
        TS(8);
#define R2(i) __builtin_bit_cast(f32x2, rr[i])
        const f32x2 gs = (R2(0) + R2(1)) + (R2(2) + R2(3)), us = (R2(4) + R2(5)) + (R2(6) + R2(7));
        const unsigned gp = pack2(gs[0], gs[1]), up = pack2(us[0], us[1]);
        const unsigned sp = pack2(bflo(gp) * __builtin_amdgcn_rcpf(1.0f + __expf(-bflo(gp))), bfhi(gp) * __builtin_amdgcn_rcpf(1.0f + __expf(-bfhi(gp))));
        asm volatile("ds_write_b32 %0, %1\n\tds_write_b32 %2, %3 offset:24" ::"v"(awa), "v"(pack2(bflo(sp) * bflo(up), bfhi(sp) * bfhi(up))), "v"(sfa), "v"(one) : "memory");
      }
      TS(25);
      SKEWPT();
      POLL2(sv != one, 0, 1 << 26, asm volatile("ds_read_b32 %1, %3 offset:24\n\tds_read_b128 %0, %2\n\ts_waitcnt lgkmcnt(0)" : "=&v"(fg), "=&v"(sv) : "v"(ara), "v"(sfa) : "memory"));
      asm volatile("global_load_dwordx2 %0, %1, %2 sc1" : "=&v"(vd) : "v"(O_VD + (b & 31) * 128), "s"(ws) : "memory");
      const f32x2 dd = MV64(dT, fg);
      asm volatile("global_store_dwordx2 %0, %1, %2 sc1" ::"v"(pso), "v"(u32x2{nsent(dd[0]), nsent(dd[1])}), "s"(wb) : "memory");
      TS(6);
      P2PUT(1);
      asm volatile("s_waitcnt vmcnt(1)" : "+v"(vd)::"memory");
      if (__any(max(vd[0], vd[1]) == SENT)) VDLD(2);
      TS(5);
    } else if (hw == 3) {
      OWNL();
      u32x2 g0, g1;
      nap_tick(th + T_C4);
      POLL2(min(min(g0[0], g0[1]), min(g1[0], g1[1])) == 0u, 2, PCAP,
            asm volatile("global_load_dwordx2 %0, %2, %4 sc1\n\tglobal_load_dwordx2 %1, %3, %4 sc1" VW0
                         : "=&v"(g0), "=&v"(g1) : "v"(O_KEY + lane * 8 * KSTR), "v"(O_KEY + (lane + 64) * 8 * KSTR), "s"(ws) : "memory"));
      const unsigned k1 = t1, k2 = t2;
      t1 = max(g0[0], g1[0]), t2 = max(min(g0[0], g1[0]), max(g0[1], g1[1]));
      TOP2(0xB1); TOP2(0x4E); TOP2(0x141); TOP2(0x140);
      TOP2S(__builtin_amdgcn_permlane16_swap);
      TOP2S(__builtin_amdgcn_permlane32_swap);
      vd = u32x2{max(t1, k1), max(min(t1, k1), max(t2, k2))};
      if (lane == 0) asm volatile("global_store_dwordx2 %0, %1, %2 sc1\n\tds_write_b64 %3, %1 offset:8" ::"v"(O_VD + (b & 31) * 128), "v"(vd), "s"(ws), "v"(sfa) : "memory");
      TS(29);
      break;
    } else if (hw < 2) {
      OWNL();
      const int tok = b & 1, c0 = ((b - NPART) >> 1) * 64 + hw * 32 + (lane & 7) * 4, col = c0 + g4;
      const unsigned td = tick(), kk = tok ? t2 : t1;
      const int ln = tok ? l2 : l1;
      unsigned v;
      POLL2(v == SENT, 1, PCAP, asm volatile("global_load_dword %0, %1, %2 sc1" VW0 : "=&v"(v) : "v"(O_X1C + ((__builtin_amdgcn_readlane(xo_, ln) - O_X1C) >> 11) * 2048 + (col >> 1) * 4), "s"(ws) : "memory"));
      float x1 = (col & 1) ? bfhi(v) : bflo(v);
      float h2 = rbf(((x1 - __int_as_float(__builtin_amdgcn_readlane(__float_as_int(mean), ln))) * __int_as_float(__builtin_amdgcn_readlane(__float_as_int(rstd), ln))) * bf2f(svb[2][col]) + bf2f(svb[1][col]));
      float gb = bf2f(svb[3][col]), rsg = rbf(1.0f / (1.0f + __expf(-key_val(kk))));
      unsigned oo_ = (key_row(kk) * D + col) * 2, po = O_PART + ((lane >> 3) * 2048 + tok * 1024 + c0) * 4, p1 = po + 4 * 65536;
      asm volatile("" : "+v"(x1), "+v"(h2), "+v"(gb), "+v"(rsg), "+v"(oo_), "+v"(po), "+v"(p1));
      u32x4 y[7];
      nap_tick(td + F_T - 10);
      wait_tick(td + F_T);
      TS(37);
      P2GET(8192 + (min(lane, 55) * 16 + ((b - NPART) >> 1)) * 8);
      POLL2(max(max(max(max4(y[0]), max4(y[1])), max(max4(y[2]), max4(y[3]))), max(max(max4(y[4]), max4(y[5])), max4(y[6]))) == SENT || vd[1] == 0u, 0, PCAP,
            TS(38);
            asm volatile("global_load_dwordx4 %0, %7, %9 sc1\n\tglobal_load_dwordx4 %1, %7, %10 sc1\n\tglobal_load_dwordx4 %2, %7, %11 sc1\n\t"
                         "global_load_dwordx4 %3, %7, %12 sc1\n\tglobal_load_dwordx4 %4, %8, %9 sc1\n\tglobal_load_dwordx4 %5, %8, %10 sc1\n\t"
                         "global_load_dwordx4 %6, %8, %11 sc1"
                         : "=&v"(y[0]), "=&v"(y[1]), "=&v"(y[2]), "=&v"(y[3]), "=&v"(y[4]), "=&v"(y[5]), "=&v"(y[6])
                         : "v"(po), "v"(p1), "s"(wb), "s"(wb + 65536), "s"(wb + 131072), "s"(wb + 196608)
                         : "memory");
            asm volatile("s_waitcnt vmcnt(2)\n\tds_read_b64 %7, %8 offset:8\n\ts_waitcnt vmcnt(0) lgkmcnt(0)"
                         : "+v"(y[0]), "+v"(y[1]), "+v"(y[2]), "+v"(y[3]), "+v"(y[4]), "+v"(y[5]), "+v"(y[6]), "=&v"(vd) : "v"(sfa) : "memory"));
      TS(28);
#define YF(i) __builtin_bit_cast(f32x4, y[i])
      const f32x4 t = ((YF(0) + YF(1)) + (YF(2) + YF(3))) + ((YF(4) + YF(5)) + YF(6));
      const float sum = dpp_add<0x128>(swap16_add(swap32_add(t[0], t[2]), swap32_add(t[1], t[3])));
      asm volatile("global_store_short %0, %1, %2" ::"v"(oo_), "v"(pack2(x1 + gb * (wb == ws && kk < vd[1] ? h2 : rbf(h2 + rbf(rbf(sum) * rsg))), 0.f)), "s"(out) : "memory");
      TS(15);
    } else {
      break;
    }
    if (__builtin_expect(wb != ws || __all(vd[1] == t2), 1)) break;
    OWNL();
    const unsigned tk = (lane & 8) ? vd[1] : vd[0], ns = tk == t1 ? l1 >> 3 : 8 + ((lane >> 3) & 1);
    rk2 = tk & 0xffffu, kmk = lane < 16 ? ~0u : 0u, one = 3u;
    xo_ = O_X1C + ns * 2048 + kca, sto = O_ST2 + ns * 128 + (lane & 7) * 16;
    wb = ws + O_P2;
    }
    TSE();
  }
}

extern "C" void kernel_launch(void* const* d_in, const int* in_sizes, int n_in, void* d_out, int out_size, void* d_ws, size_t ws_size,
                              hipStream_t stream) {
  const u16* x = (const u16*)d_in[0];
  const u16* wsr = (const u16*)d_in[1];
  const u16* wsi = (const u16*)d_in[2];
  const u16* whr = (const u16*)d_in[3];
  const u16* whi = (const u16*)d_in[4];
  const u16* w_r = (const u16*)d_in[6];
  const u16* w_gate = (const u16*)d_in[7];
  const u16* w_up = (const u16*)d_in[8];
  const u16* w_down = (const u16*)d_in[9];
  const u16* c = (const u16*)d_in[10];
  const u16* w_mod = (const u16*)d_in[11];
  u16* out = (u16*)d_out;
  k_main<<<256, 512, 0, stream>>>(x, (char*)d_ws, wsr, wsi, c, w_mod, whr, whi, w_r, w_gate, w_up, w_down, out);
}
```

```cpp
#include <hip/hip_runtime.h>
#include <hip/hip_bf16.h>
#include <stdint.h>
#include <utility>

#define NEWLAY 1
typedef unsigned short u16;
typedef unsigned long long u64;
using bf16x8 = __attribute__((ext_vector_type(8))) short;
using bf16v8 = __attribute__((ext_vector_type(8))) __bf16;
using f32x4 = __attribute__((ext_vector_type(4))) float;
using f32x2 = __attribute__((ext_vector_type(2))) float;
using u32x4 = __attribute__((ext_vector_type(4))) unsigned;
using u32x2 = __attribute__((ext_vector_type(2))) unsigned;

constexpr int S = 2048, D = 1024, F = 3584;

__device__ __forceinline__ float bf2f(unsigned v) { return __uint_as_float(v << 16); }
__device__ __forceinline__ float bflo(unsigned v) { return __uint_as_float(v << 16); }
__device__ __forceinline__ float bfhi(unsigned v) { return __uint_as_float(v & 0xffff0000u); }
__device__ __forceinline__ unsigned f2bf(float f) {
  __hip_bfloat16 b = __float2bfloat16(f);
  return (unsigned)*reinterpret_cast<u16*>(&b);
}
__device__ __forceinline__ float rbf(float f) { return bf2f(f2bf(f)); }
using bf16v2 = __attribute__((ext_vector_type(2))) __bf16;
__device__ __forceinline__ unsigned pack2(float lo, float hi) {
  return __builtin_bit_cast(unsigned, __builtin_convertvector(f32x2{lo, hi}, bf16v2));
}

template <int CTRL>
__device__ __forceinline__ float dpp_f(float v) {
  return __int_as_float(__builtin_amdgcn_mov_dpp(__float_as_int(v), CTRL, 0xf, 0xf, true));
}
template <int CTRL>
__device__ __forceinline__ float dpp_add(float v) { return v + dpp_f<CTRL>(v); }
__device__ __forceinline__ float row_sum(float v) { return dpp_add<0x140>(dpp_add<0x141>(dpp_add<0x4E>(dpp_add<0xB1>(v)))); }
template <int CTRL>
__device__ __forceinline__ unsigned dpp_u(unsigned v) { return (unsigned)__builtin_amdgcn_mov_dpp((int)v, CTRL, 0xf, 0xf, true); }
__device__ __forceinline__ u64 mk64(unsigned hi, unsigned lo) { return ((u64)hi << 32) | lo; }
__device__ __forceinline__ float swap32_add(float a, float b) {
  auto r = __builtin_amdgcn_permlane32_swap(__float_as_uint(a), __float_as_uint(b), false, false);
  return __uint_as_float(r[0]) + __uint_as_float(r[1]);
}
__device__ __forceinline__ float swap16_add(float a, float b) {
  auto r = __builtin_amdgcn_permlane16_swap(__float_as_uint(a), __float_as_uint(b), false, false);
  return __uint_as_float(r[0]) + __uint_as_float(r[1]);
}
__device__ __forceinline__ float ar4(float v) {
  const float t = swap32_add(v, v);
  return swap16_add(t, t);
}
__device__ __forceinline__ float wave_sum_s(float v) {
  v = row_sum(v);
  v += __int_as_float(__builtin_amdgcn_update_dpp(0, __float_as_int(v), 0x142, 0xa, 0xf, false));
  v += __int_as_float(__builtin_amdgcn_update_dpp(0, __float_as_int(v), 0x143, 0xc, 0xf, false));
  return __int_as_float(__builtin_amdgcn_readlane(__float_as_int(v), 63));
}
__device__ __forceinline__ float silu_bf(float c) { return rbf(c * __builtin_amdgcn_rcpf(1.0f + __expf(-c))); }
__device__ __forceinline__ float dsum(unsigned v, float a) {
  return __builtin_amdgcn_fdot2_f32_bf16(__builtin_bit_cast(bf16v2, v), __builtin_bit_cast(bf16v2, 0x3f803f80u), a, false);
}
template <class Fn, int... I>
__device__ __forceinline__ void sfor_(Fn&& f, std::integer_sequence<int, I...>) { (f(std::integral_constant<int, I>{}), ...); }
template <int N, class Fn>
__device__ __forceinline__ void sfor(Fn&& f) { sfor_(f, std::make_integer_sequence<int, N>{}); }
constexpr unsigned SENT = 0xFFFFFFFFu;
constexpr int NPART = 224;

constexpr size_t WS_EPO = 0;
constexpr size_t WS_XFL = 4096;
constexpr size_t WS_MODP = 40960;
constexpr size_t WS_KEYG = 320u << 10;
constexpr size_t WS_UFL = WS_KEYG + 16384;
constexpr size_t WS_MOD = WS_KEYG + (80u << 10);
constexpr size_t WS_RS = WS_KEYG + (92u << 10);
constexpr size_t WS_PART = WS_KEYG + (332u << 10);
constexpr unsigned O_MODP = WS_MODP, O_XFL = WS_XFL, O_KEY = WS_KEYG, O_MOD = WS_MOD, O_RS = WS_RS, O_M2 = WS_RS + 80 * 1024;
constexpr unsigned O_PART = WS_PART, O_GU = O_PART + 56 * 8192, O_STAT = O_GU + 224 * 1024, O_X1C = O_STAT + 262144;
constexpr unsigned O_ST2 = O_X1C + 20480, O_VD = O_ST2 + 6144, O_P2 = 1536u << 10, O_F2 = 3584u << 10;
constexpr unsigned O_GC = O_M2 + 8704, O_VP = O_M2 + 12288, O_NP = O_VP + 65536, O_K2 = O_NP + 65536, O_QT = O_K2 + 4096, O_G2 = 3u << 20, O_G1 = O_G2 + 262144;
static_assert(O_QT + 16384 == O_PART && O_VD + 4096 <= O_KEY + (512 + 832) * 1024 && O_PART + O_P2 >= O_VD + 4096 && O_STAT + O_P2 <= O_G2, "armed area");
constexpr size_t WS_H = 4u << 20;
constexpr size_t WS_T = WS_H + (size_t)S * D * 2;
constexpr size_t WS_P = WS_T + (size_t)2 * D * S * 2;

#define TSG(base, i)
#define TSD(i)
#define PCAP (1 << 20)
#define PDBG(it)
#define TS(i)
#define TSE()
#define BT(k)
#define BTE()
#define BAR(k) do { BT(k); BARRIER(); } while (0)
#define WAIT_V(n) asm volatile("s_waitcnt vmcnt(%0)" ::"n"(n) : "memory")
#define WAIT_L(n) asm volatile("s_waitcnt lgkmcnt(%0)" ::"n"(n) : "memory")
#define BARRIER() asm volatile("s_barrier" ::: "memory")
#define LBAR() asm volatile("s_waitcnt lgkmcnt(0)\n\ts_barrier" ::: "memory")
#define DSR(dst, addr, off) asm volatile("ds_read_b128 %0, %1 offset:%2" : "=v"(dst) : "v"(addr), "n"(off) : "memory")
#define MFMA(c, a, b) asm volatile("v_mfma_f32_16x16x32_bf16 %0, %1, %2, %0" : "+v"(c) : "v"(a), "v"(b))
#define MFMA0(c, a, b) asm volatile("v_mfma_f32_16x16x32_bf16 %0, %1, %2, 0" : "=&v"(c) : "v"(a), "v"(b))
#define GLDS(m0v, voff, gbase) \
  asm volatile("s_mov_b32 m0, %0\n\ts_nop 0\n\tglobal_load_lds_dwordx4 %1, %2" ::"s"(m0v), "v"(voff), "s"(gbase) : "memory")
#define GLDS4(m0v, v, gbase)                                                                                                        \
  asm volatile("s_mov_b32 m0, %0\n\ts_nop 0\n\tglobal_load_lds_dwordx4 %1, %5\n\tglobal_load_lds_dwordx4 %2, %5 offset:1024\n\t"    \
               "global_load_lds_dwordx4 %3, %5 offset:2048\n\tglobal_load_lds_dwordx4 %4, %5 offset:3072" ::"s"(m0v),               \
               "v"((v)[0]), "v"((v)[1]), "v"((v)[2]), "v"((v)[3]), "s"(gbase) : "memory")
#define LDG(dst, voff, base) asm volatile("global_load_dwordx4 %0, %1, %2" : "=v"(dst) : "v"(voff), "s"(base) : "memory")
#define HPOL ""
#define XPOL ""
#define WPOL " nt"
#define LDM(dst, voff, base) asm volatile("global_load_dwordx4 %0, %1, %2" WPOL : "=v"(dst) : "v"(voff), "s"(base) : "memory")
#define LDX(dst, voff, base, off) asm volatile("global_load_dwordx4 %0, %1, %2 offset:%3" : "=v"(dst) : "v"(voff), "s"(base), "n"(off) : "memory")
#define LDXS(dst, voff, base, off) asm volatile("global_load_dwordx4 %0, %1, %2 offset:%3 sc1" : "=v"(dst) : "v"(voff), "s"(base), "n"(off) : "memory")
#define BF8(v) __builtin_bit_cast(bf16v8, v)
#define LSPIN(v, p, cond)                                   \
  _Pragma("unroll 1") for (int it_ = 0; it_ < (1 << 26); ++it_) { \
    v = __atomic_load_n(p, __ATOMIC_RELAXED);               \
    if (cond) break;                                        \
    __builtin_amdgcn_s_sleep(1);                            \
  }
#define POLL(ok, sl, reload)                                                  \
  if (__builtin_expect(!(ok), 0)) {                                           \
    _Pragma("unroll 1") for (int it = 0; it < PCAP; ++it) {                   \
      PDBG(it);                                                               \
      __builtin_amdgcn_s_sleep(sl);                                           \
      reload;                                                                 \
      if (ok) break;                                                          \
    }                                                                         \
  }
#define POLL2(bad, sl, cap, load)                                             \
  {                                                                           \
    int it = (cap);                                                           \
    u64 live;                                                                 \
    _Pragma("unroll 1") do {                                                  \
      if (sl) __builtin_amdgcn_s_sleep(sl);                                   \
      asm volatile("s_sub_u32 %0, %0, 1\n\ts_cselect_b64 %1, 0, -1" : "+s"(it), "=s"(live)::"scc"); \
      load;                                                                   \
    } while (__builtin_amdgcn_ballot_w64(bad) & live);                        \
    PDBG(PCAP - 2 - it);                                                      \
  }
__device__ __forceinline__ unsigned tick() {
  u64 t;
  asm volatile("s_memrealtime %0\n\ts_waitcnt lgkmcnt(0)" : "=s"(t)::"memory");
  return (unsigned)t;
}
__device__ __forceinline__ void wait_tick(unsigned t) {
#pragma unroll 1
  for (int i = 0; i < 256; ++i)
    if ((int)(tick() - t) >= 0) break;
}
__device__ __forceinline__ void nap_tick(unsigned t) {
#pragma unroll 1
  for (int i = 0; i < 1024; ++i) {
    if ((int)(tick() - t) >= 0) break;
    __builtin_amdgcn_s_sleep(2);
  }
}
#define T_C1 245
#define T_A 44
#define T_N 2500
#define T_D1 78
#define VW0 "\n\ts_waitcnt vmcnt(0)"
#define W8(a) "+v"(a[0]), "+v"(a[1]), "+v"(a[2]), "+v"(a[3]), "+v"(a[4]), "+v"(a[5]), "+v"(a[6]), "+v"(a[7])
#define PFL(ptr) asm volatile("s_mov_b32 m0, %0\n\ts_nop 0\n\tglobal_load_lds_dword %1, off" ::"s"(pfl), "v"(ptr) : "memory")

#define CPFL() asm volatile("s_mov_b32 m0, %0\n\ts_mov_b64 exec, 0xfff\n\tglobal_load_lds_dword %1, off\n\ts_mov_b64 exec, -1" ::"s"(pfl), "v"(cpf) : "memory")
#define HHOOK(j)
#define XPV 1557
#define XPW 1557
#define XPM (MODE ? XPW : XPV)
#define XPA0 (XPM / 1000)
#define XPA1 (XPM / 100 % 10)
#define XPB0 (XPM / 10 % 10)
#define XPB1 (XPM % 10)
#define U_X0 6
#define XLD(dst, XP, XH, i)                                                                                   \
  do {                                                                                                        \
    if (MODE) LDXS(dst, (i) ? xo1 : xo0, XP, ((XH)&3) * 2048 - 4096);                                         \
    else LDXS(dst, ((XH)&4) ? xo1 : xo0, XP, ((XH)&3) * 2048 + (i)*1024 - 4096);                              \
  } while (0)
#define HALF(XC, YA, RD, XN, XP, XH, LD, YP)                                                 \
  do {                                                                                       \
    _Pragma("unroll") for (int j = 0; j < 8; ++j) {                                          \
      if ((j & 1) == 0) { if (RD) WAIT_L(5); else WAIT_L(j == 0 ? 5 : 6 - j); }              \
      if (MODE == 2) MFMA0(acc[0][j], Y[j], XC[0]); else if (MODE) MFMA(acc[0][j], Y[j], XC[0]); else MFMA(acc[0][j], XC[0], Y[j]); \
      if (j == 0) DSR(Y[7], YP, 7 * 2048);                                                   \
      else if (RD) DSR(Y[j - 1], YA, (j - 1) * 2048);                                        \
      if (MODE == 2) MFMA0(acc[1][j], Y[j], XC[1]); else if (MODE) MFMA(acc[1][j], Y[j], XC[1]); else MFMA(acc[1][j], XC[1], Y[j]); \
      if ((LD) && j == (((XH)&1) ? XPA0 : XPB0)) XLD(XN[0], XP, XH, 0);                                             \
      if ((LD) && j == (((XH)&1) ? XPA1 : XPB1)) XLD(XN[1], XP, XH, 1);                                             \
      HHOOK(j);                                                                              \
    }                                                                                        \
  } while (0)

__device__ __forceinline__ int key_row(unsigned k) { return (int)((~k >> 1) & 2047u); }
__device__ __forceinline__ unsigned mkkey(float v) {
  const unsigned rb = pack2(0.f, v);
  return rb ^ ((unsigned)((int)rb >> 31) & 0x7fff0000u) ^ 0x80000000u;
}
__device__ __forceinline__ float key_val(unsigned k) {
  const unsigned ord = k >> 16;
  return bf2f(ord ^ ((ord & 0x8000u) ? 0x8000u : 0xffffu));
}
__device__ __forceinline__ unsigned nsent(float v) { return min(__float_as_uint(v), SENT - 1u); }
__device__ __forceinline__ f32x4 lo4(u32x4 q) { return f32x4{bflo(q[0]), bfhi(q[0]), bflo(q[1]), bfhi(q[1])}; }
__device__ __forceinline__ f32x4 hi4(u32x4 q) { return f32x4{bflo(q[2]), bfhi(q[2]), bflo(q[3]), bfhi(q[3])}; }
__device__ __forceinline__ u32x4 pk8(f32x4 a, f32x4 b) { return u32x4{pack2(a[0], a[1]), pack2(a[2], a[3]), pack2(b[0], b[1]), pack2(b[2], b[3])}; }
__device__ __forceinline__ u32x4 nsent4(u32x4 v) { return u32x4{min(v[0], SENT - 1u), min(v[1], SENT - 1u), min(v[2], SENT - 1u), min(v[3], SENT - 1u)}; }
__device__ __forceinline__ unsigned max4(u32x4 v) { return max(max(v[0], v[1]), max(v[2], v[3])); }
__device__ __forceinline__ float amin16(const u32x4* v) {
  float a, b;
  asm("v_min3_f32 %0, |%2|, |%3|, |%4|\n\tv_min3_f32 %1, |%5|, |%6|, |%7|\n\tv_min3_f32 %0, |%8|, |%9|, %0\n\tv_min3_f32 %1, |%10|, |%11|, %1\n\t"
      "v_min3_f32 %0, |%12|, |%13|, %0\n\tv_min3_f32 %1, |%14|, |%15|, %1\n\tv_min3_f32 %0, |%16|, |%17|, %0\n\tv_min_f32 %0, %0, %1"
      : "=&v"(a), "=&v"(b)
      : "v"(v[0][0]), "v"(v[0][1]), "v"(v[0][2]), "v"(v[0][3]), "v"(v[1][0]), "v"(v[1][1]), "v"(v[1][2]), "v"(v[1][3]), "v"(v[2][0]), "v"(v[2][1]),
        "v"(v[2][2]), "v"(v[2][3]), "v"(v[3][0]), "v"(v[3][1]), "v"(v[3][2]), "v"(v[3][3]));
  return a;
}
__device__ __forceinline__ u32x4 selu(unsigned mk, u32x4 a, u32x4 b) { return (a & mk) | (b & ~mk); }
#define KSTR 16
#define TOP2(CTRL)                                                  \
  do {                                                              \
    const unsigned o1 = dpp_u<CTRL>(t1), o2 = dpp_u<CTRL>(t2);      \
    t2 = max(min(t1, o1), max(t2, o2));                             \
    t1 = max(t1, o1);                                               \
  } while (0)
#define TOP2S(SW)                                                   \
  do {                                                              \
    auto a_ = SW(t1, t1, false, false), b_ = SW(t2, t2, false, false); \
    t2 = max(min(a_[0], a_[1]), max(b_[0], b_[1]));                 \
    t1 = max(a_[0], a_[1]);                                         \
  } while (0)
#define T_C3 253
#define T_C4 340
#define E_D 6
#define O_DW 6
#define O_DS 4
#define OPOL " sc1"
#define X_PA 2
#define G1_C 2
#define R_AT 24
#define B_TK 7
#define W_T 4
#define V_K 3
#define V_T 5
#define N_K 9
#define N_D 2
#define T_K 0
#define H_AT 23
#define G1_S 1
#define F_T 142
#define K_S 1
#define N_C (V_T + 1)
#define T_O 260
#define A_S1 9
#define A_S2 9
#define A_P2 12
#define X_K 13
#define H_P 2
#define H_O 2
#define S_R 2
#define T_X 80
#define A_PS 3
#define U_S 4
#define C_LD 2
#define C_DP 8
#define F_D 2
#define F_AT 29
#define PW0 2
#define T_W 70
#define P_D2 1
#define P_D4 1
#define XSTR 16
#define P_D3 20
#ifndef M_NS
#define M_NS 6
#endif
#define A_Q1 27
#define B_RAT 5
#define B_MAT 13
#define U_D 4
#define FSTR 16
#define B_J 4
#define RETIRE()                                                                                                              \
  do {                                                                                                                        \
    if (hw == 3 && lane < (b == 255 ? 2 : 1))                                                                                 \
      asm volatile("global_store_dwordx2 %0, %1, %2 sc1" ::"v"(lane ? (unsigned)WS_EPO : O_XFL + b * XSTR * 4), "v"(lane ? tag : ~tag), "s"(ws) : "memory"); \
  } while (0)
#define U_N1 3
#define B_QAT 12
#define B_QD 2
#define TP_PS 5
#define TP_XC 34
#define TP_P0 2
#define TP_X0 24
#define TP_XL 54
#define W_UO 3
#define CPF_K 24
#define CPF_B 8
#define B_WN 4
#define B_WK 15
struct BSched {
  int cnt[16], rcnt, mcnt, qcnt, vcnt, ncnt;
  constexpr BSched() : cnt{}, rcnt(0), mcnt(0), qcnt(0), vcnt(0), ncnt(0) {
    int pos[16] = {}, n = 0, rpos = 0, mpos = 0, qpos = 0, npos = 0;
    for (int k = 0; k <= B_J - 2 + M_NS; ++k) pos[k] = -8 * (B_J - 2 + M_NS - k);
    for (int k = B_J + 1; k < 16; ++k) {
      cnt[k] = n - pos[k] > 63 ? 63 : n - pos[k];
      if (k - 2 + M_NS < 16) { n += 4; pos[k - 2 + M_NS] = n; }
      if (k == B_RAT + 2) rcnt = n - rpos;
      if (k == B_QAT + B_QD) qcnt = n - qpos;
      if (k == V_T) vcnt = n;
      if (k == N_K + N_D) ncnt = n - npos;
      if (k == N_K) npos = n;
      if (k == B_RAT) rpos = n;
      if (k == X_K) n += 4;
      if (k == B_QAT) { n += 1; qpos = n; }
      if (k == B_MAT) { n += 2; mpos = n; }
      if (k == B_WK) n += B_WN;
    }
    mcnt = n - mpos > 63 ? 63 : n - mpos;
  }
};
__global__ __launch_bounds__(512, 2) void k_main(const u16* __restrict__ x, char* ws, const u16* __restrict__ Y0, const u16* __restrict__ Y1,
                                              const u16* __restrict__ cvec, const u16* __restrict__ wmod, const u16* __restrict__ X0,
                                              const u16* __restrict__ X1, const u16* __restrict__ wr_, const u16* __restrict__ wg,
                                              const u16* __restrict__ wu, const u16* __restrict__ wd, u16* __restrict__ out) {
  constexpr int DX = 4, NS = M_NS, STG = NS * 16384, SPARE = STG + 32768;
  constexpr int LST = SPARE + 17664, SLT = LST + 256, XC = SLT + 512, VT = XC + 4096;
  __shared__ __attribute__((aligned(1024))) char shm[VT + 256];
  u16(*svb)[1024] = reinterpret_cast<u16(*)[1024]>(shm + SPARE);
  float* cwj = reinterpret_cast<float*>(shm + SPARE + 10240);
  unsigned* ssel = reinterpret_cast<unsigned*>(shm + SPARE + 10368);
  float* smod = reinterpret_cast<float*>(shm + SPARE + 15872);
  u16* ssa = reinterpret_cast<u16*>(smod);
  constexpr int TAB = SPARE + 10752, RKA = TAB + 1536, RKB = TAB + 3584;
  u16(*scw)[128] = reinterpret_cast<u16(*)[128]>(shm + TAB + 512);
  const int b = blockIdx.x, xcd = b & 7, j8 = b >> 3;
  const int cc = xcd >> 2, nt = (xcd & 3) * 4 + (j8 & 3), mt = j8 >> 2, et = mt;
  const int tid = threadIdx.x, lane = tid & 63;
  const int wid = __builtin_amdgcn_readfirstlane(tid >> 6);
  const unsigned lds0 = (unsigned)(size_t)(__attribute__((address_space(3))) char*)shm;
  const char* cpf;
  {
    unsigned long long pc;
    unsigned len;
    asm volatile("s_getpc_b64 %0\n.Lcpf_here:\n\ts_mov_b32 %1, .Lcpf_end-.Lcpf_here-4\n\t.subsection 1\n.Lcpf_end:\n\t.subsection 0" : "=s"(pc), "=s"(len));
    cpf = reinterpret_cast<const char*>(pc) + min((unsigned)((lane * 32 + j8) * 128), len);
  }
  const unsigned pfl = lds0 + SPARE + 17408;
  if (wid == 4) CPFL();
  u32x4 xv[2];
  {
    const unsigned xo = (unsigned)((b * 8 + wid) * D + lane * 8) * 2;
    asm volatile("global_load_dwordx4 %0, %2, %3" XPOL "\n\tglobal_load_dwordx4 %1, %2, %3 offset:1024" XPOL : "=&v"(xv[0]), "=&v"(xv[1]) : "v"(xo), "s"(x) : "memory");
  }
  asm volatile("" ::"s"(ws), "s"(Y0), "s"(Y1), "s"(cvec), "s"(wmod), "s"(wr_));
  u16* xn = reinterpret_cast<u16*>(ws + WS_H);
  u16* U = reinterpret_cast<u16*>(ws + WS_T);
  unsigned* part = reinterpret_cast<unsigned*>(ws + WS_PART);
  const int g = (b & 7) * 28 + (b >> 3);
  unsigned* ufl = reinterpret_cast<unsigned*>(ws + WS_UFL) + (cc * 16 + nt) * 32 * FSTR;
  unsigned* epo = reinterpret_cast<unsigned*>(ws + WS_EPO);
#define SKEWPT()
  const int g4 = lane >> 4;
  u64 tag0, tag;
#define TAGW(n) asm volatile("s_waitcnt vmcnt(%2)\n\tv_lshl_add_u64 %0, %1, 0, 1" : "=v"(tag) : "v"(tag0), "n"(n) : "memory")
  u32x4 wv[8], cq;
  const char* pfy = reinterpret_cast<const char*>(cc ? Y1 : Y0) + (size_t)((xcd & 3) * 512 + j8 * 16 + (lane & 15)) * 4096 + g4 * 128;
#define PFG(g) (pfy + ((((g) + xcd) & 7) << 9))
  const int u = b * 2 + (wid & 1), cg0 = u & 31, ks0 = u >> 5;
  unsigned ta = lds0 + STG + ((lane >> 2) & 3) * 2368 + (wid * 128 + (lane >> 5) * 32 + (lane & 3) * 8 + (g4 & 1) * 4) * 2;
  unsigned oo = (unsigned)((wid * 4 + (lane >> 5)) * 131072 + (b >> 2) * 2048 + (g4 & 1) * 1024 + ((b & 3) * 16 + (lane & 15)) * 16), oo2 = oo + 262144;
  asm volatile("" : "+v"(ta), "+v"(oo), "+v"(oo2));
  unsigned wa = lds0 + STG + wid * 2368 + lane * 16;
  asm volatile("" : "+v"(wa));
  const unsigned wo = (unsigned)((ks0 * 64 + (lane >> 3) * 8) * 6144 + 2048 + (cg0 >> 4) * 1024 + cg0 * 64 + (lane & 7) * 8) * 2;
  if (wid == 0) TSG(part, 0);
  BT(50);
  if (wid >= 3) {
    asm volatile("global_load_dwordx2 %0, %1, off sc1" : "=v"(tag0) : "v"(epo) : "memory");
    if (wid >= 6) {
      LDG(cq, (ks0 * 64 + g4 * 16 + (lane & 1) * 8) * 2, cvec);
#pragma unroll
      for (int i = 0; i < 8; ++i) LDM(wv[i], wo + i * 12288, wmod);
      const unsigned av = u < 80 ? 0u : ~0u;
      asm volatile("global_store_dwordx4 %0, %1, %2 sc1\n\ts_nop 1" ::"v"(O_KEY + u * 1024 + lane * 16), "v"(u32x4{av, av, av, av}), "s"(ws) : "memory");
      WAIT_V(11);
    } else if (wid == 4) {
      __builtin_amdgcn_s_sleep(C_DP);
      PFL(PFG(0));
      WAIT_V(2);
    } else {
      WAIT_V(1);
    }
  } else {
    WAIT_V(0);
  }
  {
    asm volatile("" : "+v"(xv[0]), "+v"(xv[1])::"memory");
    if (wid == 1) TSG(part, 2);
    BT(51);
    const float s0 = dsum(xv[0][3], dsum(xv[0][2], dsum(xv[0][1], dsum(xv[0][0], 0.f))));
    const float s1 = dsum(xv[1][3], dsum(xv[1][2], dsum(xv[1][1], dsum(xv[1][0], 0.f))));
    const float mf = wave_sum_s(s0 + s1) * (1.0f / D), m = rbf(mf);
    const f32x4 d0 = lo4(xv[0]) - m, d1 = hi4(xv[0]) - m, d2 = lo4(xv[1]) - m, d3 = hi4(xv[1]) - m;
    const f32x4 q = (d0 * d0 + d1 * d1) + (d2 * d2 + d3 * d3);
    const float vf = wave_sum_s((q[0] + q[1]) + (q[2] + q[3])) * (1.0f / D) - (mf - m) * (mf - m);
    const float rden = __builtin_amdgcn_rcpf(rbf(__builtin_amdgcn_sqrtf(rbf(rbf(vf) + 1e-5f))));
    asm volatile("ds_write_b128 %0, %1" ::"v"(wa), "v"(pk8(d0 * rden, d1 * rden)) : "memory");
    asm volatile("ds_write_b128 %0, %1 offset:1024" ::"v"(wa), "v"(pk8(d2 * rden, d3 * rden)) : "memory");
    if (wid == 1) TSG(part, 12);
    BT(52);
    LBAR();
    if (wid == 1) TSG(part, 10);
    BT(53);
    u32x2 t[4];
    asm volatile(
        "ds_read_b64_tr_b16 %0, %4\n\tds_read_b64_tr_b16 %1, %4 offset:%5\n\t"
        "ds_read_b64_tr_b16 %2, %4 offset:128\n\tds_read_b64_tr_b16 %3, %4 offset:%6\n\ts_waitcnt lgkmcnt(0)"
        : "=&v"(t[0]), "=&v"(t[1]), "=&v"(t[2]), "=&v"(t[3])
        : "v"(ta), "n"(4 * 2368), "n"(4 * 2368 + 128)
        : "memory");
    asm volatile("global_store_dwordx4 %0, %2, %4 sc1\n\tglobal_store_dwordx4 %1, %3, %4 sc1\n\ts_nop 1" ::"v"(oo), "v"(oo2),
                 "v"(u32x4{t[0][0], t[0][1], t[1][0], t[1][1]}), "v"(u32x4{t[2][0], t[2][1], t[3][0], t[3][1]}), "s"(xn)
                 : "memory");
    if (wid == 2) TSG(part, 14);
    BT(54);
  }
  u32x2 vd;
#define VDLD(sl) POLL2(max(vd[0], vd[1]) == SENT, sl, PCAP, asm volatile("global_load_dwordx2 %0, %1, %2 sc1" VW0 : "=&v"(vd) : "v"(O_VD + (b & 31) * 128), "s"(ws) : "memory"))
  if (wid < 4) {
    const unsigned ya0 = lds0 + (lane & 15) * 128 + (g4 ^ (lane & 7)) * 16;
    bf16x8 X[DX][2][2], Y[8];
    f32x4 acc[2][8];
    {
      constexpr int MODE = 0;
      const unsigned xo0 = lane * 16 + 4096, xo1 = xo0 + 8192;
      const char* xb = reinterpret_cast<const char*>(xn) + (size_t)(mt * 4 + wid) * 131072;
#define XB(i) (xb + (((xcd + (i)) & 7) << 14))
#pragma unroll
      for (int i = 0; i < 2; ++i)
#pragma unroll
        for (int j = 0; j < 8; ++j) acc[i][j] = f32x4{0.f, 0.f, 0.f, 0.f};
      asm volatile("" : W8(acc[0]), W8(acc[1]));
      __builtin_amdgcn_s_setprio(1);
      WAIT_V(0);
      if (wid == 0) TSG(part, 1);
      const char* xpb = XB(0);
      BAR(55);
      if (wid == 3) {
        TAGW(0);
        const unsigned fp = O_XFL + ((xcd * 32 + lane) & 255) * XSTR * 4;
        if (P_D3) __builtin_amdgcn_s_sleep(P_D3);
#pragma unroll 1
        for (int it = 0; it < PCAP; ++it) { PDBG(it);
          u64 f;
          if (it < 2) BT(58 + 2 * it);
          asm volatile("global_load_dwordx2 %0, %1, %2 sc1\n\ts_waitcnt vmcnt(0)" : "=&v"(f) : "v"(fp), "s"(ws) : "memory");
          if (it < 2) BT(59 + 2 * it);
          if (__all(f == tag)) break;
          __builtin_amdgcn_s_sleep(P_D4);
        }
      }
      BAR(0);
      if (wid == 1) TSG(part, 3);
#pragma unroll
      for (int h = 0; h < 7; ++h) {
        XLD(X[h >> 1][h & 1][0], xpb, h, 0);
        XLD(X[h >> 1][h & 1][1], xpb, h, 1);
      }
#pragma unroll
      for (int j = 0; j < 7; ++j) DSR(Y[j], ya0, j * 2048);
      unsigned so = 0, a0 = ya0;
      f32x4 c0, c1;
#pragma unroll 1
      for (int it = 0; it < 7; ++it) {
        const char* xpa = xpb;
        xpb = XB(it + 1);
#pragma unroll
        for (int s = 0; s < 4; ++s) {
          const unsigned a1 = (ya0 ^ 64) + so;
          so = so == (NS - 1) * 16384 ? 0 : so + 16384;
          WAIT_V(12);
          HALF(X[s][0], a1, true, X[(s + 3) & 3][1], (s == 0 ? xpa : xpb), (2 * s + 7) & 7, true, a0);
          a0 = ya0 + so;
          BAR(1 + it * 4 + s);
          WAIT_V(12);
          HALF(X[s][1], a0, true, X[s][0], xpb, 2 * s, true, a1);
        }
      }
#pragma unroll
      for (int s = 0; s < 4; ++s) {
        const unsigned a1 = (ya0 ^ 64) + so;
        so = so == (NS - 1) * 16384 ? 0 : so + 16384;
        WAIT_V(s == 0 ? 12 : 14 - 4 * s);
        HALF(X[s][0], a1, true, X[3][1], xpb, 7, s == 0, a0);
        a0 = ya0 + so;
        if (s < 3) BAR(29 + s);
        WAIT_V(12 - 4 * s);
        if (s == 2) {
          DSR(c0, lds0 + SPARE + 15872 + 512 + wid * 128 + g4 * 32, 0);
          DSR(c1, lds0 + SPARE + 15872 + 512 + wid * 128 + g4 * 32, 16);
        }
        if (s < 3) HALF(X[s][1], a0, true, X[s][0], xpb, 0, false, a1);
      }
      const char* up = reinterpret_cast<const char*>(U) + (size_t)cc * 4194304 + (size_t)(nt * 4) * 65536 + (mt * 4 + wid) * 2048;
      const unsigned ul = lds0 + STG + wid * 1024 + lane * 16;
#define UEP(j)                                                                                                                             \
  do {                                                                                                                                     \
    u32x4 v_;                                                                                                                              \
    asm volatile("v_pk_mul_f32 %0, %0, %4\n\tv_pk_mul_f32 %1, %1, %5\n\tv_pk_mul_f32 %2, %2, %6\n\tv_pk_mul_f32 %3, %3, %7"            \
                 : "+v"(acc[0][j].lo), "+v"(acc[0][j].hi), "+v"(acc[1][j].lo), "+v"(acc[1][j].hi)                                          \
                 : "v"(c0.lo), "v"(c0.hi), "v"(c1.lo), "v"(c1.hi));                                                                        \
    asm volatile("v_cvt_pk_bf16_f32 %0, %4, %5\n\tv_cvt_pk_bf16_f32 %1, %6, %7\n\tv_cvt_pk_bf16_f32 %2, %8, %9\n\tv_cvt_pk_bf16_f32 %3, %10, %11" \
                 : "=&v"(v_[0]), "=&v"(v_[1]), "=&v"(v_[2]), "=&v"(v_[3])                                                                  \
                 : "v"(acc[0][j][0]), "v"(acc[0][j][1]), "v"(acc[0][j][2]), "v"(acc[0][j][3]), "v"(acc[1][j][0]), "v"(acc[1][j][1]),       \
                   "v"(acc[1][j][2]), "v"(acc[1][j][3]));                                                                                  \
    asm volatile("global_store_dwordx4 %0, %2, %3 offset:%4 sc1\n\tds_write_b128 %1, %2 offset:%5\n\ts_nop 0"                          \
                 ::"v"(lane * 16), "v"(ul), "v"(v_), "s"(up + ((j) >> 1) * 65536), "n"(((j)&1) * 1024), "n"((j)*4096) : "memory");         \
  } while (0)
      {
        const unsigned a1 = (ya0 ^ 64) + (31 % NS) * 16384;
        DSR(Y[7], a1, 7 * 2048);
        WAIT_L(6);
#pragma unroll
        for (int j = 0; j < 8; ++j) {
          if (j == 2) WAIT_L(0);
          MFMA(acc[0][j], X[3][1][0], Y[j]);
          MFMA(acc[1][j], X[3][1][1], Y[j]);
          if (j >= 2) UEP(j - 2);
        }
      }
      if (wid == 0) TSG(part, 4);
      TS(0);
      SKEWPT();
      asm volatile("s_nop 3");
#pragma unroll
      for (int j = 6; j < 8; ++j) UEP(j);
    }
    const int rg = nt * 8 + wid * 2 + cc, row0 = rg * 16 + (lane & 15), col0 = et * 128 + g4 * 8;
    u32x4 xq[4], pv[4], sa4[4];
    f32x4 ga[8];
    f32x2 cj;
    float sw, ce;
    u32x4 dk;
    unsigned spo, soo, th, slt, slf, sla, tha, xca, sca;
    u32x4 e0, e1;
    const char* xr = reinterpret_cast<const char*>(xn) + mt * 524288 + (cc * 16 + nt) * 16384 + wid * 4096;
#define XCHLD()                                                                                                     \
  asm volatile(                                                                                                     \
      "global_load_dwordx4 %0, %4, %5 sc1\n\tglobal_load_dwordx4 %1, %4, %5 offset:1024 sc1\n\t"                   \
      "global_load_dwordx4 %2, %4, %5 offset:2048 sc1\n\tglobal_load_dwordx4 %3, %4, %5 offset:3072 sc1"            \
      : "=&v"(pv[0]), "=&v"(pv[1]), "=&v"(pv[2]), "=&v"(pv[3])                                                      \
      : "v"(lane * 16), "s"(xr)                                                                                     \
      : "memory")
    {
      constexpr int MODE = 1;
      const char* ub = reinterpret_cast<const char*>(U) + (size_t)cc * 4194304 + (size_t)(nt * 4 + wid) * 65536;
      const unsigned xo0 = lane * 16 + cc * 1024 + 4096, xo1 = xo0 ^ 1024;
      BT(32);
      LBAR();
      if (wid == 1) TSG(part, 6);
      {
        const unsigned sa = lds0 + STG + wid * 8192 + lane * 16 + cc * 4096;
#pragma unroll
        for (int q = 0; q < 8; ++q) DSR(X[(q >> 1) & 1][q & 1][q >> 2], (q >> 2) ? sa ^ 4096 : sa, (q & 3) * 1024);
        const unsigned a0 = ya0 + (32 % NS) * 16384;
#pragma unroll
        for (int j = 0; j < 7; ++j) DSR(Y[j], a0, j * 2048);
      }
#define UFLAG()                                                                                                         \
  do {                                                                                                                  \
    WAIT_V(0);                                                                                                          \
    if (lane == 0) __hip_atomic_store(ufl + (mt * 4 + wid) * FSTR, 1u, __ATOMIC_RELAXED, __HIP_MEMORY_SCOPE_AGENT);     \
    if (wid == 0) TSG(part, 5);                                                                                         \
  } while (0)
#define XP(h) (ub + (((mt + ((h) >> 2)) & 7) << 13))
#define XS(h) X[((h) >> 1) & 3][(h)&1]
#define XW(h) do { if ((h) >= 4) WAIT_V(2 * (((h) + 6 > 31 ? 31 : (h) + 6) - (h)) ); } while (0)
      constexpr int H0 = 24, NP = 64, NE = 2 * NP + 8;
      const char* xw;
      unsigned c128, xoff[4], rka, rkb, ea, eb, cwa, cwu;
      auto ESET = [&]() __attribute__((always_inline)) {
        int ln_ = lane;
        asm volatile("" : "+v"(ln_));
        const int lane = ln_, g4 = lane >> 4, row0 = rg * 16 + (lane & 15), col0 = et * 128 + g4 * 8;
        xw = reinterpret_cast<const char*>(xn) + mt * 524288 + ((cc ^ 1) * 16 + nt) * 16384 + wid * 4096;
        asm volatile("" : "+s"(xw), "+s"(xr));
        c128 = 0x43000000u;
        rka = lds0 + RKA + (lane & 15) * 16, rkb = lds0 + RKB + (g4 ? 1024 : wid * 256 + lane * 16);
        ea = lds0 + TAB + g4 * 32, eb = lds0 + TAB + 512 + (lane & 3) * 256 + g4 * 16;
#pragma unroll
        for (int q = 0; q < 4; ++q) {
          dk[q] = (lane & 3) == q ? ~0u : 0u;
          xoff[q] = (lds0 + STG + wid * 8192 + (lane & 15) * 256 + ((g4 ^ lane) & 15) * 16) ^ (q * 64);
        }
        spo = O_STAT + rg * 2048 + g4 * 512 + (lane & 15) * 16, soo = O_STAT + rg * 2048 + et * 256 + (lane & 15) * 16;
        cwa = lds0 + SPARE + 10240 + g4 * 8, cwu = lds0 + SPARE + 10240 + et * 4;
        const unsigned one = g4 == ((lane >> 2) & 3) ? ((lane & 1) ? 0x1f800000u : 0x3f80u) : 0u;
        e0 = u32x4{(lane & 2) ? 0u : one, (lane & 2) ? one : 0u, 0u, 0u}, e1 = u32x4{0u, 0u, e0[0], e0[1]};
        tha = lds0 + SPARE + 10368 + 24;
        sla = lds0 + SLT + (row0 & 127) * 4;
        xca = O_X1C - 2048 + col0 * 2, sca = O_ST2 - 128 + et * 16;
        asm volatile("" : "+v"(dk), "+v"(spo), "+v"(soo), "+v"(cwa), "+v"(cwu), "+v"(tha), "+v"(sla), "+v"(xca), "+v"(sca), "+v"(e0), "+v"(e1), "+v"(c128));
        asm volatile("" : "+v"(xoff[0]), "+v"(xoff[1]), "+v"(xoff[2]), "+v"(xoff[3]), "+v"(rka), "+v"(rkb), "+v"(ea), "+v"(eb));
      };
#pragma unroll
      for (int s = 0; s < H0 / 2; ++s) {
        const unsigned a0 = ya0 + ((32 + s) % NS) * 16384, a1 = a0 ^ 64, an = ya0 + ((33 + s) % NS) * 16384;
        XW(2 * s);
        if (s == 0) {
          constexpr int MODE = 2;
          HALF(XS(0), a1, true, XS(7), XP(7), 7, false, a0);
          UFLAG();
        } else {
          HALF(XS(2 * s), a1, true, XS(2 * s + 7), XP(2 * s + 7), 2 * s + 7, 2 * s >= 4, a0);
        }
        if (s == 1) ESET();
        BAR(33 + s);
        if (s == 1) {
          if (wid == 1) TSG(part, 7);
          TS(4);
#pragma unroll
          for (int h = 4; h < U_X0; ++h) {
            XLD(XS(h)[0], XP(h), h, 0);
            XLD(XS(h)[1], XP(h), h, 1);
          }
        }
        XW(2 * s + 1);
#undef HHOOK
#define HHOOK(j) if (s == 1 && j + 3 >= U_X0) { XLD(XS(j + 3)[0], XP(j + 3), j + 3, 0); XLD(XS(j + 3)[1], XP(j + 3), j + 3, 1); }
        HALF(XS(2 * s + 1), an, true, XS(2 * s + 8), XP(2 * s + 8), 2 * s + 8, 2 * s + 1 >= 5, a1);
#undef HHOOK
#define HHOOK(j)
      }
      u32x4 ov;
      bf16x8 xk;
#define EH(e) (H0 + (((e) < NP ? (e) : (e) - NP - 8) >> 3))
#define EA(e) ((ya0 ^ ((EH(e) & 1) * 64)) + ((32 + (EH(e) >> 1)) % NS) * 16384)
      sfor<NE>([&](auto nn) __attribute__((always_inline)) {
        constexpr int n = decltype(nn)::value, h = EH(n), j = n & 7, m = n - NP - TP_P0, e = n + 7, z = n - (NE - 8);
        (void)sa4, (void)eb, (void)ga, (void)ea, (void)xk, (void)rka, (void)rkb, (void)xq, (void)xoff, (void)ov, (void)xw, (void)x;
        (void)cj, (void)sw, (void)ce, (void)cwa, (void)cwu, (void)th, (void)tha, (void)slt, (void)sla, (void)slf, (void)c128;
        if (n == 0) TS(2);
        if (n == NP) { TS(3); SKEWPT(); }
        if (n < NP && j == 0) {
          if ((h & 1) && h < 31) BAR(33 + (h >> 1));
          XW(h);
        }
        if (n == NP + TP_X0) { TS(14); BAR(48); TS(11); }
        if (n == NP + TP_XL) XCHLD();
        if constexpr ((j & 1) == 0) WAIT_L(z <= 0 ? 5 : 6 - z + 2 * z);
        if constexpr (n < NP) MFMA(acc[1][j], Y[j], XS(h)[1]);
        else if constexpr (n < NP + 8) MFMA(acc[0][j], Y[j], xk);
        else MFMA(acc[0][j], Y[j], XS(h)[0]);
        if constexpr (n == NP - 16) DSR(xk, rkb, 0);
        if constexpr (e >= NP && e < NP + 8) DSR(Y[e & 7], rka, (e & 7) * 256);
        else if constexpr (e < NE) DSR(Y[e & 7], EA(e), (e & 7) * 2048);
        if constexpr (n == 2 || n == 4) XLD(XS(31)[(j >> 1) - 1], XP(31), 31, (j >> 1) - 1);
        if constexpr (m >= TP_XC - 11 && m < TP_XC - 7) DSR(xq[m - TP_XC + 11], xoff[m - TP_XC + 11], 4096);
        if constexpr (m >= 0 && m < 4 * TP_PS) {
          constexpr int q = m / TP_PS, r = m % TP_PS;
#pragma unroll
          for (int u = (r * 13 + TP_PS - 1) / TP_PS; u < ((r + 1) * 13 + TP_PS - 1) / TP_PS; ++u) {
#define AO(i) acc[1][2 * q + ((u >> 1) & 1)][(u & 1) * 2 + (i)]
            if (u < 4) asm volatile("v_and_or_b32 %0, %1, %2, %3" : "=v"(ov[u & 3]) : "v"(AO(1)), "s"(0x80000000u), "v"(c128));
            else if (u < 8) asm volatile("v_fmamk_f32 %0, %1, 0x5f800000, %0" : "+v"(ov[u & 3]) : "v"(AO(1)));
            else if (u < 12) asm volatile("v_cvt_pk_bf16_f32 %0, %1, %0" : "+v"(ov[u & 3]) : "v"(AO(0)));
            else asm volatile("global_store_dwordx4 %0, %1, %2 offset:%3 sc1\n\ts_nop 1" ::"v"(lane * 16), "v"(ov), "s"(xw), "n"(q * 1024) : "memory");
          }
          if (m == 4 * TP_PS - 1) TS(10);
        }
        if constexpr (m == TP_XC) asm volatile("" : "+v"(xq[0]), "+v"(xq[1]), "+v"(xq[2]), "+v"(xq[3]));
        if constexpr (m >= TP_XC && m < TP_XC + 32) {
          constexpr int c = m - TP_XC;
          if constexpr (c & 1) asm volatile("v_and_b32 %0, 0xffff0000, %1" : "=v"(acc[1][c >> 2][c & 3]) : "v"(xq[c >> 3][(c >> 1) & 3]));
          else asm volatile("v_lshlrev_b32 %0, 16, %1" : "=v"(acc[1][c >> 2][c & 3]) : "v"(xq[c >> 3][(c >> 1) & 3]));
        }
        if constexpr (z >= 0 && z < 4) {
          DSR(ga[2 * z], ea, z * 128);
          DSR(ga[2 * z + 1], ea, z * 128 + 16);
        } else if constexpr (z >= 4 && z < 6) {
          DSR(sa4[2 * z - 8], eb, (2 * z - 8) * 64);
          DSR(sa4[2 * z - 7], eb, (2 * z - 7) * 64);
        } else if constexpr (z == 6) {
          asm volatile("ds_read_b64 %0, %2\n\tds_read_b32 %1, %3" : "=&v"(cj), "=&v"(ce) : "v"(cwa), "v"(cwu) : "memory");
        } else if constexpr (z == 7) {
          asm volatile("ds_read_b32 %0, %4 offset:32\n\tds_read_b32 %1, %5 offset:4\n\tds_read_b32 %3, %5 offset:56\n\tds_read_b32 %2, %6"
                       : "=&v"(sw), "=&v"(th), "=&v"(slt), "=&v"(slf) : "v"(cwa), "v"(tha), "v"(sla) : "memory");
        }
      });
    }
    if (wid == 0) TSG(part, 8);
    TS(13);
    BT(49);
    BTE();
    SKEWPT();
    {
      TS(26);
#define XCHW() asm volatile("s_waitcnt vmcnt(0)" : "+v"(pv[0]), "+v"(pv[1]), "+v"(pv[2]), "+v"(pv[3])::"memory")
      XCHW();
#define XBAD (amin16(pv) < 128.0f)
      if (__builtin_expect(__any(XBAD), 1)) POLL2(XBAD, 1, PCAP, XCHLD(); XCHW());
      TS(27);
      asm volatile("s_waitcnt lgkmcnt(0)" : W8(acc[0]), W8(ga), "+v"(sa4[0]), "+v"(sa4[1]), "+v"(sa4[2]), "+v"(sa4[3]), "+v"(cj), "+v"(sw), "+v"(ce), "+v"(th), "+v"(slt), "+v"(slf)::"memory");
#pragma unroll
      for (int q = 0; q < 8; ++q) acc[0][q] = __builtin_amdgcn_mfma_f32_16x16x32_bf16(BF8((q & 1) ? e1 : e0), BF8(pv[q >> 1]), acc[0][q], 0, 0, 0);
      f32x4 s4 = {0.f, 0.f, 0.f, 0.f}, gr = {0.f, 0.f, 0.f, 0.f};
      const unsigned xa1 = xca + slt * 2048, sa1 = sca + slt * 128;
#pragma unroll
      for (int q = 0; q < 4; ++q) {
        xq[q] = pk8(acc[1][2 * q] + ga[2 * q] * acc[0][2 * q], acc[1][2 * q + 1] + ga[2 * q + 1] * acc[0][2 * q + 1]);
        gr = __builtin_amdgcn_mfma_f32_16x16x32_bf16(BF8(xq[q]), BF8(xq[q]), gr, 0, 0, 0);
        s4 = __builtin_amdgcn_mfma_f32_16x16x32_bf16(BF8(sa4[q]), BF8(xq[q]), s4, 0, 0, 0);
        xq[q] = nsent4(xq[q]);
      }
#define XCST(a)                                                                                                              \
  asm volatile(                                                                                                              \
      "global_store_dwordx4 %0, %1, %5 sc1\n\tglobal_store_dwordx4 %0, %2, %5 offset:64 sc1\n\t"                            \
      "global_store_dwordx4 %0, %3, %5 offset:128 sc1\n\tglobal_store_dwordx4 %0, %4, %5 offset:192 sc1\n\ts_nop 1" ::"v"(a), \
      "v"(xq[0]), "v"(xq[1]), "v"(xq[2]), "v"(xq[3]), "s"(ws)                                                                \
      : "memory")
#define SCST(a)                                                                                                              \
  do {                                                                                                                       \
    _Pragma("unroll") for (int r = 0; r < S_R; ++r) asm volatile("global_store_dwordx4 %0, %1, %2 offset:%3 sc1" ::"v"(a), "v"(sg), "s"(ws), "n"(r * 1280) : "memory"); \
    asm volatile("s_nop 1");                                                                                                 \
  } while (0)
      if (slt) XCST(xa1);
      const bool dl = ((lane >> 2) & 3) == g4;
      const u32x4 gd = __builtin_bit_cast(u32x4, gr) & dk;
      const float m = s4[2] * (1.0f / 128);
      float q2 = __uint_as_float((gd[0] | gd[1]) | (gd[2] | gd[3])) - s4[2] * m, lg = (s4[0] + s4[1]) - m * ce;
      if (__builtin_expect(__any(dl && !(s4[2] * m <= 256.0f * q2)), 0)) {
        asm volatile("" : "+v"(xq[0]), "+v"(xq[1]), "+v"(xq[2]), "+v"(xq[3]));
        f32x4 a = {0.f, 0.f, 0.f, 0.f}, l = {0.f, 0.f, 0.f, 0.f};
#pragma unroll
        for (int q = 0; q < 4; ++q) {
          const u32x4 ch = *reinterpret_cast<const u32x4*>(&scw[0][g4 * 8 + q * 32]), cl = *reinterpret_cast<const u32x4*>(&scw[1][g4 * 8 + q * 32]);
          const f32x4 d0 = lo4(xq[q]) - m, d1 = hi4(xq[q]) - m;
          a += d0 * d0 + d1 * d1;
          l += d0 * (lo4(ch) + lo4(cl)) + d1 * (hi4(ch) + hi4(cl));
        }
        q2 = ar4((a[0] + a[1]) + (a[2] + a[3]));
        lg = ar4((l[0] + l[1]) + (l[2] + l[3]));
      }
      const u32x4 sg = {nsent(m), nsent(q2), nsent(lg), 0u};
      if (dl) {
        if (slt) SCST(sa1);
        asm volatile("global_store_dwordx4 %0, %1, %2 sc1\n\ts_nop 1" ::"v"(soo), "v"(sg), "s"(ws) : "memory");
      }
      if (__any(slf == 0u)) {
        POLL2(slf == 0u, 1, 1 << 26, asm volatile("ds_read_b32 %1, %2 offset:56\n\tds_read_b32 %0, %3\n\ts_waitcnt lgkmcnt(0)" : "=&v"(slt), "=&v"(slf) : "v"(tha), "v"(sla) : "memory"));
        if (slt) {
          XCST(xca + slt * 2048);
          if (dl) SCST(sca + slt * 128);
        }
      }
      asm volatile("ds_write_b32 %0, %1 offset:72" ::"v"(tha + wid * 4), "v"(1u) : "memory");
      TS(7);
      SKEWPT();
      const unsigned tp = tick() + T_D1, tq = (unsigned)__builtin_amdgcn_readfirstlane((int)th) + T_C1;
      u32x4 sr[2];
#define STLD(W) asm volatile("global_load_dwordx4 %0, %2, %3 sc1\n\tglobal_load_dwordx4 %1, %2, %3 offset:256 sc1" W : "=&v"(sr[0]), "=&v"(sr[1]) : "v"(spo), "s"(ws) : "memory")
#define SBAD (max(max(max(sr[0][0], sr[0][1]), sr[0][2]), max(max(sr[1][0], sr[1][1]), sr[1][2])) == SENT)
      asm volatile("" : "+v"(xq[0]), "+v"(xq[1]), "+v"(xq[2]), "+v"(xq[3])::"memory");
      f32x4 xf[8], xs[8], gc[8];
      auto PREW = [&](int q0, int q1) __attribute__((always_inline)) {
#pragma unroll
        for (int q = q0; q < q1; ++q) {
          const u32x4 sbq = *reinterpret_cast<const u32x4*>(&svb[1][col0 + q * 32]), gbq = *reinterpret_cast<const u32x4*>(&svb[3][col0 + q * 32]);
          const u32x4 cq = *reinterpret_cast<const u32x4*>(&svb[2][col0 + q * 32]);
          xf[2 * q] = lo4(xq[q]), xf[2 * q + 1] = hi4(xq[q]);
          xs[2 * q] = xf[2 * q] + lo4(gbq) * lo4(sbq), xs[2 * q + 1] = xf[2 * q + 1] + hi4(gbq) * hi4(sbq);
          gc[2 * q] = lo4(gbq) * lo4(cq), gc[2 * q + 1] = hi4(gbq) * hi4(cq);
          asm volatile("" : "+v"(xf[2 * q]), "+v"(xf[2 * q + 1]), "+v"(xs[2 * q]), "+v"(xs[2 * q + 1]), "+v"(gc[2 * q]), "+v"(gc[2 * q + 1]));
        }
      };
      PREW(0, PW0);
      wait_tick((int)(tp - tq) < 0 ? tp : tq);
      STLD("");
      unsigned rk = 0x8ffeu - (unsigned)row0 * 2;
      unsigned twa = lds0 + STG + wid * 8192 + (lane & 15) * 256 + ((g4 ^ lane) & 15) * 16, tra = lds0 + STG + wid * 8192 + g4 * 256 + ((g4 ^ lane) & 15) * 16;
      unsigned oo_ = (unsigned)((rg * 16 + g4) * D + et * 128 + (lane & 15) * 8) * 2;
      asm volatile("" : "+v"(rk), "+v"(twa), "+v"(tra), "+v"(oo_));
      PREW(PW0, 4);
      asm volatile("s_waitcnt vmcnt(0)" : "+v"(sr[0]), "+v"(sr[1])::"memory");
      if (__builtin_expect(__any(SBAD), 1)) POLL2(SBAD, 1, PCAP, STLD(VW0));
      TS(9);
      const f32x2 mk = {__uint_as_float(sr[0][0]), __uint_as_float(sr[1][0])};
      const f32x2 qk = {__uint_as_float(sr[0][1]), __uint_as_float(sr[1][1])};
      const f32x2 lk = {__uint_as_float(sr[0][2]), __uint_as_float(sr[1][2])};
      const float mean = ar4(mk[0] + mk[1]) * (1.0f / 8);
      const f32x2 dm = mk - mean;
      const f32x2 a = qk + 128.0f * (dm * dm), l = lk + dm * cj;
      const float al = swap32_add(a[0] + a[1], l[0] + l[1]), am = swap16_add(al, al);
      auto r = __builtin_amdgcn_permlane32_swap(__float_as_uint(am), __float_as_uint(am), false, false);
      const float rstd = __builtin_amdgcn_rsqf(__uint_as_float(r[0]) * (1.0f / D) + 1e-5f);
      const unsigned key = mkkey(rstd * __uint_as_float(r[1]) + sw) | rk;
      unsigned t1 = slt ? 0u : key, t2 = 0u;
      TOP2(0xB1); TOP2(0x4E); TOP2(0x141); TOP2(0x140);
      if (lane == 0 && et == (rg & 7)) asm volatile("global_store_dwordx2 %0, %1, %2 sc1" ::"v"(O_KEY + rg * KSTR * 8), "v"(u32x2{t1, t2}), "s"(ws) : "memory");
      u32x4 o[4];
#pragma unroll
      for (int q = 0; q < 4; ++q) o[q] = pk8(xs[2 * q] + (gc[2 * q] * (xf[2 * q] - mean)) * rstd, xs[2 * q + 1] + (gc[2 * q + 1] * (xf[2 * q + 1] - mean)) * rstd);
      {
        asm volatile("ds_write_b128 %0, %4\n\tds_write_b128 %1, %5\n\tds_write_b128 %2, %6\n\tds_write_b128 %3, %7" ::"v"(twa), "v"(twa ^ 64), "v"(twa ^ 128), "v"(twa ^ 192),
                     "v"(o[0]), "v"(o[1]), "v"(o[2]), "v"(o[3]) : "memory");
        asm volatile("ds_read_b128 %0, %4\n\tds_read_b128 %1, %5 offset:1024\n\tds_read_b128 %2, %6 offset:2048\n\tds_read_b128 %3, %7 offset:3072\n\ts_waitcnt lgkmcnt(0)"
                     : "=&v"(o[0]), "=&v"(o[1]), "=&v"(o[2]), "=&v"(o[3]) : "v"(tra), "v"(tra ^ 64), "v"(tra ^ 128), "v"(tra ^ 192) : "memory");
      }
      auto OST = [&](bool on) __attribute__((always_inline)) {
        const unsigned mk_ = (unsigned)__builtin_amdgcn_ballot_w64(on) >> g4;
#pragma unroll
        for (int q = 0; q < 4; ++q)
          if (mk_ & (1u << (4 * q))) asm volatile("global_store_dwordx4 %0, %1, %2" OPOL "\n\ts_nop 1" ::"v"(oo_), "v"(o[q]), "s"(out + q * 4096) : "memory");
      };
      bool df = slt;
      {
        const int tn = (int)(tick() - (tq + (T_C3 - T_C1)));
        if (tn > T_X) df = true;
        else if (tn > -T_W) { unsigned sv; LSPIN(sv, ssel + 16 + wid, sv != 0u); }
      }
      OST(!df);
      TS(1);
      u32x2 ks;
      POLL2(ks[1] == 0u, 1, 1 << 26, asm volatile("ds_read_b64 %0, %1\n\ts_waitcnt lgkmcnt(0)" : "=v"(ks) : "v"(lds0 + SPARE + 10368) : "memory"));
      df = df && (ks[0] & 0xffffu) != rk && (ks[1] & 0xffffu) != rk;
      SKEWPT();
      if (__any(df)) {
        if (b < NPART) {
          unsigned sv;
          LSPIN(sv, ssel + O_DW, sv != 0u);
          if (O_DS) __builtin_amdgcn_s_sleep(O_DS);
        }
        OST(df);
      }
      if (__builtin_expect(__any(t1 > ks[1]), 0)) {
        VDLD(8);
        slt = slt ? 0u : key == vd[0] ? 9u : key == vd[1] ? 10u : 0u;
        if (slt) {
          XCST(xca + slt * 2048);
          if (dl) SCST(sca + slt * 128);
        }
      }
    }
    TS(16);
    TSE();
  } else {
    const int hw = wid - 4;
    unsigned ts0 = tick();
    asm volatile("" : "+v"(ts0));
    const char* Hc = reinterpret_cast<const char*>((cc ? X1 : X0) + (size_t)et * 128 * D);
    auto UPOLL = [&](int n) {
      const unsigned fo = (unsigned)WS_UFL + ((cc * 16 + nt) * 32 + ((mt + 1 + (lane >> 2)) & 7) * 4 + (lane & 3)) * FSTR * 4;
      unsigned f;
      POLL2(f == 0u && lane < 4 * n, U_S, PCAP, asm volatile("global_load_dword %0, %1, %2 sc1" VW0 : "=&v"(f) : "v"(fo), "s"(ws) : "memory"));
      return (__builtin_amdgcn_ballot_w64(f == 0u) & 0xFFFFFFFull) == 0;
    };
    bool urdy = true;
    u32x4 gT[8], uT[8], wdv[8];
    unsigned og = (unsigned)(((g & 3) * 256 + hw * 64 + (lane >> 3)) * F + (g >> 2) * 64 + ((lane & 7) ^ ((lane >> 3) & 2) ^ 4) * 8) * 2;
    unsigned wta = lds0 + STG + (g4 * 8 + ((lane >> 2) & 3)) * 128 + (((hw * 2 + ((lane >> 1) & 1)) ^ ((lane >> 2) & 2) ^ ((g4 & 1) * 4)) << 4) + (lane & 1) * 8;
    unsigned od = (unsigned)(((g >> 2) * 64 + (lane >> 3) * 8) * D + (g & 3) * 256 + hw * 64 + (lane & 7) * 8) * 2;
    unsigned ou = og + (unsigned)(((mt + W_UO) % 7 - mt) * 128);
    asm volatile("" : "+v"(og), "+v"(ou), "+v"(od), "+v"(wta));
#define LDWI(dst, voff, base, inc) asm volatile("global_load_dwordx4 %0, %1, %2" WPOL "\n\tv_add_u32 %1, %3, %1" : "=v"(dst), "+v"(voff) : "s"(base), "n"(inc) : "memory")
#define WST(o, w)                                                                                                          \
  if (b < NPART) {                                                                                                         \
    asm volatile("s_mov_b32 m0, %0" ::"s"(lds0 + STG + hw * 8192));                                                        \
    _Pragma("unroll") for (int i = 0; i < 8; ++i) {                                                                        \
      asm volatile("v_xor_b32 %0, 64, %0\n\tglobal_load_lds_dwordx4 %0, %1 offset:%2" WPOL "\n\tv_add_u32 %0, %3, %0"       \
                   : "+v"(o) : "s"(w), "n"((i & 3) * 1024), "n"(F * 16 + (i == 3 ? 3072 : -1024)) : "memory");             \
      if (i == 3) asm volatile("s_add_u32 m0, m0, 4096" ::: "scc");                                                        \
    }                                                                                                                      \
  }
#define WTR2(i) "ds_read_b64_tr_b16 %" #i ", %16 offset:(" #i "/2)*4096+(" #i "&1)*512\n\t"
#define WTK(T)                                                                                                                         \
  if (b < NPART) {                                                                                                                     \
    u32x2 t_[16];                                                                                                                      \
    asm volatile(WTR2(0) WTR2(1) WTR2(2) WTR2(3) WTR2(4) WTR2(5) WTR2(6) WTR2(7) WTR2(8) WTR2(9) WTR2(10) WTR2(11) WTR2(12) WTR2(13)   \
                     WTR2(14) WTR2(15) "s_waitcnt lgkmcnt(0)"                                                                          \
                 : "=&v"(t_[0]), "=&v"(t_[1]), "=&v"(t_[2]), "=&v"(t_[3]), "=&v"(t_[4]), "=&v"(t_[5]), "=&v"(t_[6]), "=&v"(t_[7]),     \
                   "=&v"(t_[8]), "=&v"(t_[9]), "=&v"(t_[10]), "=&v"(t_[11]), "=&v"(t_[12]), "=&v"(t_[13]), "=&v"(t_[14]), "=&v"(t_[15]) \
                 : "v"(wta) : "memory");                                                                                               \
    _Pragma("unroll") for (int f = 0; f < 8; ++f) T[f] = u32x4{t_[2 * f][0], t_[2 * f][1], t_[2 * f + 1][0], t_[2 * f + 1][1]};        \
  }
#define DLD(q0, q1) if (b < NPART) { _Pragma("unroll") for (int q = q0; q < q1; ++q) LDWI(wdv[q], od, wd, D * 2); }
    const int pf0 = A_PS + (((j8 & 7) * A_S1) >> 3), pf1 = A_P2 + (((j8 & 7) * A_S2) >> 3), q1 = A_Q1;
    static_assert(A_PS >= 1 && A_P2 + A_S2 * 7 / 8 + 3 <= R_AT && R_AT < A_Q1 && A_P2 + A_S2 * 7 / 8 + 1 + W_T <= A_Q1 && H_AT + 3 <= A_Q1 && G1_C + G1_S < W_T && A_PS + W_T < A_P2 && A_PS + A_S1 * 7 / 8 + W_T + 1 < A_P2 + A_S2 * 7 / 8 && W_T >= M_NS - 2 && A_Q1 < F_AT && F_AT <= 29 && V_K <= B_J && V_T > B_J && B_WK == 15 && X_K >= N_C && (X_K + M_NS - 1 < 16 || (X_K <= B_MAT && TP_XC - 11 >= TP_X0 - TP_P0)) && TP_XC >= 4 * TP_PS && TP_XC + TP_P0 + 32 <= 72 && N_K > N_C && N_C > V_T && B_RAT > B_J && B_QAT > B_J && B_QAT + B_QD < 15, "barrier counts");
#define HOFF(r) (unsigned)((((r) >> 5) * 32 + (((r) >> 2) & 3) * 8 + (((r) >> 4) & 1) * 4 + ((r)&3)) * 2048 + ((lane & 7) ^ (lane >> 3)) * 16)
#define TR8(T, W)                                                                                                    \
  _Pragma("unroll") for (int c = 0; c < 8; ++c) {                                                                    \
    _Pragma("unroll") for (int p = 0; p < 4; ++p)                                                                    \
        T[c][p] = __builtin_amdgcn_perm(W[2 * p + 1][c >> 1], W[2 * p][c >> 1], (c & 1) ? 0x07060302u : 0x05040100u); \
    asm volatile("" : "+v"(T[c]));                                                                                   \
  }
    unsigned qm[4];
#pragma unroll
    for (int i = 0; i < 4; ++i) qm[i] = ((lane >> 2) & 3) == i ? ~0u : 0u;
    asm volatile("" : "+v"(qm[0]), "+v"(qm[1]), "+v"(qm[2]), "+v"(qm[3]));
    auto MVR = [&](f32x4 d) __attribute__((always_inline)) { return ((lane & 8) ? d[3] : d[0]) + dpp_f<0x128>((lane & 8) ? d[1] : d[2]); };
    auto MV64 = [&](const u32x4* T, u32x4 a) __attribute__((always_inline)) {
      f32x4 d[2] = {};
#pragma unroll
      for (int c = 0; c < 8; ++c) {
        const unsigned k_ = qm[c >> 1];
        d[c & 1] = __builtin_amdgcn_mfma_f32_16x16x32_bf16(BF8((u32x4{a[0] & k_, a[1] & k_, a[2] & k_, a[3] & k_})), BF8(T[c]), d[c & 1], 0, 0, 0);
      }
      return f32x2{MVR(d[0]), MVR(d[1])};
    };
#define LD1(i, a) "global_load_dwordx4 %" #i ", %" #a ", %10 sc1\n\tv_add_u32 %" #a ", 0x4000, %" #a "\n\t"
#define LD8(r, o0, o1, W)                                                                                                          \
  do {                                                                                                                             \
    unsigned a0_ = (o0), a1_ = (o1);                                                                                               \
    asm volatile(LD1(0, 8) LD1(4, 9) LD1(1, 8) LD1(5, 9) LD1(2, 8) LD1(6, 9) LD1(3, 8) LD1(7, 9) "s_nop 0" W                        \
                 : "=&v"(r[0]), "=&v"(r[1]), "=&v"(r[2]), "=&v"(r[3]), "=&v"(r[4]), "=&v"(r[5]), "=&v"(r[6]), "=&v"(r[7]), "+v"(a0_), "+v"(a1_) \
                 : "s"(ws) : "memory");                                                                                            \
  } while (0)
    u32x4 sel, gp[4];
    unsigned vc;
#define VCLD(W) asm volatile("global_load_dword %0, %1, %2 sc1" W : "=&v"(vc) : "v"(O_VP + ((cc ^ 1) * 8 + mt) * 4096 + (lane & 15) * 256), "s"(ws) : "memory")
#define GP4(o, W)                                                                                                         \
  asm volatile(                                                                                                           \
      "global_load_dwordx4 %0, %4, %5 sc1\n\tglobal_load_dwordx4 %1, %4, %5 offset:1024 sc1\n\t"                         \
      "global_load_dwordx4 %2, %4, %5 offset:2048 sc1\n\tglobal_load_dwordx4 %3, %4, %5 offset:3072 sc1" W                \
      : "=&v"(gp[0]), "=&v"(gp[1]), "=&v"(gp[2]), "=&v"(gp[3])                                                            \
      : "v"(o), "s"(ws)                                                                                                   \
      : "memory")
#define GPBAD (max(max(max4(gp[0]), max4(gp[1])), max(max4(gp[2]), max4(gp[3]))) == SENT)
      auto GPSUM = [&]() __attribute__((always_inline)) {
        f32x4 d = {0.f, 0.f, 0.f, 0.f};
#pragma unroll
        for (int i = 0; i < 4; ++i) d = __builtin_amdgcn_mfma_f32_16x16x32_bf16(BF8(sel), BF8(gp[i]), d, 0, 0, 0);
        return d;
      };
    const bool rdc = b >= NPART && hw == 3, rnp = b >= NPART && hw == 2 && cc == 0;
    const int gbq = (b >> 4) & 1, cj = (b & 15) * 64 + (lane & 31) * 2;
    unsigned voff[4], ro, xo, ra1, wqa, qo, qa, ma, mo2, ma2;
    unsigned sfa, rk2, xo_, hwa, hra, hrb, ara, pso, gso, awa, sfw, gl0, gl1, sto, kca;
    auto JSET = [&]() {
#pragma unroll
      for (int i = 0; i < 4; ++i) {
        voff[i] = HOFF((hw * 4 + i) * 8 + (lane >> 3)) - i * 1024;
        sel[i] = ((lane & 15) >> 1) == i ? ((lane & 1) ? 0x3f800000u : 0x3f80u) : 0u;
      }
      ro = O_G2 + ((lane >> 5) * 16384 + gbq * 1024 + cj) * 8;
      xo = (unsigned)((nt * 128 + hw * 32 + cc * 16 + g4) * D + et * 128 + ((g4 ^ lane) & 15) * 8) * 2;
      ra1 = lane < 32 ? O_MOD + 2048 + gbq * 4096 + cj * 2 : O_M2 + 8192 + 64 - gbq * 64 + (b & 15) * 4;
      wqa = lds0 + SPARE + 8192 + cj * 2;
      ma = max(hw, 1) * 2048 + (lane < 16 ? et * 256 + lane * 16 : lane < 48 ? (g & 3) * 512 + (lane - 16) * 16 : ((b >> 1) & 15) * 128 + (lane & 7) * 16);
      qo = hw < 2 ? O_QT + et * 2048 + hw * 1024 + lane * 16 : O_RS + ((hw & 1) * S + nt * 128 + (lane >> 4) * 32 + cc * 16 + (lane & 15)) * 4;
      qa = lds0 + (hw < 2 ? RKA + hw * 1024 + lane * 16 : RKB + lane * 16 + (hw & 1) * 8);
      mo2 = O_M2 + (hw == 2 ? 8448u + (lane & 15) * 16 : hw ? 8192u + (lane & 7) * 16 : lane < 32 ? et * 512 + lane * 16 : 4096u + (lane & 16) * 128 + et * 256 + (lane & 15) * 16);
      ma2 = lds0 + (hw ? SPARE + 10240 + (lane & 7) * 8 : TAB + lane * 16);
      asm volatile("" : "+v"(voff[0]), "+v"(voff[1]), "+v"(voff[2]), "+v"(voff[3]), "+v"(ro), "+v"(xo), "+v"(ra1), "+v"(wqa), "+v"(qo), "+v"(qa), "+v"(ma), "+v"(mo2), "+v"(ma2), "+v"(sel));
      *reinterpret_cast<unsigned*>((hw & 1) ? shm + RKB + 1024 + lane * 4 : reinterpret_cast<char*>(scw[2]) + lane * 4) = (hw & 1) ? 0u : 0x3f803f80u;
      if (hw == 3) {
        if (lane < 32) ssel[lane] = 0u;
        *reinterpret_cast<u32x2*>(shm + SLT + lane * 8) = u32x2{0u, 0u};
      }
    };
#define OWNL() int ln_ = lane; asm volatile("" : "+v"(ln_)); const int lane = ln_, g4 = lane >> 4; (void)g4
    auto TSET = [&]() {
      OWNL();
      kca = ((g & 3) * 256 + hw * 64 + (lane & 7) * 8) * 2;
      sfa = lds0 + SPARE + 10368, rk2 = lds0 + LST + (lane >> 3) * 4, sfw = sfa + hw * 4;
      xo_ = O_X1C + (lane >> 3) * 2048 + kca, sto = O_ST2 + (hw & (S_R - 1)) * 1280 + lane * 16;
      hwa = lds0 + XC + hw * 1024 + (lane & 7) * 128 + (((lane >> 3) ^ lane) & 7) * 16, hra = lds0 + XC + g4 * 128 + ((lane ^ g4) & 7) * 16, hrb = hra ^ 64;
      awa = lds0 + SPARE + 15872 + lane * 4, ara = lds0 + SPARE + 15872 + (lane & 2) * 64 + g4 * 32 + (lane & 1) * 16;
      pso = O_PART + ((g >> 2) * 2048 + (lane & 8) * 128 + (g & 3) * 256 + hw * 64 + (lane & 7) * 8 + g4 * 2) * 4;
      gso = O_GU + g * 1024 + (lane >> 5) * 256 + hw * 64 + (lane & 15) * 4;
      gl0 = O_GU + (g >> 2) * 4096 + lane * 8, gl1 = O_GU + (xcd * 7 + (mt + 7 - W_UO) % 7) * 4096 + 512 + lane * 8;
      asm volatile("" : "+v"(sfa), "+v"(rk2), "+v"(xo_), "+v"(hwa), "+v"(hra), "+v"(hrb), "+v"(ara), "+v"(pso), "+v"(gso), "+v"(awa), "+v"(sfw), "+v"(gl0), "+v"(gl1), "+v"(sto), "+v"(kca));
    };
    if (hw < 2) {
      constexpr int P = 8;
      __builtin_amdgcn_s_setprio(2);
      unsigned voy[P];
#pragma unroll
      for (int i = 0; i < P; ++i) voy[i] = (unsigned)(((hw * 8 + i) * 8 + (lane >> 3)) * 4096 + ((lane & 7) ^ (lane >> 3)) * 16 - (i & 3) * 1024);
      const unsigned lbase = lds0 + hw * 8192;
      const char* Yc = reinterpret_cast<const char*>((cc ? Y1 : Y0) + (size_t)nt * 128 * S);
      auto stage = [&](const char* yp, unsigned l) {
        GLDS4(l, voy, yp);
        GLDS4(l + 4096, voy + 4, yp);
      };
#define YSRC(n) (Yc + ((((n) + xcd * 4) & 31) << 7))
      __builtin_amdgcn_s_sleep(C_LD);
      PFL(hw ? PFG(1) : cpf);
      JSET();
      TAGW(1);
      BAR(55);
      BT(56);
      if (hw == 1 && lane == 0) asm volatile("global_store_dwordx2 %0, %1, %2 sc1" ::"v"(O_XFL + b * XSTR * 4), "v"(tag), "s"(ws) : "memory");
#pragma unroll
      for (int s = 0; s < NS - 1; ++s) stage(YSRC(s), lbase + s * 16384);
      BT(57);
      WAIT_V((NS - 2) * P);
      BAR(0);
      unsigned slot = lbase + (NS - 1) * 16384;
      const u64 evm = (u64)((1u | 1u << W_T) << pf0 | (1u | 1u << W_T) << pf1) | 1ull << CPF_K | 1ull << (33 - NS) | 3ull << 32;
      const char* yb = Yc;
      unsigned yo = (NS - 1 + xcd * 4) << 7, ym = 31 << 7;
#pragma unroll 1
      for (int k = 1; k <= 32 + B_J; ++k) {
        const char* ys = yb + (yo & ym);
        yo += 128;
        asm volatile("" : "+s"(ys));
        WAIT_V((NS - 3) * P);
        BAR(k);
        stage(ys, slot);
        slot = slot == lbase + (NS - 1) * 16384 ? lbase : slot + 16384;
        if (__builtin_expect((evm >> k) & 1, 0)) {
          if (k == pf0) {
            WST(og, wg);
            if (b >= NPART) GLDS(lds0 + SPARE + 8192 + hw * 1024, (unsigned)(hw * 1024 + lane * 16), wr_);
          }
          if (k == pf0 + W_T) { WTK(gT); }
          if (k == pf1) { WST(ou, wu); }
          if (k == pf1 + W_T) { WTK(uT); }
          if (k == CPF_K && hw == 1) CPFL();
          if (k == 33 - NS) {
#pragma unroll
            for (int i = 0; i < P; ++i) voy[i] = HOFF((hw * 8 + i) * 8 + (lane >> 3)) - (i & 3) * 1024;
            yb = Hc, yo = mt << 8, ym = 15 << 7;
          }
          if (k == 32) {
            DLD(0, 8 - B_WN);
            TSET();
          }
          if (k == 33 && hw == 0) {
            if (U_D) __builtin_amdgcn_s_sleep(U_D);
            urdy = UPOLL(U_N1);
          }
        }
      }
    } else {
      const int aw = hw - 2;
      unsigned ow = wo - (2048 + (cg0 >> 4) * 1024) * 2;
      const unsigned oq = (unsigned)((nt * 64 + aw * 32 + (lane >> 4)) * D + mt * 128) * 2;
      u32x4 hv[8], wt[8], sc;
      {
        asm volatile("s_waitcnt vmcnt(11)" : "+v"(cq)::"memory");
#pragma unroll
        for (int i = 0; i < 4; ++i) sc[i] = pack2(silu_bf(bflo(cq[i])), silu_bf(bfhi(cq[i])));
        asm volatile("" : "+v"(sc));
      }
#define GPUB(o, off)                                                                                   \
  if ((lane & 8) == 0)                                                                                 \
    asm volatile("global_store_dwordx4 %0, %1, %2 sc1 ; ALWAYS\n\ts_nop 1" ::"v"(off),                          \
                 "v"(u32x4{__float_as_uint(o[0]), (unsigned)tag, __float_as_uint(o[1]), (unsigned)tag}), "s"(ws) : "memory")
      const unsigned gpo = ks0 * 2048 + cg0 * 64 + (lane & 7) * 8 + (lane >> 4) * 2;
      {
        unsigned mpo = O_G1 + gpo * 8;
        asm volatile("s_waitcnt vmcnt(3)" : W8(wv), "+v"(mpo)::"memory");
        TR8(wt, wv);
        f32x2 o = MV64(wt, sc);
        asm volatile("" : "+v"(o));
        TAGW(0);
        BAR(55);
        if (P_D2) __builtin_amdgcn_s_sleep(P_D2);
        PFL(PFG(2 + aw));
        GPUB(o, mpo);
      }
      unsigned vta = lds0 + STG + (g4 * 8 + ((lane >> 2) & 3)) * 256 + (((aw * 8 + ((lane >> 1) & 1)) ^ ((g4 & 1) * 8 + ((lane >> 2) & 3))) << 4) + (lane & 1) * 8;
      unsigned vso = O_VP + ((cc * 8 + mt) * 16 + nt) * 256 + aw * 128 + (lane & 3) * 32 + g4 * 8;
      asm volatile("" : "+v"(vta), "+v"(vso), "+v"(ow));
      unsigned f0 = O_XFL + ((xcd * 32 + 64 + aw * 128 + lane) & 255) * XSTR * 4, f1 = O_XFL + ((xcd * 32 + 128 + aw * 64 + lane) & 255) * XSTR * 4;
      unsigned lao = O_KEY + 512 * 1024 + b * 3328 + aw * 1664 + lane * 16;
      const char* pf4 = PFG(4 + aw);
      const char* pf6 = PFG(6 + aw);
      asm volatile("" : "+v"(f0), "+v"(f1), "+v"(lao), "+v"(pf4), "+v"(pf6));
      BT(57);
      BAR(0);
      f32x4 rs = {0.f, 0.f, 0.f, 0.f};
      u32x4 rsa = (lane & 15) == g4 * 4 ? u32x4{0x3f803f80u, 0x3f803f80u, 0x3f803f80u, 0x3f803f80u} : u32x4{0u, 0u, 0u, 0u}, R_;
      asm volatile("" : "+v"(rs), "+v"(rsa), "=v"(R_));
      const unsigned ra = lds0 + (mt * 16 + aw * 8) * 128 + lane * 16;
      unsigned so = 0;
#define TSX()
#define RSUM()                                                                                                    \
  do {                                                                                                            \
    TSX();                                                                                                        \
    asm volatile("ds_read_b128 %1, %2\n\ts_waitcnt lgkmcnt(0)\n\tv_mfma_f32_16x16x32_bf16 %0, %3, %1, %0" : "+v"(rs), "+v"(R_) : "v"(ra + so), "v"(rsa) : "memory"); \
    so = so == (NS - 1) * 16384 ? 0 : so + 16384;                                                                 \
  } while (0)
      {
        u64 f[2];
#define FLD() asm volatile("global_load_dwordx2 %0, %2, %4 sc1\n\tglobal_load_dwordx2 %1, %3, %4 sc1" : "=&v"(f[0]), "=&v"(f[1]) : "v"(f0), "v"(f1), "s"(ws) : "memory")
        __builtin_amdgcn_s_sleep(F_D);
        FLD();
        asm volatile("global_store_dwordx4 %0, %1, %2 sc1" ::"v"(lao), "v"(u32x4{~0u, ~0u, ~0u, ~0u}), "s"(ws) : "memory");
        if (lane < 40) asm volatile("global_store_dwordx4 %0, %1, %2 offset:1024 sc1 ; ALWAYS" ::"v"(lao), "v"(u32x4{~0u, ~0u, ~0u, ~0u}), "s"(ws) : "memory");
        asm volatile("s_nop 1");
#define GLD() _Pragma("unroll") for (int i = 0; i < 8; ++i) LDWI(wv[i], ow, wmod, 12288)
        RSUM();
        BAR(1);
        asm volatile("s_waitcnt vmcnt(%2)" : "+v"(f[0]), "+v"(f[1]) : "n"(2) : "memory");
        POLL(__all(f[0] == tag && f[1] == tag), 1, FLD(); asm volatile("s_waitcnt vmcnt(0)" : "+v"(f[0]), "+v"(f[1])::"memory"));
      }
      if (X_PA) __builtin_amdgcn_s_setprio(X_PA);
#define ARUN(b0, b1) _Pragma("unroll 1") for (int bi = (b0); bi < (b1); ++bi) { RSUM(); BAR(bi + 1); }
      GLD();
      ARUN(1, pf0);
      PFL(pf4);
#define HVLD()                                                                                                                    \
  do {                                                                                                                            \
    unsigned o_ = oq + (((lane & 15) ^ g4) << 4);                                                                                 \
    asm volatile("s_mov_b32 m0, %0" ::"s"(lds0 + STG + aw * 8192));                                                               \
    _Pragma("unroll") for (int i = 0; i < 8; ++i) {                                                                               \
      asm volatile("v_xor_b32 %0, %4, %0\n\tglobal_load_lds_dwordx4 %0, %1 offset:%2" HPOL "\n\tv_add_u32 %0, %3, %0"              \
                   : "+v"(o_) : "s"(cc ? X1 : X0), "n"((i & 3) * 1024), "n"(8192 + (i == 3 ? 3072 : -1024)), "n"(i ? ((i ^ (i - 1)) & 3) * 64 : 0) : "memory"); \
      if (i == 3) asm volatile("s_add_u32 m0, m0, 4096" ::: "scc");                                                               \
    }                                                                                                                             \
  } while (0)
      WST(og, wg);
      ARUN(pf0, pf0 + G1_C);
      {
        if (b >= NPART) WAIT_V(0);
        asm volatile("s_waitcnt vmcnt(8)" : W8(wv)::"memory");
        TR8(wt, wv);
        if (G1_S) { RSUM(); BAR(pf0 + G1_C + 1); }
        const f32x2 o = MV64(wt, sc);
        GPUB(o, O_MODP + gpo * 8);
        ow = wo + 2048;
      }
      ARUN(pf0 + G1_C + G1_S, pf0 + W_T - 1);
      WAIT_V(1);
      RSUM();
      BAR(pf0 + W_T);
      WTK(gT);
      ARUN(pf0 + W_T, pf1);
      PFL(pf6);
      WST(ou, wu);
      const int rb_ = b < NPART ? pf1 + W_T : R_AT, hb_ = max(rb_ + 1, H_AT + (j8 & 3));
      const unsigned c0 = (b - NPART) * 64 + aw * 32 + (lane & 15) * 2, c1 = (b - NPART) * 32 + aw * 16 + (lane & 7) * 2;
#define ERLD(W) LD8(wt, O_MODP + (g4 * 8192 + c0) * 8, O_G1 + (g4 * 8192 + (lane & 8) * 128 + c1) * 8, W)
      ARUN(pf1, rb_ - 3);
      if (b >= NPART) ERLD("");
      ARUN(rb_ - 3, rb_ - 1);
      WAIT_V(0);
      RSUM();
      BAR(rb_);
      WTK(uT);
      if (b >= NPART) {
        const unsigned wq = *reinterpret_cast<const unsigned*>(&svb[4][c1]);
#define TG2(r, i) ((r[i][1] ^ (unsigned)tag) | (r[i][3] ^ (unsigned)tag) | (r[i + 1][1] ^ (unsigned)tag) | (r[i + 1][3] ^ (unsigned)tag))
        asm volatile("" : W8(wt));
        POLL(__all((TG2(wt, 0) | TG2(wt, 2) | TG2(wt, 4) | TG2(wt, 6)) == 0u), 2, ERLD(VW0));
        auto FIN = [&](int q, int i, bool inc) __attribute__((always_inline)) {
          return rbf((inc ? 1.0f : 0.0f) + rbf(ar4((__uint_as_float(wt[q][i]) + __uint_as_float(wt[q + 1][i])) + (__uint_as_float(wt[q + 2][i]) + __uint_as_float(wt[q + 3][i])))));
        };
        const float s0 = FIN(0, 0, gbq), s1 = FIN(0, 2, gbq);
        if (lane < 16)
          asm volatile("global_store_dwordx4 %0, %1, %2 sc1\n\ts_nop 1" ::"v"(O_MODP + (16 * 2048 + c0) * 8),
                       "v"(u32x4{__float_as_uint(s0), (unsigned)tag, __float_as_uint(s1), (unsigned)tag}), "s"(ws) : "memory");
        const bool hi = lane & 8;
        const float t0 = FIN(4, 0, hi), t1 = FIN(4, 2, hi), o0 = dpp_f<0x128>(t0), o1 = dpp_f<0x128>(t1);
        const float a0 = hi ? o0 : t0, a1 = hi ? o1 : t1, e0 = hi ? t0 : o0, e1 = hi ? t1 : o1, p0 = e0 * bflo(wq), p1 = e1 * bfhi(wq);
        const unsigned h = pack2(p0, p1);
        const unsigned v = g4 == 0 ? __float_as_uint(hi ? a1 : a0) : g4 == 1 ? (hi ? h : pack2(e0, e1)) : hi ? pack2(a0 * p0, a1 * p1) : pack2(p0 - bflo(h), p1 - bfhi(h));
        const unsigned po = g4 == 0 ? O_M2 + c1 * 4 + (hi ? 4 : 0) : (g4 == 1 ? (hi ? O_M2 + 4096 : O_MOD + 4096) : hi ? O_GC : O_M2 + 6144) + c1 * 2;
        if (lane < 48) asm volatile("global_store_dword %0, %1, %2 sc1" ::"v"(po), "v"(min(v, SENT - 1u)), "s"(ws) : "memory");
      }
      ARUN(rb_, hb_);
      HVLD();
      ARUN(hb_, q1);
      u32x4 fm, g0, g1;
#define FMLD(W)                                                                                                                                     \
  asm volatile("global_load_dwordx4 %0, %3, %5 sc1\n\tglobal_load_dwordx4 %1, %4, %5 sc1\n\tglobal_load_dwordx4 %2, %4, %5 offset:64 sc1" W        \
               : "=&v"(fm), "=&v"(g0), "=&v"(g1) : "v"(O_MODP + (16 * 2048 + aw * 1024 + mt * 128) * 8 + lane * 16), "v"(O_GC + nt * 128 + g4 * 16), "s"(ws) : "memory")
      FMLD("");
      RSUM();
      BAR(q1 + 1);
      unsigned ha[4], sma = lds0 + SPARE + 15872 + (aw ? 512 + lane * 8 : lane * 4);
#pragma unroll
      for (int i = 0; i < 4; ++i) ha[i] = lds0 + STG + aw * 8192 + (lane & 15) * 256 + (((i * 4 + (lane >> 4)) ^ (lane & 15)) << 4);
      asm volatile("" : "+v"(ha[0]), "+v"(ha[1]), "+v"(ha[2]), "+v"(ha[3]), "+v"(sma), "+v"(ow));
      ARUN(q1 + 1, F_AT);
      asm volatile("s_waitcnt vmcnt(0)" : "+v"(fm), "+v"(g0), "+v"(g1)::"memory");
#pragma unroll
      for (int i = 0; i < 8; ++i) DSR(hv[i], ha[i & 3], (i >> 2) * 4096);
      POLL(__all(fm[1] == (unsigned)tag && fm[3] == (unsigned)tag && max(max4(g0), max4(g1)) != SENT), 1, FMLD(VW0));
      {
        const unsigned sg = cc ? 0x80000000u : 0u;
        asm volatile("ds_write_b32 %0, %2\n\tds_write_b32 %1, %3" ::"v"(sma), "v"(sma + (aw ? 4 : 256)), "v"(aw ? fm[0] ^ sg : (fm[0] >> 16) | (fm[2] & 0xffff0000u)), "v"(fm[2] ^ sg) : "memory");
      }
      ARUN(F_AT, 29);
      GLD();
      RSUM();
      BAR(30);
        u32x2 tv[16];
#pragma unroll
        for (int q = 0; q < 16; ++q)
          asm volatile("ds_read_b64_tr_b16 %0, %1 offset:%2" : "=v"(tv[q]) : "v"(vta ^ ((((q >> 2) * 2) ^ ((q & 1) * 4)) << 4)), "n"((q & 1) * 1024 + ((q >> 1) & 1) * 8192) : "memory");
      RSUM();
      BAR(31);
      RSUM();
      {
        asm volatile("s_nop 7\n\ts_nop 7" : "+v"(rs));
        const float v0 = dpp_add<0x141>(dpp_add<0x4E>(dpp_add<0xB1>(rs[0]))), v = cc ? -v0 : v0;
        if ((lane & 7) == 0)
          asm volatile("global_store_dword %0, %1, %2 sc1 ; ALWAYS" ::"v"(O_RS + (cc * S + nt * 128 + mt * 16 + aw * 8 + (lane >> 3)) * 4), "v"(min(pack2(v, v - rbf(v)), SENT - 1u)), "s"(ws) : "memory");
      }
      BAR(32);
      __builtin_amdgcn_s_sleep(2);
      DLD(0, 8 - B_WN);
      __builtin_amdgcn_s_setprio(2);
      {
        asm volatile("" : W8(tv), W8((tv + 8))::"memory");
        f32x4 d[4];
#pragma unroll
        for (int j = 0; j < 4; ++j) {
          d[j] = __builtin_amdgcn_mfma_f32_16x16x32_bf16(BF8((u32x4{tv[4 * j][0], tv[4 * j][1], tv[4 * j + 1][0], tv[4 * j + 1][1]})), BF8(g0), f32x4{0.f, 0.f, 0.f, 0.f}, 0, 0, 0);
          d[j] = __builtin_amdgcn_mfma_f32_16x16x32_bf16(BF8((u32x4{tv[4 * j + 2][0], tv[4 * j + 2][1], tv[4 * j + 3][0], tv[4 * j + 3][1]})), BF8(g1), d[j], 0, 0, 0);
        }
        u32x2 vo;
#pragma unroll
        for (int i = 0; i < 2; ++i) {
          const unsigned e0 = pack2(d[0][2 * i], d[0][2 * i + 1]), e1 = pack2(d[1][2 * i], d[1][2 * i + 1]), e2 = pack2(d[2][2 * i], d[2][2 * i + 1]), e3 = pack2(d[3][2 * i], d[3][2 * i + 1]);
          vo[i] = min((lane & 2) ? ((lane & 1) ? e3 : e2) : ((lane & 1) ? e1 : e0), SENT - 1u);
        }
        if ((lane & 12) == 0) asm volatile("global_store_dwordx2 %0, %1, %2 sc1 ; ALWAYS" ::"v"(vso), "v"(vo), "s"(ws) : "memory");
      }
      asm volatile("" : W8(hv)::"memory");
      {
        f32x4 qa[2] = {};
#pragma unroll
        for (int kk = 0; kk < 4; ++kk) {
          const bf16x8 sb8 = *reinterpret_cast<const bf16x8*>(ssa + kk * 32 + (lane >> 4) * 8);
#pragma unroll
          for (int r = 0; r < 2; ++r)
            qa[r] = __builtin_amdgcn_mfma_f32_16x16x32_bf16(__builtin_bit_cast(bf16v8, hv[r * 4 + kk]), __builtin_bit_cast(bf16v8, sb8), qa[r], 0, 0, 0);
        }
        if ((lane & 15) < 2)
          asm volatile("global_store_dwordx4 %0, %1, %2 sc1 ; ALWAYS\n\ts_nop 1" ::"v"(O_RS + (4096 + (cc * 8 + mt) * D + nt * 64 + aw * 32 + (lane & 1) * 16 + (lane >> 4) * 4) * 4),
                       "v"(nsent4(selu((lane & 1) ? ~0u : 0u, __builtin_bit_cast(u32x4, qa[1]), __builtin_bit_cast(u32x4, qa[0])))), "s"(ws) : "memory");
      }
      BAR(33);
      {
        if (b >= NPART) WAIT_V(3);
        asm volatile("s_waitcnt vmcnt(%8)" : W8(wv) : "n"(8 - B_WN + 3) : "memory");
        TR8(wt, wv);
        const f32x2 o = MV64(wt, sc);
        GPUB(o, O_G2 + gpo * 8);
      }
      JSET();
      TSET();
      BAR(34);
#pragma unroll
      for (int k = 3; k <= B_J; ++k) {
        BAR(32 + k);
        if (k == V_K) {
          if (!aw) GP4(O_VP + (cc * 8 + mt) * 4096 + lane * 16, "");
          else VCLD("");
        }
      }
    }
    u32x4 dT[8];
    u32x4 m[3];
    u32x2 kk = {SENT, SENT};
#define K2LD(W) asm volatile("global_load_dwordx2 %0, %1, %2 sc1" W : "+v"(kk) : "v"(O_K2 + (lane & 15) * 64), "s"(ws) : "memory")
    {
      constexpr BSched sch;

      const unsigned lbase = lds0 + hw * 4096;
      u32x4 rr[8];
      unsigned cbv, t1, t2;
      float t[2];
#define RRLD(W)                                                                                                          \
  do {                                                                                                                   \
    asm volatile("global_load_dword %0, %1, %2 sc1" : "=&v"(cbv) : "v"(O_MOD + 4096 + cj * 2), "s"(ws) : "memory");      \
    LD8(rr, ro, ro + 65536, W);                                                                                          \
  } while (0)
      unsigned wq;
      u32x4 mq = {0u, 0u, 0u, 0u};
#define QLD(W)                                                                                                        \
  do {                                                                                                                \
    if (hw < 2) asm volatile("global_load_dwordx4 %0, %1, %2 sc1" W : "+v"(mq) : "v"(qo), "s"(ws) : "memory");        \
    else asm volatile("global_load_dword %0, %1, %2 sc1" W : "+v"(mq[0]) : "v"(qo), "s"(ws) : "memory");              \
  } while (0)
#define QRLD(W)                                                                                                       \
  asm volatile("global_load_dwordx4 %0, %2, %4 sc1\n\tglobal_load_dwordx4 %1, %3, %4 sc1" W : "=&v"(gp[0]), "=&v"(gp[1]) \
               : "v"(qr), "v"(qr + 32768), "s"(ws) : "memory")
      const bool rq_ = b >= NPART && hw == 1;
      const unsigned qr = O_RS + (4096 + (lane >> 3) * D + (b - NPART) * 32 + (lane & 7) * 4) * 4;
#define MODLD() asm volatile("global_load_dwordx4 %0, %2, %4 sc1\n\tglobal_load_dwordx4 %1, %3, %4 sc1" : "=&v"(m[0]), "=&v"(m[2]) : "v"(O_MOD + ma), "v"(mo2), "s"(ws) : "memory")
#define MODW(n) asm volatile("s_waitcnt vmcnt(%2)" : "+v"(m[0]), "+v"(m[2]) : "n"(n) : "memory")
#pragma unroll
      for (int k = B_J; k < 16; ++k) {
        if (k > B_J) {
          if (k == 2 * U_N1 - 1 && !urdy) UPOLL(7);
          WAIT_V(sch.cnt[k]);
          if (k == 15) WAIT_L(0);
          BAR(32 + k);
        }
        if (k > B_J && k - 2 + NS < 16) {
          const char* yp = Hc + ((mt * 2 + k - 2 + NS) & 15) * 128;
          const unsigned l = lbase + ((30 + k + NS) % NS) * 16384;
          GLDS4(l, voff, yp);
        }
        if (k == B_TK && hw == 1 && b >= 192) {
          const unsigned t = tick(), h = t + T_O + (((int)(t - ts0) - T_N) * T_A >> 8);
          if (lane == 0) asm volatile("global_store_dword %0, %1, %2 sc1" ::"v"(O_M2 + 8448 + (b - 192) * 4), "v"(min(h, SENT - 1u)), "s"(ws) : "memory");
        }
        if (k == B_RAT + 2) {
          if (rdc) {
            WAIT_V(sch.rcnt);
            asm volatile("" : W8(rr), "+v"(cbv));
            POLL(__all((TG2(rr, 0) | TG2(rr, 2) | TG2(rr, 4) | TG2(rr, 6)) == 0u && cbv != SENT), 2, RRLD(VW0); asm volatile("" : "+v"(cbv)));
#pragma unroll
            for (int j = 0; j < 2; ++j) {
#define RF(q) __uint_as_float(rr[q][2 * j])
              t[j] = ((RF(0) + RF(1)) + (RF(2) + RF(3))) + ((RF(4) + RF(5)) + (RF(6) + RF(7)));
            }
            asm volatile("" : "+v"(t[0]), "+v"(t[1]));
          }
        }
        if (k == B_RAT + 2 && rdc) {
          const unsigned o = pack2(swap32_add(t[0], t[0]), swap32_add(t[1], t[1])), f = gbq ? cbv : o;
          const float pr = row_sum(bflo(f) * bflo(wq) + bfhi(f) * bfhi(wq)), ps = swap16_add(pr, pr);
          if (lane <= 32) asm volatile("global_store_dword %0, %1, %2 sc1" ::"v"(ra1), "v"(min(lane < 32 ? o : __float_as_uint(ps), SENT - 1u)), "s"(ws) : "memory");
          TS(12);
        }
        if (k == V_T && hw == 3) {
          WAIT_V(sch.vcnt);
          asm volatile("" : "+v"(vc));
          POLL(__all(vc != SENT), 2, VCLD(VW0));
        }
        if (k == V_T && hw == 2) {
          WAIT_V(sch.vcnt);
          asm volatile("" : "+v"(gp[0]), "+v"(gp[1]), "+v"(gp[2]), "+v"(gp[3]));
          POLL(!__any(GPBAD), 2, GP4(O_VP + (cc * 8 + mt) * 4096 + lane * 16, VW0));
          const f32x4 d = GPSUM();
          if (lane < 32) *reinterpret_cast<u32x2*>(shm + VT + (lane & 15) * 16 + g4 * 8) = u32x2{pack2(d[0], d[1]), pack2(d[2], d[3])};
          WAIT_L(0);
        }
        if (k == N_C) {
          f32x4 n[2] = {};
#pragma unroll
          for (int q = 0; q < 8; ++q)
            n[q >> 2] = __builtin_amdgcn_mfma_f32_16x16x32_bf16(BF8(*reinterpret_cast<const u32x4*>(shm + VT + (q & 3) * 64 + g4 * 16)),
                                                                BF8(*reinterpret_cast<const u32x4*>(shm + STG + hw * 8192 + q * 1024 + lane * 16)), n[q >> 2], 0, 0, 0);
          if (lane < 16)
            asm volatile("global_store_dword %0, %1, %2 sc1" ::"v"(O_NP + nt * 4096 + (cc * 8 + mt) * 256 + hw * 64 + lane * 4), "v"(min(pack2(n[0][0], n[1][0]), SENT - 1u)), "s"(ws) : "memory");
          TS(33);
        }
        if (k == N_K + N_D && rnp) {
            WAIT_V(sch.ncnt);
            asm volatile("" : "+v"(gp[0]), "+v"(gp[1]), "+v"(gp[2]), "+v"(gp[3]));
            POLL(!__any(GPBAD), 2, GP4(O_NP + nt * 4096 + lane * 16, VW0));
            const unsigned r0 = nt * 128 + ((lane >> 2) & 3) * 32 + (lane & 3) * 4 + (g4 & 1) * 2;
        const f32x4 d = GPSUM();
        unsigned ky[4];
#pragma unroll
        for (int i = 0; i < 4; ++i) {
          const unsigned f = __float_as_uint(d[i]);
          ky[i] = (((f ^ ((unsigned)((int)f >> 31) | 0x80000000u)) >> 1) & ~2047u) | (r0 + (i & 1) * 16 + (i >> 1));
        }
        t1 = max(max(ky[0], ky[1]), max(ky[2], ky[3])), t2 = max(min(max(ky[0], ky[1]), max(ky[2], ky[3])), max(min(ky[0], ky[1]), min(ky[2], ky[3])));
        asm volatile("" : "+v"(t1), "+v"(t2));
        }
        if (k == N_K + N_D && rnp) {
        TOP2(0xB1); TOP2(0x4E); TOP2(0x141); TOP2(0x140);
        TOP2S(__builtin_amdgcn_permlane16_swap);
            if (lane == 0) asm volatile("global_store_dwordx2 %0, %1, %2 sc1" ::"v"(O_K2 + nt * 64), "v"(u32x2{t1, t2}), "s"(ws) : "memory");
            TS(34);
        }
        if (k == N_K && rnp) GP4(O_NP + nt * 4096 + lane * 16, "");
        if (k == B_RAT + 2 && rq_) {
          WAIT_V(sch.rcnt);
          asm volatile("" : "+v"(gp[0]), "+v"(gp[1]));
          POLL(__all(max(max4(gp[0]), max4(gp[1])) != SENT), 2, QRLD(VW0));
          u32x4 a;
#pragma unroll
          for (int c = 0; c < 2; ++c) {
            const f32x4 t = __builtin_bit_cast(f32x4, gp[c]);
            const float r = dpp_add<0x128>(swap16_add(swap32_add(t[0], t[2]), swap32_add(t[1], t[3])));
            a[2 * c] = pack2(r, r);
            a[2 * c + 1] = pack2(r - bflo(a[2 * c]), r - bflo(a[2 * c]));
          }
          if ((lane & 8) == 0)
            asm volatile("global_store_dwordx4 %0, %1, %2 sc1\n\ts_nop 1" ::"v"(O_QT + ((b - NPART) * 32 + (lane & 1) * 16 + ((lane >> 1) & 3) * 4 + g4) * 16), "v"(nsent4(a)), "s"(ws) : "memory");
        }
        if (k == B_QAT + B_QD) {
          asm volatile("s_waitcnt vmcnt(%1)" : "+v"(mq) : "n"(sch.qcnt) : "memory");
          POLL(__all(max4(mq) != SENT), 2, QLD(VW0));
          if (hw < 2) asm volatile("ds_write_b128 %0, %1" ::"v"(qa), "v"(mq) : "memory");
          else asm volatile("ds_write2_b32 %0, %1, %1 offset1:1" ::"v"(qa), "v"(mq[0]) : "memory");
        }
        if (k == CPF_B && hw == 2) CPFL();
        if (k == B_RAT) {
          if (rdc) {
            RRLD("");
            asm volatile("ds_read_b32 %0, %1\n\ts_waitcnt lgkmcnt(0)" : "=v"(wq) : "v"(wqa) : "memory");
          }
          if (rq_) QRLD("");
        }
        if (k == X_K) {
          const unsigned v4[4] = {xo, (xo ^ 64) + 8192 - 1024, (xo ^ 128) + 16384 - 2048, (xo ^ 192) + 24576 - 3072};
          unsigned l2 = lbase;
          asm volatile("" : "+s"(l2));
          GLDS4(l2 * 2 - lds0 + STG + 4096, v4, x);
        }
        if (k == V_T + 1 || k == N_K + 1) SKEWPT();
        if (k == B_QAT) QLD("");
        if (k == B_MAT) MODLD();
        if (k == B_WK) { DLD(8 - B_WN, 8); }
      }
      if (b >= NPART) WAIT_V(0);
      MODW(sch.mcnt);
      TS(30);
      SKEWPT();
      POLL(__all(max(max4(m[0]), max4(m[2])) != SENT), 4, MODLD(); MODW(0));
      *reinterpret_cast<u32x4*>(shm + SPARE + ma) = m[0];
      if (hw == 0) {
        asm volatile("ds_write_b128 %0, %1" ::"v"(ma2), "v"(m[2]) : "memory");
      } else if (hw == 2) {
        int d = max(max((int)(m[2][0] - ts0), (int)(m[2][1] - ts0)), max((int)(m[2][2] - ts0), (int)(m[2][3] - ts0)));
        d = max(d, (int)dpp_u<0xB1>(d));
        d = max(d, (int)dpp_u<0x4E>(d));
        d = max(d, (int)dpp_u<0x141>(d));
        d = max(d, (int)dpp_u<0x140>(d));
        const unsigned hh = ts0 + d;
        ssel[7] = hh;
        if ((int)(tick() - (unsigned)__builtin_amdgcn_readfirstlane((int)hh) - T_K) >= 0) K2LD("");
      } else {
        const f32x4 v = __builtin_bit_cast(f32x4, m[2]);
        const f32x2 c = {v[0] + v[1], v[2] + v[3]};
        const float s4 = dpp_add<0x4E>(dpp_add<0xB1>(c[0] + c[1]));
        if (lane < 8) asm volatile("ds_write_b64 %0, %1" ::"v"(ma2), "v"(lane < 4 ? c : f32x2{s4, s4}) : "memory");
      }
    }
    if (hw != 2) __builtin_amdgcn_s_setprio(0);
    TS(17);
    BT(48);
    LBAR();
    BTE();
    TS(18);
    if (hw == 2) {
      WAIT_V(0);
      asm volatile("" : "+v"(kk));
      if (__any(max(kk[0], kk[1]) == SENT)) {
        nap_tick((unsigned)__builtin_amdgcn_readfirstlane((int)ssel[7]) + T_K);
        POLL2(max(kk[0], kk[1]) == SENT, K_S, PCAP, K2LD(VW0));
      }
      TS(35);
      const unsigned me = lane < 32 ? kk[0] : kk[1], ot = (lane & 16) ? kk[1] : kk[0];
      unsigned rk = ot > me;
      sfor<15>([&](auto r) __attribute__((always_inline)) { rk += dpp_u<0x121 + decltype(r)::value>(ot) > me; });
      auto rs_ = __builtin_amdgcn_permlane16_swap(rk, rk, false, false);
      rk = rs_[0] + rs_[1];
      if (rk < 8u) {
        *reinterpret_cast<unsigned*>(shm + LST + rk * 4) = 0x8ffeu - (me & 2047u) * 2;
        if ((lane & 15) == nt) *reinterpret_cast<unsigned*>(shm + SLT + (me & 127u) * 4) = rk + 1;
      }
      asm volatile("" ::: "memory");
      ssel[20] = 1u;
    }
    TS(19);
    unsigned sv, one = 1u, kmk = ~0u;
    RETIRE();
    const u32x4 cbq = *reinterpret_cast<const u32x4*>(shm + SPARE + 4096 + kca), sbq = *reinterpret_cast<const u32x4*>(shm + SPARE + 2048 + kca);
    f32x4 cb2[2] = {lo4(cbq), hi4(cbq)}, sb2[2] = {lo4(sbq), hi4(sbq)};
    float sw = cwj[8], cj8 = cwj[lane & 7];
    asm volatile("" : "+v"(cb2[0]), "+v"(cb2[1]), "+v"(sb2[0]), "+v"(sb2[1]), "+v"(sw), "+v"(cj8), "+v"(one), "+v"(kmk));
    const unsigned th = (unsigned)__builtin_amdgcn_readfirstlane((int)ssel[7]);
    const char* wb = ws;
#define P2PUT(i) if (__builtin_expect(wb != ws, 0)) { OWNL(); WAIT_V(0); if (lane == 0) asm volatile("global_store_dwordx2 %0, %1, %2 sc1" ::"v"(O_F2 + (i)*8192 + (g * 4 + hw) * 8 + lane), "v"(tag), "s"(ws) : "memory"); }
#define P2GET(o) if (__builtin_expect(wb != ws, 0)) { OWNL(); u64 f; POLL2(f != tag, 4, PCAP, asm volatile("global_load_dwordx2 %0, %1, %2 sc1" VW0 : "=&v"(f) : "v"(O_F2 + (o)), "s"(ws) : "memory")); }
#pragma unroll 1
    for (;;) {
    TS(20);
    if (wb == ws) {
      unsigned r_;
      POLL2(sv == 0u, 2, 1 << 26, asm volatile("ds_read_b32 %0, %2 offset:80\n\tds_read_b32 %1, %3\n\ts_waitcnt lgkmcnt(0)" : "=&v"(sv), "=&v"(r_) : "v"(sfa), "v"(rk2) : "memory"));
      rk2 = r_;
    }
    TS(21);
    nap_tick(th + T_C3 - 10);
    {
      u32x4 dn;
      POLL2((dn[0] & dn[1] & dn[2] & dn[3]) == 0u, 1, 1 << 26, asm volatile("ds_read_b128 %0, %1 offset:96\n\ts_waitcnt lgkmcnt(0)" : "=v"(dn) : "v"(sfa) : "memory"));
    }
    __builtin_amdgcn_s_setprio(H_P);
    wait_tick(th + T_C3);
    TS(36);
    u32x4 st, xv_;
    POLL2(max(max4(xv_), max4(st)) == SENT, 0, PCAP,
          asm volatile("global_load_dwordx4 %0, %2, %4 sc1\n\tglobal_load_dwordx4 %1, %3, %4 sc1" VW0 : "=&v"(st), "=&v"(xv_) : "v"(sto), "v"(xo_), "s"(ws) : "memory"));
    TS(22);
    SKEWPT();
#define SUM8(v) dpp_add<0x141>(dpp_add<0x4E>(dpp_add<0xB1>(v)))
    const f32x4 sf = __builtin_bit_cast(f32x4, st);
    const float mean = SUM8(sf[0]) * 0.125f, dm = sf[0] - mean;
    const float rstd = __builtin_amdgcn_rsqf(SUM8(sf[1] + 128.0f * (dm * dm)) * (1.0f / D) + 1e-5f);
    asm volatile("ds_write_b128 %0, %1\n\tds_write_b32 %2, %3 offset:64" ::"v"(hwa),
                 "v"(pk8(((lo4(xv_) - mean) * rstd) * cb2[0] + sb2[0], ((hi4(xv_) - mean) * rstd) * cb2[1] + sb2[1])), "v"(sfw), "v"(one) : "memory");
    TS(3);
    u32x4 ha[8], fg;
#define HSLD(W)                                                                                                                             \
  asm volatile(                                                                                                                             \
      "ds_read_b128 %8, %11 offset:64\n\tds_read_b128 %0, %9\n\tds_read_b128 %1, %10 offset:512\n\tds_read_b128 %2, %9 offset:1024\n\t"       \
      "ds_read_b128 %3, %10 offset:1536\n\tds_read_b128 %4, %9 offset:2048\n\tds_read_b128 %5, %10 offset:2560\n\t"                          \
      "ds_read_b128 %6, %9 offset:3072\n\tds_read_b128 %7, %10 offset:3584" W                                                               \
      : "=&v"(ha[0]), "=&v"(ha[1]), "=&v"(ha[2]), "=&v"(ha[3]), "=&v"(ha[4]), "=&v"(ha[5]), "=&v"(ha[6]), "=&v"(ha[7]), "=&v"(fg)           \
      : "v"(hra), "v"(hrb), "v"(sfa)                                                                                                        \
      : "memory")
    unsigned key = (mkkey(rstd * SUM8(sf[2] + dm * cj8) + sw) | rk2) & kmk;
    asm volatile("" : "+v"(key));
    if (H_O == 0) HSLD("");
    unsigned t1 = key, t2 = 0u;
    TOP2(0x140);
    if (H_O == 1) { asm volatile("" : "+v"(t1), "+v"(t2)); HSLD(""); asm volatile("" : "+v"(t1), "+v"(t2)); }
    TOP2S(__builtin_amdgcn_permlane16_swap);
    if (H_O == 2) { asm volatile("" : "+v"(t1), "+v"(t2)); HSLD(""); asm volatile("" : "+v"(t1), "+v"(t2)); }
    TOP2S(__builtin_amdgcn_permlane32_swap);
    const int l1 = __builtin_ctzll(__builtin_amdgcn_ballot_w64(key == t1)), l2 = __builtin_ctzll(__builtin_amdgcn_ballot_w64(key == t2));
    unsigned mq[6];
    asm volatile("s_bfe_i32 %0, %6, 0x10003\n\ts_bfe_i32 %1, %7, 0x10003\n\ts_bfe_i32 %2, %6, 0x10004\n\ts_bfe_i32 %3, %7, 0x10004\n\t"
                 "s_bfe_i32 %4, %6, 0x10005\n\ts_bfe_i32 %5, %7, 0x10005\n\ts_xor_b32 %4, %4, 0xffff\n\ts_xor_b32 %5, %5, 0xffff"
                 : "=&s"(mq[0]), "=&s"(mq[1]), "=&s"(mq[2]), "=&s"(mq[3]), "=&s"(mq[4]), "=&s"(mq[5]) : "s"(l1), "s"(l2) : "scc");
    const u64 m0 = mk64(mq[1], mq[0]), m1 = mk64(mq[3], mq[2]), m2 = mk64(mq[5], mq[4]);
    asm volatile("s_waitcnt lgkmcnt(0)" : W8(ha), "+v"(fg)::"memory");
    TS(2);
    if (b < NPART) {
#define HBAD ((fg[0] & fg[1] & fg[2] & fg[3]) != one)
      if (__any(HBAD)) POLL2(HBAD, 0, 1 << 26, HSLD("\n\ts_waitcnt lgkmcnt(0)"));
      TS(23);
      f32x4 dg = {0.f, 0.f, 0.f, 0.f}, du = {0.f, 0.f, 0.f, 0.f};
#pragma unroll
      for (int f = 0; f < 8; ++f) {
        dg = __builtin_amdgcn_mfma_f32_16x16x32_bf16(BF8(ha[f]), BF8(gT[f]), dg, 0, 0, 0);
        du = __builtin_amdgcn_mfma_f32_16x16x32_bf16(BF8(ha[f]), BF8(uT[f]), du, 0, 0, 0);
      }
      const bool s0 = __builtin_amdgcn_inverse_ballot_w64(m0), s1 = __builtin_amdgcn_inverse_ballot_w64(m1);
      if (__builtin_amdgcn_inverse_ballot_w64(m2))
        asm volatile("global_store_dword %0, %1, %3 sc1 ; ALWAYS\n\tglobal_store_dword %0, %2, %3 offset:512 sc1" ::"v"(gso),
                     "v"(nsent(s1 ? (s0 ? dg[3] : dg[2]) : (s0 ? dg[1] : dg[0]))), "v"(nsent(s1 ? (s0 ? du[3] : du[2]) : (s0 ? du[1] : du[0]))), "s"(wb) : "memory");
    }
    if (hw == 0 && wb == ws) asm volatile("ds_write_b64 %0, %1" ::"v"(sfa), "v"(u32x2{t1, t2}) : "memory");
    if (b < NPART) {
      TS(24);
      asm volatile("" : W8(wdv), "+v"(qm[0]), "+v"(qm[1]), "+v"(qm[2]), "+v"(qm[3])::"memory");
      TR8(dT, wdv);
      P2PUT(0);
      if (hw == 0) {
        u32x2 rr[8];
        if (E_D) __builtin_amdgcn_s_sleep(E_D);
        TS(39);
        P2GET(((((lane & 16) ? gl1 : gl0) - O_GU) >> 12) * 128 + (lane & 15) * 8);
#define MX2(i) max(max(rr[i][0], rr[i][1]), max(rr[i + 1][0], rr[i + 1][1]))
        POLL2(max(max(MX2(0), MX2(2)), max(MX2(4), MX2(6))) == SENT, 1, PCAP,
              TS(40);
              asm volatile("global_load_dwordx2 %0, %8, %10 sc1\n\tglobal_load_dwordx2 %1, %8, %10 offset:1024 sc1\n\t"
                           "global_load_dwordx2 %2, %8, %10 offset:2048 sc1\n\tglobal_load_dwordx2 %3, %8, %10 offset:3072 sc1\n\t"
                           "global_load_dwordx2 %4, %9, %10 sc1\n\tglobal_load_dwordx2 %5, %9, %10 offset:1024 sc1\n\t"
                           "global_load_dwordx2 %6, %9, %10 offset:2048 sc1\n\tglobal_load_dwordx2 %7, %9, %10 offset:3072 sc1" VW0
                           : "=&v"(rr[0]), "=&v"(rr[1]), "=&v"(rr[2]), "=&v"(rr[3]), "=&v"(rr[4]), "=&v"(rr[5]), "=&v"(rr[6]), "=&v"(rr[7])
                           : "v"(gl0), "v"(gl1), "s"(wb)
                           : "memory"));
        TS(8);
#define R2(i) __builtin_bit_cast(f32x2, rr[i])
        const f32x2 gs = (R2(0) + R2(1)) + (R2(2) + R2(3)), us = (R2(4) + R2(5)) + (R2(6) + R2(7));
        const unsigned gp = pack2(gs[0], gs[1]), up = pack2(us[0], us[1]);
        const unsigned sp = pack2(bflo(gp) * __builtin_amdgcn_rcpf(1.0f + __expf(-bflo(gp))), bfhi(gp) * __builtin_amdgcn_rcpf(1.0f + __expf(-bfhi(gp))));
        asm volatile("ds_write_b32 %0, %1\n\tds_write_b32 %2, %3 offset:24" ::"v"(awa), "v"(pack2(bflo(sp) * bflo(up), bfhi(sp) * bfhi(up))), "v"(sfa), "v"(one) : "memory");
      }
      TS(25);
      SKEWPT();
      POLL2(sv != one, 0, 1 << 26, asm volatile("ds_read_b32 %1, %3 offset:24\n\tds_read_b128 %0, %2\n\ts_waitcnt lgkmcnt(0)" : "=&v"(fg), "=&v"(sv) : "v"(ara), "v"(sfa) : "memory"));
      asm volatile("global_load_dwordx2 %0, %1, %2 sc1" : "=&v"(vd) : "v"(O_VD + (b & 31) * 128), "s"(ws) : "memory");
      const f32x2 dd = MV64(dT, fg);
      asm volatile("global_store_dwordx2 %0, %1, %2 sc1" ::"v"(pso), "v"(u32x2{nsent(dd[0]), nsent(dd[1])}), "s"(wb) : "memory");
      TS(6);
      P2PUT(1);
      asm volatile("s_waitcnt vmcnt(1)" : "+v"(vd)::"memory");
      if (__any(max(vd[0], vd[1]) == SENT)) VDLD(2);
      TS(5);
    } else if (hw == 3) {
      OWNL();
      u32x2 g0, g1;
      nap_tick(th + T_C4);
      POLL2(min(min(g0[0], g0[1]), min(g1[0], g1[1])) == 0u, 2, PCAP,
            asm volatile("global_load_dwordx2 %0, %2, %4 sc1\n\tglobal_load_dwordx2 %1, %3, %4 sc1" VW0
                         : "=&v"(g0), "=&v"(g1) : "v"(O_KEY + lane * 8 * KSTR), "v"(O_KEY + (lane + 64) * 8 * KSTR), "s"(ws) : "memory"));
      const unsigned k1 = t1, k2 = t2;
      t1 = max(g0[0], g1[0]), t2 = max(min(g0[0], g1[0]), max(g0[1], g1[1]));
      TOP2(0xB1); TOP2(0x4E); TOP2(0x141); TOP2(0x140);
      TOP2S(__builtin_amdgcn_permlane16_swap);
      TOP2S(__builtin_amdgcn_permlane32_swap);
      vd = u32x2{max(t1, k1), max(min(t1, k1), max(t2, k2))};
      if (lane == 0) asm volatile("global_store_dwordx2 %0, %1, %2 sc1\n\tds_write_b64 %3, %1 offset:8" ::"v"(O_VD + (b & 31) * 128), "v"(vd), "s"(ws), "v"(sfa) : "memory");
      TS(29);
      break;
    } else if (hw < 2) {
      OWNL();
      const int tok = b & 1, c0 = ((b - NPART) >> 1) * 64 + hw * 32 + (lane & 7) * 4, col = c0 + g4;
      const unsigned td = tick(), kk = tok ? t2 : t1;
      const int ln = tok ? l2 : l1;
      unsigned v;
      POLL2(v == SENT, 1, PCAP, asm volatile("global_load_dword %0, %1, %2 sc1" VW0 : "=&v"(v) : "v"(O_X1C + ((__builtin_amdgcn_readlane(xo_, ln) - O_X1C) >> 11) * 2048 + (col >> 1) * 4), "s"(ws) : "memory"));
      float x1 = (col & 1) ? bfhi(v) : bflo(v);
      float h2 = rbf(((x1 - __int_as_float(__builtin_amdgcn_readlane(__float_as_int(mean), ln))) * __int_as_float(__builtin_amdgcn_readlane(__float_as_int(rstd), ln))) * bf2f(svb[2][col]) + bf2f(svb[1][col]));
      float gb = bf2f(svb[3][col]), rsg = rbf(1.0f / (1.0f + __expf(-key_val(kk))));
      unsigned oo_ = (key_row(kk) * D + col) * 2, po = O_PART + ((lane >> 3) * 2048 + tok * 1024 + c0) * 4, p1 = po + 4 * 65536;
      asm volatile("" : "+v"(x1), "+v"(h2), "+v"(gb), "+v"(rsg), "+v"(oo_), "+v"(po), "+v"(p1));
      u32x4 y[7];
      nap_tick(td + F_T - 10);
      wait_tick(td + F_T);
      TS(37);
      P2GET(8192 + (min(lane, 55) * 16 + ((b - NPART) >> 1)) * 8);
      POLL2(max(max(max(max4(y[0]), max4(y[1])), max(max4(y[2]), max4(y[3]))), max(max(max4(y[4]), max4(y[5])), max4(y[6]))) == SENT || vd[1] == 0u, 0, PCAP,
            TS(38);
            asm volatile("global_load_dwordx4 %0, %7, %9 sc1\n\tglobal_load_dwordx4 %1, %7, %10 sc1\n\tglobal_load_dwordx4 %2, %7, %11 sc1\n\t"
                         "global_load_dwordx4 %3, %7, %12 sc1\n\tglobal_load_dwordx4 %4, %8, %9 sc1\n\tglobal_load_dwordx4 %5, %8, %10 sc1\n\t"
                         "global_load_dwordx4 %6, %8, %11 sc1"
                         : "=&v"(y[0]), "=&v"(y[1]), "=&v"(y[2]), "=&v"(y[3]), "=&v"(y[4]), "=&v"(y[5]), "=&v"(y[6])
                         : "v"(po), "v"(p1), "s"(wb), "s"(wb + 65536), "s"(wb + 131072), "s"(wb + 196608)
                         : "memory");
            asm volatile("s_waitcnt vmcnt(2)\n\tds_read_b64 %7, %8 offset:8\n\ts_waitcnt vmcnt(0) lgkmcnt(0)"
                         : "+v"(y[0]), "+v"(y[1]), "+v"(y[2]), "+v"(y[3]), "+v"(y[4]), "+v"(y[5]), "+v"(y[6]), "=&v"(vd) : "v"(sfa) : "memory"));
      TS(28);
#define YF(i) __builtin_bit_cast(f32x4, y[i])
      const f32x4 t = ((YF(0) + YF(1)) + (YF(2) + YF(3))) + ((YF(4) + YF(5)) + YF(6));
      const float sum = dpp_add<0x128>(swap16_add(swap32_add(t[0], t[2]), swap32_add(t[1], t[3])));
      asm volatile("global_store_short %0, %1, %2" ::"v"(oo_), "v"(pack2(x1 + gb * (wb == ws && kk < vd[1] ? h2 : rbf(h2 + rbf(rbf(sum) * rsg))), 0.f)), "s"(out) : "memory");
      TS(15);
    } else {
      break;
    }
    if (__builtin_expect(wb != ws || __all(vd[1] == t2), 1)) break;
    OWNL();
    const unsigned tk = (lane & 8) ? vd[1] : vd[0], ns = tk == t1 ? l1 >> 3 : 8 + ((lane >> 3) & 1);
    rk2 = tk & 0xffffu, kmk = lane < 16 ? ~0u : 0u, one = 3u;
    xo_ = O_X1C + ns * 2048 + kca, sto = O_ST2 + ns * 128 + (lane & 7) * 16;
    wb = ws + O_P2;
    }
    TSE();
  }
}

extern "C" void kernel_launch(void* const* d_in, const int* in_sizes, int n_in, void* d_out, int out_size, void* d_ws, size_t ws_size,
                              hipStream_t stream) {
  const u16* x = (const u16*)d_in[0];
  const u16* wsr = (const u16*)d_in[1];
  const u16* wsi = (const u16*)d_in[2];
  const u16* whr = (const u16*)d_in[3];
  const u16* whi = (const u16*)d_in[4];
  const u16* w_r = (const u16*)d_in[6];
  const u16* w_gate = (const u16*)d_in[7];
  const u16* w_up = (const u16*)d_in[8];
  const u16* w_down = (const u16*)d_in[9];
  const u16* c = (const u16*)d_in[10];
  const u16* w_mod = (const u16*)d_in[11];
  u16* out = (u16*)d_out;
  k_main<<<256, 512, 0, stream>>>(x, (char*)d_ws, wsr, wsi, c, w_mod, whr, whi, w_r, w_gate, w_up, w_down, out);
}
```

```cpp
#include <hip/hip_runtime.h>
#include <hip/hip_bf16.h>
#include <stdint.h>
#include <utility>

#define NEWLAY 1
typedef unsigned short u16;
typedef unsigned long long u64;
using bf16x8 = __attribute__((ext_vector_type(8))) short;
using bf16v8 = __attribute__((ext_vector_type(8))) __bf16;
using f32x4 = __attribute__((ext_vector_type(4))) float;
using f32x2 = __attribute__((ext_vector_type(2))) float;
using u32x4 = __attribute__((ext_vector_type(4))) unsigned;
using u32x2 = __attribute__((ext_vector_type(2))) unsigned;

constexpr int S = 2048, D = 1024, F = 3584;

__device__ __forceinline__ float bf2f(unsigned v) { return __uint_as_float(v << 16); }
__device__ __forceinline__ float bflo(unsigned v) { return __uint_as_float(v << 16); }
__device__ __forceinline__ float bfhi(unsigned v) { return __uint_as_float(v & 0xffff0000u); }
__device__ __forceinline__ unsigned f2bf(float f) {
  __hip_bfloat16 b = __float2bfloat16(f);
  return (unsigned)*reinterpret_cast<u16*>(&b);
}
__device__ __forceinline__ float rbf(float f) { return bf2f(f2bf(f)); }
using bf16v2 = __attribute__((ext_vector_type(2))) __bf16;
__device__ __forceinline__ unsigned pack2(float lo, float hi) {
  return __builtin_bit_cast(unsigned, __builtin_convertvector(f32x2{lo, hi}, bf16v2));
}

template <int CTRL>
__device__ __forceinline__ float dpp_f(float v) {
  return __int_as_float(__builtin_amdgcn_mov_dpp(__float_as_int(v), CTRL, 0xf, 0xf, true));
}
template <int CTRL>
__device__ __forceinline__ float dpp_add(float v) { return v + dpp_f<CTRL>(v); }
__device__ __forceinline__ float row_sum(float v) { return dpp_add<0x140>(dpp_add<0x141>(dpp_add<0x4E>(dpp_add<0xB1>(v)))); }
template <int CTRL>
__device__ __forceinline__ unsigned dpp_u(unsigned v) { return (unsigned)__builtin_amdgcn_mov_dpp((int)v, CTRL, 0xf, 0xf, true); }
__device__ __forceinline__ u64 mk64(unsigned hi, unsigned lo) { return ((u64)hi << 32) | lo; }
__device__ __forceinline__ float swap32_add(float a, float b) {
  auto r = __builtin_amdgcn_permlane32_swap(__float_as_uint(a), __float_as_uint(b), false, false);
  return __uint_as_float(r[0]) + __uint_as_float(r[1]);
}
__device__ __forceinline__ float swap16_add(float a, float b) {
  auto r = __builtin_amdgcn_permlane16_swap(__float_as_uint(a), __float_as_uint(b), false, false);
  return __uint_as_float(r[0]) + __uint_as_float(r[1]);
}
__device__ __forceinline__ float ar4(float v) {
  const float t = swap32_add(v, v);
  return swap16_add(t, t);
}
__device__ __forceinline__ float wave_sum_s(float v) {
  v = row_sum(v);
  v += __int_as_float(__builtin_amdgcn_update_dpp(0, __float_as_int(v), 0x142, 0xa, 0xf, false));
  v += __int_as_float(__builtin_amdgcn_update_dpp(0, __float_as_int(v), 0x143, 0xc, 0xf, false));
  return __int_as_float(__builtin_amdgcn_readlane(__float_as_int(v), 63));
}
__device__ __forceinline__ float silu_bf(float c) { return rbf(c * __builtin_amdgcn_rcpf(1.0f + __expf(-c))); }
__device__ __forceinline__ float dsum(unsigned v, float a) {
  return __builtin_amdgcn_fdot2_f32_bf16(__builtin_bit_cast(bf16v2, v), __builtin_bit_cast(bf16v2, 0x3f803f80u), a, false);
}
template <class Fn, int... I>
__device__ __forceinline__ void sfor_(Fn&& f, std::integer_sequence<int, I...>) { (f(std::integral_constant<int, I>{}), ...); }
template <int N, class Fn>
__device__ __forceinline__ void sfor(Fn&& f) { sfor_(f, std::make_integer_sequence<int, N>{}); }
constexpr unsigned SENT = 0xFFFFFFFFu;
constexpr int NPART = 224;

constexpr size_t WS_EPO = 0;
constexpr size_t WS_XFL = 4096;
constexpr size_t WS_MODP = 40960;
constexpr size_t WS_KEYG = 320u << 10;
constexpr size_t WS_UFL = WS_KEYG + 16384;
constexpr size_t WS_MOD = WS_KEYG + (80u << 10);
constexpr size_t WS_RS = WS_KEYG + (92u << 10);
constexpr size_t WS_PART = WS_KEYG + (460u << 10);
constexpr unsigned O_MODP = WS_MODP, O_XFL = WS_XFL, O_KEY = WS_KEYG, O_MOD = WS_MOD, O_RS = WS_RS, O_M2 = WS_RS + 80 * 1024;
constexpr unsigned O_PART = WS_PART, O_GU = O_PART + 56 * 8192, O_STAT = O_GU + 224 * 1024, O_X1C = O_STAT + 262144;
constexpr unsigned O_ST2 = O_X1C + 20480, O_VD = O_ST2 + 6144, O_P2 = 1536u << 10, O_F2 = 3584u << 10;
constexpr unsigned O_GC = O_M2 + 8704, O_VP = O_M2 + 12288, O_NP = O_VP + 65536, O_K2 = O_NP + 65536, O_QT = O_K2 + 4096, O_G2 = O_QT + 16384, O_G1 = 3328u << 10;
static_assert(O_G2 + 131072 == O_PART && O_VD + 4096 <= O_KEY + (512 + 960) * 1024 && O_PART + O_P2 >= O_KEY + (512 + 960) * 1024 && O_STAT + O_P2 <= O_G1, "armed area");
constexpr size_t WS_H = 4u << 20;
constexpr size_t WS_T = WS_H + (size_t)S * D * 2;
constexpr size_t WS_P = WS_T + (size_t)2 * D * S * 2;

#define TSG(base, i)
#define TSD(i)
#define PCAP (1 << 20)
#define PDBG(it)
#define TS(i)
#define TSE()
#define BT(k)
#define BTE()
#define BAR(k) do { BT(k); BARRIER(); } while (0)
#define WAIT_V(n) asm volatile("s_waitcnt vmcnt(%0)" ::"n"(n) : "memory")
#define WAIT_L(n) asm volatile("s_waitcnt lgkmcnt(%0)" ::"n"(n) : "memory")
#define BARRIER() asm volatile("s_barrier" ::: "memory")
#define LBAR() asm volatile("s_waitcnt lgkmcnt(0)\n\ts_barrier" ::: "memory")
#define DSR(dst, addr, off) asm volatile("ds_read_b128 %0, %1 offset:%2" : "=v"(dst) : "v"(addr), "n"(off) : "memory")
#define MFMA(c, a, b) asm volatile("v_mfma_f32_16x16x32_bf16 %0, %1, %2, %0" : "+v"(c) : "v"(a), "v"(b))
#define MFMA0(c, a, b) asm volatile("v_mfma_f32_16x16x32_bf16 %0, %1, %2, 0" : "=&v"(c) : "v"(a), "v"(b))
#define GLDS(m0v, voff, gbase) \
  asm volatile("s_mov_b32 m0, %0\n\ts_nop 0\n\tglobal_load_lds_dwordx4 %1, %2" ::"s"(m0v), "v"(voff), "s"(gbase) : "memory")
#define GLDS4(m0v, v, gbase)                                                                                                        \
  asm volatile("s_mov_b32 m0, %0\n\ts_nop 0\n\tglobal_load_lds_dwordx4 %1, %5\n\tglobal_load_lds_dwordx4 %2, %5 offset:1024\n\t"    \
               "global_load_lds_dwordx4 %3, %5 offset:2048\n\tglobal_load_lds_dwordx4 %4, %5 offset:3072" ::"s"(m0v),               \
               "v"((v)[0]), "v"((v)[1]), "v"((v)[2]), "v"((v)[3]), "s"(gbase) : "memory")
#define LDG(dst, voff, base) asm volatile("global_load_dwordx4 %0, %1, %2" : "=v"(dst) : "v"(voff), "s"(base) : "memory")
#define HPOL ""
#define XPOL ""
#define WPOL " nt"
#define LDM(dst, voff, base) asm volatile("global_load_dwordx4 %0, %1, %2" WPOL : "=v"(dst) : "v"(voff), "s"(base) : "memory")
#define LDX(dst, voff, base, off) asm volatile("global_load_dwordx4 %0, %1, %2 offset:%3" : "=v"(dst) : "v"(voff), "s"(base), "n"(off) : "memory")
#define LDXS(dst, voff, base, off) asm volatile("global_load_dwordx4 %0, %1, %2 offset:%3 sc1" : "=v"(dst) : "v"(voff), "s"(base), "n"(off) : "memory")
#define BF8(v) __builtin_bit_cast(bf16v8, v)
#define LSPIN(v, p, cond)                                   \
  _Pragma("unroll 1") for (int it_ = 0; it_ < (1 << 26); ++it_) { \
    v = __atomic_load_n(p, __ATOMIC_RELAXED);               \
    if (cond) break;                                        \
    __builtin_amdgcn_s_sleep(1);                            \
  }
#define POLL(ok, sl, reload)                                                  \
  if (__builtin_expect(!(ok), 0)) {                                           \
    _Pragma("unroll 1") for (int it = 0; it < PCAP; ++it) {                   \
      PDBG(it);                                                               \
      __builtin_amdgcn_s_sleep(sl);                                           \
      reload;                                                                 \
      if (ok) break;                                                          \
    }                                                                         \
  }
#define POLL2(bad, sl, cap, load)                                             \
  {                                                                           \
    int it = (cap);                                                           \
    u64 live;                                                                 \
    _Pragma("unroll 1") do {                                                  \
      if (sl) __builtin_amdgcn_s_sleep(sl);                                   \
      asm volatile("s_sub_u32 %0, %0, 1\n\ts_cselect_b64 %1, 0, -1" : "+s"(it), "=s"(live)::"scc"); \
      load;                                                                   \
    } while (__builtin_amdgcn_ballot_w64(bad) & live);                        \
    PDBG(PCAP - 2 - it);                                                      \
  }
__device__ __forceinline__ unsigned tick() {
  u64 t;
  asm volatile("s_memrealtime %0\n\ts_waitcnt lgkmcnt(0)" : "=s"(t)::"memory");
  return (unsigned)t;
}
__device__ __forceinline__ void wait_tick(unsigned t) {
#pragma unroll 1
  for (int i = 0; i < 256; ++i)
    if ((int)(tick() - t) >= 0) break;
}
__device__ __forceinline__ void nap_tick(unsigned t) {
#pragma unroll 1
  for (int i = 0; i < 1024; ++i) {
    if ((int)(tick() - t) >= 0) break;
    __builtin_amdgcn_s_sleep(2);
  }
}
#define T_C1 245
#define T_A 32
#define T_N 2500
#define T_D1 78
#define VW0 "\n\ts_waitcnt vmcnt(0)"
#define W8(a) "+v"(a[0]), "+v"(a[1]), "+v"(a[2]), "+v"(a[3]), "+v"(a[4]), "+v"(a[5]), "+v"(a[6]), "+v"(a[7])
#define PFL(ptr) asm volatile("s_mov_b32 m0, %0\n\ts_nop 0\n\tglobal_load_lds_dword %1, off" ::"s"(pfl), "v"(ptr) : "memory")

#define CPFL() asm volatile("s_mov_b32 m0, %0\n\ts_mov_b64 exec, 0xfff\n\tglobal_load_lds_dword %1, off\n\ts_mov_b64 exec, -1" ::"s"(pfl), "v"(cpf) : "memory")
#define HHOOK(j)
#define XPV 1557
#define XPW 1557
#define XPM (MODE ? XPW : XPV)
#define XPA0 (XPM / 1000)
#define XPA1 (XPM / 100 % 10)
#define XPB0 (XPM / 10 % 10)
#define XPB1 (XPM % 10)
#define U_X0 6
#define XLD(dst, XP, XH, i)                                                                                   \
  do {                                                                                                        \
    if (MODE) LDXS(dst, (i) ? xo1 : xo0, XP, ((XH)&3) * 2048 - 4096);                                         \
    else LDXS(dst, ((XH)&4) ? xo1 : xo0, XP, ((XH)&3) * 2048 + (i)*1024 - 4096);                              \
  } while (0)
#define HALF(XC, YA, RD, XN, XP, XH, LD, YP)                                                 \
  do {                                                                                       \
    _Pragma("unroll") for (int j = 0; j < 8; ++j) {                                          \
      if ((j & 1) == 0) { if (RD) WAIT_L(5); else WAIT_L(j == 0 ? 5 : 6 - j); }              \
      if (MODE == 2) MFMA0(acc[0][j], Y[j], XC[0]); else if (MODE) MFMA(acc[0][j], Y[j], XC[0]); else MFMA(acc[0][j], XC[0], Y[j]); \
      if (j == 0) DSR(Y[7], YP, 7 * 2048);                                                   \
      else if (RD) DSR(Y[j - 1], YA, (j - 1) * 2048);                                        \
      if (MODE == 2) MFMA0(acc[1][j], Y[j], XC[1]); else if (MODE) MFMA(acc[1][j], Y[j], XC[1]); else MFMA(acc[1][j], XC[1], Y[j]); \
      if ((LD) && j == (((XH)&1) ? XPA0 : XPB0)) XLD(XN[0], XP, XH, 0);                                             \
      if ((LD) && j == (((XH)&1) ? XPA1 : XPB1)) XLD(XN[1], XP, XH, 1);                                             \
      HHOOK(j);                                                                              \
    }                                                                                        \
  } while (0)

__device__ __forceinline__ int key_row(unsigned k) { return (int)((~k >> 1) & 2047u); }
__device__ __forceinline__ unsigned mkkey(float v) {
  const unsigned rb = pack2(0.f, v);
  return rb ^ ((unsigned)((int)rb >> 31) & 0x7fff0000u) ^ 0x80000000u;
}
__device__ __forceinline__ float key_val(unsigned k) {
  const unsigned ord = k >> 16;
  return bf2f(ord ^ ((ord & 0x8000u) ? 0x8000u : 0xffffu));
}
__device__ __forceinline__ unsigned nsent(float v) { return min(__float_as_uint(v), SENT - 1u); }
__device__ __forceinline__ f32x4 lo4(u32x4 q) { return f32x4{bflo(q[0]), bfhi(q[0]), bflo(q[1]), bfhi(q[1])}; }
__device__ __forceinline__ f32x4 hi4(u32x4 q) { return f32x4{bflo(q[2]), bfhi(q[2]), bflo(q[3]), bfhi(q[3])}; }
__device__ __forceinline__ u32x4 pk8(f32x4 a, f32x4 b) { return u32x4{pack2(a[0], a[1]), pack2(a[2], a[3]), pack2(b[0], b[1]), pack2(b[2], b[3])}; }
__device__ __forceinline__ u32x4 nsent4(u32x4 v) { return u32x4{min(v[0], SENT - 1u), min(v[1], SENT - 1u), min(v[2], SENT - 1u), min(v[3], SENT - 1u)}; }
__device__ __forceinline__ unsigned max4(u32x4 v) { return max(max(v[0], v[1]), max(v[2], v[3])); }
__device__ __forceinline__ float amin16(const u32x4* v) {
  float a, b;
  asm("v_min3_f32 %0, |%2|, |%3|, |%4|\n\tv_min3_f32 %1, |%5|, |%6|, |%7|\n\tv_min3_f32 %0, |%8|, |%9|, %0\n\tv_min3_f32 %1, |%10|, |%11|, %1\n\t"
      "v_min3_f32 %0, |%12|, |%13|, %0\n\tv_min3_f32 %1, |%14|, |%15|, %1\n\tv_min3_f32 %0, |%16|, |%17|, %0\n\tv_min_f32 %0, %0, %1"
      : "=&v"(a), "=&v"(b)
      : "v"(v[0][0]), "v"(v[0][1]), "v"(v[0][2]), "v"(v[0][3]), "v"(v[1][0]), "v"(v[1][1]), "v"(v[1][2]), "v"(v[1][3]), "v"(v[2][0]), "v"(v[2][1]),
        "v"(v[2][2]), "v"(v[2][3]), "v"(v[3][0]), "v"(v[3][1]), "v"(v[3][2]), "v"(v[3][3]));
  return a;
}
__device__ __forceinline__ u32x4 selu(unsigned mk, u32x4 a, u32x4 b) { return (a & mk) | (b & ~mk); }
#define KSTR 16
#define TOP2(CTRL)                                                  \
  do {                                                              \
    const unsigned o1 = dpp_u<CTRL>(t1), o2 = dpp_u<CTRL>(t2);      \
    t2 = max(min(t1, o1), max(t2, o2));                             \
    t1 = max(t1, o1);                                               \
  } while (0)
#define TOP2S(SW)                                                   \
  do {                                                              \
    auto a_ = SW(t1, t1, false, false), b_ = SW(t2, t2, false, false); \
    t2 = max(min(a_[0], a_[1]), max(b_[0], b_[1]));                 \
    t1 = max(a_[0], a_[1]);                                         \
  } while (0)
#define T_C3 253
#define T_C4 340
#define E_D 6
#define O_DW 6
#define O_DS 4
#define OPOL " sc1"
#define X_PA 2
#define G1_C 2
#define R_AT 24
#define B_TK 7
#define W_T 4
#define V_K 3
#define V_T 5
#define N_K 9
#define N_D 2
#define T_K 0
#define H_AT 23
#define G1_S 1
#define F_T 142
#define K_S 1
#define N_C (V_T + 1)
#define T_O 250
#define A_S1 9
#define A_S2 9
#define A_P2 12
#define X_K 13
#define H_P 2
#define H_O 2
#define S_R 2
#define T_X 80
#define A_PS 3
#define U_S 4
#define C_LD 2
#define C_DP 8
#define F_D 2
#define F_AT 29
#define PW0 2
#define T_W 70
#define P_D2 1
#define P_D4 1
#define XSTR 16
#define P_D3 20
#ifndef M_NS
#define M_NS 6
#endif
#define A_Q1 27
#define B_RAT 5
#define B_MAT 13
#define U_D 4
#define FSTR 16
#define B_J 4
#define RETIRE()                                                                                                              \
  do {                                                                                                                        \
    if (hw == 3 && lane < (b == 255 ? 2 : 1))                                                                                 \
      asm volatile("global_store_dwordx2 %0, %1, %2 sc1" ::"v"(lane ? (unsigned)WS_EPO : O_XFL + b * XSTR * 4), "v"(lane ? tag : ~tag), "s"(ws) : "memory"); \
  } while (0)
#define U_N1 3
#define B_QAT 12
#define B_QD 2
#define TP_PS 5
#define TP_XC 34
#define TP_P0 2
#define TP_X0 24
#define TP_XL 54
#define W_UO 3
#define CPF_K 24
#define CPF_B 8
#define B_WN 4
#define B_WK 15
struct BSched {
  int cnt[16], rcnt, mcnt, qcnt, vcnt, ncnt;
  constexpr BSched() : cnt{}, rcnt(0), mcnt(0), qcnt(0), vcnt(0), ncnt(0) {
    int pos[16] = {}, n = 0, rpos = 0, mpos = 0, qpos = 0, npos = 0;
    for (int k = 0; k <= B_J - 2 + M_NS; ++k) pos[k] = -8 * (B_J - 2 + M_NS - k);
    for (int k = B_J + 1; k < 16; ++k) {
      cnt[k] = n - pos[k] > 63 ? 63 : n - pos[k];
      if (k - 2 + M_NS < 16) { n += 4; pos[k - 2 + M_NS] = n; }
      if (k == B_RAT + 2) rcnt = n - rpos;
      if (k == B_QAT + B_QD) qcnt = n - qpos;
      if (k == V_T) vcnt = n;
      if (k == N_K + N_D) ncnt = n - npos;
      if (k == N_K) npos = n;
      if (k == B_RAT) rpos = n;
      if (k == X_K) n += 4;
      if (k == B_QAT) { n += 1; qpos = n; }
      if (k == B_MAT) { n += 2; mpos = n; }
      if (k == B_WK) n += B_WN;
    }
    mcnt = n - mpos > 63 ? 63 : n - mpos;
  }
};
__global__ __launch_bounds__(512, 2) void k_main(const u16* __restrict__ x, char* ws, const u16* __restrict__ Y0, const u16* __restrict__ Y1,
                                              const u16* __restrict__ cvec, const u16* __restrict__ wmod, const u16* __restrict__ X0,
                                              const u16* __restrict__ X1, const u16* __restrict__ wr_, const u16* __restrict__ wg,
                                              const u16* __restrict__ wu, const u16* __restrict__ wd, u16* __restrict__ out) {
  constexpr int DX = 4, NS = M_NS, STG = NS * 16384, SPARE = STG + 32768;
  constexpr int LST = SPARE + 17664, SLT = LST + 256, XC = SLT + 512, VT = XC + 4096;
  __shared__ __attribute__((aligned(1024))) char shm[VT + 256];
  u16(*svb)[1024] = reinterpret_cast<u16(*)[1024]>(shm + SPARE);
  float* cwj = reinterpret_cast<float*>(shm + SPARE + 10240);
  unsigned* ssel = reinterpret_cast<unsigned*>(shm + SPARE + 10368);
  float* smod = reinterpret_cast<float*>(shm + SPARE + 15872);
  u16* ssa = reinterpret_cast<u16*>(smod);
  constexpr int TAB = SPARE + 10752, RKA = TAB + 1536, RKB = TAB + 3584;
  u16(*scw)[128] = reinterpret_cast<u16(*)[128]>(shm + TAB + 512);
  const int b = blockIdx.x, xcd = b & 7, j8 = b >> 3;
  const int cc = xcd >> 2, nt = (xcd & 3) * 4 + (j8 & 3), mt = j8 >> 2, et = mt;
  const int tid = threadIdx.x, lane = tid & 63;
  const int wid = __builtin_amdgcn_readfirstlane(tid >> 6);
  const unsigned lds0 = (unsigned)(size_t)(__attribute__((address_space(3))) char*)shm;
  const char* cpf;
  {
    unsigned long long pc;
    unsigned len;
    asm volatile("s_getpc_b64 %0\n.Lcpf_here:\n\ts_mov_b32 %1, .Lcpf_end-.Lcpf_here-4\n\t.subsection 1\n.Lcpf_end:\n\t.subsection 0" : "=s"(pc), "=s"(len));
    cpf = reinterpret_cast<const char*>(pc) + min((unsigned)((lane * 32 + j8) * 128), len);
  }
  const unsigned pfl = lds0 + SPARE + 17408;
  if (wid == 4) CPFL();
  u32x4 xv[2];
  {
    const unsigned xo = (unsigned)((b * 8 + wid) * D + lane * 8) * 2;
    asm volatile("global_load_dwordx4 %0, %2, %3" XPOL "\n\tglobal_load_dwordx4 %1, %2, %3 offset:1024" XPOL : "=&v"(xv[0]), "=&v"(xv[1]) : "v"(xo), "s"(x) : "memory");
  }
  asm volatile("" ::"s"(ws), "s"(Y0), "s"(Y1), "s"(cvec), "s"(wmod), "s"(wr_));
  u16* xn = reinterpret_cast<u16*>(ws + WS_H);
  u16* U = reinterpret_cast<u16*>(ws + WS_T);
  unsigned* part = reinterpret_cast<unsigned*>(ws + WS_PART);
  const int g = (b & 7) * 28 + (b >> 3);
  unsigned* ufl = reinterpret_cast<unsigned*>(ws + WS_UFL) + (cc * 16 + nt) * 32 * FSTR;
  unsigned* epo = reinterpret_cast<unsigned*>(ws + WS_EPO);
#define SKEWPT()
  const int g4 = lane >> 4;
  u64 tag0, tag;
#define TAGW(n) asm volatile("s_waitcnt vmcnt(%2)\n\tv_lshl_add_u64 %0, %1, 0, 1" : "=v"(tag) : "v"(tag0), "n"(n) : "memory")
  u32x4 wv[8], cq;
  const char* pfy = reinterpret_cast<const char*>(cc ? Y1 : Y0) + (size_t)((xcd & 3) * 512 + j8 * 16 + (lane & 15)) * 4096 + g4 * 128;
#define PFG(g) (pfy + ((((g) + xcd) & 7) << 9))
  const int u = b * 2 + (wid & 1), cg0 = u & 31, ks0 = u >> 5;
  unsigned ta = lds0 + STG + ((lane >> 2) & 3) * 2368 + (wid * 128 + (lane >> 5) * 32 + (lane & 3) * 8 + (g4 & 1) * 4) * 2;
  unsigned oo = (unsigned)((wid * 4 + (lane >> 5)) * 131072 + (b >> 2) * 2048 + (g4 & 1) * 1024 + ((b & 3) * 16 + (lane & 15)) * 16), oo2 = oo + 262144;
  asm volatile("" : "+v"(ta), "+v"(oo), "+v"(oo2));
  unsigned wa = lds0 + STG + wid * 2368 + lane * 16;
  asm volatile("" : "+v"(wa));
  const unsigned wo = (unsigned)((ks0 * 64 + (lane >> 3) * 8) * 6144 + 2048 + (cg0 >> 4) * 1024 + cg0 * 64 + (lane & 7) * 8) * 2;
  if (wid == 0) TSG(part, 0);
  BT(50);
  if (wid >= 3) {
    asm volatile("global_load_dwordx2 %0, %1, off sc1" : "=v"(tag0) : "v"(epo) : "memory");
    if (wid >= 6) {
      LDG(cq, (ks0 * 64 + g4 * 16 + (lane & 1) * 8) * 2, cvec);
#pragma unroll
      for (int i = 0; i < 8; ++i) LDM(wv[i], wo + i * 12288, wmod);
      const unsigned av = u < 80 ? 0u : ~0u;
      asm volatile("global_store_dwordx4 %0, %1, %2 sc1\n\ts_nop 1" ::"v"(O_KEY + u * 1024 + lane * 16), "v"(u32x4{av, av, av, av}), "s"(ws) : "memory");
      WAIT_V(11);
    } else if (wid == 4) {
      __builtin_amdgcn_s_sleep(C_DP);
      PFL(PFG(0));
      WAIT_V(2);
    } else {
      WAIT_V(1);
    }
  } else {
    WAIT_V(0);
  }
  {
    asm volatile("" : "+v"(xv[0]), "+v"(xv[1])::"memory");
    if (wid == 1) TSG(part, 2);
    BT(51);
    const float s0 = dsum(xv[0][3], dsum(xv[0][2], dsum(xv[0][1], dsum(xv[0][0], 0.f))));
    const float s1 = dsum(xv[1][3], dsum(xv[1][2], dsum(xv[1][1], dsum(xv[1][0], 0.f))));
    const float mf = wave_sum_s(s0 + s1) * (1.0f / D), m = rbf(mf);
    const f32x4 d0 = lo4(xv[0]) - m, d1 = hi4(xv[0]) - m, d2 = lo4(xv[1]) - m, d3 = hi4(xv[1]) - m;
    const f32x4 q = (d0 * d0 + d1 * d1) + (d2 * d2 + d3 * d3);
    const float vf = wave_sum_s((q[0] + q[1]) + (q[2] + q[3])) * (1.0f / D) - (mf - m) * (mf - m);
    const float rden = __builtin_amdgcn_rcpf(rbf(__builtin_amdgcn_sqrtf(rbf(rbf(vf) + 1e-5f))));
    asm volatile("ds_write_b128 %0, %1" ::"v"(wa), "v"(pk8(d0 * rden, d1 * rden)) : "memory");
    asm volatile("ds_write_b128 %0, %1 offset:1024" ::"v"(wa), "v"(pk8(d2 * rden, d3 * rden)) : "memory");
    if (wid == 1) TSG(part, 12);
    BT(52);
    LBAR();
    if (wid == 1) TSG(part, 10);
    BT(53);
    u32x2 t[4];
    asm volatile(
        "ds_read_b64_tr_b16 %0, %4\n\tds_read_b64_tr_b16 %1, %4 offset:%5\n\t"
        "ds_read_b64_tr_b16 %2, %4 offset:128\n\tds_read_b64_tr_b16 %3, %4 offset:%6\n\ts_waitcnt lgkmcnt(0)"
        : "=&v"(t[0]), "=&v"(t[1]), "=&v"(t[2]), "=&v"(t[3])
        : "v"(ta), "n"(4 * 2368), "n"(4 * 2368 + 128)
        : "memory");
    asm volatile("global_store_dwordx4 %0, %2, %4 sc1\n\tglobal_store_dwordx4 %1, %3, %4 sc1\n\ts_nop 1" ::"v"(oo), "v"(oo2),
                 "v"(u32x4{t[0][0], t[0][1], t[1][0], t[1][1]}), "v"(u32x4{t[2][0], t[2][1], t[3][0], t[3][1]}), "s"(xn)
                 : "memory");
    if (wid == 2) TSG(part, 14);
    BT(54);
  }
  u32x2 vd;
#define VDLD(sl) POLL2(max(vd[0], vd[1]) == SENT, sl, PCAP, asm volatile("global_load_dwordx2 %0, %1, %2 sc1" VW0 : "=&v"(vd) : "v"(O_VD + (b & 31) * 128), "s"(ws) : "memory"))
  if (wid < 4) {
    const unsigned ya0 = lds0 + (lane & 15) * 128 + (g4 ^ (lane & 7)) * 16;
    bf16x8 X[DX][2][2], Y[8];
    f32x4 acc[2][8];
    {
      constexpr int MODE = 0;
      const unsigned xo0 = lane * 16 + 4096, xo1 = xo0 + 8192;
      const char* xb = reinterpret_cast<const char*>(xn) + (size_t)(mt * 4 + wid) * 131072;
#define XB(i) (xb + (((xcd + (i)) & 7) << 14))
#pragma unroll
      for (int i = 0; i < 2; ++i)
#pragma unroll
        for (int j = 0; j < 8; ++j) acc[i][j] = f32x4{0.f, 0.f, 0.f, 0.f};
      asm volatile("" : W8(acc[0]), W8(acc[1]));
      __builtin_amdgcn_s_setprio(1);
      WAIT_V(0);
      if (wid == 0) TSG(part, 1);
      const char* xpb = XB(0);
      BAR(55);
      if (wid == 3) {
        TAGW(0);
        const unsigned fp = O_XFL + ((xcd * 32 + lane) & 255) * XSTR * 4;
        if (P_D3) __builtin_amdgcn_s_sleep(P_D3);
#pragma unroll 1
        for (int it = 0; it < PCAP; ++it) { PDBG(it);
          u64 f;
          if (it < 2) BT(58 + 2 * it);
          asm volatile("global_load_dwordx2 %0, %1, %2 sc1\n\ts_waitcnt vmcnt(0)" : "=&v"(f) : "v"(fp), "s"(ws) : "memory");
          if (it < 2) BT(59 + 2 * it);
          if (__all(f == tag)) break;
          __builtin_amdgcn_s_sleep(P_D4);
        }
      }
      BAR(0);
      if (wid == 1) TSG(part, 3);
#pragma unroll
      for (int h = 0; h < 7; ++h) {
        XLD(X[h >> 1][h & 1][0], xpb, h, 0);
        XLD(X[h >> 1][h & 1][1], xpb, h, 1);
      }
#pragma unroll
      for (int j = 0; j < 7; ++j) DSR(Y[j], ya0, j * 2048);
      unsigned so = 0, a0 = ya0;
      f32x4 c0, c1;
#pragma unroll 1
      for (int it = 0; it < 7; ++it) {
        const char* xpa = xpb;
        xpb = XB(it + 1);
#pragma unroll
        for (int s = 0; s < 4; ++s) {
          const unsigned a1 = (ya0 ^ 64) + so;
          so = so == (NS - 1) * 16384 ? 0 : so + 16384;
          WAIT_V(12);
          HALF(X[s][0], a1, true, X[(s + 3) & 3][1], (s == 0 ? xpa : xpb), (2 * s + 7) & 7, true, a0);
          a0 = ya0 + so;
          BAR(1 + it * 4 + s);
          WAIT_V(12);
          HALF(X[s][1], a0, true, X[s][0], xpb, 2 * s, true, a1);
        }
      }
#pragma unroll
      for (int s = 0; s < 4; ++s) {
        const unsigned a1 = (ya0 ^ 64) + so;
        so = so == (NS - 1) * 16384 ? 0 : so + 16384;
        WAIT_V(s == 0 ? 12 : 14 - 4 * s);
        HALF(X[s][0], a1, true, X[3][1], xpb, 7, s == 0, a0);
        a0 = ya0 + so;
        if (s < 3) BAR(29 + s);
        WAIT_V(12 - 4 * s);
        if (s == 2) {
          DSR(c0, lds0 + SPARE + 15872 + 512 + wid * 128 + g4 * 32, 0);
          DSR(c1, lds0 + SPARE + 15872 + 512 + wid * 128 + g4 * 32, 16);
        }
        if (s < 3) HALF(X[s][1], a0, true, X[s][0], xpb, 0, false, a1);
      }
      const char* up = reinterpret_cast<const char*>(U) + (size_t)cc * 4194304 + (size_t)(nt * 4) * 65536 + (mt * 4 + wid) * 2048;
      const unsigned ul = lds0 + STG + wid * 1024 + lane * 16;
#define UEP(j)                                                                                                                             \
  do {                                                                                                                                     \
    u32x4 v_;                                                                                                                              \
    asm volatile("v_pk_mul_f32 %0, %0, %4\n\tv_pk_mul_f32 %1, %1, %5\n\tv_pk_mul_f32 %2, %2, %6\n\tv_pk_mul_f32 %3, %3, %7"            \
                 : "+v"(acc[0][j].lo), "+v"(acc[0][j].hi), "+v"(acc[1][j].lo), "+v"(acc[1][j].hi)                                          \
                 : "v"(c0.lo), "v"(c0.hi), "v"(c1.lo), "v"(c1.hi));                                                                        \
    asm volatile("v_cvt_pk_bf16_f32 %0, %4, %5\n\tv_cvt_pk_bf16_f32 %1, %6, %7\n\tv_cvt_pk_bf16_f32 %2, %8, %9\n\tv_cvt_pk_bf16_f32 %3, %10, %11" \
                 : "=&v"(v_[0]), "=&v"(v_[1]), "=&v"(v_[2]), "=&v"(v_[3])                                                                  \
                 : "v"(acc[0][j][0]), "v"(acc[0][j][1]), "v"(acc[0][j][2]), "v"(acc[0][j][3]), "v"(acc[1][j][0]), "v"(acc[1][j][1]),       \
                   "v"(acc[1][j][2]), "v"(acc[1][j][3]));                                                                                  \
    asm volatile("global_store_dwordx4 %0, %2, %3 offset:%4 sc1\n\tds_write_b128 %1, %2 offset:%5\n\ts_nop 0"                          \
                 ::"v"(lane * 16), "v"(ul), "v"(v_), "s"(up + ((j) >> 1) * 65536), "n"(((j)&1) * 1024), "n"((j)*4096) : "memory");         \
  } while (0)
      {
        const unsigned a1 = (ya0 ^ 64) + (31 % NS) * 16384;
        DSR(Y[7], a1, 7 * 2048);
        WAIT_L(6);
#pragma unroll
        for (int j = 0; j < 8; ++j) {
          if (j == 2) WAIT_L(0);
          MFMA(acc[0][j], X[3][1][0], Y[j]);
          MFMA(acc[1][j], X[3][1][1], Y[j]);
          if (j >= 2) UEP(j - 2);
        }
      }
      if (wid == 0) TSG(part, 4);
      TS(0);
      SKEWPT();
      asm volatile("s_nop 3");
#pragma unroll
      for (int j = 6; j < 8; ++j) UEP(j);
    }
    const int rg = nt * 8 + wid * 2 + cc, row0 = rg * 16 + (lane & 15), col0 = et * 128 + g4 * 8;
    u32x4 xq[4], pv[4], sa4[4];
    f32x4 ga[8];
    f32x2 cj;
    float sw, ce;
    u32x4 dk;
    unsigned spo, soo, th, slt, slf, sla, tha, xca, sca;
    u32x4 e0, e1;
    const char* xr = reinterpret_cast<const char*>(xn) + mt * 524288 + (cc * 16 + nt) * 16384 + wid * 4096;
#define XCHLD()                                                                                                     \
  asm volatile(                                                                                                     \
      "global_load_dwordx4 %0, %4, %5 sc1\n\tglobal_load_dwordx4 %1, %4, %5 offset:1024 sc1\n\t"                   \
      "global_load_dwordx4 %2, %4, %5 offset:2048 sc1\n\tglobal_load_dwordx4 %3, %4, %5 offset:3072 sc1"            \
      : "=&v"(pv[0]), "=&v"(pv[1]), "=&v"(pv[2]), "=&v"(pv[3])                                                      \
      : "v"(lane * 16), "s"(xr)                                                                                     \
      : "memory")
    {
      constexpr int MODE = 1;
      const char* ub = reinterpret_cast<const char*>(U) + (size_t)cc * 4194304 + (size_t)(nt * 4 + wid) * 65536;
      const unsigned xo0 = lane * 16 + cc * 1024 + 4096, xo1 = xo0 ^ 1024;
      BT(32);
      LBAR();
      if (wid == 1) TSG(part, 6);
      {
        const unsigned sa = lds0 + STG + wid * 8192 + lane * 16 + cc * 4096;
#pragma unroll
        for (int q = 0; q < 8; ++q) DSR(X[(q >> 1) & 1][q & 1][q >> 2], (q >> 2) ? sa ^ 4096 : sa, (q & 3) * 1024);
        const unsigned a0 = ya0 + (32 % NS) * 16384;
#pragma unroll
        for (int j = 0; j < 7; ++j) DSR(Y[j], a0, j * 2048);
      }
#define UFLAG()                                                                                                         \
  do {                                                                                                                  \
    WAIT_V(0);                                                                                                          \
    if (lane == 0) __hip_atomic_store(ufl + (mt * 4 + wid) * FSTR, 1u, __ATOMIC_RELAXED, __HIP_MEMORY_SCOPE_AGENT);     \
    if (wid == 0) TSG(part, 5);                                                                                         \
  } while (0)
#define XP(h) (ub + (((mt + ((h) >> 2)) & 7) << 13))
#define XS(h) X[((h) >> 1) & 3][(h)&1]
#define XW(h) do { if ((h) >= 4) WAIT_V(2 * (((h) + 6 > 31 ? 31 : (h) + 6) - (h)) ); } while (0)
      constexpr int H0 = 24, NP = 64, NE = 2 * NP + 8;
      const char* xw;
      unsigned c128, xoff[4], rka, rkb, ea, eb, cwa, cwu;
      auto ESET = [&]() __attribute__((always_inline)) {
        int ln_ = lane;
        asm volatile("" : "+v"(ln_));
        const int lane = ln_, g4 = lane >> 4, row0 = rg * 16 + (lane & 15), col0 = et * 128 + g4 * 8;
        xw = reinterpret_cast<const char*>(xn) + mt * 524288 + ((cc ^ 1) * 16 + nt) * 16384 + wid * 4096;
        asm volatile("" : "+s"(xw), "+s"(xr));
        c128 = 0x43000000u;
        rka = lds0 + RKA + (lane & 15) * 16, rkb = lds0 + RKB + (g4 ? 1024 : wid * 256 + lane * 16);
        ea = lds0 + TAB + g4 * 32, eb = lds0 + TAB + 512 + (lane & 3) * 256 + g4 * 16;
#pragma unroll
        for (int q = 0; q < 4; ++q) {
          dk[q] = (lane & 3) == q ? ~0u : 0u;
          xoff[q] = (lds0 + STG + wid * 8192 + (lane & 15) * 256 + ((g4 ^ lane) & 15) * 16) ^ (q * 64);
        }
        spo = O_STAT + rg * 2048 + g4 * 512 + (lane & 15) * 16, soo = O_STAT + rg * 2048 + et * 256 + (lane & 15) * 16;
        cwa = lds0 + SPARE + 10240 + g4 * 8, cwu = lds0 + SPARE + 10240 + et * 4;
        const unsigned one = g4 == ((lane >> 2) & 3) ? ((lane & 1) ? 0x1f800000u : 0x3f80u) : 0u;
        e0 = u32x4{(lane & 2) ? 0u : one, (lane & 2) ? one : 0u, 0u, 0u}, e1 = u32x4{0u, 0u, e0[0], e0[1]};
        tha = lds0 + SPARE + 10368 + 24;
        sla = lds0 + SLT + (row0 & 127) * 4;
        xca = O_X1C - 2048 + col0 * 2, sca = O_ST2 - 128 + et * 16;
        asm volatile("" : "+v"(dk), "+v"(spo), "+v"(soo), "+v"(cwa), "+v"(cwu), "+v"(tha), "+v"(sla), "+v"(xca), "+v"(sca), "+v"(e0), "+v"(e1), "+v"(c128));
        asm volatile("" : "+v"(xoff[0]), "+v"(xoff[1]), "+v"(xoff[2]), "+v"(xoff[3]), "+v"(rka), "+v"(rkb), "+v"(ea), "+v"(eb));
      };
#pragma unroll
      for (int s = 0; s < H0 / 2; ++s) {
        const unsigned a0 = ya0 + ((32 + s) % NS) * 16384, a1 = a0 ^ 64, an = ya0 + ((33 + s) % NS) * 16384;
        XW(2 * s);
        if (s == 0) {
          constexpr int MODE = 2;
          HALF(XS(0), a1, true, XS(7), XP(7), 7, false, a0);
          UFLAG();
        } else {
          HALF(XS(2 * s), a1, true, XS(2 * s + 7), XP(2 * s + 7), 2 * s + 7, 2 * s >= 4, a0);
        }
        if (s == 1) ESET();
        BAR(33 + s);
        if (s == 1) {
          if (wid == 1) TSG(part, 7);
          TS(4);
#pragma unroll
          for (int h = 4; h < U_X0; ++h) {
            XLD(XS(h)[0], XP(h), h, 0);
            XLD(XS(h)[1], XP(h), h, 1);
          }
        }
        XW(2 * s + 1);
#undef HHOOK
#define HHOOK(j) if (s == 1 && j + 3 >= U_X0) { XLD(XS(j + 3)[0], XP(j + 3), j + 3, 0); XLD(XS(j + 3)[1], XP(j + 3), j + 3, 1); }
        HALF(XS(2 * s + 1), an, true, XS(2 * s + 8), XP(2 * s + 8), 2 * s + 8, 2 * s + 1 >= 5, a1);
#undef HHOOK
#define HHOOK(j)
      }
      u32x4 ov;
      bf16x8 xk;
#define EH(e) (H0 + (((e) < NP ? (e) : (e) - NP - 8) >> 3))
#define EA(e) ((ya0 ^ ((EH(e) & 1) * 64)) + ((32 + (EH(e) >> 1)) % NS) * 16384)
      sfor<NE>([&](auto nn) __attribute__((always_inline)) {
        constexpr int n = decltype(nn)::value, h = EH(n), j = n & 7, m = n - NP - TP_P0, e = n + 7, z = n - (NE - 8);
        (void)sa4, (void)eb, (void)ga, (void)ea, (void)xk, (void)rka, (void)rkb, (void)xq, (void)xoff, (void)ov, (void)xw, (void)x;
        (void)cj, (void)sw, (void)ce, (void)cwa, (void)cwu, (void)th, (void)tha, (void)slt, (void)sla, (void)slf, (void)c128;
        if (n == 0) TS(2);
        if (n == NP) { TS(3); SKEWPT(); }
        if (n < NP && j == 0) {
          if ((h & 1) && h < 31) BAR(33 + (h >> 1));
          XW(h);
        }
        if (n == NP + TP_X0) { TS(14); BAR(48); TS(11); }
        if (n == NP + TP_XL) XCHLD();
        if constexpr ((j & 1) == 0) WAIT_L(z <= 0 ? 5 : 6 - z + 2 * z);
        if constexpr (n < NP) MFMA(acc[1][j], Y[j], XS(h)[1]);
        else if constexpr (n < NP + 8) MFMA(acc[0][j], Y[j], xk);
        else MFMA(acc[0][j], Y[j], XS(h)[0]);
        if constexpr (n == NP - 16) DSR(xk, rkb, 0);
        if constexpr (e >= NP && e < NP + 8) DSR(Y[e & 7], rka, (e & 7) * 256);
        else if constexpr (e < NE) DSR(Y[e & 7], EA(e), (e & 7) * 2048);
        if constexpr (n == 2 || n == 4) XLD(XS(31)[(j >> 1) - 1], XP(31), 31, (j >> 1) - 1);
        if constexpr (m >= TP_XC - 11 && m < TP_XC - 7) DSR(xq[m - TP_XC + 11], xoff[m - TP_XC + 11], 4096);
        if constexpr (m >= 0 && m < 4 * TP_PS) {
          constexpr int q = m / TP_PS, r = m % TP_PS;
#pragma unroll
          for (int u = (r * 13 + TP_PS - 1) / TP_PS; u < ((r + 1) * 13 + TP_PS - 1) / TP_PS; ++u) {
#define AO(i) acc[1][2 * q + ((u >> 1) & 1)][(u & 1) * 2 + (i)]
            if (u < 4) asm volatile("v_and_or_b32 %0, %1, %2, %3" : "=v"(ov[u & 3]) : "v"(AO(1)), "s"(0x80000000u), "v"(c128));
            else if (u < 8) asm volatile("v_fmamk_f32 %0, %1, 0x5f800000, %0" : "+v"(ov[u & 3]) : "v"(AO(1)));
            else if (u < 12) asm volatile("v_cvt_pk_bf16_f32 %0, %1, %0" : "+v"(ov[u & 3]) : "v"(AO(0)));
            else asm volatile("global_store_dwordx4 %0, %1, %2 offset:%3 sc1\n\ts_nop 1" ::"v"(lane * 16), "v"(ov), "s"(xw), "n"(q * 1024) : "memory");
          }
          if (m == 4 * TP_PS - 1) TS(10);
        }
        if constexpr (m == TP_XC) asm volatile("" : "+v"(xq[0]), "+v"(xq[1]), "+v"(xq[2]), "+v"(xq[3]));
        if constexpr (m >= TP_XC && m < TP_XC + 32) {
          constexpr int c = m - TP_XC;
          if constexpr (c & 1) asm volatile("v_and_b32 %0, 0xffff0000, %1" : "=v"(acc[1][c >> 2][c & 3]) : "v"(xq[c >> 3][(c >> 1) & 3]));
          else asm volatile("v_lshlrev_b32 %0, 16, %1" : "=v"(acc[1][c >> 2][c & 3]) : "v"(xq[c >> 3][(c >> 1) & 3]));
        }
        if constexpr (z >= 0 && z < 4) {
          DSR(ga[2 * z], ea, z * 128);
          DSR(ga[2 * z + 1], ea, z * 128 + 16);
        } else if constexpr (z >= 4 && z < 6) {
          DSR(sa4[2 * z - 8], eb, (2 * z - 8) * 64);
          DSR(sa4[2 * z - 7], eb, (2 * z - 7) * 64);
        } else if constexpr (z == 6) {
          asm volatile("ds_read_b64 %0, %2\n\tds_read_b32 %1, %3" : "=&v"(cj), "=&v"(ce) : "v"(cwa), "v"(cwu) : "memory");
        } else if constexpr (z == 7) {
          asm volatile("ds_read_b32 %0, %4 offset:32\n\tds_read_b32 %1, %5 offset:4\n\tds_read_b32 %3, %5 offset:56\n\tds_read_b32 %2, %6"
                       : "=&v"(sw), "=&v"(th), "=&v"(slt), "=&v"(slf) : "v"(cwa), "v"(tha), "v"(sla) : "memory");
        }
      });
    }
    if (wid == 0) TSG(part, 8);
    TS(13);
    BT(49);
    BTE();
    SKEWPT();
    {
      TS(26);
#define XCHW() asm volatile("s_waitcnt vmcnt(0)" : "+v"(pv[0]), "+v"(pv[1]), "+v"(pv[2]), "+v"(pv[3])::"memory")
      XCHW();
#define XBAD (amin16(pv) < 128.0f)
      if (__builtin_expect(__any(XBAD), 1)) POLL2(XBAD, 1, PCAP, XCHLD(); XCHW());
      TS(27);
      asm volatile("s_waitcnt lgkmcnt(0)" : W8(acc[0]), W8(ga), "+v"(sa4[0]), "+v"(sa4[1]), "+v"(sa4[2]), "+v"(sa4[3]), "+v"(cj), "+v"(sw), "+v"(ce), "+v"(th), "+v"(slt), "+v"(slf)::"memory");
#pragma unroll
      for (int q = 0; q < 8; ++q) acc[0][q] = __builtin_amdgcn_mfma_f32_16x16x32_bf16(BF8((q & 1) ? e1 : e0), BF8(pv[q >> 1]), acc[0][q], 0, 0, 0);
      f32x4 s4 = {0.f, 0.f, 0.f, 0.f}, gr = {0.f, 0.f, 0.f, 0.f};
      const unsigned xa1 = xca + slt * 2048, sa1 = sca + slt * 128;
#pragma unroll
      for (int q = 0; q < 4; ++q) {
        xq[q] = pk8(acc[1][2 * q] + ga[2 * q] * acc[0][2 * q], acc[1][2 * q + 1] + ga[2 * q + 1] * acc[0][2 * q + 1]);
        gr = __builtin_amdgcn_mfma_f32_16x16x32_bf16(BF8(xq[q]), BF8(xq[q]), gr, 0, 0, 0);
        s4 = __builtin_amdgcn_mfma_f32_16x16x32_bf16(BF8(sa4[q]), BF8(xq[q]), s4, 0, 0, 0);
        xq[q] = nsent4(xq[q]);
      }
#define XCST(a)                                                                                                              \
  asm volatile(                                                                                                              \
      "global_store_dwordx4 %0, %1, %5 sc1\n\tglobal_store_dwordx4 %0, %2, %5 offset:64 sc1\n\t"                            \
      "global_store_dwordx4 %0, %3, %5 offset:128 sc1\n\tglobal_store_dwordx4 %0, %4, %5 offset:192 sc1\n\ts_nop 1" ::"v"(a), \
      "v"(xq[0]), "v"(xq[1]), "v"(xq[2]), "v"(xq[3]), "s"(ws)                                                                \
      : "memory")
#define SCST(a)                                                                                                              \
  do {                                                                                                                       \
    _Pragma("unroll") for (int r = 0; r < S_R; ++r) asm volatile("global_store_dwordx4 %0, %1, %2 offset:%3 sc1" ::"v"(a), "v"(sg), "s"(ws), "n"(r * 1280) : "memory"); \
    asm volatile("s_nop 1");                                                                                                 \
  } while (0)
      if (slt) XCST(xa1);
      const bool dl = ((lane >> 2) & 3) == g4;
      const u32x4 gd = __builtin_bit_cast(u32x4, gr) & dk;
      const float m = s4[2] * (1.0f / 128);
      float q2 = __uint_as_float((gd[0] | gd[1]) | (gd[2] | gd[3])) - s4[2] * m, lg = (s4[0] + s4[1]) - m * ce;
      if (__builtin_expect(__any(dl && !(s4[2] * m <= 256.0f * q2)), 0)) {
        asm volatile("" : "+v"(xq[0]), "+v"(xq[1]), "+v"(xq[2]), "+v"(xq[3]));
        f32x4 a = {0.f, 0.f, 0.f, 0.f}, l = {0.f, 0.f, 0.f, 0.f};
#pragma unroll
        for (int q = 0; q < 4; ++q) {
          const u32x4 ch = *reinterpret_cast<const u32x4*>(&scw[0][g4 * 8 + q * 32]), cl = *reinterpret_cast<const u32x4*>(&scw[1][g4 * 8 + q * 32]);
          const f32x4 d0 = lo4(xq[q]) - m, d1 = hi4(xq[q]) - m;
          a += d0 * d0 + d1 * d1;
          l += d0 * (lo4(ch) + lo4(cl)) + d1 * (hi4(ch) + hi4(cl));
        }
        q2 = ar4((a[0] + a[1]) + (a[2] + a[3]));
        lg = ar4((l[0] + l[1]) + (l[2] + l[3]));
      }
      const u32x4 sg = {nsent(m), nsent(q2), nsent(lg), 0u};
      if (dl) {
        if (slt) SCST(sa1);
        asm volatile("global_store_dwordx4 %0, %1, %2 sc1\n\ts_nop 1" ::"v"(soo), "v"(sg), "s"(ws) : "memory");
      }
      if (__any(slf == 0u)) {
        POLL2(slf == 0u, 1, 1 << 26, asm volatile("ds_read_b32 %1, %2 offset:56\n\tds_read_b32 %0, %3\n\ts_waitcnt lgkmcnt(0)" : "=&v"(slt), "=&v"(slf) : "v"(tha), "v"(sla) : "memory"));
        if (slt) {
          XCST(xca + slt * 2048);
          if (dl) SCST(sca + slt * 128);
        }
      }
      asm volatile("ds_write_b32 %0, %1 offset:72" ::"v"(tha + wid * 4), "v"(1u) : "memory");
      TS(7);
      SKEWPT();
      const unsigned tp = tick() + T_D1, tq = (unsigned)__builtin_amdgcn_readfirstlane((int)th) + T_C1;
      u32x4 sr[2];
#define STLD(W) asm volatile("global_load_dwordx4 %0, %2, %3 sc1\n\tglobal_load_dwordx4 %1, %2, %3 offset:256 sc1" W : "=&v"(sr[0]), "=&v"(sr[1]) : "v"(spo), "s"(ws) : "memory")
#define SBAD (max(max(max(sr[0][0], sr[0][1]), sr[0][2]), max(max(sr[1][0], sr[1][1]), sr[1][2])) == SENT)
      asm volatile("" : "+v"(xq[0]), "+v"(xq[1]), "+v"(xq[2]), "+v"(xq[3])::"memory");
      f32x4 xf[8], xs[8], gc[8];
      auto PREW = [&](int q0, int q1) __attribute__((always_inline)) {
#pragma unroll
        for (int q = q0; q < q1; ++q) {
          const u32x4 sbq = *reinterpret_cast<const u32x4*>(&svb[1][col0 + q * 32]), gbq = *reinterpret_cast<const u32x4*>(&svb[3][col0 + q * 32]);
          const u32x4 cq = *reinterpret_cast<const u32x4*>(&svb[2][col0 + q * 32]);
          xf[2 * q] = lo4(xq[q]), xf[2 * q + 1] = hi4(xq[q]);
          xs[2 * q] = xf[2 * q] + lo4(gbq) * lo4(sbq), xs[2 * q + 1] = xf[2 * q + 1] + hi4(gbq) * hi4(sbq);
          gc[2 * q] = lo4(gbq) * lo4(cq), gc[2 * q + 1] = hi4(gbq) * hi4(cq);
          asm volatile("" : "+v"(xf[2 * q]), "+v"(xf[2 * q + 1]), "+v"(xs[2 * q]), "+v"(xs[2 * q + 1]), "+v"(gc[2 * q]), "+v"(gc[2 * q + 1]));
        }
      };
      PREW(0, PW0);
      wait_tick((int)(tp - tq) < 0 ? tp : tq);
      STLD("");
      unsigned rk = 0x8ffeu - (unsigned)row0 * 2;
      unsigned twa = lds0 + STG + wid * 8192 + (lane & 15) * 256 + ((g4 ^ lane) & 15) * 16, tra = lds0 + STG + wid * 8192 + g4 * 256 + ((g4 ^ lane) & 15) * 16;
      unsigned oo_ = (unsigned)((rg * 16 + g4) * D + et * 128 + (lane & 15) * 8) * 2;
      asm volatile("" : "+v"(rk), "+v"(twa), "+v"(tra), "+v"(oo_));
      PREW(PW0, 4);
      asm volatile("s_waitcnt vmcnt(0)" : "+v"(sr[0]), "+v"(sr[1])::"memory");
      if (__builtin_expect(__any(SBAD), 1)) POLL2(SBAD, 1, PCAP, STLD(VW0));
      TS(9);
      const f32x2 mk = {__uint_as_float(sr[0][0]), __uint_as_float(sr[1][0])};
      const f32x2 qk = {__uint_as_float(sr[0][1]), __uint_as_float(sr[1][1])};
      const f32x2 lk = {__uint_as_float(sr[0][2]), __uint_as_float(sr[1][2])};
      const float mean = ar4(mk[0] + mk[1]) * (1.0f / 8);
      const f32x2 dm = mk - mean;
      const f32x2 a = qk + 128.0f * (dm * dm), l = lk + dm * cj;
      const float al = swap32_add(a[0] + a[1], l[0] + l[1]), am = swap16_add(al, al);
      auto r = __builtin_amdgcn_permlane32_swap(__float_as_uint(am), __float_as_uint(am), false, false);
      const float rstd = __builtin_amdgcn_rsqf(__uint_as_float(r[0]) * (1.0f / D) + 1e-5f);
      const unsigned key = mkkey(rstd * __uint_as_float(r[1]) + sw) | rk;
      unsigned t1 = slt ? 0u : key, t2 = 0u;
      TOP2(0xB1); TOP2(0x4E); TOP2(0x141); TOP2(0x140);
      if (lane == 0 && et == (rg & 7)) asm volatile("global_store_dwordx2 %0, %1, %2 sc1" ::"v"(O_KEY + rg * KSTR * 8), "v"(u32x2{t1, t2}), "s"(ws) : "memory");
      u32x4 o[4];
#pragma unroll
      for (int q = 0; q < 4; ++q) o[q] = pk8(xs[2 * q] + (gc[2 * q] * (xf[2 * q] - mean)) * rstd, xs[2 * q + 1] + (gc[2 * q + 1] * (xf[2 * q + 1] - mean)) * rstd);
      {
        asm volatile("ds_write_b128 %0, %4\n\tds_write_b128 %1, %5\n\tds_write_b128 %2, %6\n\tds_write_b128 %3, %7" ::"v"(twa), "v"(twa ^ 64), "v"(twa ^ 128), "v"(twa ^ 192),
                     "v"(o[0]), "v"(o[1]), "v"(o[2]), "v"(o[3]) : "memory");
        asm volatile("ds_read_b128 %0, %4\n\tds_read_b128 %1, %5 offset:1024\n\tds_read_b128 %2, %6 offset:2048\n\tds_read_b128 %3, %7 offset:3072\n\ts_waitcnt lgkmcnt(0)"
                     : "=&v"(o[0]), "=&v"(o[1]), "=&v"(o[2]), "=&v"(o[3]) : "v"(tra), "v"(tra ^ 64), "v"(tra ^ 128), "v"(tra ^ 192) : "memory");
      }
      auto OST = [&](bool on) __attribute__((always_inline)) {
        const unsigned mk_ = (unsigned)__builtin_amdgcn_ballot_w64(on) >> g4;
#pragma unroll
        for (int q = 0; q < 4; ++q)
          if (mk_ & (1u << (4 * q))) asm volatile("global_store_dwordx4 %0, %1, %2" OPOL "\n\ts_nop 1" ::"v"(oo_), "v"(o[q]), "s"(out + q * 4096) : "memory");
      };
      bool df = slt;
      {
        const int tn = (int)(tick() - (tq + (T_C3 - T_C1)));
        if (tn > T_X) df = true;
        else if (tn > -T_W) { unsigned sv; LSPIN(sv, ssel + 16 + wid, sv != 0u); }
      }
      OST(!df);
      TS(1);
      u32x2 ks;
      POLL2(ks[1] == 0u, 1, 1 << 26, asm volatile("ds_read_b64 %0, %1\n\ts_waitcnt lgkmcnt(0)" : "=v"(ks) : "v"(lds0 + SPARE + 10368) : "memory"));
      df = df && (ks[0] & 0xffffu) != rk && (ks[1] & 0xffffu) != rk;
      SKEWPT();
      if (__any(df)) {
        if (b < NPART) {
          unsigned sv;
          LSPIN(sv, ssel + O_DW, sv != 0u);
          if (O_DS) __builtin_amdgcn_s_sleep(O_DS);
        }
        OST(df);
      }
      if (__builtin_expect(__any(t1 > ks[1]), 0)) {
        VDLD(8);
        slt = slt ? 0u : key == vd[0] ? 9u : key == vd[1] ? 10u : 0u;
        if (slt) {
          XCST(xca + slt * 2048);
          if (dl) SCST(sca + slt * 128);
        }
      }
    }
    TS(16);
    TSE();
  } else {
    const int hw = wid - 4;
    unsigned ts0 = tick();
    asm volatile("" : "+v"(ts0));
    const char* Hc = reinterpret_cast<const char*>((cc ? X1 : X0) + (size_t)et * 128 * D);
    auto UPOLL = [&](int n) {
      const unsigned fo = (unsigned)WS_UFL + ((cc * 16 + nt) * 32 + ((mt + 1 + (lane >> 2)) & 7) * 4 + (lane & 3)) * FSTR * 4;
      unsigned f;
      POLL2(f == 0u && lane < 4 * n, U_S, PCAP, asm volatile("global_load_dword %0, %1, %2 sc1" VW0 : "=&v"(f) : "v"(fo), "s"(ws) : "memory"));
      return (__builtin_amdgcn_ballot_w64(f == 0u) & 0xFFFFFFFull) == 0;
    };
    bool urdy = true;
    u32x4 gT[8], uT[8], wdv[8];
    unsigned og = (unsigned)(((g & 3) * 256 + hw * 64 + (lane >> 3)) * F + (g >> 2) * 64 + ((lane & 7) ^ ((lane >> 3) & 2) ^ 4) * 8) * 2;
    unsigned wta = lds0 + STG + (g4 * 8 + ((lane >> 2) & 3)) * 128 + (((hw * 2 + ((lane >> 1) & 1)) ^ ((lane >> 2) & 2) ^ ((g4 & 1) * 4)) << 4) + (lane & 1) * 8;
    unsigned od = (unsigned)(((g >> 2) * 64 + (lane >> 3) * 8) * D + (g & 3) * 256 + hw * 64 + (lane & 7) * 8) * 2;
    unsigned ou = og + (unsigned)(((mt + W_UO) % 7 - mt) * 128);
    asm volatile("" : "+v"(og), "+v"(ou), "+v"(od), "+v"(wta));
#define LDWI(dst, voff, base, inc) asm volatile("global_load_dwordx4 %0, %1, %2" WPOL "\n\tv_add_u32 %1, %3, %1" : "=v"(dst), "+v"(voff) : "s"(base), "n"(inc) : "memory")
#define WST(o, w)                                                                                                          \
  if (b < NPART) {                                                                                                         \
    asm volatile("s_mov_b32 m0, %0" ::"s"(lds0 + STG + hw * 8192));                                                        \
    _Pragma("unroll") for (int i = 0; i < 8; ++i) {                                                                        \
      asm volatile("v_xor_b32 %0, 64, %0\n\tglobal_load_lds_dwordx4 %0, %1 offset:%2" WPOL "\n\tv_add_u32 %0, %3, %0"       \
                   : "+v"(o) : "s"(w), "n"((i & 3) * 1024), "n"(F * 16 + (i == 3 ? 3072 : -1024)) : "memory");             \
      if (i == 3) asm volatile("s_add_u32 m0, m0, 4096" ::: "scc");                                                        \
    }                                                                                                                      \
  }
#define WTR2(i) "ds_read_b64_tr_b16 %" #i ", %16 offset:(" #i "/2)*4096+(" #i "&1)*512\n\t"
#define WTK(T)                                                                                                                         \
  if (b < NPART) {                                                                                                                     \
    u32x2 t_[16];                                                                                                                      \
    asm volatile(WTR2(0) WTR2(1) WTR2(2) WTR2(3) WTR2(4) WTR2(5) WTR2(6) WTR2(7) WTR2(8) WTR2(9) WTR2(10) WTR2(11) WTR2(12) WTR2(13)   \
                     WTR2(14) WTR2(15) "s_waitcnt lgkmcnt(0)"                                                                          \
                 : "=&v"(t_[0]), "=&v"(t_[1]), "=&v"(t_[2]), "=&v"(t_[3]), "=&v"(t_[4]), "=&v"(t_[5]), "=&v"(t_[6]), "=&v"(t_[7]),     \
                   "=&v"(t_[8]), "=&v"(t_[9]), "=&v"(t_[10]), "=&v"(t_[11]), "=&v"(t_[12]), "=&v"(t_[13]), "=&v"(t_[14]), "=&v"(t_[15]) \
                 : "v"(wta) : "memory");                                                                                               \
    _Pragma("unroll") for (int f = 0; f < 8; ++f) T[f] = u32x4{t_[2 * f][0], t_[2 * f][1], t_[2 * f + 1][0], t_[2 * f + 1][1]};        \
  }
#define DLD(q0, q1) if (b < NPART) { _Pragma("unroll") for (int q = q0; q < q1; ++q) LDWI(wdv[q], od, wd, D * 2); }
    const int pf0 = A_PS + (((j8 & 7) * A_S1) >> 3), pf1 = A_P2 + (((j8 & 7) * A_S2) >> 3), q1 = A_Q1;
    static_assert(A_PS >= 1 && A_P2 + A_S2 * 7 / 8 + 3 <= R_AT && R_AT < A_Q1 && A_P2 + A_S2 * 7 / 8 + 1 + W_T <= A_Q1 && H_AT + 3 <= A_Q1 && G1_C + G1_S < W_T && A_PS + W_T < A_P2 && A_PS + A_S1 * 7 / 8 + W_T + 1 < A_P2 + A_S2 * 7 / 8 && W_T >= M_NS - 2 && A_Q1 < F_AT && F_AT <= 29 && V_K <= B_J && V_T > B_J && B_WK == 15 && X_K >= N_C && (X_K + M_NS - 1 < 16 || (X_K <= B_MAT && TP_XC - 11 >= TP_X0 - TP_P0)) && TP_XC >= 4 * TP_PS && TP_XC + TP_P0 + 32 <= 72 && N_K > N_C && N_C > V_T && B_RAT > B_J && B_QAT > B_J && B_QAT + B_QD < 15, "barrier counts");
#define HOFF(r) (unsigned)((((r) >> 5) * 32 + (((r) >> 2) & 3) * 8 + (((r) >> 4) & 1) * 4 + ((r)&3)) * 2048 + ((lane & 7) ^ (lane >> 3)) * 16)
#define TR8(T, W)                                                                                                    \
  _Pragma("unroll") for (int c = 0; c < 8; ++c) {                                                                    \
    _Pragma("unroll") for (int p = 0; p < 4; ++p)                                                                    \
        T[c][p] = __builtin_amdgcn_perm(W[2 * p + 1][c >> 1], W[2 * p][c >> 1], (c & 1) ? 0x07060302u : 0x05040100u); \
    asm volatile("" : "+v"(T[c]));                                                                                   \
  }
    unsigned qm[4];
#pragma unroll
    for (int i = 0; i < 4; ++i) qm[i] = ((lane >> 2) & 3) == i ? ~0u : 0u;
    asm volatile("" : "+v"(qm[0]), "+v"(qm[1]), "+v"(qm[2]), "+v"(qm[3]));
    auto MVR = [&](f32x4 d) __attribute__((always_inline)) { return ((lane & 8) ? d[3] : d[0]) + dpp_f<0x128>((lane & 8) ? d[1] : d[2]); };
    auto MV64 = [&](const u32x4* T, u32x4 a) __attribute__((always_inline)) {
      f32x4 d[2] = {};
#pragma unroll
      for (int c = 0; c < 8; ++c) {
        const unsigned k_ = qm[c >> 1];
        d[c & 1] = __builtin_amdgcn_mfma_f32_16x16x32_bf16(BF8((u32x4{a[0] & k_, a[1] & k_, a[2] & k_, a[3] & k_})), BF8(T[c]), d[c & 1], 0, 0, 0);
      }
      return f32x2{MVR(d[0]), MVR(d[1])};
    };
#define LD1(i, a) "global_load_dwordx4 %" #i ", %" #a ", %10 sc1\n\tv_add_u32 %" #a ", 0x4000, %" #a "\n\t"
#define LD8(r, o0, o1, W)                                                                                                          \
  do {                                                                                                                             \
    unsigned a0_ = (o0), a1_ = (o1);                                                                                               \
    asm volatile(LD1(0, 8) LD1(4, 9) LD1(1, 8) LD1(5, 9) LD1(2, 8) LD1(6, 9) LD1(3, 8) LD1(7, 9) "s_nop 0" W                        \
                 : "=&v"(r[0]), "=&v"(r[1]), "=&v"(r[2]), "=&v"(r[3]), "=&v"(r[4]), "=&v"(r[5]), "=&v"(r[6]), "=&v"(r[7]), "+v"(a0_), "+v"(a1_) \
                 : "s"(ws) : "memory");                                                                                            \
  } while (0)
    u32x4 sel, gp[4];
    unsigned vc;
#define VCLD(W) asm volatile("global_load_dword %0, %1, %2 sc1" W : "=&v"(vc) : "v"(O_VP + ((cc ^ 1) * 8 + mt) * 4096 + (lane & 15) * 256), "s"(ws) : "memory")
#define GP4(o, W)                                                                                                         \
  asm volatile(                                                                                                           \
      "global_load_dwordx4 %0, %4, %5 sc1\n\tglobal_load_dwordx4 %1, %4, %5 offset:1024 sc1\n\t"                         \
      "global_load_dwordx4 %2, %4, %5 offset:2048 sc1\n\tglobal_load_dwordx4 %3, %4, %5 offset:3072 sc1" W                \
      : "=&v"(gp[0]), "=&v"(gp[1]), "=&v"(gp[2]), "=&v"(gp[3])                                                            \
      : "v"(o), "s"(ws)                                                                                                   \
      : "memory")
#define GPBAD (max(max(max4(gp[0]), max4(gp[1])), max(max4(gp[2]), max4(gp[3]))) == SENT)
      auto GPSUM = [&]() __attribute__((always_inline)) {
        f32x4 d = {0.f, 0.f, 0.f, 0.f};
#pragma unroll
        for (int i = 0; i < 4; ++i) d = __builtin_amdgcn_mfma_f32_16x16x32_bf16(BF8(sel), BF8(gp[i]), d, 0, 0, 0);
        return d;
      };
    const bool rdc = b >= NPART && hw == 3, rnp = b >= NPART && hw == 2 && cc == 0;
    const int gbq = (b >> 4) & 1, cj = (b & 15) * 64 + (lane & 15) * 4 + (lane >> 5) * 2;
    unsigned voff[4], ro, xo, ra1, wqa, qo, qa, ma, mo2, ma2;
    unsigned sfa, rk2, xo_, hwa, hra, hrb, ara, pso, gso, awa, sfw, gl0, gl1, sto, kca;
    auto JSET = [&]() {
#pragma unroll
      for (int i = 0; i < 4; ++i) {
        voff[i] = HOFF((hw * 4 + i) * 8 + (lane >> 3)) - i * 1024;
        sel[i] = ((lane & 15) >> 1) == i ? ((lane & 1) ? 0x3f800000u : 0x3f80u) : 0u;
      }
      ro = O_G2 + (g4 * 8192 + gbq * 1024 + (b & 15) * 64 + (lane & 15) * 4) * 4;
      xo = (unsigned)((nt * 128 + hw * 32 + cc * 16 + g4) * D + et * 128 + ((g4 ^ lane) & 15) * 8) * 2;
      ra1 = (lane & 16) == 0 ? O_MOD + 2048 + gbq * 4096 + cj * 2 : O_M2 + 8192 + 64 - gbq * 64 + (b & 15) * 4;
      wqa = lds0 + SPARE + 8192 + cj * 2;
      ma = max(hw, 1) * 2048 + (lane < 16 ? et * 256 + lane * 16 : lane < 48 ? (g & 3) * 512 + (lane - 16) * 16 : ((b >> 1) & 15) * 128 + (lane & 7) * 16);
      qo = hw < 2 ? O_QT + et * 2048 + hw * 1024 + lane * 16 : O_RS + ((hw & 1) * S + nt * 128 + (lane >> 4) * 32 + cc * 16 + (lane & 15)) * 4;
      qa = lds0 + (hw < 2 ? RKA + hw * 1024 + lane * 16 : RKB + lane * 16 + (hw & 1) * 8);
      mo2 = O_M2 + (hw == 2 ? 8448u + (lane & 15) * 16 : hw ? 8192u + (lane & 7) * 16 : lane < 32 ? et * 512 + lane * 16 : 4096u + (lane & 16) * 128 + et * 256 + (lane & 15) * 16);
      ma2 = lds0 + (hw ? SPARE + 10240 + (lane & 7) * 8 : TAB + lane * 16);
      asm volatile("" : "+v"(voff[0]), "+v"(voff[1]), "+v"(voff[2]), "+v"(voff[3]), "+v"(ro), "+v"(xo), "+v"(ra1), "+v"(wqa), "+v"(qo), "+v"(qa), "+v"(ma), "+v"(mo2), "+v"(ma2), "+v"(sel));
      *reinterpret_cast<unsigned*>((hw & 1) ? shm + RKB + 1024 + lane * 4 : reinterpret_cast<char*>(scw[2]) + lane * 4) = (hw & 1) ? 0u : 0x3f803f80u;
      if (hw == 3) {
        if (lane < 32) ssel[lane] = 0u;
        *reinterpret_cast<u32x2*>(shm + SLT + lane * 8) = u32x2{0u, 0u};
      }
    };
#define OWNL() int ln_ = lane; asm volatile("" : "+v"(ln_)); const int lane = ln_, g4 = lane >> 4; (void)g4
    auto TSET = [&]() {
      OWNL();
      kca = ((g & 3) * 256 + hw * 64 + (lane & 7) * 8) * 2;
      sfa = lds0 + SPARE + 10368, rk2 = lds0 + LST + (lane >> 3) * 4, sfw = sfa + hw * 4;
      xo_ = O_X1C + (lane >> 3) * 2048 + kca, sto = O_ST2 + (hw & (S_R - 1)) * 1280 + lane * 16;
      hwa = lds0 + XC + hw * 1024 + (lane & 7) * 128 + (((lane >> 3) ^ lane) & 7) * 16, hra = lds0 + XC + g4 * 128 + ((lane ^ g4) & 7) * 16, hrb = hra ^ 64;
      awa = lds0 + SPARE + 15872 + lane * 4, ara = lds0 + SPARE + 15872 + (lane & 2) * 64 + g4 * 32 + (lane & 1) * 16;
      pso = O_PART + ((g >> 2) * 2048 + (lane & 8) * 128 + (g & 3) * 256 + hw * 64 + (lane & 7) * 8 + g4 * 2) * 4;
      gso = O_GU + g * 1024 + (lane >> 5) * 256 + hw * 64 + (lane & 15) * 4;
      gl0 = O_GU + (g >> 2) * 4096 + lane * 8, gl1 = O_GU + (xcd * 7 + (mt + 7 - W_UO) % 7) * 4096 + 512 + lane * 8;
      asm volatile("" : "+v"(sfa), "+v"(rk2), "+v"(xo_), "+v"(hwa), "+v"(hra), "+v"(hrb), "+v"(ara), "+v"(pso), "+v"(gso), "+v"(awa), "+v"(sfw), "+v"(gl0), "+v"(gl1), "+v"(sto), "+v"(kca));
    };
    if (hw < 2) {
      constexpr int P = 8;
      __builtin_amdgcn_s_setprio(2);
      unsigned voy[P];
#pragma unroll
      for (int i = 0; i < P; ++i) voy[i] = (unsigned)(((hw * 8 + i) * 8 + (lane >> 3)) * 4096 + ((lane & 7) ^ (lane >> 3)) * 16 - (i & 3) * 1024);
      const unsigned lbase = lds0 + hw * 8192;
      const char* Yc = reinterpret_cast<const char*>((cc ? Y1 : Y0) + (size_t)nt * 128 * S);
      auto stage = [&](const char* yp, unsigned l) {
        GLDS4(l, voy, yp);
        GLDS4(l + 4096, voy + 4, yp);
      };
#define YSRC(n) (Yc + ((((n) + xcd * 4) & 31) << 7))
      __builtin_amdgcn_s_sleep(C_LD);
      PFL(hw ? PFG(1) : cpf);
      JSET();
      TAGW(1);
      BAR(55);
      BT(56);
      if (hw == 1 && lane == 0) asm volatile("global_store_dwordx2 %0, %1, %2 sc1" ::"v"(O_XFL + b * XSTR * 4), "v"(tag), "s"(ws) : "memory");
#pragma unroll
      for (int s = 0; s < NS - 1; ++s) stage(YSRC(s), lbase + s * 16384);
      BT(57);
      WAIT_V((NS - 2) * P);
      BAR(0);
      unsigned slot = lbase + (NS - 1) * 16384;
      const u64 evm = (u64)((1u | 1u << W_T) << pf0 | (1u | 1u << W_T) << pf1) | 1ull << CPF_K | 1ull << (33 - NS) | 3ull << 32;
      const char* yb = Yc;
      unsigned yo = (NS - 1 + xcd * 4) << 7, ym = 31 << 7;
#pragma unroll 1
      for (int k = 1; k <= 32 + B_J; ++k) {
        const char* ys = yb + (yo & ym);
        yo += 128;
        asm volatile("" : "+s"(ys));
        WAIT_V((NS - 3) * P);
        BAR(k);
        stage(ys, slot);
        slot = slot == lbase + (NS - 1) * 16384 ? lbase : slot + 16384;
        if (__builtin_expect((evm >> k) & 1, 0)) {
          if (k == pf0) {
            WST(og, wg);
            if (b >= NPART) GLDS(lds0 + SPARE + 8192 + hw * 1024, (unsigned)(hw * 1024 + lane * 16), wr_);
          }
          if (k == pf0 + W_T) { WTK(gT); }
          if (k == pf1) { WST(ou, wu); }
          if (k == pf1 + W_T) { WTK(uT); }
          if (k == CPF_K && hw == 1) CPFL();
          if (k == 33 - NS) {
#pragma unroll
            for (int i = 0; i < P; ++i) voy[i] = HOFF((hw * 8 + i) * 8 + (lane >> 3)) - (i & 3) * 1024;
            yb = Hc, yo = mt << 8, ym = 15 << 7;
          }
          if (k == 32) {
            DLD(0, 8 - B_WN);
            TSET();
          }
          if (k == 33 && hw == 0) {
            if (U_D) __builtin_amdgcn_s_sleep(U_D);
            urdy = UPOLL(U_N1);
          }
        }
      }
    } else {
      const int aw = hw - 2;
      unsigned ow = wo - (2048 + (cg0 >> 4) * 1024) * 2;
      const unsigned oq = (unsigned)((nt * 64 + aw * 32 + (lane >> 4)) * D + mt * 128) * 2;
      u32x4 hv[8], wt[8], sc;
      {
        asm volatile("s_waitcnt vmcnt(11)" : "+v"(cq)::"memory");
#pragma unroll
        for (int i = 0; i < 4; ++i) sc[i] = pack2(silu_bf(bflo(cq[i])), silu_bf(bfhi(cq[i])));
        asm volatile("" : "+v"(sc));
      }
#define GPUB(o, off)                                                                                   \
  if ((lane & 8) == 0)                                                                                 \
    asm volatile("global_store_dwordx4 %0, %1, %2 sc1 ; ALWAYS\n\ts_nop 1" ::"v"(off),                          \
                 "v"(u32x4{__float_as_uint(o[0]), (unsigned)tag, __float_as_uint(o[1]), (unsigned)tag}), "s"(ws) : "memory")
      const unsigned gpo = ks0 * 2048 + cg0 * 64 + (lane & 7) * 8 + (lane >> 4) * 2;
      {
        unsigned mpo = O_G1 + gpo * 8;
        asm volatile("s_waitcnt vmcnt(3)" : W8(wv), "+v"(mpo)::"memory");
        TR8(wt, wv);
        f32x2 o = MV64(wt, sc);
        asm volatile("" : "+v"(o));
        TAGW(0);
        BAR(55);
        if (P_D2) __builtin_amdgcn_s_sleep(P_D2);
        PFL(PFG(2 + aw));
        GPUB(o, mpo);
      }
      unsigned vta = lds0 + STG + (g4 * 8 + ((lane >> 2) & 3)) * 256 + (((aw * 8 + ((lane >> 1) & 1)) ^ ((g4 & 1) * 8 + ((lane >> 2) & 3))) << 4) + (lane & 1) * 8;
      unsigned vso = O_VP + ((cc * 8 + mt) * 16 + nt) * 256 + aw * 128 + (lane & 3) * 32 + g4 * 8;
      asm volatile("" : "+v"(vta), "+v"(vso), "+v"(ow));
      unsigned f0 = O_XFL + ((xcd * 32 + 64 + aw * 128 + lane) & 255) * XSTR * 4, f1 = O_XFL + ((xcd * 32 + 128 + aw * 64 + lane) & 255) * XSTR * 4;
      unsigned lao = O_KEY + 512 * 1024 + b * 3840 + aw * 1920 + lane * 16;
      const char* pf4 = PFG(4 + aw);
      const char* pf6 = PFG(6 + aw);
      asm volatile("" : "+v"(f0), "+v"(f1), "+v"(lao), "+v"(pf4), "+v"(pf6));
      BT(57);
      BAR(0);
      f32x4 rs = {0.f, 0.f, 0.f, 0.f};
      u32x4 rsa = (lane & 15) == g4 * 4 ? u32x4{0x3f803f80u, 0x3f803f80u, 0x3f803f80u, 0x3f803f80u} : u32x4{0u, 0u, 0u, 0u}, R_;
      asm volatile("" : "+v"(rs), "+v"(rsa), "=v"(R_));
      const unsigned ra = lds0 + (mt * 16 + aw * 8) * 128 + lane * 16;
      unsigned so = 0;
#define TSX()
#define RSUM()                                                                                                    \
  do {                                                                                                            \
    TSX();                                                                                                        \
    asm volatile("ds_read_b128 %1, %2\n\ts_waitcnt lgkmcnt(0)\n\tv_mfma_f32_16x16x32_bf16 %0, %3, %1, %0" : "+v"(rs), "+v"(R_) : "v"(ra + so), "v"(rsa) : "memory"); \
    so = so == (NS - 1) * 16384 ? 0 : so + 16384;                                                                 \
  } while (0)
      {
        u64 f[2];
#define FLD() asm volatile("global_load_dwordx2 %0, %2, %4 sc1\n\tglobal_load_dwordx2 %1, %3, %4 sc1" : "=&v"(f[0]), "=&v"(f[1]) : "v"(f0), "v"(f1), "s"(ws) : "memory")
        __builtin_amdgcn_s_sleep(F_D);
        FLD();
        asm volatile("global_store_dwordx4 %0, %1, %2 sc1" ::"v"(lao), "v"(u32x4{~0u, ~0u, ~0u, ~0u}), "s"(ws) : "memory");
        if (lane < 56) asm volatile("global_store_dwordx4 %0, %1, %2 offset:1024 sc1 ; ALWAYS" ::"v"(lao), "v"(u32x4{~0u, ~0u, ~0u, ~0u}), "s"(ws) : "memory");
        asm volatile("s_nop 1");
#define GLD() _Pragma("unroll") for (int i = 0; i < 8; ++i) LDWI(wv[i], ow, wmod, 12288)
        RSUM();
        BAR(1);
        asm volatile("s_waitcnt vmcnt(%2)" : "+v"(f[0]), "+v"(f[1]) : "n"(2) : "memory");
        POLL(__all(f[0] == tag && f[1] == tag), 1, FLD(); asm volatile("s_waitcnt vmcnt(0)" : "+v"(f[0]), "+v"(f[1])::"memory"));
      }
      if (X_PA) __builtin_amdgcn_s_setprio(X_PA);
#define ARUN(b0, b1) _Pragma("unroll 1") for (int bi = (b0); bi < (b1); ++bi) { RSUM(); BAR(bi + 1); }
      GLD();
      ARUN(1, pf0);
      PFL(pf4);
#define HVLD()                                                                                                                    \
  do {                                                                                                                            \
    unsigned o_ = oq + (((lane & 15) ^ g4) << 4);                                                                                 \
    asm volatile("s_mov_b32 m0, %0" ::"s"(lds0 + STG + aw * 8192));                                                               \
    _Pragma("unroll") for (int i = 0; i < 8; ++i) {                                                                               \
      asm volatile("v_xor_b32 %0, %4, %0\n\tglobal_load_lds_dwordx4 %0, %1 offset:%2" HPOL "\n\tv_add_u32 %0, %3, %0"              \
                   : "+v"(o_) : "s"(cc ? X1 : X0), "n"((i & 3) * 1024), "n"(8192 + (i == 3 ? 3072 : -1024)), "n"(i ? ((i ^ (i - 1)) & 3) * 64 : 0) : "memory"); \
      if (i == 3) asm volatile("s_add_u32 m0, m0, 4096" ::: "scc");                                                               \
    }                                                                                                                             \
  } while (0)
      WST(og, wg);
      ARUN(pf0, pf0 + G1_C);
      {
        if (b >= NPART) WAIT_V(0);
        asm volatile("s_waitcnt vmcnt(8)" : W8(wv)::"memory");
        TR8(wt, wv);
        if (G1_S) { RSUM(); BAR(pf0 + G1_C + 1); }
        const f32x2 o = MV64(wt, sc);
        GPUB(o, O_MODP + gpo * 8);
        ow = wo + 2048;
      }
      ARUN(pf0 + G1_C + G1_S, pf0 + W_T - 1);
      WAIT_V(1);
      RSUM();
      BAR(pf0 + W_T);
      WTK(gT);
      ARUN(pf0 + W_T, pf1);
      PFL(pf6);
      WST(ou, wu);
      const int rb_ = b < NPART ? pf1 + W_T : R_AT, hb_ = max(rb_ + 1, H_AT + (j8 & 3));
      const unsigned c0 = (b - NPART) * 64 + aw * 32 + (lane & 15) * 2, c1 = (b - NPART) * 32 + aw * 16 + (lane & 7) * 2;
#define ERLD(W) LD8(wt, O_MODP + (g4 * 8192 + c0) * 8, O_G1 + (g4 * 8192 + (lane & 8) * 128 + c1) * 8, W)
      ARUN(pf1, rb_ - 3);
      if (b >= NPART) ERLD("");
      ARUN(rb_ - 3, rb_ - 1);
      WAIT_V(0);
      RSUM();
      BAR(rb_);
      WTK(uT);
      if (b >= NPART) {
        const unsigned wq = *reinterpret_cast<const unsigned*>(&svb[4][c1]);
#define TG2(r, i) ((r[i][1] ^ (unsigned)tag) | (r[i][3] ^ (unsigned)tag) | (r[i + 1][1] ^ (unsigned)tag) | (r[i + 1][3] ^ (unsigned)tag))
        asm volatile("" : W8(wt));
        POLL(__all((TG2(wt, 0) | TG2(wt, 2) | TG2(wt, 4) | TG2(wt, 6)) == 0u), 2, ERLD(VW0));
        auto FIN = [&](int q, int i, bool inc) __attribute__((always_inline)) {
          return rbf((inc ? 1.0f : 0.0f) + rbf(ar4((__uint_as_float(wt[q][i]) + __uint_as_float(wt[q + 1][i])) + (__uint_as_float(wt[q + 2][i]) + __uint_as_float(wt[q + 3][i])))));
        };
        const float s0 = FIN(0, 0, gbq), s1 = FIN(0, 2, gbq);
        if (lane < 16)
          asm volatile("global_store_dwordx4 %0, %1, %2 sc1\n\ts_nop 1" ::"v"(O_MODP + (16 * 2048 + c0) * 8),
                       "v"(u32x4{__float_as_uint(s0), (unsigned)tag, __float_as_uint(s1), (unsigned)tag}), "s"(ws) : "memory");
        const bool hi = lane & 8;
        const float t0 = FIN(4, 0, hi), t1 = FIN(4, 2, hi), o0 = dpp_f<0x128>(t0), o1 = dpp_f<0x128>(t1);
        const float a0 = hi ? o0 : t0, a1 = hi ? o1 : t1, e0 = hi ? t0 : o0, e1 = hi ? t1 : o1, p0 = e0 * bflo(wq), p1 = e1 * bfhi(wq);
        const unsigned h = pack2(p0, p1);
        const unsigned v = g4 == 0 ? __float_as_uint(hi ? a1 : a0) : g4 == 1 ? (hi ? h : pack2(e0, e1)) : hi ? pack2(a0 * p0, a1 * p1) : pack2(p0 - bflo(h), p1 - bfhi(h));
        const unsigned po = g4 == 0 ? O_M2 + c1 * 4 + (hi ? 4 : 0) : (g4 == 1 ? (hi ? O_M2 + 4096 : O_MOD + 4096) : hi ? O_GC : O_M2 + 6144) + c1 * 2;
        if (lane < 48) asm volatile("global_store_dword %0, %1, %2 sc1" ::"v"(po), "v"(min(v, SENT - 1u)), "s"(ws) : "memory");
      }
      ARUN(rb_, hb_);
      HVLD();
      ARUN(hb_, q1);
      u32x4 fm, g0, g1;
#define FMLD(W)                                                                                                                                     \
  asm volatile("global_load_dwordx4 %0, %3, %5 sc1\n\tglobal_load_dwordx4 %1, %4, %5 sc1\n\tglobal_load_dwordx4 %2, %4, %5 offset:64 sc1" W        \
               : "=&v"(fm), "=&v"(g0), "=&v"(g1) : "v"(O_MODP + (16 * 2048 + aw * 1024 + mt * 128) * 8 + lane * 16), "v"(O_GC + nt * 128 + g4 * 16), "s"(ws) : "memory")
      FMLD("");
      RSUM();
      BAR(q1 + 1);
      unsigned ha[4], sma = lds0 + SPARE + 15872 + (aw ? 512 + lane * 8 : lane * 4);
#pragma unroll
      for (int i = 0; i < 4; ++i) ha[i] = lds0 + STG + aw * 8192 + (lane & 15) * 256 + (((i * 4 + (lane >> 4)) ^ (lane & 15)) << 4);
      asm volatile("" : "+v"(ha[0]), "+v"(ha[1]), "+v"(ha[2]), "+v"(ha[3]), "+v"(sma), "+v"(ow));
      ARUN(q1 + 1, F_AT);
      asm volatile("s_waitcnt vmcnt(0)" : "+v"(fm), "+v"(g0), "+v"(g1)::"memory");
#pragma unroll
      for (int i = 0; i < 8; ++i) DSR(hv[i], ha[i & 3], (i >> 2) * 4096);
      POLL(__all(fm[1] == (unsigned)tag && fm[3] == (unsigned)tag && max(max4(g0), max4(g1)) != SENT), 1, FMLD(VW0));
      {
        const unsigned sg = cc ? 0x80000000u : 0u;
        asm volatile("ds_write_b32 %0, %2\n\tds_write_b32 %1, %3" ::"v"(sma), "v"(sma + (aw ? 4 : 256)), "v"(aw ? fm[0] ^ sg : (fm[0] >> 16) | (fm[2] & 0xffff0000u)), "v"(fm[2] ^ sg) : "memory");
      }
      ARUN(F_AT, 29);
      GLD();
      RSUM();
      BAR(30);
        u32x2 tv[16];
#pragma unroll
        for (int q = 0; q < 16; ++q)
          asm volatile("ds_read_b64_tr_b16 %0, %1 offset:%2" : "=v"(tv[q]) : "v"(vta ^ ((((q >> 2) * 2) ^ ((q & 1) * 4)) << 4)), "n"((q & 1) * 1024 + ((q >> 1) & 1) * 8192) : "memory");
      RSUM();
      BAR(31);
      RSUM();
      {
        asm volatile("s_nop 7\n\ts_nop 7" : "+v"(rs));
        const float v0 = dpp_add<0x141>(dpp_add<0x4E>(dpp_add<0xB1>(rs[0]))), v = cc ? -v0 : v0;
        if ((lane & 7) == 0)
          asm volatile("global_store_dword %0, %1, %2 sc1 ; ALWAYS" ::"v"(O_RS + (cc * S + nt * 128 + mt * 16 + aw * 8 + (lane >> 3)) * 4), "v"(min(pack2(v, v - rbf(v)), SENT - 1u)), "s"(ws) : "memory");
      }
      BAR(32);
      __builtin_amdgcn_s_sleep(2);
      DLD(0, 8 - B_WN);
      __builtin_amdgcn_s_setprio(2);
      {
        asm volatile("" : W8(tv), W8((tv + 8))::"memory");
        f32x4 d[4];
#pragma unroll
        for (int j = 0; j < 4; ++j) {
          d[j] = __builtin_amdgcn_mfma_f32_16x16x32_bf16(BF8((u32x4{tv[4 * j][0], tv[4 * j][1], tv[4 * j + 1][0], tv[4 * j + 1][1]})), BF8(g0), f32x4{0.f, 0.f, 0.f, 0.f}, 0, 0, 0);
          d[j] = __builtin_amdgcn_mfma_f32_16x16x32_bf16(BF8((u32x4{tv[4 * j + 2][0], tv[4 * j + 2][1], tv[4 * j + 3][0], tv[4 * j + 3][1]})), BF8(g1), d[j], 0, 0, 0);
        }
        u32x2 vo;
#pragma unroll
        for (int i = 0; i < 2; ++i) {
          const unsigned e0 = pack2(d[0][2 * i], d[0][2 * i + 1]), e1 = pack2(d[1][2 * i], d[1][2 * i + 1]), e2 = pack2(d[2][2 * i], d[2][2 * i + 1]), e3 = pack2(d[3][2 * i], d[3][2 * i + 1]);
          vo[i] = min((lane & 2) ? ((lane & 1) ? e3 : e2) : ((lane & 1) ? e1 : e0), SENT - 1u);
        }
        if ((lane & 12) == 0) asm volatile("global_store_dwordx2 %0, %1, %2 sc1 ; ALWAYS" ::"v"(vso), "v"(vo), "s"(ws) : "memory");
      }
      asm volatile("" : W8(hv)::"memory");
      {
        f32x4 qa[2] = {};
#pragma unroll
        for (int kk = 0; kk < 4; ++kk) {
          const bf16x8 sb8 = *reinterpret_cast<const bf16x8*>(ssa + kk * 32 + (lane >> 4) * 8);
#pragma unroll
          for (int r = 0; r < 2; ++r)
            qa[r] = __builtin_amdgcn_mfma_f32_16x16x32_bf16(__builtin_bit_cast(bf16v8, hv[r * 4 + kk]), __builtin_bit_cast(bf16v8, sb8), qa[r], 0, 0, 0);
        }
        if ((lane & 15) < 2)
          asm volatile("global_store_dwordx4 %0, %1, %2 sc1 ; ALWAYS\n\ts_nop 1" ::"v"(O_RS + (4096 + (cc * 8 + mt) * D + nt * 64 + aw * 32 + (lane & 1) * 16 + (lane >> 4) * 4) * 4),
                       "v"(nsent4(selu((lane & 1) ? ~0u : 0u, __builtin_bit_cast(u32x4, qa[1]), __builtin_bit_cast(u32x4, qa[0])))), "s"(ws) : "memory");
      }
      BAR(33);
      {
        if (b >= NPART) WAIT_V(3);
        asm volatile("s_waitcnt vmcnt(%8)" : W8(wv) : "n"(8 - B_WN + 3) : "memory");
        TR8(wt, wv);
        const f32x2 o = MV64(wt, sc);
        if ((lane & 8) == 0) asm volatile("global_store_dwordx2 %0, %1, %2 sc1" ::"v"(O_G2 + gpo * 4), "v"(u32x2{nsent(o[0]), nsent(o[1])}), "s"(ws) : "memory");
      }
      JSET();
      TSET();
      BAR(34);
#pragma unroll
      for (int k = 3; k <= B_J; ++k) {
        BAR(32 + k);
        if (k == V_K) {
          if (!aw) GP4(O_VP + (cc * 8 + mt) * 4096 + lane * 16, "");
          else VCLD("");
        }
      }
    }
    u32x4 dT[8];
    u32x4 m[3];
    u32x2 kk = {SENT, SENT};
#define K2LD(W) asm volatile("global_load_dwordx2 %0, %1, %2 sc1" W : "+v"(kk) : "v"(O_K2 + (lane & 15) * 64), "s"(ws) : "memory")
    {
      constexpr BSched sch;

      const unsigned lbase = lds0 + hw * 4096;
      unsigned cbv, t1, t2;
#define RRLD(W)                                                                                                                   \
  asm volatile("global_load_dword %4, %9, %10 sc1\n\tglobal_load_dwordx4 %0, %5, %10 sc1\n\tglobal_load_dwordx4 %1, %6, %10 sc1\n\t" \
               "global_load_dwordx4 %2, %7, %10 sc1\n\tglobal_load_dwordx4 %3, %8, %10 sc1" W                                      \
               : "=&v"(gp[0]), "=&v"(gp[1]), "=&v"(gp[2]), "=&v"(gp[3]), "=&v"(cbv)                                               \
               : "v"(ro), "v"(ro + 8192), "v"(ro + 16384), "v"(ro + 24576), "v"(O_MOD + 4096 + cj * 2), "s"(ws) : "memory")
      unsigned wq;
      u32x4 mq = {0u, 0u, 0u, 0u};
#define QLD(W)                                                                                                        \
  do {                                                                                                                \
    if (hw < 2) asm volatile("global_load_dwordx4 %0, %1, %2 sc1" W : "+v"(mq) : "v"(qo), "s"(ws) : "memory");        \
    else asm volatile("global_load_dword %0, %1, %2 sc1" W : "+v"(mq[0]) : "v"(qo), "s"(ws) : "memory");              \
  } while (0)
#define QRLD(W)                                                                                                       \
  asm volatile("global_load_dwordx4 %0, %2, %4 sc1\n\tglobal_load_dwordx4 %1, %3, %4 sc1" W : "=&v"(gp[0]), "=&v"(gp[1]) \
               : "v"(qr), "v"(qr + 32768), "s"(ws) : "memory")
      const bool rq_ = b >= NPART && hw == 1;
      const unsigned qr = O_RS + (4096 + (lane >> 3) * D + (b - NPART) * 32 + (lane & 7) * 4) * 4;
#define MODLD() asm volatile("global_load_dwordx4 %0, %2, %4 sc1\n\tglobal_load_dwordx4 %1, %3, %4 sc1" : "=&v"(m[0]), "=&v"(m[2]) : "v"(O_MOD + ma), "v"(mo2), "s"(ws) : "memory")
#define MODW(n) asm volatile("s_waitcnt vmcnt(%2)" : "+v"(m[0]), "+v"(m[2]) : "n"(n) : "memory")
#pragma unroll
      for (int k = B_J; k < 16; ++k) {
        if (k > B_J) {
          if (k == 2 * U_N1 - 1 && !urdy) UPOLL(7);
          WAIT_V(sch.cnt[k]);
          if (k == 15) WAIT_L(0);
          BAR(32 + k);
        }
        if (k > B_J && k - 2 + NS < 16) {
          const char* yp = Hc + ((mt * 2 + k - 2 + NS) & 15) * 128;
          const unsigned l = lbase + ((30 + k + NS) % NS) * 16384;
          GLDS4(l, voff, yp);
        }
        if (k == B_TK && hw == 1 && b >= 192) {
          const unsigned t = tick(), h = t + T_O + (((int)(t - ts0) - T_N) * T_A >> 8);
          if (lane == 0) asm volatile("global_store_dword %0, %1, %2 sc1" ::"v"(O_M2 + 8448 + (b - 192) * 4), "v"(min(h, SENT - 1u)), "s"(ws) : "memory");
        }
        if (k == B_RAT + 2 && rdc) {
          WAIT_V(sch.rcnt);
          asm volatile("" : "+v"(gp[0]), "+v"(gp[1]), "+v"(gp[2]), "+v"(gp[3]), "+v"(cbv));
          POLL(!__any(GPBAD || cbv == SENT), 2, RRLD(VW0));
#define GF(i) __builtin_bit_cast(f32x4, gp[i])
          const f32x4 t = (GF(0) + GF(1)) + (GF(2) + GF(3));
          const float ta = swap32_add(t[0], t[2]), tb = swap32_add(t[1], t[3]);
          const unsigned o = pack2(swap16_add(ta, ta), swap16_add(tb, tb)), f = gbq ? cbv : o;
          const float pr = row_sum(bflo(f) * bflo(wq) + bfhi(f) * bfhi(wq)), ps = swap32_add(pr, pr);
          if ((lane & 16) == 0 || lane == 16)
            asm volatile("global_store_dword %0, %1, %2 sc1" ::"v"(ra1), "v"(min((lane & 16) ? __float_as_uint(ps) : o, SENT - 1u)), "s"(ws) : "memory");
          TS(12);
        }
        if (k == V_T && hw == 3) {
          WAIT_V(sch.vcnt);
          asm volatile("" : "+v"(vc));
          POLL(__all(vc != SENT), 2, VCLD(VW0));
        }
        if (k == V_T && hw == 2) {
          WAIT_V(sch.vcnt);
          asm volatile("" : "+v"(gp[0]), "+v"(gp[1]), "+v"(gp[2]), "+v"(gp[3]));
          POLL(!__any(GPBAD), 2, GP4(O_VP + (cc * 8 + mt) * 4096 + lane * 16, VW0));
          const f32x4 d = GPSUM();
          if (lane < 32) *reinterpret_cast<u32x2*>(shm + VT + (lane & 15) * 16 + g4 * 8) = u32x2{pack2(d[0], d[1]), pack2(d[2], d[3])};
          WAIT_L(0);
        }
        if (k == N_C) {
          f32x4 n[2] = {};
#pragma unroll
          for (int q = 0; q < 8; ++q)
            n[q >> 2] = __builtin_amdgcn_mfma_f32_16x16x32_bf16(BF8(*reinterpret_cast<const u32x4*>(shm + VT + (q & 3) * 64 + g4 * 16)),
                                                                BF8(*reinterpret_cast<const u32x4*>(shm + STG + hw * 8192 + q * 1024 + lane * 16)), n[q >> 2], 0, 0, 0);
          if (lane < 16)
            asm volatile("global_store_dword %0, %1, %2 sc1" ::"v"(O_NP + nt * 4096 + (cc * 8 + mt) * 256 + hw * 64 + lane * 4), "v"(min(pack2(n[0][0], n[1][0]), SENT - 1u)), "s"(ws) : "memory");
          TS(33);
        }
        if (k == N_K + N_D && rnp) {
            WAIT_V(sch.ncnt);
            asm volatile("" : "+v"(gp[0]), "+v"(gp[1]), "+v"(gp[2]), "+v"(gp[3]));
            POLL(!__any(GPBAD), 2, GP4(O_NP + nt * 4096 + lane * 16, VW0));
            const unsigned r0 = nt * 128 + ((lane >> 2) & 3) * 32 + (lane & 3) * 4 + (g4 & 1) * 2;
        const f32x4 d = GPSUM();
        unsigned ky[4];
#pragma unroll
        for (int i = 0; i < 4; ++i) {
          const unsigned f = __float_as_uint(d[i]);
          ky[i] = (((f ^ ((unsigned)((int)f >> 31) | 0x80000000u)) >> 1) & ~2047u) | (r0 + (i & 1) * 16 + (i >> 1));
        }
        t1 = max(max(ky[0], ky[1]), max(ky[2], ky[3])), t2 = max(min(max(ky[0], ky[1]), max(ky[2], ky[3])), max(min(ky[0], ky[1]), min(ky[2], ky[3])));
        asm volatile("" : "+v"(t1), "+v"(t2));
        }
        if (k == N_K + N_D && rnp) {
        TOP2(0xB1); TOP2(0x4E); TOP2(0x141); TOP2(0x140);
        TOP2S(__builtin_amdgcn_permlane16_swap);
            if (lane == 0) asm volatile("global_store_dwordx2 %0, %1, %2 sc1" ::"v"(O_K2 + nt * 64), "v"(u32x2{t1, t2}), "s"(ws) : "memory");
            TS(34);
        }
        if (k == N_K && rnp) GP4(O_NP + nt * 4096 + lane * 16, "");
        if (k == B_RAT + 2 && rq_) {
          WAIT_V(sch.rcnt);
          asm volatile("" : "+v"(gp[0]), "+v"(gp[1]));
          POLL(__all(max(max4(gp[0]), max4(gp[1])) != SENT), 2, QRLD(VW0));
          u32x4 a;
#pragma unroll
          for (int c = 0; c < 2; ++c) {
            const f32x4 t = __builtin_bit_cast(f32x4, gp[c]);
            const float r = dpp_add<0x128>(swap16_add(swap32_add(t[0], t[2]), swap32_add(t[1], t[3])));
            a[2 * c] = pack2(r, r);
            a[2 * c + 1] = pack2(r - bflo(a[2 * c]), r - bflo(a[2 * c]));
          }
          if ((lane & 8) == 0)
            asm volatile("global_store_dwordx4 %0, %1, %2 sc1\n\ts_nop 1" ::"v"(O_QT + ((b - NPART) * 32 + (lane & 1) * 16 + ((lane >> 1) & 3) * 4 + g4) * 16), "v"(nsent4(a)), "s"(ws) : "memory");
        }
        if (k == B_QAT + B_QD) {
          asm volatile("s_waitcnt vmcnt(%1)" : "+v"(mq) : "n"(sch.qcnt) : "memory");
          POLL(__all(max4(mq) != SENT), 2, QLD(VW0));
          if (hw < 2) asm volatile("ds_write_b128 %0, %1" ::"v"(qa), "v"(mq) : "memory");
          else asm volatile("ds_write2_b32 %0, %1, %1 offset1:1" ::"v"(qa), "v"(mq[0]) : "memory");
        }
        if (k == CPF_B && hw == 2) CPFL();
        if (k == B_RAT) {
          if (rdc) {
            RRLD("");
            asm volatile("ds_read_b32 %0, %1\n\ts_waitcnt lgkmcnt(0)" : "=v"(wq) : "v"(wqa) : "memory");
          }
          if (rq_) QRLD("");
        }
        if (k == X_K) {
          const unsigned v4[4] = {xo, (xo ^ 64) + 8192 - 1024, (xo ^ 128) + 16384 - 2048, (xo ^ 192) + 24576 - 3072};
          unsigned l2 = lbase;
          asm volatile("" : "+s"(l2));
          GLDS4(l2 * 2 - lds0 + STG + 4096, v4, x);
        }
        if (k == V_T + 1 || k == N_K + 1) SKEWPT();
        if (k == B_QAT) QLD("");
        if (k == B_MAT) MODLD();
        if (k == B_WK) { DLD(8 - B_WN, 8); }
      }
      if (b >= NPART) WAIT_V(0);
      MODW(sch.mcnt);
      TS(30);
      SKEWPT();
      POLL(__all(max(max4(m[0]), max4(m[2])) != SENT), 4, MODLD(); MODW(0));
      *reinterpret_cast<u32x4*>(shm + SPARE + ma) = m[0];
      if (hw == 0) {
        asm volatile("ds_write_b128 %0, %1" ::"v"(ma2), "v"(m[2]) : "memory");
      } else if (hw == 2) {
        int d = max(max((int)(m[2][0] - ts0), (int)(m[2][1] - ts0)), max((int)(m[2][2] - ts0), (int)(m[2][3] - ts0)));
        d = max(d, (int)dpp_u<0xB1>(d));
        d = max(d, (int)dpp_u<0x4E>(d));
        d = max(d, (int)dpp_u<0x141>(d));
        d = max(d, (int)dpp_u<0x140>(d));
        const unsigned hh = ts0 + d;
        ssel[7] = hh;
        if ((int)(tick() - (unsigned)__builtin_amdgcn_readfirstlane((int)hh) - T_K) >= 0) K2LD("");
      } else {
        const f32x4 v = __builtin_bit_cast(f32x4, m[2]);
        const f32x2 c = {v[0] + v[1], v[2] + v[3]};
        const float s4 = dpp_add<0x4E>(dpp_add<0xB1>(c[0] + c[1]));
        if (lane < 8) asm volatile("ds_write_b64 %0, %1" ::"v"(ma2), "v"(lane < 4 ? c : f32x2{s4, s4}) : "memory");
      }
    }
    if (hw != 2) __builtin_amdgcn_s_setprio(0);
    TS(17);
    BT(48);
    LBAR();
    BTE();
    TS(18);
    if (hw == 2) {
      WAIT_V(0);
      asm volatile("" : "+v"(kk));
      if (__any(max(kk[0], kk[1]) == SENT)) {
        nap_tick((unsigned)__builtin_amdgcn_readfirstlane((int)ssel[7]) + T_K);
        POLL2(max(kk[0], kk[1]) == SENT, K_S, PCAP, K2LD(VW0));
      }
      TS(35);
      const unsigned me = lane < 32 ? kk[0] : kk[1], ot = (lane & 16) ? kk[1] : kk[0];
      unsigned rk = ot > me;
      sfor<15>([&](auto r) __attribute__((always_inline)) { rk += dpp_u<0x121 + decltype(r)::value>(ot) > me; });
      auto rs_ = __builtin_amdgcn_permlane16_swap(rk, rk, false, false);
      rk = rs_[0] + rs_[1];
      if (rk < 8u) {
        *reinterpret_cast<unsigned*>(shm + LST + rk * 4) = 0x8ffeu - (me & 2047u) * 2;
        if ((lane & 15) == nt) *reinterpret_cast<unsigned*>(shm + SLT + (me & 127u) * 4) = rk + 1;
      }
      asm volatile("" ::: "memory");
      ssel[20] = 1u;
    }
    TS(19);
    unsigned sv, one = 1u, kmk = ~0u;
    RETIRE();
    const u32x4 cbq = *reinterpret_cast<const u32x4*>(shm + SPARE + 4096 + kca), sbq = *reinterpret_cast<const u32x4*>(shm + SPARE + 2048 + kca);
    f32x4 cb2[2] = {lo4(cbq), hi4(cbq)}, sb2[2] = {lo4(sbq), hi4(sbq)};
    float sw = cwj[8], cj8 = cwj[lane & 7];
    asm volatile("" : "+v"(cb2[0]), "+v"(cb2[1]), "+v"(sb2[0]), "+v"(sb2[1]), "+v"(sw), "+v"(cj8), "+v"(one), "+v"(kmk));
    const unsigned th = (unsigned)__builtin_amdgcn_readfirstlane((int)ssel[7]);
    const char* wb = ws;
#define P2PUT(i) if (__builtin_expect(wb != ws, 0)) { OWNL(); WAIT_V(0); if (lane == 0) asm volatile("global_store_dwordx2 %0, %1, %2 sc1" ::"v"(O_F2 + (i)*8192 + (g * 4 + hw) * 8 + lane), "v"(tag), "s"(ws) : "memory"); }
#define P2GET(o) if (__builtin_expect(wb != ws, 0)) { OWNL(); u64 f; POLL2(f != tag, 4, PCAP, asm volatile("global_load_dwordx2 %0, %1, %2 sc1" VW0 : "=&v"(f) : "v"(O_F2 + (o)), "s"(ws) : "memory")); }
#pragma unroll 1
    for (;;) {
    TS(20);
    if (wb == ws) {
      unsigned r_;
      POLL2(sv == 0u, 2, 1 << 26, asm volatile("ds_read_b32 %0, %2 offset:80\n\tds_read_b32 %1, %3\n\ts_waitcnt lgkmcnt(0)" : "=&v"(sv), "=&v"(r_) : "v"(sfa), "v"(rk2) : "memory"));
      rk2 = r_;
    }
    TS(21);
    nap_tick(th + T_C3 - 10);
    {
      u32x4 dn;
      POLL2((dn[0] & dn[1] & dn[2] & dn[3]) == 0u, 1, 1 << 26, asm volatile("ds_read_b128 %0, %1 offset:96\n\ts_waitcnt lgkmcnt(0)" : "=v"(dn) : "v"(sfa) : "memory"));
    }
    __builtin_amdgcn_s_setprio(H_P);
    wait_tick(th + T_C3);
    TS(36);
    u32x4 st, xv_;
    POLL2(max(max4(xv_), max4(st)) == SENT, 0, PCAP,
          asm volatile("global_load_dwordx4 %0, %2, %4 sc1\n\tglobal_load_dwordx4 %1, %3, %4 sc1" VW0 : "=&v"(st), "=&v"(xv_) : "v"(sto), "v"(xo_), "s"(ws) : "memory"));
    TS(22);
    SKEWPT();
#define SUM8(v) dpp_add<0x141>(dpp_add<0x4E>(dpp_add<0xB1>(v)))
    const f32x4 sf = __builtin_bit_cast(f32x4, st);
    const float mean = SUM8(sf[0]) * 0.125f, dm = sf[0] - mean;
    const float rstd = __builtin_amdgcn_rsqf(SUM8(sf[1] + 128.0f * (dm * dm)) * (1.0f / D) + 1e-5f);
    asm volatile("ds_write_b128 %0, %1\n\tds_write_b32 %2, %3 offset:64" ::"v"(hwa),
                 "v"(pk8(((lo4(xv_) - mean) * rstd) * cb2[0] + sb2[0], ((hi4(xv_) - mean) * rstd) * cb2[1] + sb2[1])), "v"(sfw), "v"(one) : "memory");
    TS(3);
    u32x4 ha[8], fg;
#define HSLD(W)                                                                                                                             \
  asm volatile(                                                                                                                             \
      "ds_read_b128 %8, %11 offset:64\n\tds_read_b128 %0, %9\n\tds_read_b128 %1, %10 offset:512\n\tds_read_b128 %2, %9 offset:1024\n\t"       \
      "ds_read_b128 %3, %10 offset:1536\n\tds_read_b128 %4, %9 offset:2048\n\tds_read_b128 %5, %10 offset:2560\n\t"                          \
      "ds_read_b128 %6, %9 offset:3072\n\tds_read_b128 %7, %10 offset:3584" W                                                               \
      : "=&v"(ha[0]), "=&v"(ha[1]), "=&v"(ha[2]), "=&v"(ha[3]), "=&v"(ha[4]), "=&v"(ha[5]), "=&v"(ha[6]), "=&v"(ha[7]), "=&v"(fg)           \
      : "v"(hra), "v"(hrb), "v"(sfa)                                                                                                        \
      : "memory")
    unsigned key = (mkkey(rstd * SUM8(sf[2] + dm * cj8) + sw) | rk2) & kmk;
    asm volatile("" : "+v"(key));
    if (H_O == 0) HSLD("");
    unsigned t1 = key, t2 = 0u;
    TOP2(0x140);
    if (H_O == 1) { asm volatile("" : "+v"(t1), "+v"(t2)); HSLD(""); asm volatile("" : "+v"(t1), "+v"(t2)); }
    TOP2S(__builtin_amdgcn_permlane16_swap);
    if (H_O == 2) { asm volatile("" : "+v"(t1), "+v"(t2)); HSLD(""); asm volatile("" : "+v"(t1), "+v"(t2)); }
    TOP2S(__builtin_amdgcn_permlane32_swap);
    const int l1 = __builtin_ctzll(__builtin_amdgcn_ballot_w64(key == t1)), l2 = __builtin_ctzll(__builtin_amdgcn_ballot_w64(key == t2));
    unsigned mq[6];
    asm volatile("s_bfe_i32 %0, %6, 0x10003\n\ts_bfe_i32 %1, %7, 0x10003\n\ts_bfe_i32 %2, %6, 0x10004\n\ts_bfe_i32 %3, %7, 0x10004\n\t"
                 "s_bfe_i32 %4, %6, 0x10005\n\ts_bfe_i32 %5, %7, 0x10005\n\ts_xor_b32 %4, %4, 0xffff\n\ts_xor_b32 %5, %5, 0xffff"
                 : "=&s"(mq[0]), "=&s"(mq[1]), "=&s"(mq[2]), "=&s"(mq[3]), "=&s"(mq[4]), "=&s"(mq[5]) : "s"(l1), "s"(l2) : "scc");
    const u64 m0 = mk64(mq[1], mq[0]), m1 = mk64(mq[3], mq[2]), m2 = mk64(mq[5], mq[4]);
    asm volatile("s_waitcnt lgkmcnt(0)" : W8(ha), "+v"(fg)::"memory");
    TS(2);
    if (b < NPART) {
#define HBAD ((fg[0] & fg[1] & fg[2] & fg[3]) != one)
      if (__any(HBAD)) POLL2(HBAD, 0, 1 << 26, HSLD("\n\ts_waitcnt lgkmcnt(0)"));
      TS(23);
      f32x4 dg = {0.f, 0.f, 0.f, 0.f}, du = {0.f, 0.f, 0.f, 0.f};
#pragma unroll
      for (int f = 0; f < 8; ++f) {
        dg = __builtin_amdgcn_mfma_f32_16x16x32_bf16(BF8(ha[f]), BF8(gT[f]), dg, 0, 0, 0);
        du = __builtin_amdgcn_mfma_f32_16x16x32_bf16(BF8(ha[f]), BF8(uT[f]), du, 0, 0, 0);
      }
      const bool s0 = __builtin_amdgcn_inverse_ballot_w64(m0), s1 = __builtin_amdgcn_inverse_ballot_w64(m1);
      if (__builtin_amdgcn_inverse_ballot_w64(m2))
        asm volatile("global_store_dword %0, %1, %3 sc1 ; ALWAYS\n\tglobal_store_dword %0, %2, %3 offset:512 sc1" ::"v"(gso),
                     "v"(nsent(s1 ? (s0 ? dg[3] : dg[2]) : (s0 ? dg[1] : dg[0]))), "v"(nsent(s1 ? (s0 ? du[3] : du[2]) : (s0 ? du[1] : du[0]))), "s"(wb) : "memory");
    }
    if (hw == 0 && wb == ws) asm volatile("ds_write_b64 %0, %1" ::"v"(sfa), "v"(u32x2{t1, t2}) : "memory");
    if (b < NPART) {
      TS(24);
      asm volatile("" : W8(wdv), "+v"(qm[0]), "+v"(qm[1]), "+v"(qm[2]), "+v"(qm[3])::"memory");
      TR8(dT, wdv);
      P2PUT(0);
      if (hw == 0) {
        u32x2 rr[8];
        if (E_D) __builtin_amdgcn_s_sleep(E_D);
        TS(39);
        P2GET(((((lane & 16) ? gl1 : gl0) - O_GU) >> 12) * 128 + (lane & 15) * 8);
#define MX2(i) max(max(rr[i][0], rr[i][1]), max(rr[i + 1][0], rr[i + 1][1]))
        POLL2(max(max(MX2(0), MX2(2)), max(MX2(4), MX2(6))) == SENT, 1, PCAP,
              TS(40);
              asm volatile("global_load_dwordx2 %0, %8, %10 sc1\n\tglobal_load_dwordx2 %1, %8, %10 offset:1024 sc1\n\t"
                           "global_load_dwordx2 %2, %8, %10 offset:2048 sc1\n\tglobal_load_dwordx2 %3, %8, %10 offset:3072 sc1\n\t"
                           "global_load_dwordx2 %4, %9, %10 sc1\n\tglobal_load_dwordx2 %5, %9, %10 offset:1024 sc1\n\t"
                           "global_load_dwordx2 %6, %9, %10 offset:2048 sc1\n\tglobal_load_dwordx2 %7, %9, %10 offset:3072 sc1" VW0
                           : "=&v"(rr[0]), "=&v"(rr[1]), "=&v"(rr[2]), "=&v"(rr[3]), "=&v"(rr[4]), "=&v"(rr[5]), "=&v"(rr[6]), "=&v"(rr[7])
                           : "v"(gl0), "v"(gl1), "s"(wb)
                           : "memory"));
        TS(8);
#define R2(i) __builtin_bit_cast(f32x2, rr[i])
        const f32x2 gs = (R2(0) + R2(1)) + (R2(2) + R2(3)), us = (R2(4) + R2(5)) + (R2(6) + R2(7));
        const unsigned gp = pack2(gs[0], gs[1]), up = pack2(us[0], us[1]);
        const unsigned sp = pack2(bflo(gp) * __builtin_amdgcn_rcpf(1.0f + __expf(-bflo(gp))), bfhi(gp) * __builtin_amdgcn_rcpf(1.0f + __expf(-bfhi(gp))));
        asm volatile("ds_write_b32 %0, %1\n\tds_write_b32 %2, %3 offset:24" ::"v"(awa), "v"(pack2(bflo(sp) * bflo(up), bfhi(sp) * bfhi(up))), "v"(sfa), "v"(one) : "memory");
      }
      TS(25);
      SKEWPT();
      POLL2(sv != one, 0, 1 << 26, asm volatile("ds_read_b32 %1, %3 offset:24\n\tds_read_b128 %0, %2\n\ts_waitcnt lgkmcnt(0)" : "=&v"(fg), "=&v"(sv) : "v"(ara), "v"(sfa) : "memory"));
      asm volatile("global_load_dwordx2 %0, %1, %2 sc1" : "=&v"(vd) : "v"(O_VD + (b & 31) * 128), "s"(ws) : "memory");
      const f32x2 dd = MV64(dT, fg);
      asm volatile("global_store_dwordx2 %0, %1, %2 sc1" ::"v"(pso), "v"(u32x2{nsent(dd[0]), nsent(dd[1])}), "s"(wb) : "memory");
      TS(6);
      P2PUT(1);
      asm volatile("s_waitcnt vmcnt(1)" : "+v"(vd)::"memory");
      if (__any(max(vd[0], vd[1]) == SENT)) VDLD(2);
      TS(5);
    } else if (hw == 3) {
      OWNL();
      u32x2 g0, g1;
      nap_tick(th + T_C4);
      POLL2(min(min(g0[0], g0[1]), min(g1[0], g1[1])) == 0u, 2, PCAP,
            asm volatile("global_load_dwordx2 %0, %2, %4 sc1\n\tglobal_load_dwordx2 %1, %3, %4 sc1" VW0
                         : "=&v"(g0), "=&v"(g1) : "v"(O_KEY + lane * 8 * KSTR), "v"(O_KEY + (lane + 64) * 8 * KSTR), "s"(ws) : "memory"));
      const unsigned k1 = t1, k2 = t2;
      t1 = max(g0[0], g1[0]), t2 = max(min(g0[0], g1[0]), max(g0[1], g1[1]));
      TOP2(0xB1); TOP2(0x4E); TOP2(0x141); TOP2(0x140);
      TOP2S(__builtin_amdgcn_permlane16_swap);
      TOP2S(__builtin_amdgcn_permlane32_swap);
      vd = u32x2{max(t1, k1), max(min(t1, k1), max(t2, k2))};
      if (lane == 0) asm volatile("global_store_dwordx2 %0, %1, %2 sc1\n\tds_write_b64 %3, %1 offset:8" ::"v"(O_VD + (b & 31) * 128), "v"(vd), "s"(ws), "v"(sfa) : "memory");
      TS(29);
      break;
    } else if (hw < 2) {
      OWNL();
      const int tok = b & 1, c0 = ((b - NPART) >> 1) * 64 + hw * 32 + (lane & 7) * 4, col = c0 + g4;
      const unsigned td = tick(), kk = tok ? t2 : t1;
      const int ln = tok ? l2 : l1;
      unsigned v;
      POLL2(v == SENT, 1, PCAP, asm volatile("global_load_dword %0, %1, %2 sc1" VW0 : "=&v"(v) : "v"(O_X1C + ((__builtin_amdgcn_readlane(xo_, ln) - O_X1C) >> 11) * 2048 + (col >> 1) * 4), "s"(ws) : "memory"));
      float x1 = (col & 1) ? bfhi(v) : bflo(v);
      float h2 = rbf(((x1 - __int_as_float(__builtin_amdgcn_readlane(__float_as_int(mean), ln))) * __int_as_float(__builtin_amdgcn_readlane(__float_as_int(rstd), ln))) * bf2f(svb[2][col]) + bf2f(svb[1][col]));
      float gb = bf2f(svb[3][col]), rsg = rbf(1.0f / (1.0f + __expf(-key_val(kk))));
      unsigned oo_ = (key_row(kk) * D + col) * 2, po = O_PART + ((lane >> 3) * 2048 + tok * 1024 + c0) * 4, p1 = po + 4 * 65536;
      asm volatile("" : "+v"(x1), "+v"(h2), "+v"(gb), "+v"(rsg), "+v"(oo_), "+v"(po), "+v"(p1));
      u32x4 y[7];
      nap_tick(td + F_T - 10);
      wait_tick(td + F_T);
      TS(37);
      P2GET(8192 + (min(lane, 55) * 16 + ((b - NPART) >> 1)) * 8);
      POLL2(max(max(max(max4(y[0]), max4(y[1])), max(max4(y[2]), max4(y[3]))), max(max(max4(y[4]), max4(y[5])), max4(y[6]))) == SENT || vd[1] == 0u, 0, PCAP,
            TS(38);
            asm volatile("global_load_dwordx4 %0, %7, %9 sc1\n\tglobal_load_dwordx4 %1, %7, %10 sc1\n\tglobal_load_dwordx4 %2, %7, %11 sc1\n\t"
                         "global_load_dwordx4 %3, %7, %12 sc1\n\tglobal_load_dwordx4 %4, %8, %9 sc1\n\tglobal_load_dwordx4 %5, %8, %10 sc1\n\t"
                         "global_load_dwordx4 %6, %8, %11 sc1"
                         : "=&v"(y[0]), "=&v"(y[1]), "=&v"(y[2]), "=&v"(y[3]), "=&v"(y[4]), "=&v"(y[5]), "=&v"(y[6])
                         : "v"(po), "v"(p1), "s"(wb), "s"(wb + 65536), "s"(wb + 131072), "s"(wb + 196608)
                         : "memory");
            asm volatile("s_waitcnt vmcnt(2)\n\tds_read_b64 %7, %8 offset:8\n\ts_waitcnt vmcnt(0) lgkmcnt(0)"
                         : "+v"(y[0]), "+v"(y[1]), "+v"(y[2]), "+v"(y[3]), "+v"(y[4]), "+v"(y[5]), "+v"(y[6]), "=&v"(vd) : "v"(sfa) : "memory"));
      TS(28);
#define YF(i) __builtin_bit_cast(f32x4, y[i])
      const f32x4 t = ((YF(0) + YF(1)) + (YF(2) + YF(3))) + ((YF(4) + YF(5)) + YF(6));
      const float sum = dpp_add<0x128>(swap16_add(swap32_add(t[0], t[2]), swap32_add(t[1], t[3])));
      asm volatile("global_store_short %0, %1, %2" ::"v"(oo_), "v"(pack2(x1 + gb * (wb == ws && kk < vd[1] ? h2 : rbf(h2 + rbf(rbf(sum) * rsg))), 0.f)), "s"(out) : "memory");
      TS(15);
    } else {
      break;
    }
    if (__builtin_expect(wb != ws || __all(vd[1] == t2), 1)) break;
    OWNL();
    const unsigned tk = (lane & 8) ? vd[1] : vd[0], ns = tk == t1 ? l1 >> 3 : 8 + ((lane >> 3) & 1);
    rk2 = tk & 0xffffu, kmk = lane < 16 ? ~0u : 0u, one = 3u;
    xo_ = O_X1C + ns * 2048 + kca, sto = O_ST2 + ns * 128 + (lane & 7) * 16;
    wb = ws + O_P2;
    }
    TSE();
  }
}

extern "C" void kernel_launch(void* const* d_in, const int* in_sizes, int n_in, void* d_out, int out_size, void* d_ws, size_t ws_size,
                              hipStream_t stream) {
  const u16* x = (const u16*)d_in[0];
  const u16* wsr = (const u16*)d_in[1];
  const u16* wsi = (const u16*)d_in[2];
  const u16* whr = (const u16*)d_in[3];
  const u16* whi = (const u16*)d_in[4];
  const u16* w_r = (const u16*)d_in[6];
  const u16* w_gate = (const u16*)d_in[7];
  const u16* w_up = (const u16*)d_in[8];
  const u16* w_down = (const u16*)d_in[9];
  const u16* c = (const u16*)d_in[10];
  const u16* w_mod = (const u16*)d_in[11];
  u16* out = (u16*)d_out;
  k_main<<<256, 512, 0, stream>>>(x, (char*)d_ws, wsr, wsi, c, w_mod, whr, whi, w_r, w_gate, w_up, w_down, out);
}
```

```cpp
#include <hip/hip_runtime.h>
#include <hip/hip_bf16.h>
#include <stdint.h>
#include <utility>

#define NEWLAY 1
typedef unsigned short u16;
typedef unsigned long long u64;
using bf16x8 = __attribute__((ext_vector_type(8))) short;
using bf16v8 = __attribute__((ext_vector_type(8))) __bf16;
using f32x4 = __attribute__((ext_vector_type(4))) float;
using f32x2 = __attribute__((ext_vector_type(2))) float;
using u32x4 = __attribute__((ext_vector_type(4))) unsigned;
using u32x2 = __attribute__((ext_vector_type(2))) unsigned;

constexpr int S = 2048, D = 1024, F = 3584;

__device__ __forceinline__ float bf2f(unsigned v) { return __uint_as_float(v << 16); }
__device__ __forceinline__ float bflo(unsigned v) { return __uint_as_float(v << 16); }
__device__ __forceinline__ float bfhi(unsigned v) { return __uint_as_float(v & 0xffff0000u); }
__device__ __forceinline__ unsigned f2bf(float f) {
  __hip_bfloat16 b = __float2bfloat16(f);
  return (unsigned)*reinterpret_cast<u16*>(&b);
}
__device__ __forceinline__ float rbf(float f) { return bf2f(f2bf(f)); }
using bf16v2 = __attribute__((ext_vector_type(2))) __bf16;
__device__ __forceinline__ unsigned pack2(float lo, float hi) {
  return __builtin_bit_cast(unsigned, __builtin_convertvector(f32x2{lo, hi}, bf16v2));
}

template <int CTRL>
__device__ __forceinline__ float dpp_f(float v) {
  return __int_as_float(__builtin_amdgcn_mov_dpp(__float_as_int(v), CTRL, 0xf, 0xf, true));
}
template <int CTRL>
__device__ __forceinline__ float dpp_add(float v) { return v + dpp_f<CTRL>(v); }
__device__ __forceinline__ float row_sum(float v) { return dpp_add<0x140>(dpp_add<0x141>(dpp_add<0x4E>(dpp_add<0xB1>(v)))); }
template <int CTRL>
__device__ __forceinline__ unsigned dpp_u(unsigned v) { return (unsigned)__builtin_amdgcn_mov_dpp((int)v, CTRL, 0xf, 0xf, true); }
__device__ __forceinline__ u64 mk64(unsigned hi, unsigned lo) { return ((u64)hi << 32) | lo; }
__device__ __forceinline__ float swap32_add(float a, float b) {
  auto r = __builtin_amdgcn_permlane32_swap(__float_as_uint(a), __float_as_uint(b), false, false);
  return __uint_as_float(r[0]) + __uint_as_float(r[1]);
}
__device__ __forceinline__ float swap16_add(float a, float b) {
  auto r = __builtin_amdgcn_permlane16_swap(__float_as_uint(a), __float_as_uint(b), false, false);
  return __uint_as_float(r[0]) + __uint_as_float(r[1]);
}
__device__ __forceinline__ float ar4(float v) {
  const float t = swap32_add(v, v);
  return swap16_add(t, t);
}
__device__ __forceinline__ float wave_sum_s(float v) {
  v = row_sum(v);
  v += __int_as_float(__builtin_amdgcn_update_dpp(0, __float_as_int(v), 0x142, 0xa, 0xf, false));
  v += __int_as_float(__builtin_amdgcn_update_dpp(0, __float_as_int(v), 0x143, 0xc, 0xf, false));
  return __int_as_float(__builtin_amdgcn_readlane(__float_as_int(v), 63));
}
__device__ __forceinline__ float silu_bf(float c) { return rbf(c * __builtin_amdgcn_rcpf(1.0f + __expf(-c))); }
__device__ __forceinline__ float dsum(unsigned v, float a) {
  return __builtin_amdgcn_fdot2_f32_bf16(__builtin_bit_cast(bf16v2, v), __builtin_bit_cast(bf16v2, 0x3f803f80u), a, false);
}
template <class Fn, int... I>
__device__ __forceinline__ void sfor_(Fn&& f, std::integer_sequence<int, I...>) { (f(std::integral_constant<int, I>{}), ...); }
template <int N, class Fn>
__device__ __forceinline__ void sfor(Fn&& f) { sfor_(f, std::make_integer_sequence<int, N>{}); }
constexpr unsigned SENT = 0xFFFFFFFFu;
constexpr int NPART = 224;

constexpr size_t WS_EPO = 0;
constexpr size_t WS_XFL = 4096;
constexpr size_t WS_MODP = 40960;
constexpr size_t WS_KEYG = 320u << 10;
constexpr size_t WS_UFL = WS_KEYG + 16384;
constexpr size_t WS_MOD = WS_KEYG + (80u << 10);
constexpr size_t WS_RS = WS_KEYG + (92u << 10);
constexpr size_t WS_PART = WS_KEYG + (460u << 10);
constexpr unsigned O_MODP = WS_MODP, O_XFL = WS_XFL, O_KEY = WS_KEYG, O_MOD = WS_MOD, O_RS = WS_RS, O_M2 = WS_RS + 80 * 1024;
constexpr unsigned O_PART = WS_PART, O_GU = O_PART + 56 * 8192, O_STAT = O_GU + 224 * 1024, O_X1C = O_STAT + 262144;
constexpr unsigned O_ST2 = O_X1C + 20480, O_VD = O_ST2 + 6144, O_P2 = 1536u << 10, O_F2 = 3584u << 10;
constexpr unsigned O_GC = O_M2 + 8704, O_VP = O_M2 + 12288, O_NP = O_VP + 65536, O_K2 = O_NP + 65536, O_QT = O_K2 + 4096, O_G2 = O_QT + 16384, O_G1 = 3328u << 10;
static_assert(O_G2 + 131072 == O_PART && O_VD + 4096 <= O_KEY + (512 + 960) * 1024 && O_PART + O_P2 >= O_KEY + (512 + 960) * 1024 && O_STAT + O_P2 <= O_G1, "armed area");
constexpr size_t WS_H = 4u << 20;
constexpr size_t WS_T = WS_H + (size_t)S * D * 2;
constexpr size_t WS_P = WS_T + (size_t)2 * D * S * 2;

#define TSG(base, i)
#define TSD(i)
#define PCAP (1 << 20)
#define PDBG(it)
#define TS(i)
#define TSE()
#define BT(k)
#define BTE()
#define BAR(k) do { BT(k); BARRIER(); } while (0)
#define WAIT_V(n) asm volatile("s_waitcnt vmcnt(%0)" ::"n"(n) : "memory")
#define WAIT_L(n) asm volatile("s_waitcnt lgkmcnt(%0)" ::"n"(n) : "memory")
#define BARRIER() asm volatile("s_barrier" ::: "memory")
#define LBAR() asm volatile("s_waitcnt lgkmcnt(0)\n\ts_barrier" ::: "memory")
#define DSR(dst, addr, off) asm volatile("ds_read_b128 %0, %1 offset:%2" : "=v"(dst) : "v"(addr), "n"(off) : "memory")
#define MFMA(c, a, b) asm volatile("v_mfma_f32_16x16x32_bf16 %0, %1, %2, %0" : "+v"(c) : "v"(a), "v"(b))
#define MFMA0(c, a, b) asm volatile("v_mfma_f32_16x16x32_bf16 %0, %1, %2, 0" : "=&v"(c) : "v"(a), "v"(b))
#define GLDS(m0v, voff, gbase) \
  asm volatile("s_mov_b32 m0, %0\n\ts_nop 0\n\tglobal_load_lds_dwordx4 %1, %2" ::"s"(m0v), "v"(voff), "s"(gbase) : "memory")
#define GLDS4(m0v, v, gbase)                                                                                                        \
  asm volatile("s_mov_b32 m0, %0\n\ts_nop 0\n\tglobal_load_lds_dwordx4 %1, %5\n\tglobal_load_lds_dwordx4 %2, %5 offset:1024\n\t"    \
               "global_load_lds_dwordx4 %3, %5 offset:2048\n\tglobal_load_lds_dwordx4 %4, %5 offset:3072" ::"s"(m0v),               \
               "v"((v)[0]), "v"((v)[1]), "v"((v)[2]), "v"((v)[3]), "s"(gbase) : "memory")
#define LDG(dst, voff, base) asm volatile("global_load_dwordx4 %0, %1, %2" : "=v"(dst) : "v"(voff), "s"(base) : "memory")
#define HPOL ""
#define XPOL ""
#define WPOL " nt"
#define LDM(dst, voff, base) asm volatile("global_load_dwordx4 %0, %1, %2" WPOL : "=v"(dst) : "v"(voff), "s"(base) : "memory")
#define LDX(dst, voff, base, off) asm volatile("global_load_dwordx4 %0, %1, %2 offset:%3" : "=v"(dst) : "v"(voff), "s"(base), "n"(off) : "memory")
#define LDXS(dst, voff, base, off) asm volatile("global_load_dwordx4 %0, %1, %2 offset:%3 sc1" : "=v"(dst) : "v"(voff), "s"(base), "n"(off) : "memory")
#define BF8(v) __builtin_bit_cast(bf16v8, v)
#define LSPIN(v, p, cond)                                   \
  _Pragma("unroll 1") for (int it_ = 0; it_ < (1 << 26); ++it_) { \
    v = __atomic_load_n(p, __ATOMIC_RELAXED);               \
    if (cond) break;                                        \
    __builtin_amdgcn_s_sleep(1);                            \
  }
#define POLL(ok, sl, reload)                                                  \
  if (__builtin_expect(!(ok), 0)) {                                           \
    _Pragma("unroll 1") for (int it = 0; it < PCAP; ++it) {                   \
      PDBG(it);                                                               \
      __builtin_amdgcn_s_sleep(sl);                                           \
      reload;                                                                 \
      if (ok) break;                                                          \
    }                                                                         \
  }
#define POLL2(bad, sl, cap, load)                                             \
  {                                                                           \
    int it = (cap);                                                           \
    u64 live;                                                                 \
    _Pragma("unroll 1") do {                                                  \
      if (sl) __builtin_amdgcn_s_sleep(sl);                                   \
      asm volatile("s_sub_u32 %0, %0, 1\n\ts_cselect_b64 %1, 0, -1" : "+s"(it), "=s"(live)::"scc"); \
      load;                                                                   \
    } while (__builtin_amdgcn_ballot_w64(bad) & live);                        \
    PDBG(PCAP - 2 - it);                                                      \
  }
__device__ __forceinline__ unsigned tick() {
  u64 t;
  asm volatile("s_memrealtime %0\n\ts_waitcnt lgkmcnt(0)" : "=s"(t)::"memory");
  return (unsigned)t;
}
__device__ __forceinline__ void wait_tick(unsigned t) {
#pragma unroll 1
  for (int i = 0; i < 256; ++i)
    if ((int)(tick() - t) >= 0) break;
}
__device__ __forceinline__ void nap_tick(unsigned t) {
#pragma unroll 1
  for (int i = 0; i < 1024; ++i) {
    if ((int)(tick() - t) >= 0) break;
    __builtin_amdgcn_s_sleep(2);
  }
}
#define T_C1 245
#define T_A 32
#define T_N 2500
#define T_D1 78
#define VW0 "\n\ts_waitcnt vmcnt(0)"
#define W8(a) "+v"(a[0]), "+v"(a[1]), "+v"(a[2]), "+v"(a[3]), "+v"(a[4]), "+v"(a[5]), "+v"(a[6]), "+v"(a[7])
#define PFL(ptr) asm volatile("s_mov_b32 m0, %0\n\ts_nop 0\n\tglobal_load_lds_dword %1, off" ::"s"(pfl), "v"(ptr) : "memory")

#define CPFL() asm volatile("s_mov_b32 m0, %0\n\ts_mov_b64 exec, 0xfff\n\tglobal_load_lds_dword %1, off\n\ts_mov_b64 exec, -1" ::"s"(pfl), "v"(cpf) : "memory")
#define HHOOK(j)
#define XPV 1557
#define XPW 1557
#define XPM (MODE ? XPW : XPV)
#define XPA0 (XPM / 1000)
#define XPA1 (XPM / 100 % 10)
#define XPB0 (XPM / 10 % 10)
#define XPB1 (XPM % 10)
#define U_X0 6
#define XLD(dst, XP, XH, i)                                                                                   \
  do {                                                                                                        \
    if (MODE) LDXS(dst, (i) ? xo1 : xo0, XP, ((XH)&3) * 2048 - 4096);                                         \
    else LDXS(dst, ((XH)&4) ? xo1 : xo0, XP, ((XH)&3) * 2048 + (i)*1024 - 4096);                              \
  } while (0)
#define HALF(XC, YA, RD, XN, XP, XH, LD, YP)                                                 \
  do {                                                                                       \
    _Pragma("unroll") for (int j = 0; j < 8; ++j) {                                          \
      if ((j & 1) == 0) { if (RD) WAIT_L(5); else WAIT_L(j == 0 ? 5 : 6 - j); }              \
      if (MODE == 2) MFMA0(acc[0][j], Y[j], XC[0]); else if (MODE) MFMA(acc[0][j], Y[j], XC[0]); else MFMA(acc[0][j], XC[0], Y[j]); \
      if (j == 0) DSR(Y[7], YP, 7 * 2048);                                                   \
      else if (RD) DSR(Y[j - 1], YA, (j - 1) * 2048);                                        \
      if (MODE == 2) MFMA0(acc[1][j], Y[j], XC[1]); else if (MODE) MFMA(acc[1][j], Y[j], XC[1]); else MFMA(acc[1][j], XC[1], Y[j]); \
      if ((LD) && j == (((XH)&1) ? XPA0 : XPB0)) XLD(XN[0], XP, XH, 0);                                             \
      if ((LD) && j == (((XH)&1) ? XPA1 : XPB1)) XLD(XN[1], XP, XH, 1);                                             \
      HHOOK(j);                                                                              \
    }                                                                                        \
  } while (0)

__device__ __forceinline__ int key_row(unsigned k) { return (int)((~k >> 1) & 2047u); }
__device__ __forceinline__ unsigned mkkey(float v) {
  const unsigned rb = pack2(0.f, v);
  return rb ^ ((unsigned)((int)rb >> 31) & 0x7fff0000u) ^ 0x80000000u;
}
__device__ __forceinline__ float key_val(unsigned k) {
  const unsigned ord = k >> 16;
  return bf2f(ord ^ ((ord & 0x8000u) ? 0x8000u : 0xffffu));
}
__device__ __forceinline__ unsigned nsent(float v) { return min(__float_as_uint(v), SENT - 1u); }
__device__ __forceinline__ f32x4 lo4(u32x4 q) { return f32x4{bflo(q[0]), bfhi(q[0]), bflo(q[1]), bfhi(q[1])}; }
__device__ __forceinline__ f32x4 hi4(u32x4 q) { return f32x4{bflo(q[2]), bfhi(q[2]), bflo(q[3]), bfhi(q[3])}; }
__device__ __forceinline__ u32x4 pk8(f32x4 a, f32x4 b) { return u32x4{pack2(a[0], a[1]), pack2(a[2], a[3]), pack2(b[0], b[1]), pack2(b[2], b[3])}; }
__device__ __forceinline__ u32x4 nsent4(u32x4 v) { return u32x4{min(v[0], SENT - 1u), min(v[1], SENT - 1u), min(v[2], SENT - 1u), min(v[3], SENT - 1u)}; }
__device__ __forceinline__ unsigned max4(u32x4 v) { return max(max(v[0], v[1]), max(v[2], v[3])); }
__device__ __forceinline__ float amin16(const u32x4* v) {
  float a, b;
  asm("v_min3_f32 %0, |%2|, |%3|, |%4|\n\tv_min3_f32 %1, |%5|, |%6|, |%7|\n\tv_min3_f32 %0, |%8|, |%9|, %0\n\tv_min3_f32 %1, |%10|, |%11|, %1\n\t"
      "v_min3_f32 %0, |%12|, |%13|, %0\n\tv_min3_f32 %1, |%14|, |%15|, %1\n\tv_min3_f32 %0, |%16|, |%17|, %0\n\tv_min_f32 %0, %0, %1"
      : "=&v"(a), "=&v"(b)
      : "v"(v[0][0]), "v"(v[0][1]), "v"(v[0][2]), "v"(v[0][3]), "v"(v[1][0]), "v"(v[1][1]), "v"(v[1][2]), "v"(v[1][3]), "v"(v[2][0]), "v"(v[2][1]),
        "v"(v[2][2]), "v"(v[2][3]), "v"(v[3][0]), "v"(v[3][1]), "v"(v[3][2]), "v"(v[3][3]));
  return a;
}
__device__ __forceinline__ u32x4 selu(unsigned mk, u32x4 a, u32x4 b) { return (a & mk) | (b & ~mk); }
#define KSTR 16
#define TOP2(CTRL)                                                  \
  do {                                                              \
    const unsigned o1 = dpp_u<CTRL>(t1), o2 = dpp_u<CTRL>(t2);      \
    t2 = max(min(t1, o1), max(t2, o2));                             \
    t1 = max(t1, o1);                                               \
  } while (0)
#define TOP2S(SW)                                                   \
  do {                                                              \
    auto a_ = SW(t1, t1, false, false), b_ = SW(t2, t2, false, false); \
    t2 = max(min(a_[0], a_[1]), max(b_[0], b_[1]));                 \
    t1 = max(a_[0], a_[1]);                                         \
  } while (0)
#define T_C3 253
#define T_C4 340
#define E_D 6
#define O_DW 6
#define O_DS 4
#define OPOL " sc1"
#define X_PA 2
#define G1_C 2
#define R_AT 24
#define B_TK 7
#define W_T 4
#define V_K 3
#define V_T 5
#define N_K 9
#define N_D 2
#define T_K 0
#define H_AT 23
#define G1_S 1
#define F_T 142
#define K_S 1
#define N_C (V_T + 1)
#define T_O 250
#define A_S1 9
#define A_S2 9
#define A_P2 12
#define X_K 13
#define H_P 2
#define H_O 2
#define S_R 2
#define T_X 80
#define A_PS 3
#define U_S 4
#define C_LD 2
#define C_DP 8
#define F_D 2
#define F_AT 29
#define PW0 2
#define T_W 70
#define P_D2 1
#define P_D4 1
#define XSTR 8
#define P_D3 20
#ifndef M_NS
#define M_NS 6
#endif
#define A_Q1 27
#define B_RAT 5
#define B_MAT 13
#define U_D 4
#define FSTR 16
#define B_J 4
#define RETIRE()                                                                                                              \
  do {                                                                                                                        \
    if (hw == 3 && lane < (b == 255 ? 2 : 1))                                                                                 \
      asm volatile("global_store_dwordx2 %0, %1, %2 sc1" ::"v"(lane ? (unsigned)WS_EPO : O_XFL + b * XSTR * 4), "v"(lane ? tag : ~tag), "s"(ws) : "memory"); \
  } while (0)
#define U_N1 3
#define B_QAT 12
#define B_QD 2
#define TP_PS 5
#define TP_XC 34
#define TP_P0 2
#define TP_X0 24
#define TP_XL 54
#define W_UO 3
#define CPF_K 24
#define CPF_B 8
#define B_WN 4
#define B_WK 15
struct BSched {
  int cnt[16], rcnt, mcnt, qcnt, vcnt, ncnt;
  constexpr BSched() : cnt{}, rcnt(0), mcnt(0), qcnt(0), vcnt(0), ncnt(0) {
    int pos[16] = {}, n = 0, rpos = 0, mpos = 0, qpos = 0, npos = 0;
    for (int k = 0; k <= B_J - 2 + M_NS; ++k) pos[k] = -8 * (B_J - 2 + M_NS - k);
    for (int k = B_J + 1; k < 16; ++k) {
      cnt[k] = n - pos[k] > 63 ? 63 : n - pos[k];
      if (k - 2 + M_NS < 16) { n += 4; pos[k - 2 + M_NS] = n; }
      if (k == B_RAT + 2) rcnt = n - rpos;
      if (k == B_QAT + B_QD) qcnt = n - qpos;
      if (k == V_T) vcnt = n;
      if (k == N_K + N_D) ncnt = n - npos;
      if (k == N_K) npos = n;
      if (k == B_RAT) rpos = n;
      if (k == X_K) n += 4;
      if (k == B_QAT) { n += 1; qpos = n; }
      if (k == B_MAT) { n += 2; mpos = n; }
      if (k == B_WK) n += B_WN;
    }
    mcnt = n - mpos > 63 ? 63 : n - mpos;
  }
};
__global__ __launch_bounds__(512, 2) void k_main(const u16* __restrict__ x, char* ws, const u16* __restrict__ Y0, const u16* __restrict__ Y1,
                                              const u16* __restrict__ cvec, const u16* __restrict__ wmod, const u16* __restrict__ X0,
                                              const u16* __restrict__ X1, const u16* __restrict__ wr_, const u16* __restrict__ wg,
                                              const u16* __restrict__ wu, const u16* __restrict__ wd, u16* __restrict__ out) {
  constexpr int DX = 4, NS = M_NS, STG = NS * 16384, SPARE = STG + 32768;
  constexpr int LST = SPARE + 17664, SLT = LST + 256, XC = SLT + 512, VT = XC + 4096;
  __shared__ __attribute__((aligned(1024))) char shm[VT + 256];
  u16(*svb)[1024] = reinterpret_cast<u16(*)[1024]>(shm + SPARE);
  float* cwj = reinterpret_cast<float*>(shm + SPARE + 10240);
  unsigned* ssel = reinterpret_cast<unsigned*>(shm + SPARE + 10368);
  float* smod = reinterpret_cast<float*>(shm + SPARE + 15872);
  u16* ssa = reinterpret_cast<u16*>(smod);
  constexpr int TAB = SPARE + 10752, RKA = TAB + 1536, RKB = TAB + 3584;
  u16(*scw)[128] = reinterpret_cast<u16(*)[128]>(shm + TAB + 512);
  const int b = blockIdx.x, xcd = b & 7, j8 = b >> 3;
  const int cc = xcd >> 2, nt = (xcd & 3) * 4 + (j8 & 3), mt = j8 >> 2, et = mt;
  const int tid = threadIdx.x, lane = tid & 63;
  const int wid = __builtin_amdgcn_readfirstlane(tid >> 6);
  const unsigned lds0 = (unsigned)(size_t)(__attribute__((address_space(3))) char*)shm;
  const char* cpf;
  {
    unsigned long long pc;
    unsigned len;
    asm volatile("s_getpc_b64 %0\n.Lcpf_here:\n\ts_mov_b32 %1, .Lcpf_end-.Lcpf_here-4\n\t.subsection 1\n.Lcpf_end:\n\t.subsection 0" : "=s"(pc), "=s"(len));
    cpf = reinterpret_cast<const char*>(pc) + min((unsigned)((lane * 32 + j8) * 128), len);
  }
  const unsigned pfl = lds0 + SPARE + 17408;
  if (wid == 4) CPFL();
  u32x4 xv[2];
  {
    const unsigned xo = (unsigned)((b * 8 + wid) * D + lane * 8) * 2;
    asm volatile("" ::"s"(ws), "s"(Y0), "s"(Y1), "s"(cvec), "s"(wmod), "s"(wr_));
    asm volatile("global_load_dwordx4 %0, %2, %3" XPOL "\n\tglobal_load_dwordx4 %1, %2, %3 offset:1024" XPOL : "=&v"(xv[0]), "=&v"(xv[1]) : "v"(xo), "s"(x) : "memory");
  }
  __builtin_amdgcn_sched_barrier(0);
  u16* xn = reinterpret_cast<u16*>(ws + WS_H);
  u16* U = reinterpret_cast<u16*>(ws + WS_T);
  unsigned* part = reinterpret_cast<unsigned*>(ws + WS_PART);
  const int g = (b & 7) * 28 + (b >> 3);
  unsigned* ufl = reinterpret_cast<unsigned*>(ws + WS_UFL) + (cc * 16 + nt) * 32 * FSTR;
  unsigned* epo = reinterpret_cast<unsigned*>(ws + WS_EPO);
#define SKEWPT()
  const int g4 = lane >> 4;
  u64 tag0, tag;
#define TAGW(n) asm volatile("s_waitcnt vmcnt(%2)\n\tv_lshl_add_u64 %0, %1, 0, 1" : "=v"(tag) : "v"(tag0), "n"(n) : "memory")
  u32x4 wv[8], cq;
  const char* pfy = reinterpret_cast<const char*>(cc ? Y1 : Y0) + (size_t)((xcd & 3) * 512 + j8 * 16 + (lane & 15)) * 4096 + g4 * 128;
#define PFG(g) (pfy + ((((g) + xcd) & 7) << 9))
  const int u = b * 2 + (wid & 1), cg0 = u & 31, ks0 = u >> 5;
  unsigned ta = lds0 + STG + ((lane >> 2) & 3) * 2368 + (wid * 128 + (lane >> 5) * 32 + (lane & 3) * 8 + (g4 & 1) * 4) * 2;
  unsigned oo = (unsigned)((wid * 4 + (lane >> 5)) * 131072 + (b >> 2) * 2048 + (g4 & 1) * 1024 + ((b & 3) * 16 + (lane & 15)) * 16), oo2 = oo + 262144;
  asm volatile("" : "+v"(ta), "+v"(oo), "+v"(oo2));
  unsigned wa = lds0 + STG + wid * 2368 + lane * 16;
  asm volatile("" : "+v"(wa));
  const unsigned wo = (unsigned)((ks0 * 64 + (lane >> 3) * 8) * 6144 + 2048 + (cg0 >> 4) * 1024 + cg0 * 64 + (lane & 7) * 8) * 2;
  if (wid == 0) TSG(part, 0);
  BT(50);
  if (wid >= 3) {
    asm volatile("global_load_dwordx2 %0, %1, off sc1" : "=v"(tag0) : "v"(epo) : "memory");
    if (wid >= 6) {
      LDG(cq, (ks0 * 64 + g4 * 16 + (lane & 1) * 8) * 2, cvec);
#pragma unroll
      for (int i = 0; i < 8; ++i) LDM(wv[i], wo + i * 12288, wmod);
      const unsigned av = u < 80 ? 0u : ~0u;
      asm volatile("global_store_dwordx4 %0, %1, %2 sc1\n\ts_nop 1" ::"v"(O_KEY + u * 1024 + lane * 16), "v"(u32x4{av, av, av, av}), "s"(ws) : "memory");
      WAIT_V(11);
    } else if (wid == 4) {
      __builtin_amdgcn_s_sleep(C_DP);
      PFL(PFG(0));
      WAIT_V(2);
    } else {
      WAIT_V(1);
    }
  } else {
    WAIT_V(0);
  }
  {
    asm volatile("" : "+v"(xv[0]), "+v"(xv[1])::"memory");
    if (wid == 1) TSG(part, 2);
    BT(51);
    const float s0 = dsum(xv[0][3], dsum(xv[0][2], dsum(xv[0][1], dsum(xv[0][0], 0.f))));
    const float s1 = dsum(xv[1][3], dsum(xv[1][2], dsum(xv[1][1], dsum(xv[1][0], 0.f))));
    const float mf = wave_sum_s(s0 + s1) * (1.0f / D), m = rbf(mf);
    const f32x4 d0 = lo4(xv[0]) - m, d1 = hi4(xv[0]) - m, d2 = lo4(xv[1]) - m, d3 = hi4(xv[1]) - m;
    const f32x4 q = (d0 * d0 + d1 * d1) + (d2 * d2 + d3 * d3);
    const float vf = wave_sum_s((q[0] + q[1]) + (q[2] + q[3])) * (1.0f / D) - (mf - m) * (mf - m);
    const float rden = __builtin_amdgcn_rcpf(rbf(__builtin_amdgcn_sqrtf(rbf(rbf(vf) + 1e-5f))));
    asm volatile("ds_write_b128 %0, %1" ::"v"(wa), "v"(pk8(d0 * rden, d1 * rden)) : "memory");
    asm volatile("ds_write_b128 %0, %1 offset:1024" ::"v"(wa), "v"(pk8(d2 * rden, d3 * rden)) : "memory");
    if (wid == 1) TSG(part, 12);
    BT(52);
    LBAR();
    if (wid == 1) TSG(part, 10);
    BT(53);
    u32x2 t[4];
    asm volatile(
        "ds_read_b64_tr_b16 %0, %4\n\tds_read_b64_tr_b16 %1, %4 offset:%5\n\t"
        "ds_read_b64_tr_b16 %2, %4 offset:128\n\tds_read_b64_tr_b16 %3, %4 offset:%6\n\ts_waitcnt lgkmcnt(0)"
        : "=&v"(t[0]), "=&v"(t[1]), "=&v"(t[2]), "=&v"(t[3])
        : "v"(ta), "n"(4 * 2368), "n"(4 * 2368 + 128)
        : "memory");
    asm volatile("global_store_dwordx4 %0, %2, %4 sc1\n\tglobal_store_dwordx4 %1, %3, %4 sc1\n\ts_nop 1" ::"v"(oo), "v"(oo2),
                 "v"(u32x4{t[0][0], t[0][1], t[1][0], t[1][1]}), "v"(u32x4{t[2][0], t[2][1], t[3][0], t[3][1]}), "s"(xn)
                 : "memory");
    if (wid == 2) TSG(part, 14);
    BT(54);
  }
  u32x2 vd;
#define VDLD(sl) POLL2(max(vd[0], vd[1]) == SENT, sl, PCAP, asm volatile("global_load_dwordx2 %0, %1, %2 sc1" VW0 : "=&v"(vd) : "v"(O_VD + (b & 31) * 128), "s"(ws) : "memory"))
  if (wid < 4) {
    const unsigned ya0 = lds0 + (lane & 15) * 128 + (g4 ^ (lane & 7)) * 16;
    bf16x8 X[DX][2][2], Y[8];
    f32x4 acc[2][8];
    {
      constexpr int MODE = 0;
      const unsigned xo0 = lane * 16 + 4096, xo1 = xo0 + 8192;
      const char* xb = reinterpret_cast<const char*>(xn) + (size_t)(mt * 4 + wid) * 131072;
#define XB(i) (xb + (((xcd + (i)) & 7) << 14))
#pragma unroll
      for (int i = 0; i < 2; ++i)
#pragma unroll
        for (int j = 0; j < 8; ++j) acc[i][j] = f32x4{0.f, 0.f, 0.f, 0.f};
      asm volatile("" : W8(acc[0]), W8(acc[1]));
      __builtin_amdgcn_s_setprio(1);
      WAIT_V(0);
      if (wid == 0) TSG(part, 1);
      const char* xpb = XB(0);
      BAR(55);
      if (wid == 3) {
        TAGW(0);
        const unsigned fp = O_XFL + ((xcd * 32 + lane) & 255) * XSTR * 4;
        if (P_D3) __builtin_amdgcn_s_sleep(P_D3);
#pragma unroll 1
        for (int it = 0; it < PCAP; ++it) { PDBG(it);
          u64 f;
          if (it < 2) BT(58 + 2 * it);
          asm volatile("global_load_dwordx2 %0, %1, %2 sc1\n\ts_waitcnt vmcnt(0)" : "=&v"(f) : "v"(fp), "s"(ws) : "memory");
          if (it < 2) BT(59 + 2 * it);
          if (__all(f == tag)) break;
          __builtin_amdgcn_s_sleep(P_D4);
        }
      }
      BAR(0);
      if (wid == 1) TSG(part, 3);
#pragma unroll
      for (int h = 0; h < 7; ++h) {
        XLD(X[h >> 1][h & 1][0], xpb, h, 0);
        XLD(X[h >> 1][h & 1][1], xpb, h, 1);
      }
#pragma unroll
      for (int j = 0; j < 7; ++j) DSR(Y[j], ya0, j * 2048);
      unsigned so = 0, a0 = ya0;
      f32x4 c0, c1;
#pragma unroll 1
      for (int it = 0; it < 7; ++it) {
        const char* xpa = xpb;
        xpb = XB(it + 1);
#pragma unroll
        for (int s = 0; s < 4; ++s) {
          const unsigned a1 = (ya0 ^ 64) + so;
          so = so == (NS - 1) * 16384 ? 0 : so + 16384;
          WAIT_V(12);
          HALF(X[s][0], a1, true, X[(s + 3) & 3][1], (s == 0 ? xpa : xpb), (2 * s + 7) & 7, true, a0);
          a0 = ya0 + so;
          BAR(1 + it * 4 + s);
          WAIT_V(12);
          HALF(X[s][1], a0, true, X[s][0], xpb, 2 * s, true, a1);
        }
      }
#pragma unroll
      for (int s = 0; s < 4; ++s) {
        const unsigned a1 = (ya0 ^ 64) + so;
        so = so == (NS - 1) * 16384 ? 0 : so + 16384;
        WAIT_V(s == 0 ? 12 : 14 - 4 * s);
        HALF(X[s][0], a1, true, X[3][1], xpb, 7, s == 0, a0);
        a0 = ya0 + so;
        if (s < 3) BAR(29 + s);
        WAIT_V(12 - 4 * s);
        if (s == 2) {
          DSR(c0, lds0 + SPARE + 15872 + 512 + wid * 128 + g4 * 32, 0);
          DSR(c1, lds0 + SPARE + 15872 + 512 + wid * 128 + g4 * 32, 16);
        }
        if (s < 3) HALF(X[s][1], a0, true, X[s][0], xpb, 0, false, a1);
      }
      const char* up = reinterpret_cast<const char*>(U) + (size_t)cc * 4194304 + (size_t)(nt * 4) * 65536 + (mt * 4 + wid) * 2048;
      const unsigned ul = lds0 + STG + wid * 1024 + lane * 16;
#define UEP(j)                                                                                                                             \
  do {                                                                                                                                     \
    u32x4 v_;                                                                                                                              \
    asm volatile("v_pk_mul_f32 %0, %0, %4\n\tv_pk_mul_f32 %1, %1, %5\n\tv_pk_mul_f32 %2, %2, %6\n\tv_pk_mul_f32 %3, %3, %7"            \
                 : "+v"(acc[0][j].lo), "+v"(acc[0][j].hi), "+v"(acc[1][j].lo), "+v"(acc[1][j].hi)                                          \
                 : "v"(c0.lo), "v"(c0.hi), "v"(c1.lo), "v"(c1.hi));                                                                        \
    asm volatile("v_cvt_pk_bf16_f32 %0, %4, %5\n\tv_cvt_pk_bf16_f32 %1, %6, %7\n\tv_cvt_pk_bf16_f32 %2, %8, %9\n\tv_cvt_pk_bf16_f32 %3, %10, %11" \
                 : "=&v"(v_[0]), "=&v"(v_[1]), "=&v"(v_[2]), "=&v"(v_[3])                                                                  \
                 : "v"(acc[0][j][0]), "v"(acc[0][j][1]), "v"(acc[0][j][2]), "v"(acc[0][j][3]), "v"(acc[1][j][0]), "v"(acc[1][j][1]),       \
                   "v"(acc[1][j][2]), "v"(acc[1][j][3]));                                                                                  \
    asm volatile("global_store_dwordx4 %0, %2, %3 offset:%4 sc1\n\tds_write_b128 %1, %2 offset:%5\n\ts_nop 0"                          \
                 ::"v"(lane * 16), "v"(ul), "v"(v_), "s"(up + ((j) >> 1) * 65536), "n"(((j)&1) * 1024), "n"((j)*4096) : "memory");         \
  } while (0)
      {
        const unsigned a1 = (ya0 ^ 64) + (31 % NS) * 16384;
        DSR(Y[7], a1, 7 * 2048);
        WAIT_L(6);
#pragma unroll
        for (int j = 0; j < 8; ++j) {
          if (j == 2) WAIT_L(0);
          MFMA(acc[0][j], X[3][1][0], Y[j]);
          MFMA(acc[1][j], X[3][1][1], Y[j]);
          if (j >= 2) UEP(j - 2);
        }
      }
      if (wid == 0) TSG(part, 4);
      TS(0);
      SKEWPT();
      asm volatile("s_nop 3");
#pragma unroll
      for (int j = 6; j < 8; ++j) UEP(j);
    }
    const int rg = nt * 8 + wid * 2 + cc, row0 = rg * 16 + (lane & 15), col0 = et * 128 + g4 * 8;
    u32x4 xq[4], pv[4], sa4[4];
    f32x4 ga[8];
    f32x2 cj;
    float sw, ce;
    u32x4 dk;
    unsigned spo, soo, th, slt, slf, sla, tha, xca, sca;
    u32x4 e0, e1;
    const char* xr = reinterpret_cast<const char*>(xn) + mt * 524288 + (cc * 16 + nt) * 16384 + wid * 4096;
#define XCHLD()                                                                                                     \
  asm volatile(                                                                                                     \
      "global_load_dwordx4 %0, %4, %5 sc1\n\tglobal_load_dwordx4 %1, %4, %5 offset:1024 sc1\n\t"                   \
      "global_load_dwordx4 %2, %4, %5 offset:2048 sc1\n\tglobal_load_dwordx4 %3, %4, %5 offset:3072 sc1"            \
      : "=&v"(pv[0]), "=&v"(pv[1]), "=&v"(pv[2]), "=&v"(pv[3])                                                      \
      : "v"(lane * 16), "s"(xr)                                                                                     \
      : "memory")
    {
      constexpr int MODE = 1;
      const char* ub = reinterpret_cast<const char*>(U) + (size_t)cc * 4194304 + (size_t)(nt * 4 + wid) * 65536;
      const unsigned xo0 = lane * 16 + cc * 1024 + 4096, xo1 = xo0 ^ 1024;
      BT(32);
      LBAR();
      if (wid == 1) TSG(part, 6);
      {
        const unsigned sa = lds0 + STG + wid * 8192 + lane * 16 + cc * 4096;
#pragma unroll
        for (int q = 0; q < 8; ++q) DSR(X[(q >> 1) & 1][q & 1][q >> 2], (q >> 2) ? sa ^ 4096 : sa, (q & 3) * 1024);
        const unsigned a0 = ya0 + (32 % NS) * 16384;
#pragma unroll
        for (int j = 0; j < 7; ++j) DSR(Y[j], a0, j * 2048);
      }
#define UFLAG()                                                                                                         \
  do {                                                                                                                  \
    WAIT_V(0);                                                                                                          \
    if (lane == 0) __hip_atomic_store(ufl + (mt * 4 + wid) * FSTR, 1u, __ATOMIC_RELAXED, __HIP_MEMORY_SCOPE_AGENT);     \
    if (wid == 0) TSG(part, 5);                                                                                         \
  } while (0)
#define XP(h) (ub + (((mt + ((h) >> 2)) & 7) << 13))
#define XS(h) X[((h) >> 1) & 3][(h)&1]
#define XW(h) do { if ((h) >= 4) WAIT_V(2 * (((h) + 6 > 31 ? 31 : (h) + 6) - (h)) ); } while (0)
      constexpr int H0 = 24, NP = 64, NE = 2 * NP + 8;
      const char* xw;
      unsigned c128, xoff[4], rka, rkb, ea, eb, cwa, cwu;
      auto ESET = [&]() __attribute__((always_inline)) {
        int ln_ = lane;
        asm volatile("" : "+v"(ln_));
        const int lane = ln_, g4 = lane >> 4, row0 = rg * 16 + (lane & 15), col0 = et * 128 + g4 * 8;
        xw = reinterpret_cast<const char*>(xn) + mt * 524288 + ((cc ^ 1) * 16 + nt) * 16384 + wid * 4096;
        asm volatile("" : "+s"(xw), "+s"(xr));
        c128 = 0x43000000u;
        rka = lds0 + RKA + (lane & 15) * 16, rkb = lds0 + RKB + (g4 ? 1024 : wid * 256 + lane * 16);
        ea = lds0 + TAB + g4 * 32, eb = lds0 + TAB + 512 + (lane & 3) * 256 + g4 * 16;
#pragma unroll
        for (int q = 0; q < 4; ++q) {
          dk[q] = (lane & 3) == q ? ~0u : 0u;
          xoff[q] = (lds0 + STG + wid * 8192 + (lane & 15) * 256 + ((g4 ^ lane) & 15) * 16) ^ (q * 64);
        }
        spo = O_STAT + rg * 2048 + g4 * 512 + (lane & 15) * 16, soo = O_STAT + rg * 2048 + et * 256 + (lane & 15) * 16;
        cwa = lds0 + SPARE + 10240 + g4 * 8, cwu = lds0 + SPARE + 10240 + et * 4;
        const unsigned one = g4 == ((lane >> 2) & 3) ? ((lane & 1) ? 0x1f800000u : 0x3f80u) : 0u;
        e0 = u32x4{(lane & 2) ? 0u : one, (lane & 2) ? one : 0u, 0u, 0u}, e1 = u32x4{0u, 0u, e0[0], e0[1]};
        tha = lds0 + SPARE + 10368 + 24;
        sla = lds0 + SLT + (row0 & 127) * 4;
        xca = O_X1C - 2048 + col0 * 2, sca = O_ST2 - 128 + et * 16;
        asm volatile("" : "+v"(dk), "+v"(spo), "+v"(soo), "+v"(cwa), "+v"(cwu), "+v"(tha), "+v"(sla), "+v"(xca), "+v"(sca), "+v"(e0), "+v"(e1), "+v"(c128));
        asm volatile("" : "+v"(xoff[0]), "+v"(xoff[1]), "+v"(xoff[2]), "+v"(xoff[3]), "+v"(rka), "+v"(rkb), "+v"(ea), "+v"(eb));
      };
#pragma unroll
      for (int s = 0; s < H0 / 2; ++s) {
        const unsigned a0 = ya0 + ((32 + s) % NS) * 16384, a1 = a0 ^ 64, an = ya0 + ((33 + s) % NS) * 16384;
        XW(2 * s);
        if (s == 0) {
          constexpr int MODE = 2;
          HALF(XS(0), a1, true, XS(7), XP(7), 7, false, a0);
          UFLAG();
        } else {
          HALF(XS(2 * s), a1, true, XS(2 * s + 7), XP(2 * s + 7), 2 * s + 7, 2 * s >= 4, a0);
        }
        if (s == 1) ESET();
        BAR(33 + s);
        if (s == 1) {
          if (wid == 1) TSG(part, 7);
          TS(4);
#pragma unroll
          for (int h = 4; h < U_X0; ++h) {
            XLD(XS(h)[0], XP(h), h, 0);
            XLD(XS(h)[1], XP(h), h, 1);
          }
        }
        XW(2 * s + 1);
#undef HHOOK
#define HHOOK(j) if (s == 1 && j + 3 >= U_X0) { XLD(XS(j + 3)[0], XP(j + 3), j + 3, 0); XLD(XS(j + 3)[1], XP(j + 3), j + 3, 1); }
        HALF(XS(2 * s + 1), an, true, XS(2 * s + 8), XP(2 * s + 8), 2 * s + 8, 2 * s + 1 >= 5, a1);
#undef HHOOK
#define HHOOK(j)
      }
      u32x4 ov;
      bf16x8 xk;
#define EH(e) (H0 + (((e) < NP ? (e) : (e) - NP - 8) >> 3))
#define EA(e) ((ya0 ^ ((EH(e) & 1) * 64)) + ((32 + (EH(e) >> 1)) % NS) * 16384)
      sfor<NE>([&](auto nn) __attribute__((always_inline)) {
        constexpr int n = decltype(nn)::value, h = EH(n), j = n & 7, m = n - NP - TP_P0, e = n + 7, z = n - (NE - 8);
        (void)sa4, (void)eb, (void)ga, (void)ea, (void)xk, (void)rka, (void)rkb, (void)xq, (void)xoff, (void)ov, (void)xw, (void)x;
        (void)cj, (void)sw, (void)ce, (void)cwa, (void)cwu, (void)th, (void)tha, (void)slt, (void)sla, (void)slf, (void)c128;
        if (n == 0) TS(2);
        if (n == NP) { TS(3); SKEWPT(); }
        if (n < NP && j == 0) {
          if ((h & 1) && h < 31) BAR(33 + (h >> 1));
          XW(h);
        }
        if (n == NP + TP_X0) { TS(14); BAR(48); TS(11); }
        if (n == NP + TP_XL) XCHLD();
        if constexpr ((j & 1) == 0) WAIT_L(z <= 0 ? 5 : 6 - z + 2 * z);
        if constexpr (n < NP) MFMA(acc[1][j], Y[j], XS(h)[1]);
        else if constexpr (n < NP + 8) MFMA(acc[0][j], Y[j], xk);
        else MFMA(acc[0][j], Y[j], XS(h)[0]);
        if constexpr (n == NP - 16) DSR(xk, rkb, 0);
        if constexpr (e >= NP && e < NP + 8) DSR(Y[e & 7], rka, (e & 7) * 256);
        else if constexpr (e < NE) DSR(Y[e & 7], EA(e), (e & 7) * 2048);
        if constexpr (n == 2 || n == 4) XLD(XS(31)[(j >> 1) - 1], XP(31), 31, (j >> 1) - 1);
        if constexpr (m >= TP_XC - 11 && m < TP_XC - 7) DSR(xq[m - TP_XC + 11], xoff[m - TP_XC + 11], 4096);
        if constexpr (m >= 0 && m < 4 * TP_PS) {
          constexpr int q = m / TP_PS, r = m % TP_PS;
#pragma unroll
          for (int u = (r * 13 + TP_PS - 1) / TP_PS; u < ((r + 1) * 13 + TP_PS - 1) / TP_PS; ++u) {
#define AO(i) acc[1][2 * q + ((u >> 1) & 1)][(u & 1) * 2 + (i)]
            if (u < 4) asm volatile("v_and_or_b32 %0, %1, %2, %3" : "=v"(ov[u & 3]) : "v"(AO(1)), "s"(0x80000000u), "v"(c128));
            else if (u < 8) asm volatile("v_fmamk_f32 %0, %1, 0x5f800000, %0" : "+v"(ov[u & 3]) : "v"(AO(1)));
            else if (u < 12) asm volatile("v_cvt_pk_bf16_f32 %0, %1, %0" : "+v"(ov[u & 3]) : "v"(AO(0)));
            else asm volatile("global_store_dwordx4 %0, %1, %2 offset:%3 sc1\n\ts_nop 1" ::"v"(lane * 16), "v"(ov), "s"(xw), "n"(q * 1024) : "memory");
          }
          if (m == 4 * TP_PS - 1) TS(10);
        }
        if constexpr (m == TP_XC) asm volatile("" : "+v"(xq[0]), "+v"(xq[1]), "+v"(xq[2]), "+v"(xq[3]));
        if constexpr (m >= TP_XC && m < TP_XC + 32) {
          constexpr int c = m - TP_XC;
          if constexpr (c & 1) asm volatile("v_and_b32 %0, 0xffff0000, %1" : "=v"(acc[1][c >> 2][c & 3]) : "v"(xq[c >> 3][(c >> 1) & 3]));
          else asm volatile("v_lshlrev_b32 %0, 16, %1" : "=v"(acc[1][c >> 2][c & 3]) : "v"(xq[c >> 3][(c >> 1) & 3]));
        }
        if constexpr (z >= 0 && z < 4) {
          DSR(ga[2 * z], ea, z * 128);
          DSR(ga[2 * z + 1], ea, z * 128 + 16);
        } else if constexpr (z >= 4 && z < 6) {
          DSR(sa4[2 * z - 8], eb, (2 * z - 8) * 64);
          DSR(sa4[2 * z - 7], eb, (2 * z - 7) * 64);
        } else if constexpr (z == 6) {
          asm volatile("ds_read_b64 %0, %2\n\tds_read_b32 %1, %3" : "=&v"(cj), "=&v"(ce) : "v"(cwa), "v"(cwu) : "memory");
        } else if constexpr (z == 7) {
          asm volatile("ds_read_b32 %0, %4 offset:32\n\tds_read_b32 %1, %5 offset:4\n\tds_read_b32 %3, %5 offset:56\n\tds_read_b32 %2, %6"
                       : "=&v"(sw), "=&v"(th), "=&v"(slt), "=&v"(slf) : "v"(cwa), "v"(tha), "v"(sla) : "memory");
        }
      });
    }
    if (wid == 0) TSG(part, 8);
    TS(13);
    BT(49);
    BTE();
    SKEWPT();
    {
      TS(26);
#define XCHW() asm volatile("s_waitcnt vmcnt(0)" : "+v"(pv[0]), "+v"(pv[1]), "+v"(pv[2]), "+v"(pv[3])::"memory")
      XCHW();
#define XBAD (amin16(pv) < 128.0f)
      if (__builtin_expect(__any(XBAD), 1)) POLL2(XBAD, 1, PCAP, XCHLD(); XCHW());
      TS(27);
      asm volatile("s_waitcnt lgkmcnt(0)" : W8(acc[0]), W8(ga), "+v"(sa4[0]), "+v"(sa4[1]), "+v"(sa4[2]), "+v"(sa4[3]), "+v"(cj), "+v"(sw), "+v"(ce), "+v"(th), "+v"(slt), "+v"(slf)::"memory");
#pragma unroll
      for (int q = 0; q < 8; ++q) acc[0][q] = __builtin_amdgcn_mfma_f32_16x16x32_bf16(BF8((q & 1) ? e1 : e0), BF8(pv[q >> 1]), acc[0][q], 0, 0, 0);
      f32x4 s4 = {0.f, 0.f, 0.f, 0.f}, gr = {0.f, 0.f, 0.f, 0.f};
      const unsigned xa1 = xca + slt * 2048, sa1 = sca + slt * 128;
#pragma unroll
      for (int q = 0; q < 4; ++q) {
        xq[q] = pk8(acc[1][2 * q] + ga[2 * q] * acc[0][2 * q], acc[1][2 * q + 1] + ga[2 * q + 1] * acc[0][2 * q + 1]);
        gr = __builtin_amdgcn_mfma_f32_16x16x32_bf16(BF8(xq[q]), BF8(xq[q]), gr, 0, 0, 0);
        s4 = __builtin_amdgcn_mfma_f32_16x16x32_bf16(BF8(sa4[q]), BF8(xq[q]), s4, 0, 0, 0);
        xq[q] = nsent4(xq[q]);
      }
#define XCST(a)                                                                                                              \
  asm volatile(                                                                                                              \
      "global_store_dwordx4 %0, %1, %5 sc1\n\tglobal_store_dwordx4 %0, %2, %5 offset:64 sc1\n\t"                            \
      "global_store_dwordx4 %0, %3, %5 offset:128 sc1\n\tglobal_store_dwordx4 %0, %4, %5 offset:192 sc1\n\ts_nop 1" ::"v"(a), \
      "v"(xq[0]), "v"(xq[1]), "v"(xq[2]), "v"(xq[3]), "s"(ws)                                                                \
      : "memory")
#define SCST(a)                                                                                                              \
  do {                                                                                                                       \
    _Pragma("unroll") for (int r = 0; r < S_R; ++r) asm volatile("global_store_dwordx4 %0, %1, %2 offset:%3 sc1" ::"v"(a), "v"(sg), "s"(ws), "n"(r * 1280) : "memory"); \
    asm volatile("s_nop 1");                                                                                                 \
  } while (0)
      if (slt) XCST(xa1);
      const bool dl = ((lane >> 2) & 3) == g4;
      const u32x4 gd = __builtin_bit_cast(u32x4, gr) & dk;
      const float m = s4[2] * (1.0f / 128);
      float q2 = __uint_as_float((gd[0] | gd[1]) | (gd[2] | gd[3])) - s4[2] * m, lg = (s4[0] + s4[1]) - m * ce;
      if (__builtin_expect(__any(dl && !(s4[2] * m <= 256.0f * q2)), 0)) {
        asm volatile("" : "+v"(xq[0]), "+v"(xq[1]), "+v"(xq[2]), "+v"(xq[3]));
        f32x4 a = {0.f, 0.f, 0.f, 0.f}, l = {0.f, 0.f, 0.f, 0.f};
#pragma unroll
        for (int q = 0; q < 4; ++q) {
          const u32x4 ch = *reinterpret_cast<const u32x4*>(&scw[0][g4 * 8 + q * 32]), cl = *reinterpret_cast<const u32x4*>(&scw[1][g4 * 8 + q * 32]);
          const f32x4 d0 = lo4(xq[q]) - m, d1 = hi4(xq[q]) - m;
          a += d0 * d0 + d1 * d1;
          l += d0 * (lo4(ch) + lo4(cl)) + d1 * (hi4(ch) + hi4(cl));
        }
        q2 = ar4((a[0] + a[1]) + (a[2] + a[3]));
        lg = ar4((l[0] + l[1]) + (l[2] + l[3]));
      }
      const u32x4 sg = {nsent(m), nsent(q2), nsent(lg), 0u};
      if (dl) {
        if (slt) SCST(sa1);
        asm volatile("global_store_dwordx4 %0, %1, %2 sc1\n\ts_nop 1" ::"v"(soo), "v"(sg), "s"(ws) : "memory");
      }
      if (__any(slf == 0u)) {
        POLL2(slf == 0u, 1, 1 << 26, asm volatile("ds_read_b32 %1, %2 offset:56\n\tds_read_b32 %0, %3\n\ts_waitcnt lgkmcnt(0)" : "=&v"(slt), "=&v"(slf) : "v"(tha), "v"(sla) : "memory"));
        if (slt) {
          XCST(xca + slt * 2048);
          if (dl) SCST(sca + slt * 128);
        }
      }
      asm volatile("ds_write_b32 %0, %1 offset:72" ::"v"(tha + wid * 4), "v"(1u) : "memory");
      TS(7);
      SKEWPT();
      const unsigned tp = tick() + T_D1, tq = (unsigned)__builtin_amdgcn_readfirstlane((int)th) + T_C1;
      u32x4 sr[2];
#define STLD(W) asm volatile("global_load_dwordx4 %0, %2, %3 sc1\n\tglobal_load_dwordx4 %1, %2, %3 offset:256 sc1" W : "=&v"(sr[0]), "=&v"(sr[1]) : "v"(spo), "s"(ws) : "memory")
#define SBAD (max(max(max(sr[0][0], sr[0][1]), sr[0][2]), max(max(sr[1][0], sr[1][1]), sr[1][2])) == SENT)
      asm volatile("" : "+v"(xq[0]), "+v"(xq[1]), "+v"(xq[2]), "+v"(xq[3])::"memory");
      f32x4 xf[8], xs[8], gc[8];
      auto PREW = [&](int q0, int q1) __attribute__((always_inline)) {
#pragma unroll
        for (int q = q0; q < q1; ++q) {
          const u32x4 sbq = *reinterpret_cast<const u32x4*>(&svb[1][col0 + q * 32]), gbq = *reinterpret_cast<const u32x4*>(&svb[3][col0 + q * 32]);
          const u32x4 cq = *reinterpret_cast<const u32x4*>(&svb[2][col0 + q * 32]);
          xf[2 * q] = lo4(xq[q]), xf[2 * q + 1] = hi4(xq[q]);
          xs[2 * q] = xf[2 * q] + lo4(gbq) * lo4(sbq), xs[2 * q + 1] = xf[2 * q + 1] + hi4(gbq) * hi4(sbq);
          gc[2 * q] = lo4(gbq) * lo4(cq), gc[2 * q + 1] = hi4(gbq) * hi4(cq);
          asm volatile("" : "+v"(xf[2 * q]), "+v"(xf[2 * q + 1]), "+v"(xs[2 * q]), "+v"(xs[2 * q + 1]), "+v"(gc[2 * q]), "+v"(gc[2 * q + 1]));
        }
      };
      PREW(0, PW0);
      wait_tick((int)(tp - tq) < 0 ? tp : tq);
      STLD("");
      unsigned rk = 0x8ffeu - (unsigned)row0 * 2;
      unsigned twa = lds0 + STG + wid * 8192 + (lane & 15) * 256 + ((g4 ^ lane) & 15) * 16, tra = lds0 + STG + wid * 8192 + g4 * 256 + ((g4 ^ lane) & 15) * 16;
      unsigned oo_ = (unsigned)((rg * 16 + g4) * D + et * 128 + (lane & 15) * 8) * 2;
      asm volatile("" : "+v"(rk), "+v"(twa), "+v"(tra), "+v"(oo_));
      PREW(PW0, 4);
      asm volatile("s_waitcnt vmcnt(0)" : "+v"(sr[0]), "+v"(sr[1])::"memory");
      if (__builtin_expect(__any(SBAD), 1)) POLL2(SBAD, 1, PCAP, STLD(VW0));
      TS(9);
      const f32x2 mk = {__uint_as_float(sr[0][0]), __uint_as_float(sr[1][0])};
      const f32x2 qk = {__uint_as_float(sr[0][1]), __uint_as_float(sr[1][1])};
      const f32x2 lk = {__uint_as_float(sr[0][2]), __uint_as_float(sr[1][2])};
      const float mean = ar4(mk[0] + mk[1]) * (1.0f / 8);
      const f32x2 dm = mk - mean;
      const f32x2 a = qk + 128.0f * (dm * dm), l = lk + dm * cj;
      const float al = swap32_add(a[0] + a[1], l[0] + l[1]), am = swap16_add(al, al);
      auto r = __builtin_amdgcn_permlane32_swap(__float_as_uint(am), __float_as_uint(am), false, false);
      const float rstd = __builtin_amdgcn_rsqf(__uint_as_float(r[0]) * (1.0f / D) + 1e-5f);
      const unsigned key = mkkey(rstd * __uint_as_float(r[1]) + sw) | rk;
      unsigned t1 = slt ? 0u : key, t2 = 0u;
      TOP2(0xB1); TOP2(0x4E); TOP2(0x141); TOP2(0x140);
      if (lane == 0 && et == (rg & 7)) asm volatile("global_store_dwordx2 %0, %1, %2 sc1" ::"v"(O_KEY + rg * KSTR * 8), "v"(u32x2{t1, t2}), "s"(ws) : "memory");
      u32x4 o[4];
#pragma unroll
      for (int q = 0; q < 4; ++q) o[q] = pk8(xs[2 * q] + (gc[2 * q] * (xf[2 * q] - mean)) * rstd, xs[2 * q + 1] + (gc[2 * q + 1] * (xf[2 * q + 1] - mean)) * rstd);
      {
        asm volatile("ds_write_b128 %0, %4\n\tds_write_b128 %1, %5\n\tds_write_b128 %2, %6\n\tds_write_b128 %3, %7" ::"v"(twa), "v"(twa ^ 64), "v"(twa ^ 128), "v"(twa ^ 192),
                     "v"(o[0]), "v"(o[1]), "v"(o[2]), "v"(o[3]) : "memory");
        asm volatile("ds_read_b128 %0, %4\n\tds_read_b128 %1, %5 offset:1024\n\tds_read_b128 %2, %6 offset:2048\n\tds_read_b128 %3, %7 offset:3072\n\ts_waitcnt lgkmcnt(0)"
                     : "=&v"(o[0]), "=&v"(o[1]), "=&v"(o[2]), "=&v"(o[3]) : "v"(tra), "v"(tra ^ 64), "v"(tra ^ 128), "v"(tra ^ 192) : "memory");
      }
      auto OST = [&](bool on) __attribute__((always_inline)) {
        const unsigned mk_ = (unsigned)__builtin_amdgcn_ballot_w64(on) >> g4;
#pragma unroll
        for (int q = 0; q < 4; ++q)
          if (mk_ & (1u << (4 * q))) asm volatile("global_store_dwordx4 %0, %1, %2" OPOL "\n\ts_nop 1" ::"v"(oo_), "v"(o[q]), "s"(out + q * 4096) : "memory");
      };
      bool df = slt;
      {
        const int tn = (int)(tick() - (tq + (T_C3 - T_C1)));
        if (tn > T_X) df = true;
        else if (tn > -T_W) { unsigned sv; LSPIN(sv, ssel + 16 + wid, sv != 0u); }
      }
      OST(!df);
      TS(1);
      u32x2 ks;
      POLL2(ks[1] == 0u, 1, 1 << 26, asm volatile("ds_read_b64 %0, %1\n\ts_waitcnt lgkmcnt(0)" : "=v"(ks) : "v"(lds0 + SPARE + 10368) : "memory"));
      df = df && (ks[0] & 0xffffu) != rk && (ks[1] & 0xffffu) != rk;
      SKEWPT();
      if (__any(df)) {
        if (b < NPART) {
          unsigned sv;
          LSPIN(sv, ssel + O_DW, sv != 0u);
          if (O_DS) __builtin_amdgcn_s_sleep(O_DS);
        }
        OST(df);
      }
      if (__builtin_expect(__any(t1 > ks[1]), 0)) {
        VDLD(8);
        slt = slt ? 0u : key == vd[0] ? 9u : key == vd[1] ? 10u : 0u;
        if (slt) {
          XCST(xca + slt * 2048);
          if (dl) SCST(sca + slt * 128);
        }
      }
    }
    TS(16);
    TSE();
  } else {
    const int hw = wid - 4;
    unsigned ts0 = tick();
    asm volatile("" : "+v"(ts0));
    const char* Hc = reinterpret_cast<const char*>((cc ? X1 : X0) + (size_t)et * 128 * D);
    auto UPOLL = [&](int n) {
      const unsigned fo = (unsigned)WS_UFL + ((cc * 16 + nt) * 32 + ((mt + 1 + (lane >> 2)) & 7) * 4 + (lane & 3)) * FSTR * 4;
      unsigned f;
      POLL2(f == 0u && lane < 4 * n, U_S, PCAP, asm volatile("global_load_dword %0, %1, %2 sc1" VW0 : "=&v"(f) : "v"(fo), "s"(ws) : "memory"));
      return (__builtin_amdgcn_ballot_w64(f == 0u) & 0xFFFFFFFull) == 0;
    };
    bool urdy = true;
    u32x4 gT[8], uT[8], wdv[8];
    unsigned og = (unsigned)(((g & 3) * 256 + hw * 64 + (lane >> 3)) * F + (g >> 2) * 64 + ((lane & 7) ^ ((lane >> 3) & 2) ^ 4) * 8) * 2;
    unsigned wta = lds0 + STG + (g4 * 8 + ((lane >> 2) & 3)) * 128 + (((hw * 2 + ((lane >> 1) & 1)) ^ ((lane >> 2) & 2) ^ ((g4 & 1) * 4)) << 4) + (lane & 1) * 8;
    unsigned od = (unsigned)(((g >> 2) * 64 + (lane >> 3) * 8) * D + (g & 3) * 256 + hw * 64 + (lane & 7) * 8) * 2;
    unsigned ou = og + (unsigned)(((mt + W_UO) % 7 - mt) * 128);
    asm volatile("" : "+v"(og), "+v"(ou), "+v"(od), "+v"(wta));
#define LDWI(dst, voff, base, inc) asm volatile("global_load_dwordx4 %0, %1, %2" WPOL "\n\tv_add_u32 %1, %3, %1" : "=v"(dst), "+v"(voff) : "s"(base), "n"(inc) : "memory")
#define WST(o, w)                                                                                                          \
  if (b < NPART) {                                                                                                         \
    asm volatile("s_mov_b32 m0, %0" ::"s"(lds0 + STG + hw * 8192));                                                        \
    _Pragma("unroll") for (int i = 0; i < 8; ++i) {                                                                        \
      asm volatile("v_xor_b32 %0, 64, %0\n\tglobal_load_lds_dwordx4 %0, %1 offset:%2" WPOL "\n\tv_add_u32 %0, %3, %0"       \
                   : "+v"(o) : "s"(w), "n"((i & 3) * 1024), "n"(F * 16 + (i == 3 ? 3072 : -1024)) : "memory");             \
      if (i == 3) asm volatile("s_add_u32 m0, m0, 4096" ::: "scc");                                                        \
    }                                                                                                                      \
  }
#define WTR2(i) "ds_read_b64_tr_b16 %" #i ", %16 offset:(" #i "/2)*4096+(" #i "&1)*512\n\t"
#define WTK(T)                                                                                                                         \
  if (b < NPART) {                                                                                                                     \
    u32x2 t_[16];                                                                                                                      \
    asm volatile(WTR2(0) WTR2(1) WTR2(2) WTR2(3) WTR2(4) WTR2(5) WTR2(6) WTR2(7) WTR2(8) WTR2(9) WTR2(10) WTR2(11) WTR2(12) WTR2(13)   \
                     WTR2(14) WTR2(15) "s_waitcnt lgkmcnt(0)"                                                                          \
                 : "=&v"(t_[0]), "=&v"(t_[1]), "=&v"(t_[2]), "=&v"(t_[3]), "=&v"(t_[4]), "=&v"(t_[5]), "=&v"(t_[6]), "=&v"(t_[7]),     \
                   "=&v"(t_[8]), "=&v"(t_[9]), "=&v"(t_[10]), "=&v"(t_[11]), "=&v"(t_[12]), "=&v"(t_[13]), "=&v"(t_[14]), "=&v"(t_[15]) \
                 : "v"(wta) : "memory");                                                                                               \
    _Pragma("unroll") for (int f = 0; f < 8; ++f) T[f] = u32x4{t_[2 * f][0], t_[2 * f][1], t_[2 * f + 1][0], t_[2 * f + 1][1]};        \
  }
#define DLD(q0, q1) if (b < NPART) { _Pragma("unroll") for (int q = q0; q < q1; ++q) LDWI(wdv[q], od, wd, D * 2); }
    const int pf0 = A_PS + (((j8 & 7) * A_S1) >> 3), pf1 = A_P2 + (((j8 & 7) * A_S2) >> 3), q1 = A_Q1;
    static_assert(A_PS >= 1 && A_P2 + A_S2 * 7 / 8 + 3 <= R_AT && R_AT < A_Q1 && A_P2 + A_S2 * 7 / 8 + 1 + W_T <= A_Q1 && H_AT + 3 <= A_Q1 && G1_C + G1_S < W_T && A_PS + W_T < A_P2 && A_PS + A_S1 * 7 / 8 + W_T + 1 < A_P2 + A_S2 * 7 / 8 && W_T >= M_NS - 2 && A_Q1 < F_AT && F_AT <= 29 && V_K <= B_J && V_T > B_J && B_WK == 15 && X_K >= N_C && (X_K + M_NS - 1 < 16 || (X_K <= B_MAT && TP_XC - 11 >= TP_X0 - TP_P0)) && TP_XC >= 4 * TP_PS && TP_XC + TP_P0 + 32 <= 72 && N_K > N_C && N_C > V_T && B_RAT > B_J && B_QAT > B_J && B_QAT + B_QD < 15, "barrier counts");
#define HOFF(r) (unsigned)((((r) >> 5) * 32 + (((r) >> 2) & 3) * 8 + (((r) >> 4) & 1) * 4 + ((r)&3)) * 2048 + ((lane & 7) ^ (lane >> 3)) * 16)
#define TR8(T, W)                                                                                                    \
  _Pragma("unroll") for (int c = 0; c < 8; ++c) {                                                                    \
    _Pragma("unroll") for (int p = 0; p < 4; ++p)                                                                    \
        T[c][p] = __builtin_amdgcn_perm(W[2 * p + 1][c >> 1], W[2 * p][c >> 1], (c & 1) ? 0x07060302u : 0x05040100u); \
    asm volatile("" : "+v"(T[c]));                                                                                   \
  }
    unsigned qm[4];
#pragma unroll
    for (int i = 0; i < 4; ++i) qm[i] = ((lane >> 2) & 3) == i ? ~0u : 0u;
    asm volatile("" : "+v"(qm[0]), "+v"(qm[1]), "+v"(qm[2]), "+v"(qm[3]));
    auto MVR = [&](f32x4 d) __attribute__((always_inline)) { return ((lane & 8) ? d[3] : d[0]) + dpp_f<0x128>((lane & 8) ? d[1] : d[2]); };
    auto MV64 = [&](const u32x4* T, u32x4 a) __attribute__((always_inline)) {
      f32x4 d[2] = {};
#pragma unroll
      for (int c = 0; c < 8; ++c) {
        const unsigned k_ = qm[c >> 1];
        d[c & 1] = __builtin_amdgcn_mfma_f32_16x16x32_bf16(BF8((u32x4{a[0] & k_, a[1] & k_, a[2] & k_, a[3] & k_})), BF8(T[c]), d[c & 1], 0, 0, 0);
      }
      return f32x2{MVR(d[0]), MVR(d[1])};
    };
#define LD1(i, a) "global_load_dwordx4 %" #i ", %" #a ", %10 sc1\n\tv_add_u32 %" #a ", 0x4000, %" #a "\n\t"
#define LD8(r, o0, o1, W)                                                                                                          \
  do {                                                                                                                             \
    unsigned a0_ = (o0), a1_ = (o1);                                                                                               \
    asm volatile(LD1(0, 8) LD1(4, 9) LD1(1, 8) LD1(5, 9) LD1(2, 8) LD1(6, 9) LD1(3, 8) LD1(7, 9) "s_nop 0" W                        \
                 : "=&v"(r[0]), "=&v"(r[1]), "=&v"(r[2]), "=&v"(r[3]), "=&v"(r[4]), "=&v"(r[5]), "=&v"(r[6]), "=&v"(r[7]), "+v"(a0_), "+v"(a1_) \
                 : "s"(ws) : "memory");                                                                                            \
  } while (0)
    u32x4 sel, gp[4];
    unsigned vc;
#define VCLD(W) asm volatile("global_load_dword %0, %1, %2 sc1" W : "=&v"(vc) : "v"(O_VP + ((cc ^ 1) * 8 + mt) * 4096 + (lane & 15) * 256), "s"(ws) : "memory")
#define GP4(o, W)                                                                                                         \
  asm volatile(                                                                                                           \
      "global_load_dwordx4 %0, %4, %5 sc1\n\tglobal_load_dwordx4 %1, %4, %5 offset:1024 sc1\n\t"                         \
      "global_load_dwordx4 %2, %4, %5 offset:2048 sc1\n\tglobal_load_dwordx4 %3, %4, %5 offset:3072 sc1" W                \
      : "=&v"(gp[0]), "=&v"(gp[1]), "=&v"(gp[2]), "=&v"(gp[3])                                                            \
      : "v"(o), "s"(ws)                                                                                                   \
      : "memory")
#define GPBAD (max(max(max4(gp[0]), max4(gp[1])), max(max4(gp[2]), max4(gp[3]))) == SENT)
      auto GPSUM = [&]() __attribute__((always_inline)) {
        f32x4 d = {0.f, 0.f, 0.f, 0.f};
#pragma unroll
        for (int i = 0; i < 4; ++i) d = __builtin_amdgcn_mfma_f32_16x16x32_bf16(BF8(sel), BF8(gp[i]), d, 0, 0, 0);
        return d;
      };
    const bool rdc = b >= NPART && hw == 3, rnp = b >= NPART && hw == 2 && cc == 0;
    const int gbq = (b >> 4) & 1, cj = (b & 15) * 64 + (lane & 15) * 4 + (lane >> 5) * 2;
    unsigned voff[4], ro, xo, ra1, wqa, qo, qa, ma, mo2, ma2;
    unsigned sfa, rk2, xo_, hwa, hra, hrb, ara, pso, gso, awa, sfw, gl0, gl1, sto, kca;
    auto JSET = [&]() {
#pragma unroll
      for (int i = 0; i < 4; ++i) {
        voff[i] = HOFF((hw * 4 + i) * 8 + (lane >> 3)) - i * 1024;
        sel[i] = ((lane & 15) >> 1) == i ? ((lane & 1) ? 0x3f800000u : 0x3f80u) : 0u;
      }
      ro = O_G2 + (g4 * 8192 + gbq * 1024 + (b & 15) * 64 + (lane & 15) * 4) * 4;
      xo = (unsigned)((nt * 128 + hw * 32 + cc * 16 + g4) * D + et * 128 + ((g4 ^ lane) & 15) * 8) * 2;
      ra1 = (lane & 16) == 0 ? O_MOD + 2048 + gbq * 4096 + cj * 2 : O_M2 + 8192 + 64 - gbq * 64 + (b & 15) * 4;
      wqa = lds0 + SPARE + 8192 + cj * 2;
      ma = max(hw, 1) * 2048 + (lane < 16 ? et * 256 + lane * 16 : lane < 48 ? (g & 3) * 512 + (lane - 16) * 16 : ((b >> 1) & 15) * 128 + (lane & 7) * 16);
      qo = hw < 2 ? O_QT + et * 2048 + hw * 1024 + lane * 16 : O_RS + ((hw & 1) * S + nt * 128 + (lane >> 4) * 32 + cc * 16 + (lane & 15)) * 4;
      qa = lds0 + (hw < 2 ? RKA + hw * 1024 + lane * 16 : RKB + lane * 16 + (hw & 1) * 8);
      mo2 = O_M2 + (hw == 2 ? 8448u + (lane & 15) * 16 : hw ? 8192u + (lane & 7) * 16 : lane < 32 ? et * 512 + lane * 16 : 4096u + (lane & 16) * 128 + et * 256 + (lane & 15) * 16);
      ma2 = lds0 + (hw ? SPARE + 10240 + (lane & 7) * 8 : TAB + lane * 16);
      asm volatile("" : "+v"(voff[0]), "+v"(voff[1]), "+v"(voff[2]), "+v"(voff[3]), "+v"(ro), "+v"(xo), "+v"(ra1), "+v"(wqa), "+v"(qo), "+v"(qa), "+v"(ma), "+v"(mo2), "+v"(ma2), "+v"(sel));
      *reinterpret_cast<unsigned*>((hw & 1) ? shm + RKB + 1024 + lane * 4 : reinterpret_cast<char*>(scw[2]) + lane * 4) = (hw & 1) ? 0u : 0x3f803f80u;
      if (hw == 3) {
        if (lane < 32) ssel[lane] = 0u;
        *reinterpret_cast<u32x2*>(shm + SLT + lane * 8) = u32x2{0u, 0u};
      }
    };
#define OWNL() int ln_ = lane; asm volatile("" : "+v"(ln_)); const int lane = ln_, g4 = lane >> 4; (void)g4
    auto TSET = [&]() {
      OWNL();
      kca = ((g & 3) * 256 + hw * 64 + (lane & 7) * 8) * 2;
      sfa = lds0 + SPARE + 10368, rk2 = lds0 + LST + (lane >> 3) * 4, sfw = sfa + hw * 4;
      xo_ = O_X1C + (lane >> 3) * 2048 + kca, sto = O_ST2 + (hw & (S_R - 1)) * 1280 + lane * 16;
      hwa = lds0 + XC + hw * 1024 + (lane & 7) * 128 + (((lane >> 3) ^ lane) & 7) * 16, hra = lds0 + XC + g4 * 128 + ((lane ^ g4) & 7) * 16, hrb = hra ^ 64;
      awa = lds0 + SPARE + 15872 + lane * 4, ara = lds0 + SPARE + 15872 + (lane & 2) * 64 + g4 * 32 + (lane & 1) * 16;
      pso = O_PART + ((g >> 2) * 2048 + (lane & 8) * 128 + (g & 3) * 256 + hw * 64 + (lane & 7) * 8 + g4 * 2) * 4;
      gso = O_GU + g * 1024 + (lane >> 5) * 256 + hw * 64 + (lane & 15) * 4;
      gl0 = O_GU + (g >> 2) * 4096 + lane * 8, gl1 = O_GU + (xcd * 7 + (mt + 7 - W_UO) % 7) * 4096 + 512 + lane * 8;
      asm volatile("" : "+v"(sfa), "+v"(rk2), "+v"(xo_), "+v"(hwa), "+v"(hra), "+v"(hrb), "+v"(ara), "+v"(pso), "+v"(gso), "+v"(awa), "+v"(sfw), "+v"(gl0), "+v"(gl1), "+v"(sto), "+v"(kca));
    };
    if (hw < 2) {
      constexpr int P = 8;
      __builtin_amdgcn_s_setprio(2);
      unsigned voy[P];
#pragma unroll
      for (int i = 0; i < P; ++i) voy[i] = (unsigned)(((hw * 8 + i) * 8 + (lane >> 3)) * 4096 + ((lane & 7) ^ (lane >> 3)) * 16 - (i & 3) * 1024);
      const unsigned lbase = lds0 + hw * 8192;
      const char* Yc = reinterpret_cast<const char*>((cc ? Y1 : Y0) + (size_t)nt * 128 * S);
      auto stage = [&](const char* yp, unsigned l) {
        GLDS4(l, voy, yp);
        GLDS4(l + 4096, voy + 4, yp);
      };
#define YSRC(n) (Yc + ((((n) + xcd * 4) & 31) << 7))
      __builtin_amdgcn_s_sleep(C_LD);
      PFL(hw ? PFG(1) : cpf);
      JSET();
      TAGW(1);
      BAR(55);
      BT(56);
      if (hw == 1 && lane == 0) asm volatile("global_store_dwordx2 %0, %1, %2 sc1" ::"v"(O_XFL + b * XSTR * 4), "v"(tag), "s"(ws) : "memory");
#pragma unroll
      for (int s = 0; s < NS - 1; ++s) stage(YSRC(s), lbase + s * 16384);
      BT(57);
      WAIT_V((NS - 2) * P);
      BAR(0);
      unsigned slot = lbase + (NS - 1) * 16384;
      const u64 evm = (u64)((1u | 1u << W_T) << pf0 | (1u | 1u << W_T) << pf1) | 1ull << CPF_K | 1ull << (33 - NS) | 3ull << 32;
      const char* yb = Yc;
      unsigned yo = (NS - 1 + xcd * 4) << 7, ym = 31 << 7;
#pragma unroll 1
      for (int k = 1; k <= 32 + B_J; ++k) {
        const char* ys = yb + (yo & ym);
        yo += 128;
        asm volatile("" : "+s"(ys));
        WAIT_V((NS - 3) * P);
        BAR(k);
        stage(ys, slot);
        slot = slot == lbase + (NS - 1) * 16384 ? lbase : slot + 16384;
        if (__builtin_expect((evm >> k) & 1, 0)) {
          if (k == pf0) {
            WST(og, wg);
            if (b >= NPART) GLDS(lds0 + SPARE + 8192 + hw * 1024, (unsigned)(hw * 1024 + lane * 16), wr_);
          }
          if (k == pf0 + W_T) { WTK(gT); }
          if (k == pf1) { WST(ou, wu); }
          if (k == pf1 + W_T) { WTK(uT); }
          if (k == CPF_K && hw == 1) CPFL();
          if (k == 33 - NS) {
#pragma unroll
            for (int i = 0; i < P; ++i) voy[i] = HOFF((hw * 8 + i) * 8 + (lane >> 3)) - (i & 3) * 1024;
            yb = Hc, yo = mt << 8, ym = 15 << 7;
          }
          if (k == 32) {
            DLD(0, 8 - B_WN);
            TSET();
          }
          if (k == 33 && hw == 0) {
            if (U_D) __builtin_amdgcn_s_sleep(U_D);
            urdy = UPOLL(U_N1);
          }
        }
      }
    } else {
      const int aw = hw - 2;
      unsigned ow = wo - (2048 + (cg0 >> 4) * 1024) * 2;
      const unsigned oq = (unsigned)((nt * 64 + aw * 32 + (lane >> 4)) * D + mt * 128) * 2;
      u32x4 hv[8], wt[8], sc;
      {
        asm volatile("s_waitcnt vmcnt(11)" : "+v"(cq)::"memory");
#pragma unroll
        for (int i = 0; i < 4; ++i) sc[i] = pack2(silu_bf(bflo(cq[i])), silu_bf(bfhi(cq[i])));
        asm volatile("" : "+v"(sc));
      }
#define GPUB(o, off)                                                                                   \
  if ((lane & 8) == 0)                                                                                 \
    asm volatile("global_store_dwordx4 %0, %1, %2 sc1 ; ALWAYS\n\ts_nop 1" ::"v"(off),                          \
                 "v"(u32x4{__float_as_uint(o[0]), (unsigned)tag, __float_as_uint(o[1]), (unsigned)tag}), "s"(ws) : "memory")
      const unsigned gpo = ks0 * 2048 + cg0 * 64 + (lane & 7) * 8 + (lane >> 4) * 2;
      {
        unsigned mpo = O_G1 + gpo * 8;
        asm volatile("s_waitcnt vmcnt(3)" : W8(wv), "+v"(mpo)::"memory");
        TR8(wt, wv);
        f32x2 o = MV64(wt, sc);
        asm volatile("" : "+v"(o));
        TAGW(0);
        BAR(55);
        if (P_D2) __builtin_amdgcn_s_sleep(P_D2);
        PFL(PFG(2 + aw));
        GPUB(o, mpo);
      }
      unsigned vta = lds0 + STG + (g4 * 8 + ((lane >> 2) & 3)) * 256 + (((aw * 8 + ((lane >> 1) & 1)) ^ ((g4 & 1) * 8 + ((lane >> 2) & 3))) << 4) + (lane & 1) * 8;
      unsigned vso = O_VP + ((cc * 8 + mt) * 16 + nt) * 256 + aw * 128 + (lane & 3) * 32 + g4 * 8;
      asm volatile("" : "+v"(vta), "+v"(vso), "+v"(ow));
      unsigned f0 = O_XFL + ((xcd * 32 + 64 + aw * 96 + lane) & 255) * XSTR * 4, f1 = O_XFL + ((xcd * 32 + 128 + aw * 96 + (lane & 31)) & 255) * XSTR * 4;
      unsigned lao = O_KEY + 512 * 1024 + b * 3840 + aw * 1920 + lane * 16;
      const char* pf4 = PFG(4 + aw);
      const char* pf6 = PFG(6 + aw);
      asm volatile("" : "+v"(f0), "+v"(f1), "+v"(lao), "+v"(pf4), "+v"(pf6));
      BT(57);
      BAR(0);
      f32x4 rs = {0.f, 0.f, 0.f, 0.f};
      u32x4 rsa = (lane & 15) == g4 * 4 ? u32x4{0x3f803f80u, 0x3f803f80u, 0x3f803f80u, 0x3f803f80u} : u32x4{0u, 0u, 0u, 0u}, R_;
      asm volatile("" : "+v"(rs), "+v"(rsa), "=v"(R_));
      const unsigned ra = lds0 + (mt * 16 + aw * 8) * 128 + lane * 16;
      unsigned so = 0;
#define TSX()
#define RSUM()                                                                                                    \
  do {                                                                                                            \
    TSX();                                                                                                        \
    asm volatile("ds_read_b128 %1, %2\n\ts_waitcnt lgkmcnt(0)\n\tv_mfma_f32_16x16x32_bf16 %0, %3, %1, %0" : "+v"(rs), "+v"(R_) : "v"(ra + so), "v"(rsa) : "memory"); \
    so = so == (NS - 1) * 16384 ? 0 : so + 16384;                                                                 \
  } while (0)
      {
        u64 f[2];
#define FLD() asm volatile("global_load_dwordx2 %0, %2, %4 sc1\n\tglobal_load_dwordx2 %1, %3, %4 sc1" : "=&v"(f[0]), "=&v"(f[1]) : "v"(f0), "v"(f1), "s"(ws) : "memory")
        __builtin_amdgcn_s_sleep(F_D);
        FLD();
        asm volatile("global_store_dwordx4 %0, %1, %2 sc1" ::"v"(lao), "v"(u32x4{~0u, ~0u, ~0u, ~0u}), "s"(ws) : "memory");
        if (lane < 56) asm volatile("global_store_dwordx4 %0, %1, %2 offset:1024 sc1 ; ALWAYS" ::"v"(lao), "v"(u32x4{~0u, ~0u, ~0u, ~0u}), "s"(ws) : "memory");
        asm volatile("s_nop 1");
#define GLD() _Pragma("unroll") for (int i = 0; i < 8; ++i) LDWI(wv[i], ow, wmod, 12288)
        RSUM();
        BAR(1);
        asm volatile("s_waitcnt vmcnt(%2)" : "+v"(f[0]), "+v"(f[1]) : "n"(2) : "memory");
        POLL(__all(f[0] == tag && f[1] == tag), 1, FLD(); asm volatile("s_waitcnt vmcnt(0)" : "+v"(f[0]), "+v"(f[1])::"memory"));
      }
      if (X_PA) __builtin_amdgcn_s_setprio(X_PA);
#define ARUN(b0, b1) _Pragma("unroll 1") for (int bi = (b0); bi < (b1); ++bi) { RSUM(); BAR(bi + 1); }
      GLD();
      ARUN(1, pf0);
      PFL(pf4);
#define HVLD()                                                                                                                    \
  do {                                                                                                                            \
    unsigned o_ = oq + (((lane & 15) ^ g4) << 4);                                                                                 \
    asm volatile("s_mov_b32 m0, %0" ::"s"(lds0 + STG + aw * 8192));                                                               \
    _Pragma("unroll") for (int i = 0; i < 8; ++i) {                                                                               \
      asm volatile("v_xor_b32 %0, %4, %0\n\tglobal_load_lds_dwordx4 %0, %1 offset:%2" HPOL "\n\tv_add_u32 %0, %3, %0"              \
                   : "+v"(o_) : "s"(cc ? X1 : X0), "n"((i & 3) * 1024), "n"(8192 + (i == 3 ? 3072 : -1024)), "n"(i ? ((i ^ (i - 1)) & 3) * 64 : 0) : "memory"); \
      if (i == 3) asm volatile("s_add_u32 m0, m0, 4096" ::: "scc");                                                               \
    }                                                                                                                             \
  } while (0)
      WST(og, wg);
      ARUN(pf0, pf0 + G1_C);
      {
        if (b >= NPART) WAIT_V(0);
        asm volatile("s_waitcnt vmcnt(8)" : W8(wv)::"memory");
        TR8(wt, wv);
        if (G1_S) { RSUM(); BAR(pf0 + G1_C + 1); }
        const f32x2 o = MV64(wt, sc);
        GPUB(o, O_MODP + gpo * 8);
        ow = wo + 2048;
      }
      ARUN(pf0 + G1_C + G1_S, pf0 + W_T - 1);
      WAIT_V(1);
      RSUM();
      BAR(pf0 + W_T);
      WTK(gT);
      ARUN(pf0 + W_T, pf1);
      PFL(pf6);
      WST(ou, wu);
      const int rb_ = b < NPART ? pf1 + W_T : R_AT, hb_ = max(rb_ + 1, H_AT + (j8 & 3));
      const unsigned c0 = (b - NPART) * 64 + aw * 32 + (lane & 15) * 2, c1 = (b - NPART) * 32 + aw * 16 + (lane & 7) * 2;
#define ERLD(W) LD8(wt, O_MODP + (g4 * 8192 + c0) * 8, O_G1 + (g4 * 8192 + (lane & 8) * 128 + c1) * 8, W)
      ARUN(pf1, rb_ - 3);
      if (b >= NPART) ERLD("");
      ARUN(rb_ - 3, rb_ - 1);
      WAIT_V(0);
      RSUM();
      BAR(rb_);
      WTK(uT);
      if (b >= NPART) {
        const unsigned wq = *reinterpret_cast<const unsigned*>(&svb[4][c1]);
#define TG2(r, i) ((r[i][1] ^ (unsigned)tag) | (r[i][3] ^ (unsigned)tag) | (r[i + 1][1] ^ (unsigned)tag) | (r[i + 1][3] ^ (unsigned)tag))
        asm volatile("" : W8(wt));
        POLL(__all((TG2(wt, 0) | TG2(wt, 2) | TG2(wt, 4) | TG2(wt, 6)) == 0u), 2, ERLD(VW0));
        auto FIN = [&](int q, int i, bool inc) __attribute__((always_inline)) {
          return rbf((inc ? 1.0f : 0.0f) + rbf(ar4((__uint_as_float(wt[q][i]) + __uint_as_float(wt[q + 1][i])) + (__uint_as_float(wt[q + 2][i]) + __uint_as_float(wt[q + 3][i])))));
        };
        const float s0 = FIN(0, 0, gbq), s1 = FIN(0, 2, gbq);
        if (lane < (gbq ? 32 : 16))
          asm volatile("global_store_dword %0, %1, %2 sc1" ::"v"(gbq ? O_MOD + 4096 + c0 * 4 + g4 * 4 : O_MOD + c0 * 2),
                       "v"(min(gbq ? __float_as_uint(g4 ? s1 : s0) : pack2(s0, s1), SENT - 1u)), "s"(ws) : "memory");
        const bool hi = lane & 8;
        const float t0 = FIN(4, 0, hi), t1 = FIN(4, 2, hi), o0 = dpp_f<0x128>(t0), o1 = dpp_f<0x128>(t1);
        const float a0 = hi ? o0 : t0, a1 = hi ? o1 : t1, e0 = hi ? t0 : o0, e1 = hi ? t1 : o1, p0 = e0 * bflo(wq), p1 = e1 * bfhi(wq);
        const unsigned h = pack2(p0, p1);
        const unsigned v = g4 == 0 ? __float_as_uint(hi ? a1 : a0) : g4 == 1 ? (hi ? h : pack2(e0, e1)) : hi ? pack2(a0 * p0, a1 * p1) : pack2(p0 - bflo(h), p1 - bfhi(h));
        const unsigned po = g4 == 0 ? O_M2 + c1 * 4 + (hi ? 4 : 0) : (g4 == 1 ? (hi ? O_M2 + 4096 : O_MOD + 4096) : hi ? O_GC : O_M2 + 6144) + c1 * 2;
        if (lane < 48) asm volatile("global_store_dword %0, %1, %2 sc1" ::"v"(po), "v"(min(v, SENT - 1u)), "s"(ws) : "memory");
      }
      ARUN(rb_, hb_);
      HVLD();
      ARUN(hb_, q1);
      u32x4 g0, g1;
      u32x2 fm;
#define FMLD(W)                                                                                                                                     \
  asm volatile("global_load_dwordx2 %0, %3, %5 sc1\n\tglobal_load_dwordx4 %1, %4, %5 sc1\n\tglobal_load_dwordx4 %2, %4, %5 offset:64 sc1" W        \
               : "=&v"(fm), "=&v"(g0), "=&v"(g1) : "v"(O_MOD + (aw ? 8192 + mt * 512 + lane * 8 : mt * 256 + (lane & 31) * 8)), "v"(O_GC + nt * 128 + g4 * 16), "s"(ws) : "memory")
      FMLD("");
      RSUM();
      BAR(q1 + 1);
      unsigned ha[4], sma = lds0 + SPARE + 15872 + (aw ? 512 + lane * 8 : (lane & 31) * 8);
#pragma unroll
      for (int i = 0; i < 4; ++i) ha[i] = lds0 + STG + aw * 8192 + (lane & 15) * 256 + (((i * 4 + (lane >> 4)) ^ (lane & 15)) << 4);
      asm volatile("" : "+v"(ha[0]), "+v"(ha[1]), "+v"(ha[2]), "+v"(ha[3]), "+v"(sma), "+v"(ow));
      ARUN(q1 + 1, F_AT);
      asm volatile("s_waitcnt vmcnt(0)" : "+v"(fm), "+v"(g0), "+v"(g1)::"memory");
#pragma unroll
      for (int i = 0; i < 8; ++i) DSR(hv[i], ha[i & 3], (i >> 2) * 4096);
      POLL(__all(max(max(fm[0], fm[1]), max(max4(g0), max4(g1))) != SENT), 1, FMLD(VW0));
      {
        const unsigned sg = cc && aw ? 0x80000000u : 0u;
        asm volatile("ds_write_b64 %0, %1" ::"v"(sma), "v"(fm ^ sg) : "memory");
      }
      ARUN(F_AT, 29);
      GLD();
      RSUM();
      BAR(30);
        u32x2 tv[16];
#pragma unroll
        for (int q = 0; q < 16; ++q)
          asm volatile("ds_read_b64_tr_b16 %0, %1 offset:%2" : "=v"(tv[q]) : "v"(vta ^ ((((q >> 2) * 2) ^ ((q & 1) * 4)) << 4)), "n"((q & 1) * 1024 + ((q >> 1) & 1) * 8192) : "memory");
      RSUM();
      BAR(31);
      RSUM();
      {
        asm volatile("s_nop 7\n\ts_nop 7" : "+v"(rs));
        const float v0 = dpp_add<0x141>(dpp_add<0x4E>(dpp_add<0xB1>(rs[0]))), v = cc ? -v0 : v0;
        if ((lane & 7) == 0)
          asm volatile("global_store_dword %0, %1, %2 sc1 ; ALWAYS" ::"v"(O_RS + (cc * S + nt * 128 + mt * 16 + aw * 8 + (lane >> 3)) * 4), "v"(min(pack2(v, v - rbf(v)), SENT - 1u)), "s"(ws) : "memory");
      }
      BAR(32);
      __builtin_amdgcn_s_sleep(2);
      DLD(0, 8 - B_WN);
      __builtin_amdgcn_s_setprio(2);
      {
        asm volatile("" : W8(tv), W8((tv + 8))::"memory");
        f32x4 d[4];
#pragma unroll
        for (int j = 0; j < 4; ++j) {
          d[j] = __builtin_amdgcn_mfma_f32_16x16x32_bf16(BF8((u32x4{tv[4 * j][0], tv[4 * j][1], tv[4 * j + 1][0], tv[4 * j + 1][1]})), BF8(g0), f32x4{0.f, 0.f, 0.f, 0.f}, 0, 0, 0);
          d[j] = __builtin_amdgcn_mfma_f32_16x16x32_bf16(BF8((u32x4{tv[4 * j + 2][0], tv[4 * j + 2][1], tv[4 * j + 3][0], tv[4 * j + 3][1]})), BF8(g1), d[j], 0, 0, 0);
        }
        u32x2 vo;
#pragma unroll
        for (int i = 0; i < 2; ++i) {
          const unsigned e0 = pack2(d[0][2 * i], d[0][2 * i + 1]), e1 = pack2(d[1][2 * i], d[1][2 * i + 1]), e2 = pack2(d[2][2 * i], d[2][2 * i + 1]), e3 = pack2(d[3][2 * i], d[3][2 * i + 1]);
          vo[i] = min((lane & 2) ? ((lane & 1) ? e3 : e2) : ((lane & 1) ? e1 : e0), SENT - 1u);
        }
        if ((lane & 12) == 0) asm volatile("global_store_dwordx2 %0, %1, %2 sc1 ; ALWAYS" ::"v"(vso), "v"(vo), "s"(ws) : "memory");
      }
      asm volatile("" : W8(hv)::"memory");
      {
        f32x4 qa[2] = {};
#pragma unroll
        for (int kk = 0; kk < 4; ++kk) {
          const bf16x8 sb8 = *reinterpret_cast<const bf16x8*>(ssa + kk * 32 + (lane >> 4) * 8);
#pragma unroll
          for (int r = 0; r < 2; ++r)
            qa[r] = __builtin_amdgcn_mfma_f32_16x16x32_bf16(__builtin_bit_cast(bf16v8, hv[r * 4 + kk]), __builtin_bit_cast(bf16v8, sb8), qa[r], 0, 0, 0);
        }
        if ((lane & 15) < 2)
          asm volatile("global_store_dwordx4 %0, %1, %2 sc1 ; ALWAYS\n\ts_nop 1" ::"v"(O_RS + (4096 + (cc * 8 + mt) * D + nt * 64 + aw * 32 + (lane & 1) * 16 + (lane >> 4) * 4) * 4),
                       "v"(nsent4(selu((lane & 1) ? ~0u : 0u, __builtin_bit_cast(u32x4, qa[1]), __builtin_bit_cast(u32x4, qa[0])))), "s"(ws) : "memory");
      }
      BAR(33);
      {
        if (b >= NPART) WAIT_V(3);
        asm volatile("s_waitcnt vmcnt(%8)" : W8(wv) : "n"(8 - B_WN + 3) : "memory");
        TR8(wt, wv);
        const f32x2 o = MV64(wt, sc);
        if ((lane & 8) == 0) asm volatile("global_store_dwordx2 %0, %1, %2 sc1" ::"v"(O_G2 + gpo * 4), "v"(u32x2{nsent(o[0]), nsent(o[1])}), "s"(ws) : "memory");
      }
      JSET();
      TSET();
      BAR(34);
#pragma unroll
      for (int k = 3; k <= B_J; ++k) {
        BAR(32 + k);
        if (k == V_K) {
          if (!aw) GP4(O_VP + (cc * 8 + mt) * 4096 + lane * 16, "");
          else VCLD("");
        }
      }
    }
    u32x4 dT[8];
    u32x4 m[3];
    u32x2 kk = {SENT, SENT};
#define K2LD(W) asm volatile("global_load_dwordx2 %0, %1, %2 sc1" W : "+v"(kk) : "v"(O_K2 + (lane & 15) * 16), "s"(ws) : "memory")
    {
      constexpr BSched sch;

      const unsigned lbase = lds0 + hw * 4096;
      unsigned cbv, t1, t2;
#define RRLD(W)                                                                                                                   \
  asm volatile("global_load_dword %4, %9, %10 sc1\n\tglobal_load_dwordx4 %0, %5, %10 sc1\n\tglobal_load_dwordx4 %1, %6, %10 sc1\n\t" \
               "global_load_dwordx4 %2, %7, %10 sc1\n\tglobal_load_dwordx4 %3, %8, %10 sc1" W                                      \
               : "=&v"(gp[0]), "=&v"(gp[1]), "=&v"(gp[2]), "=&v"(gp[3]), "=&v"(cbv)                                               \
               : "v"(ro), "v"(ro + 8192), "v"(ro + 16384), "v"(ro + 24576), "v"(O_MOD + 4096 + cj * 2), "s"(ws) : "memory")
      unsigned wq;
      u32x4 mq = {0u, 0u, 0u, 0u};
#define QLD(W)                                                                                                        \
  do {                                                                                                                \
    if (hw < 2) asm volatile("global_load_dwordx4 %0, %1, %2 sc1" W : "+v"(mq) : "v"(qo), "s"(ws) : "memory");        \
    else asm volatile("global_load_dword %0, %1, %2 sc1" W : "+v"(mq[0]) : "v"(qo), "s"(ws) : "memory");              \
  } while (0)
#define QRLD(W)                                                                                                       \
  asm volatile("global_load_dwordx4 %0, %2, %4 sc1\n\tglobal_load_dwordx4 %1, %3, %4 sc1" W : "=&v"(gp[0]), "=&v"(gp[1]) \
               : "v"(qr), "v"(qr + 32768), "s"(ws) : "memory")
      const bool rq_ = b >= NPART && hw == 1;
      const unsigned qr = O_RS + (4096 + (lane >> 3) * D + (b - NPART) * 32 + (lane & 7) * 4) * 4;
#define MODLD() asm volatile("global_load_dwordx4 %0, %2, %4 sc1\n\tglobal_load_dwordx4 %1, %3, %4 sc1" : "=&v"(m[0]), "=&v"(m[2]) : "v"(O_MOD + ma), "v"(mo2), "s"(ws) : "memory")
#define MODW(n) asm volatile("s_waitcnt vmcnt(%2)" : "+v"(m[0]), "+v"(m[2]) : "n"(n) : "memory")
#pragma unroll
      for (int k = B_J; k < 16; ++k) {
        if (k > B_J) {
          if (k == 2 * U_N1 - 1 && !urdy) UPOLL(7);
          WAIT_V(sch.cnt[k]);
          if (k == 15) WAIT_L(0);
          BAR(32 + k);
        }
        if (k > B_J && k - 2 + NS < 16) {
          const char* yp = Hc + ((mt * 2 + k - 2 + NS) & 15) * 128;
          const unsigned l = lbase + ((30 + k + NS) % NS) * 16384;
          GLDS4(l, voff, yp);
        }
        if (k == B_TK && hw == 1 && b >= 192) {
          const unsigned t = tick(), h = t + T_O + (((int)(t - ts0) - T_N) * T_A >> 8);
          if (lane == 0) asm volatile("global_store_dword %0, %1, %2 sc1" ::"v"(O_M2 + 8448 + (b - 192) * 4), "v"(min(h, SENT - 1u)), "s"(ws) : "memory");
        }
        if (k == B_RAT + 2 && rdc) {
          WAIT_V(sch.rcnt);
          asm volatile("" : "+v"(gp[0]), "+v"(gp[1]), "+v"(gp[2]), "+v"(gp[3]), "+v"(cbv));
          POLL(!__any(GPBAD || cbv == SENT), 2, RRLD(VW0));
#define GF(i) __builtin_bit_cast(f32x4, gp[i])
          const f32x4 t = (GF(0) + GF(1)) + (GF(2) + GF(3));
          const float ta = swap32_add(t[0], t[2]), tb = swap32_add(t[1], t[3]);
          const unsigned o = pack2(swap16_add(ta, ta), swap16_add(tb, tb)), f = gbq ? cbv : o;
          const float pr = row_sum(bflo(f) * bflo(wq) + bfhi(f) * bfhi(wq)), ps = swap32_add(pr, pr);
          if ((lane & 16) == 0 || lane == 16)
            asm volatile("global_store_dword %0, %1, %2 sc1" ::"v"(ra1), "v"(min((lane & 16) ? __float_as_uint(ps) : o, SENT - 1u)), "s"(ws) : "memory");
          TS(12);
        }
        if (k == V_T && hw == 3) {
          WAIT_V(sch.vcnt);
          asm volatile("" : "+v"(vc));
          POLL(__all(vc != SENT), 2, VCLD(VW0));
        }
        if (k == V_T && hw == 2) {
          WAIT_V(sch.vcnt);
          asm volatile("" : "+v"(gp[0]), "+v"(gp[1]), "+v"(gp[2]), "+v"(gp[3]));
          POLL(!__any(GPBAD), 2, GP4(O_VP + (cc * 8 + mt) * 4096 + lane * 16, VW0));
          const f32x4 d = GPSUM();
          if (lane < 32) *reinterpret_cast<u32x2*>(shm + VT + (lane & 15) * 16 + g4 * 8) = u32x2{pack2(d[0], d[1]), pack2(d[2], d[3])};
          WAIT_L(0);
        }
        if (k == N_C) {
          f32x4 n[2] = {};
#pragma unroll
          for (int q = 0; q < 8; ++q)
            n[q >> 2] = __builtin_amdgcn_mfma_f32_16x16x32_bf16(BF8(*reinterpret_cast<const u32x4*>(shm + VT + (q & 3) * 64 + g4 * 16)),
                                                                BF8(*reinterpret_cast<const u32x4*>(shm + STG + hw * 8192 + q * 1024 + lane * 16)), n[q >> 2], 0, 0, 0);
          if (lane < 16)
            asm volatile("global_store_dword %0, %1, %2 sc1" ::"v"(O_NP + nt * 4096 + (cc * 8 + mt) * 256 + hw * 64 + lane * 4), "v"(min(pack2(n[0][0], n[1][0]), SENT - 1u)), "s"(ws) : "memory");
          TS(33);
        }
        if (k == N_K + N_D && rnp) {
            WAIT_V(sch.ncnt);
            asm volatile("" : "+v"(gp[0]), "+v"(gp[1]), "+v"(gp[2]), "+v"(gp[3]));
            POLL(!__any(GPBAD), 2, GP4(O_NP + nt * 4096 + lane * 16, VW0));
            const unsigned r0 = nt * 128 + ((lane >> 2) & 3) * 32 + (lane & 3) * 4 + (g4 & 1) * 2;
        const f32x4 d = GPSUM();
        unsigned ky[4];
#pragma unroll
        for (int i = 0; i < 4; ++i) {
          const unsigned f = __float_as_uint(d[i]);
          ky[i] = (((f ^ ((unsigned)((int)f >> 31) | 0x80000000u)) >> 1) & ~2047u) | (r0 + (i & 1) * 16 + (i >> 1));
        }
        t1 = max(max(ky[0], ky[1]), max(ky[2], ky[3])), t2 = max(min(max(ky[0], ky[1]), max(ky[2], ky[3])), max(min(ky[0], ky[1]), min(ky[2], ky[3])));
        asm volatile("" : "+v"(t1), "+v"(t2));
        }
        if (k == N_K + N_D && rnp) {
        TOP2(0xB1); TOP2(0x4E); TOP2(0x141); TOP2(0x140);
        TOP2S(__builtin_amdgcn_permlane16_swap);
            if (lane == 0) asm volatile("global_store_dwordx2 %0, %1, %2 sc1" ::"v"(O_K2 + nt * 16), "v"(u32x2{t1, t2}), "s"(ws) : "memory");
            TS(34);
        }
        if (k == N_K && rnp) GP4(O_NP + nt * 4096 + lane * 16, "");
        if (k == B_RAT + 2 && rq_) {
          WAIT_V(sch.rcnt);
          asm volatile("" : "+v"(gp[0]), "+v"(gp[1]));
          POLL(__all(max(max4(gp[0]), max4(gp[1])) != SENT), 2, QRLD(VW0));
          u32x4 a;
#pragma unroll
          for (int c = 0; c < 2; ++c) {
            const f32x4 t = __builtin_bit_cast(f32x4, gp[c]);
            const float r = dpp_add<0x128>(swap16_add(swap32_add(t[0], t[2]), swap32_add(t[1], t[3])));
            a[2 * c] = pack2(r, r);
            a[2 * c + 1] = pack2(r - bflo(a[2 * c]), r - bflo(a[2 * c]));
          }
          if ((lane & 8) == 0)
            asm volatile("global_store_dwordx4 %0, %1, %2 sc1\n\ts_nop 1" ::"v"(O_QT + ((b - NPART) * 32 + (lane & 1) * 16 + ((lane >> 1) & 3) * 4 + g4) * 16), "v"(nsent4(a)), "s"(ws) : "memory");
        }
        if (k == B_QAT + B_QD) {
          asm volatile("s_waitcnt vmcnt(%1)" : "+v"(mq) : "n"(sch.qcnt) : "memory");
          POLL(__all(max4(mq) != SENT), 2, QLD(VW0));
          if (hw < 2) asm volatile("ds_write_b128 %0, %1" ::"v"(qa), "v"(mq) : "memory");
          else asm volatile("ds_write2_b32 %0, %1, %1 offset1:1" ::"v"(qa), "v"(mq[0]) : "memory");
        }
        if (k == CPF_B && hw == 2) CPFL();
        if (k == B_RAT) {
          if (rdc) {
            RRLD("");
            asm volatile("ds_read_b32 %0, %1\n\ts_waitcnt lgkmcnt(0)" : "=v"(wq) : "v"(wqa) : "memory");
          }
          if (rq_) QRLD("");
        }
        if (k == X_K) {
          const unsigned v4[4] = {xo, (xo ^ 64) + 8192 - 1024, (xo ^ 128) + 16384 - 2048, (xo ^ 192) + 24576 - 3072};
          unsigned l2 = lbase;
          asm volatile("" : "+s"(l2));
          GLDS4(l2 * 2 - lds0 + STG + 4096, v4, x);
        }
        if (k == V_T + 1 || k == N_K + 1) SKEWPT();
        if (k == B_QAT) QLD("");
        if (k == B_MAT) MODLD();
        if (k == B_WK) { DLD(8 - B_WN, 8); }
      }
      if (b >= NPART) WAIT_V(0);
      MODW(sch.mcnt);
      TS(30);
      SKEWPT();
      POLL(__all(max(max4(m[0]), max4(m[2])) != SENT), 4, MODLD(); MODW(0));
      *reinterpret_cast<u32x4*>(shm + SPARE + ma) = m[0];
      if (hw == 0) {
        asm volatile("ds_write_b128 %0, %1" ::"v"(ma2), "v"(m[2]) : "memory");
      } else if (hw == 2) {
        int d = max(max((int)(m[2][0] - ts0), (int)(m[2][1] - ts0)), max((int)(m[2][2] - ts0), (int)(m[2][3] - ts0)));
        d = max(d, (int)dpp_u<0xB1>(d));
        d = max(d, (int)dpp_u<0x4E>(d));
        d = max(d, (int)dpp_u<0x141>(d));
        d = max(d, (int)dpp_u<0x140>(d));
        const unsigned hh = ts0 + d;
        ssel[7] = hh;
        if ((int)(tick() - (unsigned)__builtin_amdgcn_readfirstlane((int)hh) - T_K) >= 0) K2LD("");
      } else {
        const f32x4 v = __builtin_bit_cast(f32x4, m[2]);
        const f32x2 c = {v[0] + v[1], v[2] + v[3]};
        const float s4 = dpp_add<0x4E>(dpp_add<0xB1>(c[0] + c[1]));
        if (lane < 8) asm volatile("ds_write_b64 %0, %1" ::"v"(ma2), "v"(lane < 4 ? c : f32x2{s4, s4}) : "memory");
      }
    }
    if (hw != 2) __builtin_amdgcn_s_setprio(0);
    TS(17);
    BT(48);
    LBAR();
    BTE();
    TS(18);
    if (hw == 2) {
      WAIT_V(0);
      asm volatile("" : "+v"(kk));
      if (__any(max(kk[0], kk[1]) == SENT)) {
        nap_tick((unsigned)__builtin_amdgcn_readfirstlane((int)ssel[7]) + T_K);
        POLL2(max(kk[0], kk[1]) == SENT, K_S, PCAP, K2LD(VW0));
      }
      TS(35);
      const unsigned me = lane < 32 ? kk[0] : kk[1], ot = (lane & 16) ? kk[1] : kk[0];
      unsigned rk = ot > me;
      sfor<15>([&](auto r) __attribute__((always_inline)) { rk += dpp_u<0x121 + decltype(r)::value>(ot) > me; });
      auto rs_ = __builtin_amdgcn_permlane16_swap(rk, rk, false, false);
      rk = rs_[0] + rs_[1];
      if (rk < 8u) {
        *reinterpret_cast<unsigned*>(shm + LST + rk * 4) = 0x8ffeu - (me & 2047u) * 2;
        if ((lane & 15) == nt) *reinterpret_cast<unsigned*>(shm + SLT + (me & 127u) * 4) = rk + 1;
      }
      asm volatile("" ::: "memory");
      ssel[20] = 1u;
    }
    TS(19);
    unsigned sv, one = 1u, kmk = ~0u;
    RETIRE();
    const u32x4 cbq = *reinterpret_cast<const u32x4*>(shm + SPARE + 4096 + kca), sbq = *reinterpret_cast<const u32x4*>(shm + SPARE + 2048 + kca);
    f32x4 cb2[2] = {lo4(cbq), hi4(cbq)}, sb2[2] = {lo4(sbq), hi4(sbq)};
    float sw = cwj[8], cj8 = cwj[lane & 7];
    asm volatile("" : "+v"(cb2[0]), "+v"(cb2[1]), "+v"(sb2[0]), "+v"(sb2[1]), "+v"(sw), "+v"(cj8), "+v"(one), "+v"(kmk));
    const unsigned th = (unsigned)__builtin_amdgcn_readfirstlane((int)ssel[7]);
    const char* wb = ws;
#define P2PUT(i) if (__builtin_expect(wb != ws, 0)) { OWNL(); WAIT_V(0); if (lane == 0) asm volatile("global_store_dwordx2 %0, %1, %2 sc1" ::"v"(O_F2 + (i)*8192 + (g * 4 + hw) * 8 + lane), "v"(tag), "s"(ws) : "memory"); }
#define P2GET(o) if (__builtin_expect(wb != ws, 0)) { OWNL(); u64 f; POLL2(f != tag, 4, PCAP, asm volatile("global_load_dwordx2 %0, %1, %2 sc1" VW0 : "=&v"(f) : "v"(O_F2 + (o)), "s"(ws) : "memory")); }
#pragma unroll 1
    for (;;) {
    TS(20);
    if (wb == ws) {
      unsigned r_;
      POLL2(sv == 0u, 2, 1 << 26, asm volatile("ds_read_b32 %0, %2 offset:80\n\tds_read_b32 %1, %3\n\ts_waitcnt lgkmcnt(0)" : "=&v"(sv), "=&v"(r_) : "v"(sfa), "v"(rk2) : "memory"));
      rk2 = r_;
    }
    TS(21);
    nap_tick(th + T_C3 - 10);
    {
      u32x4 dn;
      POLL2((dn[0] & dn[1] & dn[2] & dn[3]) == 0u, 1, 1 << 26, asm volatile("ds_read_b128 %0, %1 offset:96\n\ts_waitcnt lgkmcnt(0)" : "=v"(dn) : "v"(sfa) : "memory"));
    }
    __builtin_amdgcn_s_setprio(H_P);
    wait_tick(th + T_C3);
    TS(36);
    u32x4 st, xv_;
    POLL2(max(max4(xv_), max4(st)) == SENT, 0, PCAP,
          asm volatile("global_load_dwordx4 %0, %2, %4 sc1\n\tglobal_load_dwordx4 %1, %3, %4 sc1" VW0 : "=&v"(st), "=&v"(xv_) : "v"(sto), "v"(xo_), "s"(ws) : "memory"));
    TS(22);
    SKEWPT();
#define SUM8(v) dpp_add<0x141>(dpp_add<0x4E>(dpp_add<0xB1>(v)))
    const f32x4 sf = __builtin_bit_cast(f32x4, st);
    const float mean = SUM8(sf[0]) * 0.125f, dm = sf[0] - mean;
    const float rstd = __builtin_amdgcn_rsqf(SUM8(sf[1] + 128.0f * (dm * dm)) * (1.0f / D) + 1e-5f);
    asm volatile("ds_write_b128 %0, %1\n\tds_write_b32 %2, %3 offset:64" ::"v"(hwa),
                 "v"(pk8(((lo4(xv_) - mean) * rstd) * cb2[0] + sb2[0], ((hi4(xv_) - mean) * rstd) * cb2[1] + sb2[1])), "v"(sfw), "v"(one) : "memory");
    TS(3);
    u32x4 ha[8], fg;
#define HSLD(W)                                                                                                                             \
  asm volatile(                                                                                                                             \
      "ds_read_b128 %8, %11 offset:64\n\tds_read_b128 %0, %9\n\tds_read_b128 %1, %10 offset:512\n\tds_read_b128 %2, %9 offset:1024\n\t"       \
      "ds_read_b128 %3, %10 offset:1536\n\tds_read_b128 %4, %9 offset:2048\n\tds_read_b128 %5, %10 offset:2560\n\t"                          \
      "ds_read_b128 %6, %9 offset:3072\n\tds_read_b128 %7, %10 offset:3584" W                                                               \
      : "=&v"(ha[0]), "=&v"(ha[1]), "=&v"(ha[2]), "=&v"(ha[3]), "=&v"(ha[4]), "=&v"(ha[5]), "=&v"(ha[6]), "=&v"(ha[7]), "=&v"(fg)           \
      : "v"(hra), "v"(hrb), "v"(sfa)                                                                                                        \
      : "memory")
    unsigned key = (mkkey(rstd * SUM8(sf[2] + dm * cj8) + sw) | rk2) & kmk;
    asm volatile("" : "+v"(key));
    if (H_O == 0) HSLD("");
    unsigned t1 = key, t2 = 0u;
    TOP2(0x140);
    if (H_O == 1) { asm volatile("" : "+v"(t1), "+v"(t2)); HSLD(""); asm volatile("" : "+v"(t1), "+v"(t2)); }
    TOP2S(__builtin_amdgcn_permlane16_swap);
    if (H_O == 2) { asm volatile("" : "+v"(t1), "+v"(t2)); HSLD(""); asm volatile("" : "+v"(t1), "+v"(t2)); }
    TOP2S(__builtin_amdgcn_permlane32_swap);
    const int l1 = __builtin_ctzll(__builtin_amdgcn_ballot_w64(key == t1)), l2 = __builtin_ctzll(__builtin_amdgcn_ballot_w64(key == t2));
    unsigned mq[6];
    asm volatile("s_bfe_i32 %0, %6, 0x10003\n\ts_bfe_i32 %1, %7, 0x10003\n\ts_bfe_i32 %2, %6, 0x10004\n\ts_bfe_i32 %3, %7, 0x10004\n\t"
                 "s_bfe_i32 %4, %6, 0x10005\n\ts_bfe_i32 %5, %7, 0x10005\n\ts_xor_b32 %4, %4, 0xffff\n\ts_xor_b32 %5, %5, 0xffff"
                 : "=&s"(mq[0]), "=&s"(mq[1]), "=&s"(mq[2]), "=&s"(mq[3]), "=&s"(mq[4]), "=&s"(mq[5]) : "s"(l1), "s"(l2) : "scc");
    const u64 m0 = mk64(mq[1], mq[0]), m1 = mk64(mq[3], mq[2]), m2 = mk64(mq[5], mq[4]);
    asm volatile("s_waitcnt lgkmcnt(0)" : W8(ha), "+v"(fg)::"memory");
    TS(2);
    if (b < NPART) {
#define HBAD ((fg[0] & fg[1] & fg[2] & fg[3]) != one)
      if (__any(HBAD)) POLL2(HBAD, 0, 1 << 26, HSLD("\n\ts_waitcnt lgkmcnt(0)"));
      TS(23);
      f32x4 dg = {0.f, 0.f, 0.f, 0.f}, du = {0.f, 0.f, 0.f, 0.f};
#pragma unroll
      for (int f = 0; f < 8; ++f) {
        dg = __builtin_amdgcn_mfma_f32_16x16x32_bf16(BF8(ha[f]), BF8(gT[f]), dg, 0, 0, 0);
        du = __builtin_amdgcn_mfma_f32_16x16x32_bf16(BF8(ha[f]), BF8(uT[f]), du, 0, 0, 0);
      }
      const bool s0 = __builtin_amdgcn_inverse_ballot_w64(m0), s1 = __builtin_amdgcn_inverse_ballot_w64(m1);
      if (__builtin_amdgcn_inverse_ballot_w64(m2))
        asm volatile("global_store_dword %0, %1, %3 sc1 ; ALWAYS\n\tglobal_store_dword %0, %2, %3 offset:512 sc1" ::"v"(gso),
                     "v"(nsent(s1 ? (s0 ? dg[3] : dg[2]) : (s0 ? dg[1] : dg[0]))), "v"(nsent(s1 ? (s0 ? du[3] : du[2]) : (s0 ? du[1] : du[0]))), "s"(wb) : "memory");
    }
    if (hw == 0 && wb == ws) asm volatile("ds_write_b64 %0, %1" ::"v"(sfa), "v"(u32x2{t1, t2}) : "memory");
    if (b < NPART) {
      TS(24);
      asm volatile("" : W8(wdv), "+v"(qm[0]), "+v"(qm[1]), "+v"(qm[2]), "+v"(qm[3])::"memory");
      TR8(dT, wdv);
      P2PUT(0);
      if (hw == 0) {
        u32x2 rr[8];
        if (E_D) __builtin_amdgcn_s_sleep(E_D);
        TS(39);
        P2GET(((((lane & 16) ? gl1 : gl0) - O_GU) >> 12) * 128 + (lane & 15) * 8);
#define MX2(i) max(max(rr[i][0], rr[i][1]), max(rr[i + 1][0], rr[i + 1][1]))
        POLL2(max(max(MX2(0), MX2(2)), max(MX2(4), MX2(6))) == SENT, 1, PCAP,
              TS(40);
              asm volatile("global_load_dwordx2 %0, %8, %10 sc1\n\tglobal_load_dwordx2 %1, %8, %10 offset:1024 sc1\n\t"
                           "global_load_dwordx2 %2, %8, %10 offset:2048 sc1\n\tglobal_load_dwordx2 %3, %8, %10 offset:3072 sc1\n\t"
                           "global_load_dwordx2 %4, %9, %10 sc1\n\tglobal_load_dwordx2 %5, %9, %10 offset:1024 sc1\n\t"
                           "global_load_dwordx2 %6, %9, %10 offset:2048 sc1\n\tglobal_load_dwordx2 %7, %9, %10 offset:3072 sc1" VW0
                           : "=&v"(rr[0]), "=&v"(rr[1]), "=&v"(rr[2]), "=&v"(rr[3]), "=&v"(rr[4]), "=&v"(rr[5]), "=&v"(rr[6]), "=&v"(rr[7])
                           : "v"(gl0), "v"(gl1), "s"(wb)
                           : "memory"));
        TS(8);
#define R2(i) __builtin_bit_cast(f32x2, rr[i])
        const f32x2 gs = (R2(0) + R2(1)) + (R2(2) + R2(3)), us = (R2(4) + R2(5)) + (R2(6) + R2(7));
        const unsigned gp = pack2(gs[0], gs[1]), up = pack2(us[0], us[1]);
        const unsigned sp = pack2(bflo(gp) * __builtin_amdgcn_rcpf(1.0f + __expf(-bflo(gp))), bfhi(gp) * __builtin_amdgcn_rcpf(1.0f + __expf(-bfhi(gp))));
        asm volatile("ds_write_b32 %0, %1\n\tds_write_b32 %2, %3 offset:24" ::"v"(awa), "v"(pack2(bflo(sp) * bflo(up), bfhi(sp) * bfhi(up))), "v"(sfa), "v"(one) : "memory");
      }
      TS(25);
      SKEWPT();
      POLL2(sv != one, 0, 1 << 26, asm volatile("ds_read_b32 %1, %3 offset:24\n\tds_read_b128 %0, %2\n\ts_waitcnt lgkmcnt(0)" : "=&v"(fg), "=&v"(sv) : "v"(ara), "v"(sfa) : "memory"));
      asm volatile("global_load_dwordx2 %0, %1, %2 sc1" : "=&v"(vd) : "v"(O_VD + (b & 31) * 128), "s"(ws) : "memory");
      const f32x2 dd = MV64(dT, fg);
      asm volatile("global_store_dwordx2 %0, %1, %2 sc1" ::"v"(pso), "v"(u32x2{nsent(dd[0]), nsent(dd[1])}), "s"(wb) : "memory");
      TS(6);
      P2PUT(1);
      asm volatile("s_waitcnt vmcnt(1)" : "+v"(vd)::"memory");
      if (__any(max(vd[0], vd[1]) == SENT)) VDLD(2);
      TS(5);
    } else if (hw == 3) {
      OWNL();
      u32x2 g0, g1;
      nap_tick(th + T_C4);
      POLL2(min(min(g0[0], g0[1]), min(g1[0], g1[1])) == 0u, 2, PCAP,
            asm volatile("global_load_dwordx2 %0, %2, %4 sc1\n\tglobal_load_dwordx2 %1, %3, %4 sc1" VW0
                         : "=&v"(g0), "=&v"(g1) : "v"(O_KEY + lane * 8 * KSTR), "v"(O_KEY + (lane + 64) * 8 * KSTR), "s"(ws) : "memory"));
      const unsigned k1 = t1, k2 = t2;
      t1 = max(g0[0], g1[0]), t2 = max(min(g0[0], g1[0]), max(g0[1], g1[1]));
      TOP2(0xB1); TOP2(0x4E); TOP2(0x141); TOP2(0x140);
      TOP2S(__builtin_amdgcn_permlane16_swap);
      TOP2S(__builtin_amdgcn_permlane32_swap);
      vd = u32x2{max(t1, k1), max(min(t1, k1), max(t2, k2))};
      if (lane == 0) asm volatile("global_store_dwordx2 %0, %1, %2 sc1\n\tds_write_b64 %3, %1 offset:8" ::"v"(O_VD + (b & 31) * 128), "v"(vd), "s"(ws), "v"(sfa) : "memory");
      TS(29);
      break;
    } else if (hw < 2) {
      OWNL();
      const int tok = b & 1, c0 = ((b - NPART) >> 1) * 64 + hw * 32 + (lane & 7) * 4, col = c0 + g4;
      const unsigned td = tick(), kk = tok ? t2 : t1;
      const int ln = tok ? l2 : l1;
      unsigned v;
      POLL2(v == SENT, 1, PCAP, asm volatile("global_load_dword %0, %1, %2 sc1" VW0 : "=&v"(v) : "v"(O_X1C + ((__builtin_amdgcn_readlane(xo_, ln) - O_X1C) >> 11) * 2048 + (col >> 1) * 4), "s"(ws) : "memory"));
      float x1 = (col & 1) ? bfhi(v) : bflo(v);
      float h2 = rbf(((x1 - __int_as_float(__builtin_amdgcn_readlane(__float_as_int(mean), ln))) * __int_as_float(__builtin_amdgcn_readlane(__float_as_int(rstd), ln))) * bf2f(svb[2][col]) + bf2f(svb[1][col]));
      float gb = bf2f(svb[3][col]), rsg = rbf(1.0f / (1.0f + __expf(-key_val(kk))));
      unsigned oo_ = (key_row(kk) * D + col) * 2, po = O_PART + ((lane >> 3) * 2048 + tok * 1024 + c0) * 4, p1 = po + 4 * 65536;
      asm volatile("" : "+v"(x1), "+v"(h2), "+v"(gb), "+v"(rsg), "+v"(oo_), "+v"(po), "+v"(p1));
      u32x4 y[7];
      nap_tick(td + F_T - 10);
      wait_tick(td + F_T);
      TS(37);
      P2GET(8192 + (min(lane, 55) * 16 + ((b - NPART) >> 1)) * 8);
      POLL2(max(max(max(max4(y[0]), max4(y[1])), max(max4(y[2]), max4(y[3]))), max(max(max4(y[4]), max4(y[5])), max4(y[6]))) == SENT || vd[1] == 0u, 0, PCAP,
            TS(38);
            asm volatile("global_load_dwordx4 %0, %7, %9 sc1\n\tglobal_load_dwordx4 %1, %7, %10 sc1\n\tglobal_load_dwordx4 %2, %7, %11 sc1\n\t"
                         "global_load_dwordx4 %3, %7, %12 sc1\n\tglobal_load_dwordx4 %4, %8, %9 sc1\n\tglobal_load_dwordx4 %5, %8, %10 sc1\n\t"
                         "global_load_dwordx4 %6, %8, %11 sc1"
                         : "=&v"(y[0]), "=&v"(y[1]), "=&v"(y[2]), "=&v"(y[3]), "=&v"(y[4]), "=&v"(y[5]), "=&v"(y[6])
                         : "v"(po), "v"(p1), "s"(wb), "s"(wb + 65536), "s"(wb + 131072), "s"(wb + 196608)
                         : "memory");
            asm volatile("s_waitcnt vmcnt(2)\n\tds_read_b64 %7, %8 offset:8\n\ts_waitcnt vmcnt(0) lgkmcnt(0)"
                         : "+v"(y[0]), "+v"(y[1]), "+v"(y[2]), "+v"(y[3]), "+v"(y[4]), "+v"(y[5]), "+v"(y[6]), "=&v"(vd) : "v"(sfa) : "memory"));
      TS(28);
#define YF(i) __builtin_bit_cast(f32x4, y[i])
      const f32x4 t = ((YF(0) + YF(1)) + (YF(2) + YF(3))) + ((YF(4) + YF(5)) + YF(6));
      const float sum = dpp_add<0x128>(swap16_add(swap32_add(t[0], t[2]), swap32_add(t[1], t[3])));
      asm volatile("global_store_short %0, %1, %2" ::"v"(oo_), "v"(pack2(x1 + gb * (wb == ws && kk < vd[1] ? h2 : rbf(h2 + rbf(rbf(sum) * rsg))), 0.f)), "s"(out) : "memory");
      TS(15);
    } else {
      break;
    }
    if (__builtin_expect(wb != ws || __all(vd[1] == t2), 1)) break;
    OWNL();
    const unsigned tk = (lane & 8) ? vd[1] : vd[0], ns = tk == t1 ? l1 >> 3 : 8 + ((lane >> 3) & 1);
    rk2 = tk & 0xffffu, kmk = lane < 16 ? ~0u : 0u, one = 3u;
    xo_ = O_X1C + ns * 2048 + kca, sto = O_ST2 + ns * 128 + (lane & 7) * 16;
    wb = ws + O_P2;
    }
    TSE();
  }
}

extern "C" void kernel_launch(void* const* d_in, const int* in_sizes, int n_in, void* d_out, int out_size, void* d_ws, size_t ws_size,
                              hipStream_t stream) {
  const u16* x = (const u16*)d_in[0];
  const u16* wsr = (const u16*)d_in[1];
  const u16* wsi = (const u16*)d_in[2];
  const u16* whr = (const u16*)d_in[3];
  const u16* whi = (const u16*)d_in[4];
  const u16* w_r = (const u16*)d_in[6];
  const u16* w_gate = (const u16*)d_in[7];
  const u16* w_up = (const u16*)d_in[8];
  const u16* w_down = (const u16*)d_in[9];
  const u16* c = (const u16*)d_in[10];
  const u16* w_mod = (const u16*)d_in[11];
  u16* out = (u16*)d_out;
  k_main<<<256, 512, 0, stream>>>(x, (char*)d_ws, wsr, wsi, c, w_mod, whr, whi, w_r, w_gate, w_up, w_down, out);
}
```
